```python
import torch

def _softmax(x, axis=-1):
    return torch.softmax(x, dim=axis)

def _gelu(x):
    return torch.nn.functional.gelu(x, approximate='tanh')

def _silu(x):
    return torch.nn.functional.silu(x)

def _sigmoid(x):
    return torch.sigmoid(x)

def _layernorm(x, axis=-1, eps=1e-05):
    m = x.mean(dim=axis, keepdim=True)
    v = x.var(dim=axis, keepdim=True, unbiased=False)
    return (x - m) / torch.sqrt(v + eps)

def _scatter_add(tgt, idx, src, axis=0):
    return tgt.clone().index_add_(axis, idx, src)

def _segment_sum(src, seg, n):
    out = torch.zeros((n,) + src.shape[1:], dtype=src.dtype)
    return out.index_add_(0, seg, src)

def _pad(x, pw):
    flat = tuple((v for pair in reversed(pw) for v in pair))
    return torch.nn.functional.pad(x, flat)
kernel_name = 'adaLN_cond__sliding_local_dilated__glu_gated_swiglu__prompt'

def setup_inputs():

    _w = lambda a: (lambda t: t.to(torch.bfloat16) if t.is_floating_point() else t)(torch.as_tensor(a))

    def _A_setup():
        B, S, D = (1, 2048, 512)
        return {'x': _w((lambda *s: torch.randn(*s, dtype=torch.bfloat16) * 0.05)(B, S, D)), 'wqkv': _w((lambda *s: torch.randn(*s, dtype=torch.bfloat16) * 0.1)(D, 3 * D)), 'wo': _w((lambda *s: torch.randn(*s, dtype=torch.bfloat16) * 0.1)(D, D))}
    _A_in = _A_setup()

    def _B_setup():
        B, S, D, F = (1, 2048, 512, 1376)
        return {'x': _w((lambda *s: torch.randn(*s, dtype=torch.bfloat16) * 0.05)(B, S, D)), 'w1': _w((lambda *s: torch.randn(*s, dtype=torch.bfloat16) * 1.6)(D, F) * (2.0 / D) ** 0.5), 'w3': _w((lambda *s: torch.randn(*s, dtype=torch.bfloat16) * 1.6)(D, F) * (2.0 / D) ** 0.5), 'w2': _w((lambda *s: torch.randn(*s, dtype=torch.bfloat16) * 1.6)(F, D) * (2.0 / F) ** 0.5)}
    _B_in = _B_setup()
    return {'A_x': _A_in['x'], 'A_wqkv': _A_in['wqkv'], 'A_wo': _A_in['wo'], 'B_x': _B_in['x'], 'B_w1': _B_in['w1'], 'B_w3': _B_in['w3'], 'B_w2': _B_in['w2'], 'c': _w((lambda *s: torch.randn(*s, dtype=torch.bfloat16) * 0.2)(1, 512) * 8.0), 'w_mod': _w((lambda *s: torch.randn(*s, dtype=torch.bfloat16) * 0.2)(512, 6 * 512) * 512 ** (-0.5) * 8.0)}

def reference(A_x, A_wqkv, A_wo, B_x, B_w1, B_w3, B_w2, c, w_mod):

    def _A_ref(x, wqkv, wo):
        B, S, D = x.shape
        H = 32
        Dh = D // H
        W, dil = (8, 2)
        qkv = (lambda x, *d: x.permute(*d))((x @ wqkv).reshape(B, S, 3, H, Dh), 2, 0, 3, 1, 4)
        q, k, v = (qkv[0], qkv[1], qkv[2])
        scale = Dh ** (-0.5)
        pos = torch.arange(S)
        off = torch.arange(W) * dil
        idx = pos[:, None] - off[None, :]
        mask = idx >= 0
        idx_c = torch.where(mask, idx, 0).reshape(-1)
        kg = k[:, :, idx_c, :].reshape(B, H, S, W, Dh)
        vg = v[:, :, idx_c, :].reshape(B, H, S, W, Dh)
        logits = torch.einsum('bhsd,bhswd->bhsw', q, kg) * scale
        logits = torch.where(mask[None, None, :, :], logits, -1000000000.0)
        attn = _softmax(logits, axis=-1)
        o = torch.einsum('bhsw,bhswd->bhsd', attn, vg)
        return (lambda x, *d: x.permute(*d))(o, 0, 2, 1, 3).reshape(B, S, D) @ wo

    def _B_ref(x, w1, w3, w2):
        gate = torch.einsum('bsd,df->bsf', x, w1)
        up = torch.einsum('bsd,df->bsf', x, w3)
        h = _silu(gate) * up
        out = torch.einsum('bsf,fd->bsd', h, w2)
        return out
    x = A_x
    mod = (_silu(c) @ w_mod).reshape(c.shape[0], 6, 1, -1)
    sa, ca, ga, sb, cb, gb = (mod[:, 0].clone(), mod[:, 1].clone(), mod[:, 2].clone(), mod[:, 3].clone(), mod[:, 4].clone(), mod[:, 5].clone())
    h = _layernorm(x, axis=-1) * (1 + ca) + sa
    x = x + ga * _A_ref(h, A_wqkv, A_wo)
    h = _layernorm(x, axis=-1) * (1 + cb) + sb
    x = x + gb * _B_ref(h, B_w1, B_w3, B_w2)
    return x
```

```cpp
#include <hip/hip_runtime.h>
#include <hip/hip_bf16.h>
#include <stdint.h>

typedef unsigned short u16;
static __device__ __forceinline__ float bf2f(u16 b) { return __builtin_bit_cast(float, (unsigned)b << 16); }
static __device__ __forceinline__ u16 f2bf(float f) { __hip_bfloat16 h = __float2bfloat16(f); return __builtin_bit_cast(u16, h); }

constexpr int S = 2048, D = 512, F = 1376, H = 32, DH = 16;

__global__ void k_mod(const u16* c, const u16* wmod, float* mod) {
  int n = blockIdx.x * blockDim.x + threadIdx.x;
  if (n >= 6 * D) return;
  float acc = 0.f;
  for (int k = 0; k < D; ++k) {
    float cv = bf2f(c[k]);
    float s = cv / (1.f + __expf(-cv));
    acc += s * bf2f(wmod[(size_t)k * 6 * D + n]);
  }
  mod[n] = acc;
}
template <bool F32IN>
__global__ void k_ln(const void* xin, const float* shift, const float* scale, u16* h) {
  int row = blockIdx.x;
  __shared__ float red[2][8];
  int t = threadIdx.x;
  float v = F32IN ? ((const float*)xin)[(size_t)row * D + t] : bf2f(((const u16*)xin)[(size_t)row * D + t]);
  float s = v;
  for (int o = 32; o; o >>= 1) s += __shfl_xor(s, o);
  if ((t & 63) == 0) red[0][t >> 6] = s;
  __syncthreads();
  float m = 0; for (int i = 0; i < 8; ++i) m += red[0][i];
  m /= D;
  float d = v - m;
  float q = d * d;
  for (int o = 32; o; o >>= 1) q += __shfl_xor(q, o);
  if ((t & 63) == 0) red[1][t >> 6] = q;
  __syncthreads();
  float var = 0; for (int i = 0; i < 8; ++i) var += red[1][i];
  var /= D;
  float r = d / sqrtf(var + 1e-5f);
  h[(size_t)row * D + t] = f2bf(r * (1.f + scale[t]) + shift[t]);
}
__global__ void k_gemm(const u16* A, const u16* B, float* C, int M, int N, int K) {
  __shared__ float As[16][17], Bs[16][17];
  int tx = threadIdx.x & 15, ty = threadIdx.x >> 4;
  int row = blockIdx.y * 16 + ty, col = blockIdx.x * 16 + tx;
  float acc = 0;
  for (int k0 = 0; k0 < K; k0 += 16) {
    As[ty][tx] = bf2f(A[(size_t)row * K + k0 + tx]);
    Bs[ty][tx] = bf2f(B[(size_t)(k0 + ty) * N + col]);
    __syncthreads();
    for (int k = 0; k < 16; ++k) acc += As[ty][k] * Bs[k][tx];
    __syncthreads();
  }
  C[(size_t)row * N + col] = acc;
}
__global__ void k_attn(const float* qkv, u16* o) {
  int idx = blockIdx.x * blockDim.x + threadIdx.x;
  int s = idx / H, h = idx % H;
  if (s >= S) return;
  float q[DH];
  for (int d = 0; d < DH; ++d) q[d] = bf2f(f2bf(qkv[(size_t)s * 3 * D + h * DH + d]));
  float lg[8]; float mx = -1e30f;
  for (int w = 0; w < 8; ++w) {
    int j = s - 2 * w;
    if (j >= 0) {
      float a = 0;
      for (int d = 0; d < DH; ++d) a += q[d] * bf2f(f2bf(qkv[(size_t)j * 3 * D + D + h * DH + d]));
      lg[w] = a * 0.25f;
    } else lg[w] = -1e9f;
    mx = fmaxf(mx, lg[w]);
  }
  float sum = 0;
  for (int w = 0; w < 8; ++w) { lg[w] = __expf(lg[w] - mx); sum += lg[w]; }
  for (int d = 0; d < DH; ++d) {
    float a = 0;
    for (int w = 0; w < 8; ++w) {
      int j = s - 2 * w; if (j < 0) j = 0;
      a += lg[w] * bf2f(f2bf(qkv[(size_t)j * 3 * D + 2 * D + h * DH + d]));
    }
    o[(size_t)s * D + h * DH + d] = f2bf(a / sum);
  }
}
__global__ void k_res1(const u16* x, const float* y, const float* g, float* x1) {
  int i = blockIdx.x * blockDim.x + threadIdx.x;
  x1[i] = bf2f(x[i]) + g[i % D] * y[i];
}
__global__ void k_glu(const float* g, const float* u, u16* hh) {
  int i = blockIdx.x * blockDim.x + threadIdx.x;
  float a = g[i];
  hh[i] = f2bf(a / (1.f + __expf(-a)) * u[i]);
}
__global__ void k_res2(const float* x1, const float* y, const float* g, u16* out) {
  int i = blockIdx.x * blockDim.x + threadIdx.x;
  out[i] = f2bf(x1[i] + g[i % D] * y[i]);
}

extern "C" void kernel_launch(void* const* d_in, const int* in_sizes, int n_in,
                              void* d_out, int out_size, void* d_ws, size_t ws_size,
                              hipStream_t stream) {
  const u16* x = (const u16*)d_in[0];
  const u16* wqkv = (const u16*)d_in[1];
  const u16* wo = (const u16*)d_in[2];
  const u16* w1 = (const u16*)d_in[4];
  const u16* w3 = (const u16*)d_in[5];
  const u16* w2 = (const u16*)d_in[6];
  const u16* c = (const u16*)d_in[7];
  const u16* wmod = (const u16*)d_in[8];
  char* ws = (char*)d_ws;
  float* mod = (float*)ws; ws += 1 << 20;
  u16* h = (u16*)ws; ws += 4 << 20;
  float* qkv = (float*)ws; ws += 16 << 20;
  u16* o = (u16*)ws; ws += 4 << 20;
  float* y = (float*)ws; ws += 8 << 20;
  float* x1 = (float*)ws; ws += 8 << 20;
  float* g = (float*)ws; ws += 16 << 20;
  float* u = (float*)ws; ws += 16 << 20;
  u16* hh = (u16*)ws; ws += 8 << 20;
  k_mod<<<6 * D / 64, 64, 0, stream>>>(c, wmod, mod);
  k_ln<false><<<S, 512, 0, stream>>>(x, mod + 0 * D, mod + 1 * D, h);
  k_gemm<<<dim3(3 * D / 16, S / 16), 256, 0, stream>>>(h, wqkv, qkv, S, 3 * D, D);
  k_attn<<<S * H / 256, 256, 0, stream>>>(qkv, o);
  k_gemm<<<dim3(D / 16, S / 16), 256, 0, stream>>>(o, wo, y, S, D, D);
  k_res1<<<S * D / 256, 256, 0, stream>>>(x, y, mod + 2 * D, x1);
  k_ln<true><<<S, 512, 0, stream>>>(x1, mod + 3 * D, mod + 4 * D, h);
  k_gemm<<<dim3(F / 16, S / 16), 256, 0, stream>>>(h, w1, g, S, F, D);
  k_gemm<<<dim3(F / 16, S / 16), 256, 0, stream>>>(h, w3, u, S, F, D);
  k_glu<<<S * F / 256, 256, 0, stream>>>(g, u, hh);
  k_gemm<<<dim3(D / 16, S / 16), 256, 0, stream>>>(hh, w2, y, S, D, F);
  k_res2<<<S * D / 256, 256, 0, stream>>>(x1, y, mod + 5 * D, (u16*)d_out);
}
```

```cpp
#include <hip/hip_runtime.h>
#include <stdint.h>

#define KDBG 0
#define XMAP 2

#define DI __device__ __forceinline__
#define GAS __attribute__((address_space(1)))
#define LAS __attribute__((address_space(3)))
typedef unsigned u32;
typedef unsigned short u16;
typedef unsigned long long u64;
typedef u32 v4u __attribute__((ext_vector_type(4)));
typedef u32 v2u __attribute__((ext_vector_type(2)));
typedef float f32x4 __attribute__((ext_vector_type(4)));
typedef float f32x2 __attribute__((ext_vector_type(2)));
typedef __bf16 bf16x8 __attribute__((ext_vector_type(8)));
typedef __bf16 bf16x2 __attribute__((ext_vector_type(2)));
typedef short s16x4 __attribute__((ext_vector_type(4)));
typedef __amdgpu_buffer_rsrc_t rsrc_t;
typedef GAS u32 gu32;
typedef GAS u64 gu64;
#define RLX_AGENT __ATOMIC_RELAXED, __HIP_MEMORY_SCOPE_AGENT

namespace {
constexpr int S_ = 2048, D_ = 512, F_ = 1376;
constexpr u32 WS_EPOCH = 0;
constexpr u32 WS_MODG = 4096;
constexpr u32 WS_FLAG = 32768;
constexpr u32 WS_STAT = 262144;
constexpr u32 WS_O = 1u << 20;
constexpr u32 WS_X1 = 3u << 20;
constexpr u32 WS_HH = 5u << 20;
constexpr u32 WS_DBG = 16u << 20;
constexpr u32 WS_BYTES = 32u << 20;
constexpr int L_XY = 0;
constexpr int L_Z = 81920;
constexpr int L_MISC = 155648;
constexpr int L_TOTAL = 163840;
constexpr int L_RED = 73728;
constexpr int L_CW = 74752;
constexpr int L_MODA = L_MISC;
constexpr int L_CNT = L_MISC + 4096;
constexpr int L_GAB = L_MISC + 4096 + 256;
constexpr u32 SPIN_CAP = 1u << 18;

DI f32x4 mfma32(v4u a, v4u b, f32x4 c) {
  return __builtin_amdgcn_mfma_f32_16x16x32_bf16(__builtin_bit_cast(bf16x8, a), __builtin_bit_cast(bf16x8, b), c, 0, 0, 0);
}
DI f32x4 mfma16(v2u a, v2u b, f32x4 c) {
  return __builtin_amdgcn_mfma_f32_16x16x16bf16_1k(__builtin_bit_cast(s16x4, a), __builtin_bit_cast(s16x4, b), c, 0, 0, 0);
}
DI u32 pk(float lo, float hi) { f32x2 v = {lo, hi}; return __builtin_bit_cast(u32, __builtin_convertvector(v, bf16x2)); }
DI v2u pk4(f32x4 v) { v2u r; r.x = pk(v.x, v.y); r.y = pk(v.z, v.w); return r; }
DI float blo(u32 u) { return __builtin_bit_cast(float, u << 16); }
DI float bhi(u32 u) { return __builtin_bit_cast(float, u & 0xffff0000u); }
DI float asf(u32 u) { return __builtin_bit_cast(float, u); }
DI u32 asu(float f) { return __builtin_bit_cast(u32, f); }
DI v2u trd(LAS char* p) { return __builtin_bit_cast(v2u, __builtin_amdgcn_ds_read_tr16_b64_v4i16((LAS s16x4*)p)); }
DI v4u cat(v2u a, v2u b) { v4u r; r.x = a.x; r.y = a.y; r.z = b.x; r.w = b.y; return r; }
DI void dma16(u32 lds, u32 voff, rsrc_t srd, u32 soff) {
  asm volatile("s_mov_b32 m0, %0\n\ts_nop 0\n\tbuffer_load_dwordx4 %1, %2, %3 offen lds" ::"s"(lds), "v"(voff), "s"(srd), "s"(soff) : "memory");
}
#define BAR() asm volatile("s_waitcnt lgkmcnt(0)\n\ts_barrier" ::: "memory")
#define VMW(n) asm volatile("s_waitcnt vmcnt(" #n ")" ::: "memory")
#define LGW() asm volatile("s_waitcnt lgkmcnt(0)" ::: "memory")
#define SCHED() __builtin_amdgcn_sched_barrier(0)
#define LD16(dst, voff, srd, soff, IMM) asm volatile("buffer_load_dwordx4 %0, %1, %2, %3 offen offset:" #IMM : "=v"(dst) : "v"(voff), "s"(srd), "s"(soff) : "memory")
#define LD16S(dst, voff, srd, soff, IMM) asm volatile("buffer_load_dwordx4 %0, %1, %2, %3 offen offset:" #IMM " sc1" : "=v"(dst) : "v"(voff), "s"(srd), "s"(soff) : "memory")
#define LD8(dst, voff, srd, soff, IMM) asm volatile("buffer_load_dwordx2 %0, %1, %2, %3 offen offset:" #IMM : "=v"(dst) : "v"(voff), "s"(srd), "s"(soff) : "memory")
#define LD8S(dst, voff, srd, soff, IMM) asm volatile("buffer_load_dwordx2 %0, %1, %2, %3 offen offset:" #IMM " sc1" : "=v"(dst) : "v"(voff), "s"(srd), "s"(soff) : "memory")
#define PIN(x) asm volatile("" : "+v"(x))

template <int CTRL> DI float dppf(float v) { return asf(__builtin_amdgcn_update_dpp(0, asu(v), CTRL, 0xf, 0xf, true)); }
DI float row16_sum(float v) {
  v += dppf<0x128>(v);
  v += dppf<0x124>(v);
  v += dppf<0x122>(v);
  v += dppf<0x121>(v);
  return v;
}
DI float x16_sum(float v) { auto r = __builtin_amdgcn_permlane16_swap(asu(v), asu(v), false, false); return asf(r[0]) + asf(r[1]); }
DI float x32_sum(float v) { auto r = __builtin_amdgcn_permlane32_swap(asu(v), asu(v), false, false); return asf(r[0]) + asf(r[1]); }
DI float x16_max(float v) { auto r = __builtin_amdgcn_permlane16_swap(asu(v), asu(v), false, false); return fmaxf(asf(r[0]), asf(r[1])); }
DI float x32_max(float v) { auto r = __builtin_amdgcn_permlane32_swap(asu(v), asu(v), false, false); return fmaxf(asf(r[0]), asf(r[1])); }
DI float wave_sum(float v) { return x32_sum(x16_sum(row16_sum(v))); }
DI int fresh_lane() { int l; asm volatile("v_mbcnt_lo_u32_b32 %0, -1, 0\n\tv_mbcnt_hi_u32_b32 %0, -1, %0" : "=v"(l)); return l; }
DI float rfl(float v) { return asf(__builtin_amdgcn_readfirstlane(asu(v))); }

struct Params {
  const u16 *x, *wqkv, *wo, *w1, *w3, *w2, *c, *wmod;
  u16* out;
  char* ws;
};

#define STAMP(i) do {} while (0)

DI void poll_flags(char* ws, int phase, int g, int h, u32 mask, u32 tag, int lane) {
  gu32* f = (gu32*)(ws + WS_FLAG + (((phase * 32 + g) * 2 + h) * 8 + (lane & 7)) * 128);
  const bool care = (mask >> (lane & 7)) & 1u;
  for (u32 sp = 0; sp < SPIN_CAP; ++sp) {
    const u32 v = __hip_atomic_load(f, RLX_AGENT);
    if (__all(!care || v == tag)) break;
  }
  VMW(0);
}
DI void signal_flag(char* ws, LAS char* L, int idx, int phase, int g, int h, int j, u32 tag, int lane) {
  if (lane == 0) {
    const u32 old = __hip_atomic_fetch_add((LAS u32*)(L + L_CNT + idx * 4), 1u, __ATOMIC_RELAXED, __HIP_MEMORY_SCOPE_WORKGROUP);
    if (old == 7u) __hip_atomic_store((gu32*)(ws + WS_FLAG + (((phase * 32 + g) * 2 + h) * 8 + j) * 128), tag, RLX_AGENT);
  }
}

DI v2u attend(v2u kp, v2u vp, v2u kc, v2u vc, v2u q, int c, int g4, bool prev_ok) {
  const f32x4 z = {0.f, 0.f, 0.f, 0.f};
  f32x4 sp = mfma16(kp, q, z);
  f32x4 sc = mfma16(kc, q, z);
  const float K2 = 0.25f * 1.4426950408889634f;
  float s[8];
#pragma unroll
  for (int j = 0; j < 4; ++j) {
    const int dc = c - (4 * g4 + j), dp = dc + 16;
    const bool okp = prev_ok && dp <= 14 && !(dp & 1);
    const bool okc = dc >= 0 && dc <= 14 && !(dc & 1);
    s[j] = okp ? sp[j] * K2 : -INFINITY;
    s[4 + j] = okc ? sc[j] * K2 : -INFINITY;
  }
  float m = fmaxf(fmaxf(fmaxf(s[0], s[1]), fmaxf(s[2], s[3])), fmaxf(fmaxf(s[4], s[5]), fmaxf(s[6], s[7])));
  m = x32_max(x16_max(m));
  float e[8], sum = 0.f;
#pragma unroll
  for (int j = 0; j < 8; ++j) { e[j] = __builtin_amdgcn_exp2f(s[j] - m); sum += e[j]; }
  sum = x32_sum(x16_sum(sum));
  const float inv = __builtin_amdgcn_rcpf(sum);
  v4u pf;
  pf.x = pk(e[0] * inv, e[1] * inv); pf.y = pk(e[2] * inv, e[3] * inv); pf.z = pk(e[4] * inv, e[5] * inv); pf.w = pk(e[6] * inv, e[7] * inv);
  const f32x4 o = mfma32(cat(vp, vc), pf, z);
  return pk4(o);
}

__global__ void __launch_bounds__(512, 2) fused_block(Params p) {
  extern __shared__ __attribute__((aligned(16))) char smem[];
  LAS char* L = (LAS char*)smem;
  const int tid = threadIdx.x, lane = tid & 63, w = __builtin_amdgcn_readfirstlane(tid >> 6);
  const int c16 = lane & 15, g4 = lane >> 4;
  const int b = blockIdx.x;
  int g, j;
  if (XMAP == 0) { g = (b & 7) * 4 + ((b >> 3) & 3); j = b >> 5; }
  else if (XMAP == 2) { const int x = b & 7, r = b >> 3; g = (x >> 1) * 8 + (r & 7); j = (x & 1) * 4 + (r >> 3); }
  else { const int x = b & 7, r = b >> 3; g = (x >> 2) * 16 + (r & 15); j = (x & 3) * 2 + (r >> 4); }
  const rsrc_t rX = __builtin_amdgcn_make_buffer_rsrc((void*)p.x, 0, S_ * D_ * 2, 0x00020000);
  const rsrc_t rQKV = __builtin_amdgcn_make_buffer_rsrc((void*)p.wqkv, 0, D_ * 3 * D_ * 2, 0x00020000);
  const rsrc_t rWO = __builtin_amdgcn_make_buffer_rsrc((void*)p.wo, 0, D_ * D_ * 2, 0x00020000);
  const rsrc_t rW1 = __builtin_amdgcn_make_buffer_rsrc((void*)p.w1, 0, D_ * F_ * 2, 0x00020000);
  const rsrc_t rW3 = __builtin_amdgcn_make_buffer_rsrc((void*)p.w3, 0, D_ * F_ * 2, 0x00020000);
  const rsrc_t rW2 = __builtin_amdgcn_make_buffer_rsrc((void*)p.w2, 0, F_ * D_ * 2, 0x00020000);
  const rsrc_t rC = __builtin_amdgcn_make_buffer_rsrc((void*)p.c, 0, D_ * 2, 0x00020000);
  const rsrc_t rMOD = __builtin_amdgcn_make_buffer_rsrc((void*)p.wmod, 0, D_ * 6 * D_ * 2, 0x00020000);
  const rsrc_t rWS = __builtin_amdgcn_make_buffer_rsrc((void*)p.ws, 0, WS_BYTES, 0x00020000);
  const rsrc_t rOUT = __builtin_amdgcn_make_buffer_rsrc((void*)p.out, 0, S_ * D_ * 2, 0x00020000);
  STAMP(0);

  u32 cv, ep; v4u wm0; v2u wm1;
  {
    const u32 vo = tid * 2;
    asm volatile("buffer_load_ushort %0, %1, %2, 0 offen" : "=v"(cv) : "v"(vo), "s"(rC) : "memory");
    const u32 vm = tid * 6144;
    const u32 sm = b * 24;
    LD16(wm0, vm, rMOD, sm, 0);
    LD8(wm1, vm, rMOD, sm, 16);
    const u32 vz = 0;
    asm volatile("buffer_load_dword %0, %1, %2, 0 offen sc1" : "=v"(ep) : "v"(vz), "s"(rWS) : "memory");
  }
  if (tid < 16) *(LAS u32*)(L + L_CNT + tid * 4) = 0u;
  v4u xr[10];
  {
    const u32 vo = lane * 16;
#pragma unroll
    for (int i = 0; i < 10; ++i) {
      int row = 64 * g - 16 + w + 8 * i; row = row < 0 ? 0 : row;
      const u32 so = (u32)row * 1024u;
      LD16(xr[i], vo, rX, so, 0);
    }
  }
  const int st_qd = lane >> 2, st_r = (st_qd & 3) + 4 * (st_qd >> 3), st_tp = (st_qd >> 2) & 1;
  const int st_col = st_tp * 64 + (((lane & 3) ^ ((w & 1) << 1)) << 4);
  const int rot1 = (g * 2) & 15;
  const u32 vB1 = st_r * 3072 + st_col;
  auto stage_b1 = [&](int pair, int ldsbase) {
#pragma unroll
    for (int i = 0; i < 3; ++i) {
      const int q = (w >> 2) + 2 * i, kk = q / 3, ty = q % 3;
      const int kphys = (2 * pair + kk + rot1) & 15;
      dma16(ldsbase + kk * 12288 + ty * 4096 + (w & 3) * 1024, vB1, rQKV, (kphys * 32 + (w & 3) * 8) * 3072 + ty * 1024 + j * 128);
    }
  };
  stage_b1(0, L_XY); stage_b1(1, L_XY + 24576); stage_b1(2, L_XY + 49152);
  stage_b1(5, L_Z); stage_b1(6, L_Z + 24576); stage_b1(7, L_Z + 49152);
  STAMP(1);
  asm volatile("s_waitcnt vmcnt(28)" : "+v"(cv), "+v"(wm0), "+v"(wm1), "+v"(ep)::"memory");
  STAMP(2);
  const u32 tag = __builtin_amdgcn_readfirstlane(ep) + 1u;
  {
    const float cf = blo(cv);
    const float sc = cf * __builtin_amdgcn_rcpf(1.f + __expf(-cf));
    float v[12];
    v[0] = blo(wm0.x); v[1] = bhi(wm0.x); v[2] = blo(wm0.y); v[3] = bhi(wm0.y); v[4] = blo(wm0.z); v[5] = bhi(wm0.z);
    v[6] = blo(wm0.w); v[7] = bhi(wm0.w); v[8] = blo(wm1.x); v[9] = bhi(wm1.x); v[10] = blo(wm1.y); v[11] = bhi(wm1.y);
#pragma unroll
    for (int i = 0; i < 12; ++i) v[i] *= sc;
    float u[6], t[3];
#pragma unroll
    for (int i = 0; i < 6; ++i) { auto r = __builtin_amdgcn_permlane32_swap(asu(v[i]), asu(v[i + 6]), false, false); u[i] = asf(r[0]) + asf(r[1]); }
#pragma unroll
    for (int i = 0; i < 3; ++i) { auto r = __builtin_amdgcn_permlane16_swap(asu(u[i]), asu(u[i + 3]), false, false); t[i] = row16_sum(asf(r[0]) + asf(r[1])); }
    if (c16 == 0) {
#pragma unroll
      for (int i = 0; i < 3; ++i) *(LAS float*)(L + L_RED + (w * 12 + 3 * g4 + i) * 4) = t[i];
    }
    BAR();
    if (tid < 12) {
      float s = 0.f;
#pragma unroll
      for (int i = 0; i < 8; ++i) s += *(LAS float*)(L + L_RED + (i * 12 + tid) * 4);
      __hip_atomic_store((gu64*)(p.ws + WS_MODG + (b * 12 + tid) * 8), ((u64)tag << 32) | asu(s), RLX_AGENT);
    }
  }
  STAMP(3);
  asm volatile("s_waitcnt vmcnt(18)" : "+v"(xr[0]), "+v"(xr[1]), "+v"(xr[2]), "+v"(xr[3]), "+v"(xr[4]), "+v"(xr[5]), "+v"(xr[6]), "+v"(xr[7]), "+v"(xr[8]), "+v"(xr[9])::"memory");
  float rs1[10], nm1[10];
#pragma unroll
  for (int i = 0; i < 10; ++i) {
    float f[8] = {blo(xr[i].x), bhi(xr[i].x), blo(xr[i].y), bhi(xr[i].y), blo(xr[i].z), bhi(xr[i].z), blo(xr[i].w), bhi(xr[i].w)};
    const float mean = wave_sum(((f[0] + f[1]) + (f[2] + f[3])) + ((f[4] + f[5]) + (f[6] + f[7]))) * (1.f / 512.f);
    float q = 0.f;
#pragma unroll
    for (int e = 0; e < 8; ++e) { const float d = f[e] - mean; q += d * d; }
    const float var = wave_sum(q) * (1.f / 512.f);
    const float r = 1.f / sqrtf(var + 1e-5f);
    rs1[i] = rfl(r); nm1[i] = rfl(-mean * r);
  }
  STAMP(4);
  v4u bw[48];
  const int hd = w & 3;
  const int trq = (lane & 15) >> 2, trp = lane & 3;
  const int tr64 = g4 * 1024 + 64 * trq + 8 * trp;
  LAS char* trB1 = L + tr64 + 32 * ((hd & 1) ^ (g4 & 1)) + 256 * (hd >> 1);
  auto read_b1 = [&](int pair, int ldsbase) {
#pragma unroll
    for (int kk = 0; kk < 2; ++kk)
#pragma unroll
      for (int ty = 0; ty < 3; ++ty) {
        LAS char* a = trB1 + ldsbase + kk * 12288 + ty * 4096;
        bw[(2 * pair + kk) * 3 + ty] = cat(trd(a), trd(a + 512));
      }
  };
  VMW(12); BAR();
  read_b1(0, L_XY); read_b1(1, L_XY + 24576);
  BAR();
  stage_b1(3, L_XY); stage_b1(4, L_XY + 24576);
  VMW(15); BAR();
  read_b1(2, L_XY + 49152);
  STAMP(5);
  VMW(0); BAR();
  read_b1(3, L_XY); read_b1(4, L_XY + 24576);
  STAMP(6);
  {
    const u32 off = WS_MODG + tid * 16;
    v4u gv;
    for (u32 sp = 0; sp < SPIN_CAP; ++sp) {
      asm volatile("" ::: "memory");
      gv = __builtin_amdgcn_raw_buffer_load_b128(rWS, off, 0, 16);
      if (__all(gv.y == tag && gv.w == tag)) break;
    }
    const float one = tid >= 256 ? 1.f : 0.f;
    f32x2 o = {asf(gv.x) + one, asf(gv.z) + one};
    *(LAS f32x2*)(L + L_MODA + tid * 8) = o;
  }
  BAR();
  STAMP(7);
  {
    const f32x4 s0 = *(LAS f32x4*)(L + L_MODA + lane * 32), s1 = *(LAS f32x4*)(L + L_MODA + lane * 32 + 16);
    const f32x4 a0 = *(LAS f32x4*)(L + L_MODA + 2048 + lane * 32), a1 = *(LAS f32x4*)(L + L_MODA + 2048 + lane * 32 + 16);
    const int lch = (lane - 4 * rot1) & 63;
#pragma unroll
    for (int i = 0; i < 10; ++i) {
      const int r = w + 8 * i;
      v4u o;
      o.x = pk((blo(xr[i].x) * rs1[i] + nm1[i]) * a0.x + s0.x, (bhi(xr[i].x) * rs1[i] + nm1[i]) * a0.y + s0.y);
      o.y = pk((blo(xr[i].y) * rs1[i] + nm1[i]) * a0.z + s0.z, (bhi(xr[i].y) * rs1[i] + nm1[i]) * a0.w + s0.w);
      o.z = pk((blo(xr[i].z) * rs1[i] + nm1[i]) * a1.x + s1.x, (bhi(xr[i].z) * rs1[i] + nm1[i]) * a1.y + s1.y);
      o.w = pk((blo(xr[i].w) * rs1[i] + nm1[i]) * a1.z + s1.z, (bhi(xr[i].w) * rs1[i] + nm1[i]) * a1.w + s1.w);
      *(LAS v4u*)(L + L_XY + r * 1024 + ((lch ^ (r & 15)) << 4)) = o;
    }
  }
  BAR();
  STAMP(8);

  int afo[4];
#pragma unroll
  for (int i = 0; i < 4; ++i) afo[i] = c16 * 1024 + (((4 * i + g4) ^ c16) << 4);
  const int blk2 = w >> 2;
  const f32x4 z4 = {0.f, 0.f, 0.f, 0.f};
  v2u k1p, v1p;
  const u32 vOst = c16 * 16 + g4 * 256 + (hd & 1) * 8;
  {
    LAS char* Ah = L + L_XY + (2 * blk2) * 16384;
    f32x4 aKh = z4, aVh = z4, aQ = z4, aK = z4, aV = z4;
#pragma unroll
    for (int ks = 0; ks < 16; ++ks) {
      if (ks >= 8 && ks < 14 && !(ks & 1)) read_b1(5 + (ks - 8) / 2, L_Z + ((ks - 8) / 2) * 24576);
      const v4u ah = *(LAS v4u*)(Ah + afo[ks & 3] + (ks >> 2) * 256);
      const v4u a1 = *(LAS v4u*)(Ah + 16384 + afo[ks & 3] + (ks >> 2) * 256);
      aKh = mfma32(bw[ks * 3 + 1], ah, aKh);
      aVh = mfma32(ah, bw[ks * 3 + 2], aVh);
      aQ = mfma32(bw[ks * 3 + 0], a1, aQ);
      aK = mfma32(bw[ks * 3 + 1], a1, aK);
      aV = mfma32(a1, bw[ks * 3 + 2], aV);
    }
    BAR();
    STAMP(9);
    {
      const u32 vB3 = (16 * (st_r >> 2) + (st_r & 3)) * 1024 + st_col;
#pragma unroll
      for (int i = 0; i < 8; ++i) {
        const int ks = 2 * i + (w >> 2);
        dma16(L_Z + ks * 4096 + (w & 3) * 1024, vB3, rWO, (32 * ks + 4 * (w & 3)) * 1024 + j * 128);
      }
    }
    k1p = pk4(aK); v1p = pk4(aV);
    const v2u o = attend(pk4(aKh), pk4(aVh), k1p, v1p, pk4(aQ), c16, g4, !(g == 0 && blk2 == 0));
    __builtin_amdgcn_raw_buffer_store_b64(o, rWS, vOst, WS_O + ((g * 4 + 2 * blk2) * 16 + 2 * j + (hd >> 1)) * 1024, 16);
  }
  STAMP(10);
  {
    LAS char* A2 = L + L_XY + (2 * blk2 + 2) * 16384;
    f32x4 aQ = z4, aK = z4, aV = z4;
#pragma unroll
    for (int ks = 0; ks < 16; ++ks) {
      if (ks == 8) { VMW(0); signal_flag(p.ws, L, 0, 0, g, 0, j, tag, lane); }
      const v4u a2 = *(LAS v4u*)(A2 + afo[ks & 3] + (ks >> 2) * 256);
      aQ = mfma32(bw[ks * 3 + 0], a2, aQ);
      aK = mfma32(bw[ks * 3 + 1], a2, aK);
      aV = mfma32(a2, bw[ks * 3 + 2], aV);
    }
    BAR();
    STAMP(11);
    const v2u o = attend(k1p, v1p, pk4(aK), pk4(aV), pk4(aQ), c16, g4, true);
    __builtin_amdgcn_raw_buffer_store_b64(o, rWS, vOst, WS_O + ((g * 4 + 2 * blk2 + 1) * 16 + 2 * j + (hd >> 1)) * 1024, 16);
  }
  STAMP(12);

  const int ncj = j < 4 ? 22 : 21, csj = j < 4 ? 22 * j : 88 + 21 * (j - 4);
  const int ch0 = csj + 3 * w;
  const int nvalid = min(3, max(0, csj + ncj - ch0));
  const int rot4 = (g * 2) & 15;
  u32 vB4[3][2];
#pragma unroll
  for (int i = 0; i < 3; ++i) {
    const int n = 64 * i + lane, u = n / 3, ct = min(n % 3, nvalid - 1);
    const int q = u & 3, g0 = (u >> 2) & 1, t = (u >> 3) & 1, g1 = (u >> 4) & 1, kk = u >> 5;
    vB4[i][0] = (32 * kk + 16 * g1 + 8 * g0 + 4 * t + q) * 2752 + ct * 16;
    vB4[i][1] = (32 * kk + 16 * g1 + 8 * g0 + 4 * (t ^ 1) + q) * 2752 + ct * 16;
  }
  auto stage_b4 = [&](int pair, int ldsbase) {
    const u32 so = (u32)(((2 * pair + rot4) & 15) * 32 * 2752 + ch0 * 16);
#pragma unroll
    for (int i = 0; i < 3; ++i) dma16(ldsbase + i * 1024, vB4[i][0], rW1, so);
#pragma unroll
    for (int i = 0; i < 3; ++i) dma16(ldsbase + 3072 + i * 1024, vB4[i][1], rW3, so);
  };
  const int trm = trp >> 1;
  const int tr4 = trm * 3072 + 48 * (trq + 4 * (g4 & 1) + 16 * (g4 >> 1)) + 8 * (trp & 1);
  LAS char* trB4_0 = L + tr4 + 384 * trm;
  LAS char* trB4_1 = L + tr4 + 384 * (1 - trm);
  auto read_b4 = [&](int pair, int ldsbase) {
#pragma unroll
    for (int kk = 0; kk < 2; ++kk)
#pragma unroll
      for (int ct = 0; ct < 3; ++ct)
        bw[(2 * pair + kk) * 3 + ct] = cat(trd(trB4_0 + ldsbase + kk * 1536 + ct * 16), trd(trB4_1 + ldsbase + kk * 1536 + ct * 16));
  };
  const int R4A = L_XY + 32768 + w * 6144;
  const int R4B = L_Z + w * 9216;
  stage_b4(0, R4A);

  const int kq = w & 3, mh = w >> 2;
  v4u b3[16];
  LAS char* tr64e0 = L + tr64 + 32 * (g4 & 1);
  LAS char* tr64e1 = L + tr64 + 32 * ((g4 & 1) ^ 1);
#pragma unroll
  for (int s = 0; s < 4; ++s)
#pragma unroll
    for (int n = 0; n < 4; ++n) {
      LAS char* a = ((n & 1) ? tr64e1 : tr64e0) + L_Z + (4 * kq + s) * 4096 + 256 * (n >> 1);
      b3[s * 4 + n] = cat(trd(a), trd(a + 512));
    }
  {
    const u32 off = WS_MODG + (1536 + tid * 2) * 8;
    const u32 offa = WS_MODG + ((lane < 32 ? 1024 : 2560 - 64) + 64 * j + 2 * lane) * 8;
    v4u gv, a0;
    for (u32 sp = 0; sp < SPIN_CAP; ++sp) {
      asm volatile("" ::: "memory");
      gv = __builtin_amdgcn_raw_buffer_load_b128(rWS, off, 0, 16);
      a0 = __builtin_amdgcn_raw_buffer_load_b128(rWS, offa, 0, 16);
      if (__all(gv.y == tag && gv.w == tag && a0.y == tag && a0.w == tag)) break;
    }
    const float one = tid >= 256 ? 1.f : 0.f;
    f32x2 o = {asf(gv.x) + one, asf(gv.z) + one};
    *(LAS f32x2*)(L + L_MODA + tid * 8) = o;
    if (w == 0) { f32x2 o2 = {asf(a0.x), asf(a0.z)}; *(LAS f32x2*)(L + L_GAB + lane * 8) = o2; }
  }
  VMW(0);
  signal_flag(p.ws, L, 1, 0, g, 1, j, tag, lane);
  STAMP(13);

  const int er = tid >> 4;
  auto out_proj = [&](auto HC) {
    constexpr int h = decltype(HC)::value;
    poll_flags(p.ws, 0, g, h, 3u << (2 * kq), tag, lane);
    if (h == 1) signal_flag(p.ws, L, 2, 1, g, 0, j, tag, lane);
    v4u af[4]; v2u xv;
    {
      const u32 vo = lane * 16, so = WS_O + ((g * 4 + 2 * mh + h) * 16 + 4 * kq) * 1024;
      LD16S(af[0], vo, rWS, so, 0); LD16S(af[1], vo, rWS, so, 1024); LD16S(af[2], vo, rWS, so, 2048); LD16S(af[3], vo, rWS, so, 3072);
      const u32 vx = (16 * (2 * (er >> 4) + h) + (er & 15)) * 1024 + c16 * 8, sx = g * 65536 + j * 128;
      LD8(xv, vx, rX, sx, 0);
    }
    if (h == 0) { read_b4(0, R4A); BAR(); stage_b4(1, R4A); stage_b4(2, R4B); }
    else { read_b4(1, R4A); read_b4(2, R4B); BAR(); stage_b4(3, R4A); stage_b4(4, R4B); }
    asm volatile("s_waitcnt vmcnt(12)" : "+v"(af[0]), "+v"(af[1]), "+v"(af[2]), "+v"(af[3]), "+v"(xv)::"memory");
    f32x4 acc[4] = {z4, z4, z4, z4};
#pragma unroll
    for (int s = 0; s < 4; ++s)
#pragma unroll
      for (int n = 0; n < 4; ++n) acc[n] = mfma32(b3[s * 4 + n], af[s], acc[n]);
#pragma unroll
    for (int n = 0; n < 4; ++n) *(LAS f32x4*)(L + L_XY + kq * 8192 + (16 * mh + c16) * 256 + (((4 * n + g4) ^ (c16 & 7)) << 4)) = acc[n];
    BAR();
    f32x4 y = *(LAS f32x4*)(L + L_XY + er * 256 + ((c16 ^ (er & 7)) << 4));
#pragma unroll
    for (int q = 1; q < 4; ++q) y += *(LAS f32x4*)(L + L_XY + q * 8192 + er * 256 + ((c16 ^ (er & 7)) << 4));
    const f32x4 gav = *(LAS f32x4*)(L + L_GAB + c16 * 16);
    f32x4 x1; x1.x = blo(xv.x) + gav.x * y.x; x1.y = bhi(xv.x) + gav.y * y.y; x1.z = blo(xv.y) + gav.z * y.z; x1.w = bhi(xv.y) + gav.w * y.w;
    const int grow = 16 * (2 * (er >> 4) + h) + (er & 15);
    __builtin_amdgcn_raw_buffer_store_b64(pk4(x1), rWS, grow * 1024 + c16 * 8, WS_X1 + g * 65536 + j * 128, 16);
    const float mean = row16_sum((x1.x + x1.y) + (x1.z + x1.w)) * (1.f / 64.f);
    const f32x4 d = x1 - mean;
    const float m2 = row16_sum((d.x * d.x + d.y * d.y) + (d.z * d.z + d.w * d.w));
    if (c16 == 0) { v2u st; st.x = asu(mean); st.y = asu(m2); __builtin_amdgcn_raw_buffer_store_b64(st, rWS, grow * 64, WS_STAT + g * 4096 + j * 8, 16); }
  };
  out_proj(std::integral_constant<int, 0>{});
  STAMP(14);
  out_proj(std::integral_constant<int, 1>{});
  STAMP(15);

  v4u x1v[4]; v2u stv;
  auto ln2_load = [&](int h) {
    const u32 vo = lane * 16;
#pragma unroll
    for (int i = 0; i < 4; ++i) {
      const int r = 4 * w + i;
      const u32 so = WS_X1 + g * 65536 + (16 * (2 * (r >> 4) + h) + (r & 15)) * 1024;
      LD16S(x1v[i], vo, rWS, so, 0);
    }
    const int r = 4 * w + ((lane >> 3) & 3);
    const u32 vs = (16 * (2 * (r >> 4) + h) + (r & 15)) * 64 + (lane & 7) * 8, ss = WS_STAT + g * 4096;
    LD8S(stv, vs, rWS, ss, 0);
  };
  auto ln2_apply = [&](int dst) {
    const float mj = asf(stv.x);
    float mean = mj;
    mean += dppf<0xB1>(mean); mean += dppf<0x4E>(mean); mean += dppf<0x141>(mean);
    mean *= 0.125f;
    const float dm = mj - mean;
    float m2 = asf(stv.y) + 64.f * dm * dm;
    m2 += dppf<0xB1>(m2); m2 += dppf<0x4E>(m2); m2 += dppf<0x141>(m2);
    const float r = 1.f / sqrtf(m2 * (1.f / 512.f) + 1e-5f);
    const float nm = -mean * r;
    const f32x4 s0 = *(LAS f32x4*)(L + L_MODA + lane * 32), s1 = *(LAS f32x4*)(L + L_MODA + lane * 32 + 16);
    const f32x4 a0 = *(LAS f32x4*)(L + L_MODA + 2048 + lane * 32), a1 = *(LAS f32x4*)(L + L_MODA + 2048 + lane * 32 + 16);
    const int lch = (lane - 4 * rot4) & 63;
#pragma unroll
    for (int i = 0; i < 4; ++i) {
      const float rs = asf(__builtin_amdgcn_readlane(asu(r), 8 * i)), nn = asf(__builtin_amdgcn_readlane(asu(nm), 8 * i));
      const int rr = 4 * w + i;
      v4u o;
      o.x = pk((blo(x1v[i].x) * rs + nn) * a0.x + s0.x, (bhi(x1v[i].x) * rs + nn) * a0.y + s0.y);
      o.y = pk((blo(x1v[i].y) * rs + nn) * a0.z + s0.z, (bhi(x1v[i].y) * rs + nn) * a0.w + s0.w);
      o.z = pk((blo(x1v[i].z) * rs + nn) * a1.x + s1.x, (bhi(x1v[i].z) * rs + nn) * a1.y + s1.y);
      o.w = pk((blo(x1v[i].w) * rs + nn) * a1.z + s1.z, (bhi(x1v[i].w) * rs + nn) * a1.w + s1.w);
      *(LAS v4u*)(L + dst + rr * 1024 + ((lch ^ (rr & 15)) << 4)) = o;
    }
  };
  auto b5_slot = [&](int q, int s) -> int {
    if (s < 5) { const int i = q * 5 + s; return i < 18 ? L_Z + i * 4096 : L_XY + 65536 + (i - 18) * 4096; }
    if (s < 7) return L_XY + (q * 2 + (s - 5)) * 4096;
    if (s < 9) return L_XY + 32768 + (q * 2 + (s - 7)) * 4096;
    return L_Z + (q * 2 + (s - 9)) * 4096;
  };
  auto stage_b5 = [&](int s0, int s1) {
    const int fl = fresh_lane(), qd = fl >> 2;
    const u32 vB5 = ((qd & 3) + 4 * (qd >> 3)) * 1024 + ((qd >> 2) & 1) * 64 + (((fl & 3) ^ ((w & 1) << 1)) << 4);
#pragma unroll
    for (int s = s0; s < s1; ++s)
#pragma unroll
      for (int qq = 0; qq < 2; ++qq) {
        const int q = 2 * qq + (w >> 2);
        const int ks = min(11 * q + s, 42);
        dma16(b5_slot(q, s) + (w & 3) * 1024, vB5, rW2, (32 * ks + 8 * (w & 3)) * 1024 + j * 128);
      }
  };
  const u32 vHst = c16 * 16 + (g4 & 1) * 8;
  auto swiglu_store = [&](f32x4 (&acc)[3][2], int h) {
#pragma unroll
    for (int ct = 0; ct < 3; ++ct) {
      float hv[4];
#pragma unroll
      for (int e = 0; e < 4; ++e) {
        auto r = __builtin_amdgcn_permlane32_swap(asu(acc[ct][0][e]), asu(acc[ct][1][e]), false, false);
        const float gt = asf(r[0]), up = asf(r[1]);
        hv[e] = gt * __builtin_amdgcn_rcpf(1.f + __expf(-gt)) * up;
      }
      v2u o; o.x = pk(hv[0], hv[1]); o.y = pk(hv[2], hv[3]);
      if (ct < nvalid)
        __builtin_amdgcn_raw_buffer_store_b64(o, rWS, vHst + (2 * (g4 >> 1)) * (172 * 256), WS_HH + ((g * 4 + h) * 172 + ch0 + ct) * 256, 16);
    }
  };
  poll_flags(p.ws, 1, g, 0, 0xffu, tag, lane);
  signal_flag(p.ws, L, 3, 1, g, 1, j, tag, lane);
  ln2_load(0);
  read_b4(3, R4A); read_b4(4, R4B);
  BAR();
  stage_b4(5, R4A); stage_b4(6, R4B);
  asm volatile("s_waitcnt vmcnt(12)" : "+v"(x1v[0]), "+v"(x1v[1]), "+v"(x1v[2]), "+v"(x1v[3]), "+v"(stv)::"memory");
  ln2_apply(L_XY);
  BAR();
  STAMP(16);
  {
    f32x4 acc[3][2] = {{z4, z4}, {z4, z4}, {z4, z4}};
#pragma unroll
    for (int ks = 0; ks < 16; ++ks) {
      if (ks == 6) { VMW(0); read_b4(5, R4A); read_b4(6, R4B); LGW(); stage_b4(7, R4B); }
      if (ks == 12) { VMW(0); read_b4(7, R4B); }
      const v4u a0 = *(LAS v4u*)(L + L_XY + afo[ks & 3] + (ks >> 2) * 256);
      const v4u a1 = *(LAS v4u*)(L + L_XY + 16384 + afo[ks & 3] + (ks >> 2) * 256);
#pragma unroll
      for (int ct = 0; ct < 3; ++ct) {
        acc[ct][0] = mfma32(bw[ks * 3 + ct], a0, acc[ct][0]);
        acc[ct][1] = mfma32(bw[ks * 3 + ct], a1, acc[ct][1]);
      }
    }
    BAR();
    STAMP(17);
    poll_flags(p.ws, 1, g, 1, 0xffu, tag, lane);
    ln2_load(1);
    stage_b5(0, 7);
    swiglu_store(acc, 0);
  }
  asm volatile("s_waitcnt vmcnt(14)" : "+v"(x1v[0]), "+v"(x1v[1]), "+v"(x1v[2]), "+v"(x1v[3]), "+v"(stv)::"memory");
  ln2_apply(L_XY + 32768);
  BAR();
  STAMP(18);
  {
    f32x4 acc[3][2] = {{z4, z4}, {z4, z4}, {z4, z4}};
#pragma unroll
    for (int ks = 0; ks < 16; ++ks) {
      if (ks == 4) { VMW(0); signal_flag(p.ws, L, 4, 2, g, 0, j, tag, lane); }
      const v4u a0 = *(LAS v4u*)(L + L_XY + 32768 + afo[ks & 3] + (ks >> 2) * 256);
      const v4u a1 = *(LAS v4u*)(L + L_XY + 49152 + afo[ks & 3] + (ks >> 2) * 256);
#pragma unroll
      for (int ct = 0; ct < 3; ++ct) {
        acc[ct][0] = mfma32(bw[ks * 3 + ct], a0, acc[ct][0]);
        acc[ct][1] = mfma32(bw[ks * 3 + ct], a1, acc[ct][1]);
      }
    }
    BAR();
    STAMP(19);
    stage_b5(7, 9);
    swiglu_store(acc, 1);
  }
  const int l5 = fresh_lane(), c5 = l5 & 15, g5 = l5 >> 4, er5 = 4 * w + g5;
  LAS char* t5e0 = L + g5 * 1024 + 64 * (c5 >> 2) + 8 * (l5 & 3) + 32 * (g5 & 1);
  LAS char* t5e1 = L + g5 * 1024 + 64 * (c5 >> 2) + 8 * (l5 & 3) + 32 * ((g5 & 1) ^ 1);
  auto read_b5 = [&](int s) {
#pragma unroll
    for (int n = 0; n < 4; ++n) {
      LAS char* a = ((n & 1) ? t5e1 : t5e0) + b5_slot(kq, s) + 256 * (n >> 1);
      bw[s * 4 + n] = cat(trd(a), trd(a + 512));
    }
  };
#pragma unroll
  for (int s = 0; s < 7; ++s) read_b5(s);
  VMW(0);
  signal_flag(p.ws, L, 5, 2, g, 1, j, tag, lane);
  BAR();
  stage_b5(9, 11);
  read_b5(7); read_b5(8);
  STAMP(20);
  const u32 pmask = kq == 0 ? 0x03u : kq == 1 ? 0x0cu : kq == 2 ? 0x70u : 0xc0u;
  auto down_proj = [&](auto HC) {
    constexpr int h = decltype(HC)::value;
    poll_flags(p.ws, 2, g, h, pmask, tag, l5);
    v4u af[11];
    const u32 vo = l5 * 16, so = WS_HH + ((g * 4 + 2 * mh + h) * 172 + 44 * kq) * 256;
    const u32 so2 = so + 4096, so3 = so + 8192;
    LD16S(af[0], vo, rWS, so, 0); LD16S(af[1], vo, rWS, so, 1024); LD16S(af[2], vo, rWS, so, 2048); LD16S(af[3], vo, rWS, so, 3072);
    LD16S(af[4], vo, rWS, so2, 0); LD16S(af[5], vo, rWS, so2, 1024); LD16S(af[6], vo, rWS, so2, 2048); LD16S(af[7], vo, rWS, so2, 3072);
    LD16S(af[8], vo, rWS, so3, 0); LD16S(af[9], vo, rWS, so3, 1024);
    const u32 so4 = kq == 3 ? so3 + 1024 : so3 + 2048;
    LD16S(af[10], vo, rWS, so4, 0);
    const int grow = 16 * (2 * (er5 >> 4) + h) + (er5 & 15);
    v2u xq;
    { const u32 vq = grow * 1024 + c5 * 8, sq = WS_X1 + g * 65536 + j * 128; LD8S(xq, vq, rWS, sq, 0); }
    BAR();
    if (h == 0) { read_b5(9); read_b5(10); }
    f32x4 acc[4] = {z4, z4, z4, z4};
    asm volatile("s_waitcnt vmcnt(8)" : "+v"(af[0]), "+v"(af[1]), "+v"(af[2]), "+v"(af[3])::"memory");
#pragma unroll
    for (int s = 0; s < 4; ++s)
#pragma unroll
      for (int n = 0; n < 4; ++n) acc[n] = mfma32(bw[s * 4 + n], af[s], acc[n]);
    asm volatile("s_waitcnt vmcnt(4)" : "+v"(af[4]), "+v"(af[5]), "+v"(af[6]), "+v"(af[7])::"memory");
#pragma unroll
    for (int s = 4; s < 8; ++s)
#pragma unroll
      for (int n = 0; n < 4; ++n) acc[n] = mfma32(bw[s * 4 + n], af[s], acc[n]);
    asm volatile("s_waitcnt vmcnt(0)" : "+v"(af[8]), "+v"(af[9]), "+v"(af[10]), "+v"(xq)::"memory");
    if (kq == 3) af[10] = v4u{0u, 0u, 0u, 0u};
#pragma unroll
    for (int s = 8; s < 11; ++s)
#pragma unroll
      for (int n = 0; n < 4; ++n) acc[n] = mfma32(bw[s * 4 + n], af[s], acc[n]);
#pragma unroll
    for (int n = 0; n < 4; ++n) *(LAS f32x4*)(L + L_XY + kq * 8192 + (16 * mh + c5) * 256 + (((4 * n + g5) ^ (c5 & 7)) << 4)) = acc[n];
    BAR();
    f32x4 y = *(LAS f32x4*)(L + L_XY + er5 * 256 + ((c5 ^ (er5 & 7)) << 4));
#pragma unroll
    for (int q = 1; q < 4; ++q) y += *(LAS f32x4*)(L + L_XY + q * 8192 + er5 * 256 + ((c5 ^ (er5 & 7)) << 4));
    const f32x4 gbv = *(LAS f32x4*)(L + L_GAB + 256 + c5 * 16);
    const f32x4 xb = {blo(xq.x), bhi(xq.x), blo(xq.y), bhi(xq.y)};
    const f32x4 o = xb + gbv * y;
    __builtin_amdgcn_raw_buffer_store_b64(pk4(o), rOUT, grow * 1024 + c5 * 8, g * 65536 + j * 128, 0);
  };
  down_proj(std::integral_constant<int, 0>{});
  STAMP(21);
  down_proj(std::integral_constant<int, 1>{});
  STAMP(22);
  if (b == 0 && w < 4) {
    gu64* gp = (gu64*)(p.ws + WS_MODG + (size_t)(tid * 12) * 8);
    for (u32 sp = 0; sp < SPIN_CAP; ++sp) {
      const u64 v = __hip_atomic_load(gp, RLX_AGENT);
      if (__all((u32)(v >> 32) == tag)) break;
    }
    if (lane == 0) {
      const u32 old = __hip_atomic_fetch_add((LAS u32*)(L + L_CNT + 6 * 4), 1u, __ATOMIC_RELAXED, __HIP_MEMORY_SCOPE_WORKGROUP);
      if (old == 3u) __hip_atomic_store((gu32*)(p.ws + WS_EPOCH), tag, RLX_AGENT);
    }
  }
}
}

extern "C" void kernel_launch(void* const* d_in, const int* in_sizes, int n_in, void* d_out, int out_size, void* d_ws, size_t ws_size,
                              hipStream_t stream) {
  Params p;
  p.x = (const u16*)d_in[0]; p.wqkv = (const u16*)d_in[1]; p.wo = (const u16*)d_in[2];
  p.w1 = (const u16*)d_in[4]; p.w3 = (const u16*)d_in[5]; p.w2 = (const u16*)d_in[6];
  p.c = (const u16*)d_in[7]; p.wmod = (const u16*)d_in[8];
  p.out = (u16*)d_out; p.ws = (char*)d_ws;
  (void)hipFuncSetAttribute((const void*)fused_block, hipFuncAttributeMaxDynamicSharedMemorySize, L_TOTAL);
  fused_block<<<256, 512, L_TOTAL, stream>>>(p);
}
```

```cpp
#include <hip/hip_runtime.h>
#include <stdint.h>

#define KDBG 0
#define NODMA 0
#define XMAP 2

#define DI __device__ __forceinline__
#define GAS __attribute__((address_space(1)))
#define LAS __attribute__((address_space(3)))
typedef unsigned u32;
typedef unsigned short u16;
typedef unsigned long long u64;
typedef u32 v4u __attribute__((ext_vector_type(4)));
typedef u32 v2u __attribute__((ext_vector_type(2)));
typedef float f32x4 __attribute__((ext_vector_type(4)));
typedef float f32x2 __attribute__((ext_vector_type(2)));
typedef __bf16 bf16x8 __attribute__((ext_vector_type(8)));
typedef __bf16 bf16x2 __attribute__((ext_vector_type(2)));
typedef short s16x4 __attribute__((ext_vector_type(4)));
typedef __amdgpu_buffer_rsrc_t rsrc_t;
typedef GAS u32 gu32;
typedef GAS u64 gu64;
#define RLX_AGENT __ATOMIC_RELAXED, __HIP_MEMORY_SCOPE_AGENT

namespace {
constexpr int S_ = 2048, D_ = 512, F_ = 1376;
constexpr u32 WS_EPOCH = 0;
constexpr u32 WS_ALIVE = 4096;
constexpr u32 WS_FLAG = 32768;
constexpr u32 WS_STAT = 262144;
constexpr u32 WS_MODG = 524288;
constexpr u32 WS_O = 1u << 20;
constexpr u32 WS_X1 = 3u << 20;
constexpr u32 WS_HH = 5u << 20;
constexpr u32 WS_DBG = 16u << 20;
constexpr u32 WS_BYTES = 32u << 20;
constexpr int L_XY = 0;
constexpr int L_Z = 81920;
constexpr int L_MISC = 155648;
constexpr int L_TOTAL = 163840;
constexpr int L_RED = 73728;
constexpr int L_MODA = L_MISC;
constexpr int L_CNT = L_MISC + 4096;
constexpr int L_GAB = L_MISC + 4096 + 256;
constexpr u32 SPIN_CAP = 1u << 18;

DI f32x4 mfma32(v4u a, v4u b, f32x4 c) {
  return __builtin_amdgcn_mfma_f32_16x16x32_bf16(__builtin_bit_cast(bf16x8, a), __builtin_bit_cast(bf16x8, b), c, 0, 0, 0);
}
DI f32x4 mfma16(v2u a, v2u b, f32x4 c) {
  return __builtin_amdgcn_mfma_f32_16x16x16bf16_1k(__builtin_bit_cast(s16x4, a), __builtin_bit_cast(s16x4, b), c, 0, 0, 0);
}
DI u32 pk(float lo, float hi) { f32x2 v = {lo, hi}; return __builtin_bit_cast(u32, __builtin_convertvector(v, bf16x2)); }
DI u32 pk2(f32x2 v) { return __builtin_bit_cast(u32, __builtin_convertvector(v, bf16x2)); }
DI v2u pk4(f32x4 v) { v2u r; r.x = pk(v.x, v.y); r.y = pk(v.z, v.w); return r; }
DI float asf(u32 u) { return __builtin_bit_cast(float, u); }
DI u32 asu(float f) { return __builtin_bit_cast(u32, f); }
DI float blo(u32 u) { return asf(u << 16); }
DI float bhi(u32 u) { return asf(u & 0xffff0000u); }
DI f32x2 unp(u32 u) { f32x2 r = {blo(u), bhi(u)}; return r; }
DI float dot2(u32 a, u32 b, float c) { return __builtin_amdgcn_fdot2_f32_bf16(__builtin_bit_cast(bf16x2, a), __builtin_bit_cast(bf16x2, b), c, false); }
DI v2u trd(LAS char* p) { return __builtin_bit_cast(v2u, __builtin_amdgcn_ds_read_tr16_b64_v4i16((LAS s16x4*)p)); }
DI v4u cat(v2u a, v2u b) { v4u r; r.x = a.x; r.y = a.y; r.z = b.x; r.w = b.y; return r; }
template <int MI, int SI> DI void dma(u32 mb, u32 voff, rsrc_t srd, u32 sb) {
  u32 t;
  asm volatile("s_add_u32 m0, %1, %c2\n\ts_add_u32 %0, %3, %c4\n\tbuffer_load_dwordx4 %5, %6, %0 offen lds"
               : "=&s"(t) : "s"(mb), "i"(MI), "s"(sb), "i"(SI), "v"(voff), "s"(srd) : "scc");
}
#define BAR() asm volatile("s_waitcnt lgkmcnt(0)\n\ts_barrier" ::: "memory")
#define VMW(n) asm volatile("s_waitcnt vmcnt(" #n ")" ::: "memory")
#define LGW() asm volatile("s_waitcnt lgkmcnt(0)" ::: "memory")
#define SCHED() __builtin_amdgcn_sched_barrier(0)
#define LD16(dst, voff, srd, soff, IMM) asm volatile("buffer_load_dwordx4 %0, %1, %2, %3 offen offset:" #IMM : "=v"(dst) : "v"(voff), "s"(srd), "s"(soff))
#define LD16S(dst, voff, srd, soff, IMM) asm volatile("buffer_load_dwordx4 %0, %1, %2, %3 offen offset:" #IMM " sc1" : "=v"(dst) : "v"(voff), "s"(srd), "s"(soff))
#define LD8(dst, voff, srd, soff, IMM) asm volatile("buffer_load_dwordx2 %0, %1, %2, %3 offen offset:" #IMM : "=v"(dst) : "v"(voff), "s"(srd), "s"(soff))
#define LD8S(dst, voff, srd, soff, IMM) asm volatile("buffer_load_dwordx2 %0, %1, %2, %3 offen offset:" #IMM " sc1" : "=v"(dst) : "v"(voff), "s"(srd), "s"(soff))
#define LD4(dst, voff, srd, soff) asm volatile("buffer_load_dword %0, %1, %2, %3 offen" : "=v"(dst) : "v"(voff), "s"(srd), "s"(soff))
#define LD4S(dst, voff, srd, soff) asm volatile("buffer_load_dword %0, %1, %2, %3 offen sc1" : "=v"(dst) : "v"(voff), "s"(srd), "s"(soff))

template <int CTRL> DI float dppf(float v) { return asf(__builtin_amdgcn_update_dpp(0, asu(v), CTRL, 0xf, 0xf, true)); }
DI float row16_sum(float v) { v += dppf<0x128>(v); v += dppf<0x124>(v); v += dppf<0x122>(v); v += dppf<0x121>(v); return v; }
DI float sw32_add(float a, float b) { auto r = __builtin_amdgcn_permlane32_swap(asu(a), asu(b), false, false); return asf(r[0]) + asf(r[1]); }
DI float sw16_add(float a, float b) { auto r = __builtin_amdgcn_permlane16_swap(asu(a), asu(b), false, false); return asf(r[0]) + asf(r[1]); }
DI float x16_max(float v) { auto r = __builtin_amdgcn_permlane16_swap(asu(v), asu(v), false, false); return fmaxf(asf(r[0]), asf(r[1])); }
DI float x32_max(float v) { auto r = __builtin_amdgcn_permlane32_swap(asu(v), asu(v), false, false); return fmaxf(asf(r[0]), asf(r[1])); }
DI int fresh_lane() { int l; asm volatile("v_mbcnt_lo_u32_b32 %0, -1, 0\n\tv_mbcnt_hi_u32_b32 %0, -1, %0" : "=v"(l)); return l; }
DI float rdl(float v, int l) { return asf(__builtin_amdgcn_readlane(asu(v), l)); }

struct Params {
  const u16 *x, *wqkv, *wo, *w1, *w3, *w2, *c, *wmod;
  u16* out;
  char* ws;
};

#define STAMP(i) do {} while (0)
#define STAMP_DUMP() do {} while (0)

DI u32 flag_off(int phase, int g, int h) { return WS_FLAG + ((phase * 32 + g) * 2 + h) * 1024; }
DI void poll_flags(rsrc_t rWS, u32 vfl, u32 foff, u64 care, u32 tag) {
  for (u32 sp = 0; sp < SPIN_CAP; ++sp) {
    asm volatile("" ::: "memory");
    const u32 v = __builtin_amdgcn_raw_buffer_load_b32(rWS, vfl, foff, 16);
    if (!(__builtin_amdgcn_ballot_w64(v != tag) & care)) break;
  }
  VMW(0);
}
DI void raise_flag(rsrc_t rWS, u32 foff, u32 vtag, u32 vzero, u32 m1) {
  asm volatile("s_mov_b32 exec_hi, 0\n\ts_mov_b32 exec_lo, %4\n\tbuffer_store_dword %0, %1, %2, %3 offen sc1\n\ts_mov_b64 exec, -1" ::"v"(vtag), "v"(vzero), "s"(rWS), "s"(foff), "s"(m1) : "memory");
}
#define PIN(x) asm volatile("" : "+v"(x))

DI v2u attend(v2u kp, v2u vp, v2u kc, v2u vc, v2u q, const u64 (&okp)[4], const u64 (&okc)[4]) {
  const f32x4 z = {0.f, 0.f, 0.f, 0.f};
  const f32x4 sp = mfma16(kp, q, z);
  const f32x4 sc = mfma16(kc, q, z);
  const float K2 = 0.25f * 1.4426950408889634f;
  float s[8];
#pragma unroll
  for (int j = 0; j < 4; ++j) { s[j] = __builtin_amdgcn_inverse_ballot_w64(okp[j]) ? sp[j] : -INFINITY; s[4 + j] = __builtin_amdgcn_inverse_ballot_w64(okc[j]) ? sc[j] : -INFINITY; }
  float m = fmaxf(fmaxf(fmaxf(s[0], s[1]), s[2]), fmaxf(fmaxf(fmaxf(s[3], s[4]), s[5]), fmaxf(s[6], s[7])));
  m = x32_max(x16_max(m));
  const float mk = -m * K2;
  float e[8];
#pragma unroll
  for (int j = 0; j < 8; ++j) e[j] = __builtin_amdgcn_exp2f(__builtin_fmaf(s[j], K2, mk));
  float sum = ((e[0] + e[1]) + (e[2] + e[3])) + ((e[4] + e[5]) + (e[6] + e[7]));
  v4u pf;
  pf.x = pk(e[0], e[1]); pf.y = pk(e[2], e[3]); pf.z = pk(e[4], e[5]); pf.w = pk(e[6], e[7]);
  const f32x4 o = mfma32(cat(vp, vc), pf, z);
  sum = sw16_add(sum, sum); sum = sw32_add(sum, sum);
  return pk4(o * __builtin_amdgcn_rcpf(sum));
}

template <int N> using IC = std::integral_constant<int, N>;
template <int I, int N, class F> DI void sfor(F&& f) { if constexpr (I < N) { f(IC<I>{}); sfor<I + 1, N>(f); } }

__global__ void __launch_bounds__(512, 2) fused_block(Params p) {
  extern __shared__ __attribute__((aligned(16))) char smem[];
  LAS char* L = (LAS char*)smem;
  const int tid = threadIdx.x, lane = tid & 63, w = __builtin_amdgcn_readfirstlane(tid >> 6);
  const int c16 = lane & 15, g4 = lane >> 4;
  const int b = blockIdx.x;
  int g, j;
  if (XMAP == 0) { g = (b & 7) * 4 + ((b >> 3) & 3); j = b >> 5; }
  else if (XMAP == 2) { const int x = b & 7, r = b >> 3; g = (x >> 1) * 8 + (r & 7); j = (x & 1) * 4 + (r >> 3); }
  else { const int x = b & 7, r = b >> 3; g = (x >> 2) * 16 + (r & 15); j = (x & 3) * 2 + (r >> 4); }
  const rsrc_t rX = __builtin_amdgcn_make_buffer_rsrc((void*)p.x, 0, S_ * D_ * 2, 0x00020000);
  const rsrc_t rQKV = __builtin_amdgcn_make_buffer_rsrc((void*)p.wqkv, 0, D_ * 3 * D_ * 2, 0x00020000);
  const rsrc_t rWO = __builtin_amdgcn_make_buffer_rsrc((void*)p.wo, 0, D_ * D_ * 2, 0x00020000);
  const rsrc_t rW1 = __builtin_amdgcn_make_buffer_rsrc((void*)p.w1, 0, D_ * F_ * 2, 0x00020000);
  const rsrc_t rW3 = __builtin_amdgcn_make_buffer_rsrc((void*)p.w3, 0, D_ * F_ * 2, 0x00020000);
  const rsrc_t rW2 = __builtin_amdgcn_make_buffer_rsrc((void*)p.w2, 0, F_ * D_ * 2, 0x00020000);
  const rsrc_t rC = __builtin_amdgcn_make_buffer_rsrc((void*)p.c, 0, D_ * 2, 0x00020000);
  const rsrc_t rMOD = __builtin_amdgcn_make_buffer_rsrc((void*)p.wmod, 0, D_ * 6 * D_ * 2, 0x00020000);
  const rsrc_t rWS = __builtin_amdgcn_make_buffer_rsrc((void*)p.ws, 0, WS_BYTES, 0x00020000);
  const rsrc_t rOUT = __builtin_amdgcn_make_buffer_rsrc((void*)p.out, 0, S_ * D_ * 2, 0x00020000);
  STAMP(0);

  const int bm = b < 192 ? b : b - 192, mq = bm / 48, cl = bm - 48 * mq;
  u32 cv, ep; v4u wm0, wm1;
  {
    const u32 vm = (tid >> 3) * 12288 + (tid & 7) * 16, sm = mq * (128 * 6144) + cl * 128, sm1 = sm + 6144;
    LD16(wm0, vm, rMOD, sm, 0);
    LD16(wm1, vm, rMOD, sm1, 0);
    const u32 vc = (tid >> 3) * 4, scq = mq * 256;
    LD4(cv, vc, rC, scq);
    const u32 vz = 0;
    asm volatile("buffer_load_dword %0, %1, %2, 0 offen sc1" : "=v"(ep) : "v"(vz), "s"(rWS));
  }
  v4u xr[10];
  u32 vl16 = lane * 16; PIN(vl16);
  const int r0 = 64 * g - 16 + w;
  { const u32 s0 = (u32)max(r0, 0) * 1024u, s1 = (u32)max(r0 + 8, 0) * 1024u;
    LD16(xr[0], vl16, rX, s0, 0); LD16(xr[1], vl16, rX, s1, 0); }
  const int st_qd = lane >> 2, st_r = (st_qd & 3) + 4 * (st_qd >> 3);
  const int st_col = ((st_qd >> 2) & 1) * 64 + (((lane & 3) ^ ((w & 1) << 1)) << 4);
  const int rotp = g & 7;
  u32 vB1 = st_r * 3072 + st_col; PIN(vB1);
  const u32 mB1 = (w >> 2) * 12288 + (w & 3) * 1024, sB1 = (w >> 2) * 98304 + (w & 3) * 24576 + j * 128;
  auto stage_b1 = [&](auto PC, auto BC) {
    constexpr int pair = decltype(PC)::value, base = decltype(BC)::value;
    const u32 sp = ((pair + rotp) & 7) * 196608 + sB1;
    dma<base, 0>(mB1, vB1, rQKV, sp); dma<base + 4096, 1024>(mB1, vB1, rQKV, sp); dma<base + 8192, 2048>(mB1, vB1, rQKV, sp);
  };
  const int hd = w & 3, blk2 = w >> 2, kq = w & 3, mh = w >> 2;
  int afo[4];
#pragma unroll
  for (int i = 0; i < 4; ++i) { afo[i] = c16 * 1024 + (((4 * i + g4) ^ c16) << 4); PIN(afo[i]); }
  u64 okc[4], okp[4];
#pragma unroll
  for (int e = 0; e < 4; ++e) {
    const int dc = c16 - (4 * g4 + e);
    okc[e] = __builtin_amdgcn_ballot_w64(dc >= 0 && dc <= 14 && !(dc & 1));
    okp[e] = __builtin_amdgcn_ballot_w64(dc <= -2 && !(dc & 1));
  }
  u32 vOst = c16 * 16 + g4 * 256 + (hd & 1) * 8; PIN(vOst);
  u32 vB3 = (16 * (st_r >> 2) + (st_r & 3)) * 1024 + st_col; PIN(vB3);
  u32 vB4[3];
#pragma unroll
  for (int i = 0; i < 3; ++i) {
    const int n = 64 * i + lane, u = (n * 171) >> 9;
    const int row = (u & 3) | ((u >> 1) & 4) | ((u << 1) & 8) | (u & 48);
    vB4[i] = row * 2752 + (n - 3 * u) * 16; PIN(vB4[i]);
  }
  int tB1 = g4 * 1024 + 64 * (c16 >> 2) + 8 * (lane & 3) + 32 * ((hd & 1) ^ (g4 & 1)) + 256 * (hd >> 1); PIN(tB1);
  int hw0 = w * 1024 + ((((lane - 8 * rotp) & 63) ^ w) << 4); PIN(hw0);
  u32 vfl = (lane & 7) * 128; PIN(vfl);
  STAMP(1);
  asm volatile("s_waitcnt vmcnt(2)" : "+v"(cv), "+v"(wm0), "+v"(wm1), "+v"(ep)::"memory");
  STAMP(2);
  const u32 tag = __builtin_amdgcn_readfirstlane(ep) + 1u;
  u32 m1; asm("s_lshr_b32 %0, 1, %1" : "=s"(m1) : "s"(w) : "scc");
#define RAISE(fo) raise_flag(rWS, (fo), tag, vfl, m1)
  {
    const float c0 = blo(cv), c1 = bhi(cv);
    const float s0 = c0 * __builtin_amdgcn_rcpf(1.f + __builtin_amdgcn_exp2f(c0 * -1.4426950408889634f));
    const float s1 = c1 * __builtin_amdgcn_rcpf(1.f + __builtin_amdgcn_exp2f(c1 * -1.4426950408889634f));
    f32x2 v[4];
#pragma unroll
    for (int i = 0; i < 4; ++i) v[i] = unp(wm0[i]) * s0 + unp(wm1[i]) * s1;
    const float a0 = sw32_add(v[0].x, v[2].x), a1 = sw32_add(v[0].y, v[2].y), a2 = sw32_add(v[1].x, v[3].x), a3 = sw32_add(v[1].y, v[3].y);
    float t0 = sw16_add(a0, a2), t1 = sw16_add(a1, a3);
    t0 += dppf<0x128>(t0); t1 += dppf<0x128>(t1);
    f32x2 o = {t0, t1};
    *(LAS f32x2*)(L + L_RED + w * 256 + (lane & 7) * 32 + g4 * 8) = o;
    BAR();
    if (w == 0) {
      float s = 0.f;
#pragma unroll
      for (int i = 0; i < 8; ++i) s += *(LAS float*)(L + L_RED + i * 256 + lane * 4);
      v2u gr; gr.x = asu(s); gr.y = tag;
      if (b < 192) __builtin_amdgcn_raw_buffer_store_b64(gr, rWS, lane * 8, WS_MODG + (mq * 3072 + cl * 64) * 8, 16);
      __builtin_amdgcn_raw_buffer_store_b32(tag, rWS, 0, WS_ALIVE + b * 4, 16);
    }
  }
  STAMP(3);
#pragma unroll
  for (int i = 2; i < 10; ++i) { const u32 so = (u32)(r0 + 8 * i) * 1024u; LD16(xr[i], vl16, rX, so, 0); }
  stage_b1(IC<0>{}, IC<L_XY>{});
  f32x2 xn[10][4];
  {
    float u[10];
    sfor<0, 10>([&](auto I) {
      constexpr int i = decltype(I)::value;
      if constexpr (i == 0) asm volatile("s_waitcnt vmcnt(11)" : "+v"(xr[0]), "+v"(xr[1])::"memory");
      if constexpr (i == 2) asm volatile("s_waitcnt vmcnt(12)" : "+v"(xr[2]), "+v"(xr[3])::"memory");
      if constexpr (i == 4) asm volatile("s_waitcnt vmcnt(13)" : "+v"(xr[4]), "+v"(xr[5])::"memory");
      if constexpr (i == 6) asm volatile("s_waitcnt vmcnt(14)" : "+v"(xr[6]), "+v"(xr[7])::"memory");
      if constexpr (i == 8) asm volatile("s_waitcnt vmcnt(15)" : "+v"(xr[8]), "+v"(xr[9])::"memory");
      float s = 0.f, q = 0.f;
#pragma unroll
      for (int e = 0; e < 4; ++e) { s = dot2(xr[i][e], 0x3f803f80u, s); q = dot2(xr[i][e], xr[i][e], q); }
      u[i] = sw32_add(s, q);
      PIN(u[i]);
      if constexpr (i == 1) stage_b1(IC<1>{}, IC<L_XY + 24576>{});
      if constexpr (i == 3) stage_b1(IC<2>{}, IC<L_XY + 49152>{});
      if constexpr (i == 5) stage_b1(IC<5>{}, IC<L_Z>{});
      if constexpr (i == 7) stage_b1(IC<6>{}, IC<L_Z + 24576>{});
      if constexpr (i == 9) stage_b1(IC<7>{}, IC<L_Z + 49152>{});
    });
    float t[5];
#pragma unroll
    for (int i = 0; i < 5; ++i) t[i] = row16_sum(sw16_add(u[i], u[i + 5]));
    float v = t[0];
#pragma unroll
    for (int i = 1; i < 5; ++i) v = c16 == i ? t[i] : v;
    auto r = __builtin_amdgcn_permlane32_swap(asu(v), asu(v), false, false);
    const float mean = asf(r[0]) * (1.f / 512.f);
    const float var = fmaxf(asf(r[1]) * (1.f / 512.f) - mean * mean, 0.f);
    const float rs = __builtin_amdgcn_rsqf(var + 1e-5f), nm = -mean * rs;
#pragma unroll
    for (int i = 0; i < 10; ++i) {
      const float a = rdl(rs, (i / 5) * 16 + i % 5), c = rdl(nm, (i / 5) * 16 + i % 5);
#pragma unroll
      for (int e = 0; e < 4; ++e) { xn[i][e] = unp(xr[i][e]) * a + c; PIN(xn[i][e]); }
    }
  }
  STAMP(4);
  v4u bw[48];
  auto read_b1 = [&](int pair, int base, int off) {
#pragma unroll
    for (int kk = 0; kk < 2; ++kk)
#pragma unroll
      for (int ty = 0; ty < 3; ++ty) {
        LAS char* a = L + base + (off + kk * 12288 + ty * 4096);
        bw[(2 * pair + kk) * 3 + ty] = cat(trd(a), trd(a + 512));
      }
  };
  v4u ga_[4];
  const u32 sMG = WS_MODG + w * 1024;
  { const u32 s0 = sMG, s1 = sMG + 24576, s2 = sMG + 49152, s3 = sMG + 73728;
    LD16S(ga_[0], vl16, rWS, s0, 0); LD16S(ga_[1], vl16, rWS, s1, 0); LD16S(ga_[2], vl16, rWS, s2, 0); LD16S(ga_[3], vl16, rWS, s3, 0); }
  VMW(13); BAR();
  { int t2 = tB1 + 49152; PIN(t2); read_b1(0, tB1, 0); read_b1(1, tB1, 24576); read_b1(2, t2, 0); }
  BAR();
  stage_b1(IC<3>{}, IC<L_XY>{}); stage_b1(IC<4>{}, IC<L_XY + 24576>{});
  STAMP(5);
  asm volatile("s_waitcnt vmcnt(6)" : "+v"(ga_[0]), "+v"(ga_[1]), "+v"(ga_[2]), "+v"(ga_[3])::"memory");
  auto gsum = [&](v4u (&a)[4], f32x2& o) -> u32 {
    o.x = (asf(a[0].x) + asf(a[1].x)) + (asf(a[2].x) + asf(a[3].x)); o.y = (asf(a[0].z) + asf(a[1].z)) + (asf(a[2].z) + asf(a[3].z));
    return ((a[0].y ^ tag) | (a[0].w ^ tag) | (a[1].y ^ tag) | (a[1].w ^ tag)) | ((a[2].y ^ tag) | (a[2].w ^ tag) | (a[3].y ^ tag) | (a[3].w ^ tag));
  };
  auto gather = [&](u32 voff, u32 soff, f32x2& o) {
    for (u32 sp = 0; sp < SPIN_CAP; ++sp) {
      asm volatile("" ::: "memory");
      v4u a[4];
#pragma unroll
      for (int q = 0; q < 4; ++q) a[q] = __builtin_amdgcn_raw_buffer_load_b128(rWS, voff, soff + q * 24576, 16);
      if (!__builtin_amdgcn_ballot_w64(gsum(a, o) != 0u)) break;
    }
  };
  const float one = w >= 4 ? 1.f : 0.f;
  int vma = L_MODA + 2 * vl16; PIN(vma);
  {
    f32x2 mA;
    if (__builtin_amdgcn_ballot_w64(gsum(ga_, mA) != 0u)) gather(vl16, sMG, mA);
    *(LAS f32x2*)(L + L_MODA + w * 512 + (vl16 >> 1)) = mA + one;
  }
  VMW(0); BAR();
  STAMP(6);
  const f32x4 ms0 = *(LAS f32x4*)(L + vma), ms1 = *(LAS f32x4*)(L + vma + 16);
  const f32x4 ma0 = *(LAS f32x4*)(L + vma + 2048), ma1 = *(LAS f32x4*)(L + vma + 2064);
  read_b1(3, tB1, 0); read_b1(4, tB1, 24576);
  BAR();
  STAMP(7);
  {
    int hw1 = (hw0 ^ 128) + 8192; PIN(hw1);
    int tz0 = tB1 + L_Z; PIN(tz0);
    int tz2 = tB1 + L_Z + 49152; PIN(tz2);
    sfor<0, 10>([&](auto I) {
      constexpr int i = decltype(I)::value;
      v4u o;
      o.x = pk2(xn[i][0] * ma0.xy + ms0.xy); o.y = pk2(xn[i][1] * ma0.zw + ms0.zw);
      o.z = pk2(xn[i][2] * ma1.xy + ms1.xy); o.w = pk2(xn[i][3] * ma1.zw + ms1.zw);
      if constexpr (i == 8) { hw0 += 65536; hw1 += 65536; }
      *(LAS v4u*)(L + ((i & 1) ? hw1 : hw0) + ((i >> 1) & 3) * 16384) = o;
      if constexpr (i == 3) read_b1(5, tz0, 0);
      if constexpr (i == 6) read_b1(6, tz0, 24576);
      if constexpr (i == 9) read_b1(7, tz2, 0);
      SCHED();
    });
  }
  BAR();
  STAMP(8);

  const f32x4 z4 = {0.f, 0.f, 0.f, 0.f};
  const u32 sOst = WS_O + ((g * 4 + 2 * blk2) * 16 + 2 * j + (hd >> 1)) * 1024;
  const int cend = j < 4 ? 22 * j + 22 : 88 + 21 * (j - 3);
  const int ch0 = (j < 4 ? 22 * j : 88 + 21 * (j - 4)) + 3 * w;
  const int cb0 = min(ch0, 169);
  const u32 sB4 = cb0 * 16;
  u32 hmask[3];
#pragma unroll
  for (int ct = 0; ct < 3; ++ct)
    asm("s_cmp_lt_u32 %1, %2\n\ts_cselect_b32 %0, -1, 0" : "=s"(hmask[ct]) : "s"((u32)(cb0 + ct - ch0)), "s"((u32)max(cend - ch0, 0)) : "scc");
  auto b4_piece = [&](auto PC, auto IC_, u32 mb) {
    constexpr int pair = decltype(PC)::value, i = decltype(IC_)::value;
    const u32 sp = ((pair + rotp) & 7) * (64 * 2752) + sB4;
    if constexpr (NODMA) {} else if constexpr (i < 3) dma<i * 1024, 0>(mb, vB4[i], rW1, sp); else dma<i * 1024 + 128, 0>(mb, vB4[i - 3], rW3, sp);
  };
  auto stage_b4 = [&](auto PC, u32 mb) { sfor<0, 6>([&](auto I) { b4_piece(PC, I, mb); }); };
  constexpr int ND = NODMA ? 0 : 1;
  constexpr int SL4 = 6272;
  const u32 R40 = w < 5 ? w * SL4 : (w < 7 ? 49152 + (w - 5) * SL4 : L_Z + 65536);
  const u32 R4A = L_XY + 32768 + w * SL4;
  const u32 R4B = L_Z + 23552 + w * SL4;
#pragma unroll
  for (int i = 0; i < 4; ++i) { afo[i] += blk2 * 32768; PIN(afo[i]); }
#define AFRAG(off, ks) (*(LAS v4u*)(L + afo[(ks) & 3] + ((off) + ((ks) >> 2) * 256)))
  v2u k1p, v1p;
  {
    const u32 mB3 = L_Z + (w >> 2) * 4096 + (w & 3) * 1024, sB3 = ((w >> 2) * 32 + 4 * (w & 3)) * 1024 + j * 128;
    f32x4 aKh = z4, aVh = z4, aQ = z4, aK = z4, aV = z4;
    v4u ah = AFRAG(0, 0), a1 = AFRAG(16384, 0);
    sfor<0, 16>([&](auto KS) {
      constexpr int ks = decltype(KS)::value;
      v4u nh = ah, n1 = a1;
      if constexpr (ks < 15) { nh = AFRAG(0, ks + 1); n1 = AFRAG(16384, ks + 1); }
      if constexpr (!(ks & 1) && !NODMA) dma<(ks / 2) * 8192, (ks / 2) * 65536>(mB3, vB3, rWO, sB3);
      SCHED();
      aKh = mfma32(bw[ks * 3 + 1], ah, aKh);
      aVh = mfma32(ah, bw[ks * 3 + 2], aVh);
      aQ = mfma32(bw[ks * 3 + 0], a1, aQ);
      aK = mfma32(bw[ks * 3 + 1], a1, aK);
      aV = mfma32(a1, bw[ks * 3 + 2], aV);
      ah = nh; a1 = n1;
      SCHED();
    });
    STAMP(9);
    k1p = pk4(aK); v1p = pk4(aV);
    const u64 pm = (g == 0 && blk2 == 0) ? 0ull : ~0ull;
    const u64 okp0[4] = {okp[0] & pm, okp[1] & pm, okp[2] & pm, okp[3] & pm};
    const v2u o = attend(pk4(aKh), pk4(aVh), k1p, v1p, pk4(aQ), okp0, okc);
    __builtin_amdgcn_raw_buffer_store_b64(o, rWS, vOst, sOst, 16);
  }
  STAMP(10);
  v4u gb_[4], gg_[4];
  u32 vgg, fl0;
  f32x4 bQ = z4, bK = z4, bV = z4;
  int tr4, pw, pr, t3; u32 vRow;
  v4u b3[16];
  auto read_b3 = [&](int s) {
#pragma unroll
    for (int n = 0; n < 4; ++n) { LAS char* a = L + ((n & 1) ? (t3 ^ 32) : t3) + (s * 4096 + 256 * (n >> 1)); b3[s * 4 + n] = cat(trd(a), trd(a + 512)); }
  };
  {
    v4u a2 = AFRAG(32768, 0), a3 = AFRAG(32768, 1);
    sfor<0, 16>([&](auto KS) {
      constexpr int ks = decltype(KS)::value;
      v4u n2 = a3;
      if constexpr (ks < 14) n2 = AFRAG(32768, ks + 2);
      if constexpr (ks == 2) { const u32 s0 = sMG + 12288, s1 = s0 + 24576, s2 = s0 + 49152, s3 = s0 + 73728;
        LD16S(gb_[0], vl16, rWS, s0, 0); LD16S(gb_[1], vl16, rWS, s1, 0); LD16S(gb_[2], vl16, rWS, s2, 0); LD16S(gb_[3], vl16, rWS, s3, 0); }
      if constexpr (ks == 3) { vgg = (vl16 < 512 ? 8192 : 20480 - 512) + 512 * j + vl16; const u32 s0 = WS_MODG, s1 = s0 + 24576, s2 = s0 + 49152, s3 = s0 + 73728;
        LD16S(gg_[0], vgg, rWS, s0, 0); LD16S(gg_[1], vgg, rWS, s1, 0); LD16S(gg_[2], vgg, rWS, s2, 0); LD16S(gg_[3], vgg, rWS, s3, 0); }
      if constexpr (ks == 7) {
        asm volatile("s_waitcnt vmcnt(0)" : "+v"(gb_[0]), "+v"(gb_[1]), "+v"(gb_[2]), "+v"(gb_[3]), "+v"(gg_[0]), "+v"(gg_[1]), "+v"(gg_[2]), "+v"(gg_[3])::"memory");
        BAR(); RAISE(flag_off(0, g, 0) + j * 128);
      }
      if constexpr (ks >= 7 && ks < 13) b4_piece(IC<0>{}, IC<ks - 7>{}, R40);
      if constexpr (ks >= 10 && ks < 14) read_b3(ks - 10);
      SCHED();
      bQ = mfma32(bw[ks * 3 + 0], a2, bQ);
      bK = mfma32(bw[ks * 3 + 1], a2, bK);
      bV = mfma32(a2, bw[ks * 3 + 2], bV);
      if constexpr (ks == 5) {
        const int l3 = fresh_lane(), c3 = l3 & 15, g3 = l3 >> 4, er = 4 * w + g3;
        tr4 = ((l3 >> 1) & 1) * 3200 + 48 * ((c3 >> 2) + 4 * (g3 & 1) + 16 * (g3 >> 1)) + 8 * (l3 & 1); PIN(tr4);
        vRow = (32 * (er >> 4) + (er & 15)) * 1024 + c3 * 8; PIN(vRow);
        pr = er * 256 + ((c3 ^ (er & 7)) << 4); PIN(pr);
        pw = kq * 8192 + (16 * mh + c3) * 256 + ((g3 ^ (c3 & 3)) << 4) + ((c3 & 4) << 4); PIN(pw);
        t3 = L_Z + kq * 16384 + g3 * 1024 + 64 * (c3 >> 2) + 8 * (l3 & 3) + 32 * (g3 & 1); PIN(t3);
      }
      if constexpr (ks == 8) {
        f32x2 mB, mG;
        fl0 = gsum(gb_, mB) | gsum(gg_, mG); PIN(fl0);
        *(LAS f32x2*)(L + L_MODA + w * 512 + (vl16 >> 1)) = mB + one;
        *(LAS f32x2*)(L + L_GAB + (vl16 >> 1)) = mG;
      }
      a2 = a3; a3 = n2;
      SCHED();
    });
  }
  PIN(bQ); PIN(bK); PIN(bV);
  if (__builtin_amdgcn_ballot_w64(fl0 != 0u)) {
    f32x2 mB, mG;
    gather(vl16, sMG + 12288, mB); gather(vgg, WS_MODG, mG);
    *(LAS f32x2*)(L + L_MODA + w * 512 + (vl16 >> 1)) = mB + one;
    *(LAS f32x2*)(L + L_GAB + (vl16 >> 1)) = mG;
  }
  STAMP(11);

  auto read_b4 = [&](int pair, u32 ldsbase) {
    LAS char* a0 = L + (tr4 + ldsbase);
#pragma unroll
    for (int kk = 0; kk < 2; ++kk)
#pragma unroll
      for (int ct = 0; ct < 3; ++ct) bw[(2 * pair + kk) * 3 + ct] = cat(trd(a0 + kk * 1536 + ct * 16), trd(a0 + kk * 1536 + ct * 16 + 384));
  };
  v4u af[4]; v2u xv0, xv1;
  auto p3_load = [&](int h, v2u& xv) {
    const u32 so = WS_O + ((g * 4 + 2 * mh + h) * 16 + 4 * kq) * 1024;
    LD16S(af[0], vl16, rWS, so, 0); LD16S(af[1], vl16, rWS, so, 1024); LD16S(af[2], vl16, rWS, so, 2048); LD16S(af[3], vl16, rWS, so, 3072);
    const u32 sx = g * 65536 + h * 16384 + j * 128;
    LD8(xv, vRow, rX, sx, 0);
  };
  auto p3_mma = [&]() {
    f32x4 acc[4] = {z4, z4, z4, z4};
#pragma unroll
    for (int s = 0; s < 4; ++s)
#pragma unroll
      for (int n = 0; n < 4; ++n) acc[n] = mfma32(b3[s * 4 + n], af[s], acc[n]);
#pragma unroll
    for (int n = 0; n < 4; ++n) *(LAS f32x4*)(L + L_XY + ((n & 1) ? (pw ^ 64) : pw) + (n >> 1) * 128) = acc[n];
  };
  auto p3_epi = [&](int h, v2u xv) {
    f32x4 y = *(LAS f32x4*)(L + L_XY + pr);
#pragma unroll
    for (int q = 1; q < 4; ++q) y += *(LAS f32x4*)(L + L_XY + q * 8192 + pr);
    const f32x4 gav = *(LAS f32x4*)(L + L_GAB + (vl16 & 255));
    const f32x4 xf = {blo(xv.x), bhi(xv.x), blo(xv.y), bhi(xv.y)};
    const f32x4 x1 = xf + gav * y;
    __builtin_amdgcn_raw_buffer_store_b64(pk4(x1), rWS, vRow, WS_X1 + g * 65536 + h * 16384 + j * 128, 16);
    const float mean = row16_sum((x1.x + x1.y) + (x1.z + x1.w)) * (1.f / 64.f);
    const f32x4 d = x1 - mean;
    const float m2 = row16_sum((d.x * d.x + d.y * d.y) + (d.z * d.z + d.w * d.w));
    v2u st; st.x = asu(mean); st.y = asu(m2);
    __builtin_amdgcn_raw_buffer_store_b64(st, rWS, (vRow >> 10) << 6, WS_STAT + g * 4096 + h * 1024 + j * 8, 16);
  };
  const u64 care2 = 0x0101010101010101ull * (3u << (2 * kq));
  {
    const v2u o = attend(k1p, v1p, pk4(bK), pk4(bV), pk4(bQ), okp, okc);
    __builtin_amdgcn_raw_buffer_store_b64(o, rWS, vOst, sOst + 16384, 16);
  }
  poll_flags(rWS, vfl, flag_off(0, g, 0), care2, tag);
  p3_load(0, xv0);
  read_b4(0, R40);
  BAR();
  RAISE(flag_off(0, g, 1) + j * 128);
  STAMP(12);
  stage_b4(IC<1>{}, R4A);
  asm volatile("s_waitcnt vmcnt(%c5)" : "+v"(af[0]), "+v"(af[1]), "+v"(af[2]), "+v"(af[3]), "+v"(xv0) : "i"(6 * ND) : "memory");
  p3_mma();
  stage_b4(IC<2>{}, R4B);
  BAR();
  STAMP(13);
  poll_flags(rWS, vfl, flag_off(0, g, 1), care2, tag);
  p3_load(1, xv1);
  p3_epi(0, xv0);
  read_b4(1, R4A); read_b4(2, R4B);
  STAMP(14);
  asm volatile("s_waitcnt vmcnt(0)" : "+v"(af[0]), "+v"(af[1]), "+v"(af[2]), "+v"(af[3]), "+v"(xv1)::"memory");
  BAR();
  RAISE(flag_off(1, g, 0) + j * 128);
  p3_mma();
  stage_b4(IC<3>{}, R4A);
  BAR();
  p3_epi(1, xv1);
  stage_b4(IC<4>{}, R4B);
  STAMP(15);

  v4u x1v[4]; v2u stv;
  auto ln2_load = [&](int h) {
    const u32 so = WS_X1 + g * 65536 + (32 * (w >> 2) + 16 * h + 4 * (w & 3)) * 1024;
    LD16S(x1v[0], vl16, rWS, so, 0); LD16S(x1v[1], vl16, rWS, so, 1024); LD16S(x1v[2], vl16, rWS, so, 2048); LD16S(x1v[3], vl16, rWS, so, 3072);
    const u32 vs = (vl16 >> 1) & 255, ss = WS_STAT + g * 4096 + (32 * (w >> 2) + 16 * h + 4 * (w & 3)) * 64;
    LD8S(stv, vs, rWS, ss, 0);
  };
  float l2r, l2n;
  auto ln2_stats = [&]() {
    const float mj = asf(stv.x);
    float mean = mj;
    mean += dppf<0xB1>(mean); mean += dppf<0x4E>(mean); mean += dppf<0x141>(mean);
    mean *= 0.125f;
    const float dm = mj - mean;
    float m2 = asf(stv.y) + 64.f * dm * dm;
    m2 += dppf<0xB1>(m2); m2 += dppf<0x4E>(m2); m2 += dppf<0x141>(m2);
    l2r = __builtin_amdgcn_rsqf(m2 * (1.f / 512.f) + 1e-5f);
    l2n = -mean * l2r;
  };
  int h2w = w * 4096 + (((vl16 - 128 * rotp) & 1023) ^ ((4 * w & 15) << 4)); PIN(h2w);
  auto ln2_rows = [&](int dst, int hf, int i0, int i1) {
    const f32x4 s0 = *(LAS f32x4*)(L + vma + hf * 16), a0 = *(LAS f32x4*)(L + vma + 2048 + hf * 16);
#pragma unroll
    for (int i = i0; i < i1; ++i) {
      const float rs = rdl(l2r, 8 * i), nn = rdl(l2n, 8 * i);
      v2u o;
      o.x = pk2((unp(x1v[i][2 * hf]) * rs + nn) * a0.xy + s0.xy);
      o.y = pk2((unp(x1v[i][2 * hf + 1]) * rs + nn) * a0.zw + s0.zw);
      *(LAS v2u*)(L + (h2w ^ (i << 4)) + (dst + i * 1024 + hf * 8)) = o;
    }
  };
  auto b5_base = [](int s) constexpr -> int {
    return s < 2 ? 65536 + s * 16384 : s < 5 ? L_Z + 24576 + (s - 2) * 16384 : s < 7 ? (s - 5) * 16384 : s < 9 ? 32768 + (s - 7) * 16384 : 65536 + (s - 9) * 16384;
  };
  u32 vB5, vH;
  const u32 mB5 = (w >> 2) * 4096 + (w & 3) * 1024, sB5 = ((w >> 2) * 352 + 8 * (w & 3)) * 1024 + j * 128;
  auto b5_piece = [&](auto SC, auto QC) {
    constexpr int s = decltype(SC)::value, qq = decltype(QC)::value;
    if constexpr (NODMA) {}
    else if constexpr (s == 10 && qq == 1) dma<b5_base(s) + qq * 8192, (qq * 704 + 32 * s) * 1024>(mB5, vB5, rW2, sB5 - (w >> 2) * 32768);
    else dma<b5_base(s) + qq * 8192, (qq * 704 + 32 * s) * 1024>(mB5, vB5, rW2, sB5);
  };
  auto swiglu_tile = [&](f32x4 (&acc)[3][2], int ct, int h) {
    f32x4 G, U, R;
#pragma unroll
    for (int e = 0; e < 4; ++e) {
      auto r = __builtin_amdgcn_permlane32_swap(asu(acc[ct][0][e]), asu(acc[ct][1][e]), false, false);
      G[e] = asf(r[0]); U[e] = asf(r[1]);
    }
    const f32x4 T = G * -1.4426950408889634f;
#pragma unroll
    for (int e = 0; e < 4; ++e) R[e] = __builtin_amdgcn_rcpf(1.f + __builtin_amdgcn_exp2f(T[e]));
    const v2u o = pk4((G * U) * R);
    const u32 so = WS_HH + ((g * 4 + h) * 172 + cb0 + ct) * 256;
    asm volatile("s_mov_b32 exec_lo, %4\n\ts_mov_b32 exec_hi, %4\n\tbuffer_store_dwordx2 %0, %1, %2, %3 offen sc1\n\ts_mov_b64 exec, -1"
                 ::"v"(o), "v"(vH), "s"(rWS), "s"(so), "s"(hmask[ct]) : "memory");
  };
#pragma unroll
  for (int i = 0; i < 4; ++i) { afo[i] -= blk2 * 32768; PIN(afo[i]); }
  poll_flags(rWS, vfl, flag_off(1, g, 0), ~0ull, tag);
  ln2_load(0);
  BAR();
  RAISE(flag_off(1, g, 1) + j * 128);
  read_b4(3, R4A); read_b4(4, R4B);
  LGW();
  stage_b4(IC<5>{}, R4A);
  asm volatile("s_waitcnt vmcnt(%c5)" : "+v"(x1v[0]), "+v"(x1v[1]), "+v"(x1v[2]), "+v"(x1v[3]), "+v"(stv) : "i"(6 * ND) : "memory");
  ln2_stats();
  ln2_rows(L_XY, 0, 0, 4);
  ln2_rows(L_XY, 1, 0, 4);
  BAR();
  STAMP(16);
  f32x4 accA[3][2] = {{z4, z4}, {z4, z4}, {z4, z4}};
  u32 fl1;
  {
    v4u a0 = AFRAG(0, 0), a1 = AFRAG(16384, 0);
    sfor<0, 16>([&](auto KS) {
      constexpr int ks = decltype(KS)::value;
      v4u n0 = a0, n1 = a1;
      if constexpr (ks < 15) { n0 = AFRAG(0, ks + 1); n1 = AFRAG(16384, ks + 1); }
      if constexpr (ks == 0) { const u32 fo = flag_off(1, g, 1); LD4S(fl1, vfl, rWS, fo); }
      if constexpr (ks < 3) { b4_piece(IC<6>{}, IC<2 * ks>{}, R4B); b4_piece(IC<6>{}, IC<2 * ks + 1>{}, R4B); }
      if constexpr (ks == 4) {
        asm volatile("s_waitcnt vmcnt(%c7)" : "+v"(fl1), "+v"(accA[0][0]), "+v"(accA[0][1]), "+v"(accA[1][0]), "+v"(accA[1][1]), "+v"(accA[2][0]), "+v"(accA[2][1]) : "i"(6 * ND) : "memory");
        if (__builtin_amdgcn_ballot_w64(fl1 != tag)) poll_flags(rWS, vfl, flag_off(1, g, 1), ~0ull, tag);
        ln2_load(1);
        read_b4(5, R4A);
      }
      if constexpr (ks == 6) { STAMP(29); VMW(5); read_b4(6, R4B); BAR(); STAMP(30); }
      if constexpr (ks >= 6 && ks < 9) { b4_piece(IC<7>{}, IC<2 * (ks - 6)>{}, R4B); b4_piece(IC<7>{}, IC<2 * (ks - 6) + 1>{}, R4B); }
      if constexpr (ks == 10) asm volatile("s_waitcnt vmcnt(%c5)" : "+v"(x1v[0]), "+v"(x1v[1]), "+v"(x1v[2]), "+v"(x1v[3]), "+v"(stv) : "i"(6 * ND) : "memory");
      if constexpr (ks == 13) { STAMP(31); VMW(0); read_b4(7, R4B); }
      if constexpr (ks == 13 || ks == 14) b5_piece(IC<0>{}, IC<ks - 13>{});
      SCHED();
      if constexpr (ks == 9) { const int fl = vl16 >> 4, qd = fl >> 2; vB5 = ((qd & 3) + 4 * (qd >> 3)) * 1024 + ((qd >> 2) & 1) * 64 + (((fl & 3) ^ ((w & 1) << 1)) << 4); PIN(vB5); }
      if constexpr (ks == 10) { ln2_stats(); PIN(l2r); PIN(l2n); }
      if constexpr (ks == 11) ln2_rows(L_XY + 32768, 0, 0, 4);
      if constexpr (ks == 12) ln2_rows(L_XY + 32768, 1, 0, 4);
#pragma unroll
      for (int ct = 0; ct < 3; ++ct) {
        accA[ct][0] = mfma32(bw[ks * 3 + ct], a0, accA[ct][0]);
        accA[ct][1] = mfma32(bw[ks * 3 + ct], a1, accA[ct][1]);
      }
      if constexpr (ks == 15) { vH = (vl16 & 255) + ((vl16 >> 5) & 8) + (vl16 >> 9) * (2 * 172 * 256); PIN(vH); }
      a0 = n0; a1 = n1;
      SCHED();
    });
  }
  BAR();
  STAMP(17);
  f32x4 accB[3][2] = {{z4, z4}, {z4, z4}, {z4, z4}};
  u32 vRow5, fl2; int pr5, pw5, t5, t5z, gb5;
  swiglu_tile(accA, 0, 0);
  {
    v4u a0 = AFRAG(32768, 0), a1 = AFRAG(49152, 0);
    sfor<0, 16>([&](auto KS) {
      constexpr int ks = decltype(KS)::value;
      v4u n0 = a0, n1 = a1;
      if constexpr (ks < 15) { n0 = AFRAG(32768, ks + 1); n1 = AFRAG(49152, ks + 1); }
      if constexpr (ks < 4) b5_piece(IC<1 + ks / 2>{}, IC<ks & 1>{});
      if constexpr (ks == 0) STAMP(23);
      if constexpr (ks == 4) STAMP(24);
      if constexpr (ks == 7) { STAMP(25); VMW(0); STAMP(26); BAR(); RAISE(flag_off(2, g, 0) + j * 128); STAMP(27); }
      if constexpr (ks == 12) STAMP(28);
      if constexpr (ks >= 7 && ks < 15) b5_piece(IC<3 + (ks - 7) / 2>{}, IC<(ks - 7) & 1>{});
      if constexpr (ks == 15) { const u32 fo = flag_off(2, g, 0); LD4S(fl2, vfl, rWS, fo); }
      SCHED();
      if constexpr (ks == 1) swiglu_tile(accA, 1, 0);
      if constexpr (ks == 3) swiglu_tile(accA, 2, 0);
      if constexpr (ks == 13) {
        const int l5 = fresh_lane(), c5 = l5 & 15, g5 = l5 >> 4, er = 4 * w + g5;
        vRow5 = (32 * (er >> 4) + (er & 15)) * 1024 + c5 * 8; PIN(vRow5);
        pr5 = er * 256 + ((c5 ^ (er & 7)) << 4); PIN(pr5);
        pw5 = kq * 8192 + (16 * mh + c5) * 256 + ((g5 ^ (c5 & 3)) << 4) + ((c5 & 4) << 4); PIN(pw5);
        t5 = kq * 4096 + g5 * 1024 + 64 * (c5 >> 2) + 8 * (l5 & 3) + 32 * (g5 & 1); PIN(t5);
        t5z = t5 + 65536; PIN(t5z);
        gb5 = L_GAB + 256 + c5 * 16; PIN(gb5);
      }
#pragma unroll
      for (int ct = 0; ct < 3; ++ct) {
        accB[ct][0] = mfma32(bw[ks * 3 + ct], a0, accB[ct][0]);
        accB[ct][1] = mfma32(bw[ks * 3 + ct], a1, accB[ct][1]);
      }
      a0 = n0; a1 = n1;
      SCHED();
    });
  }
  STAMP(18);
  int t5y = t5 + 106496; PIN(t5y);
  auto read_b5 = [&](int s) {
    const int bs = b5_base(s);
    const int tb = bs < 65536 ? t5 : bs < 106496 ? t5z : t5y, off = bs < 65536 ? bs : bs < 106496 ? bs - 65536 : bs - 106496;
#pragma unroll
    for (int n = 0; n < 4; ++n) {
      LAS char* a = L + ((n & 1) ? (tb ^ 32) : tb) + (off + 256 * (n >> 1));
      bw[s * 4 + n] = cat(trd(a), trd(a + 512));
    }
  };
  const u64 care5 = 0x0101010101010101ull * (kq == 0 ? 0x03u : kq == 1 ? 0x0cu : kq == 2 ? 0x70u : 0xc0u);
  v4u hf[11]; v2u xq;
  auto p5_load = [&](int h) {
    const u32 so = WS_HH + ((g * 4 + 2 * mh + h) * 172 + 44 * kq) * 256;
    const u32 so2 = so + 4096, so3 = so + 8192;
    LD16S(hf[0], vl16, rWS, so, 0); LD16S(hf[1], vl16, rWS, so, 1024); LD16S(hf[2], vl16, rWS, so, 2048); LD16S(hf[3], vl16, rWS, so, 3072);
    LD16S(hf[4], vl16, rWS, so2, 0); LD16S(hf[5], vl16, rWS, so2, 1024); LD16S(hf[6], vl16, rWS, so2, 2048); LD16S(hf[7], vl16, rWS, so2, 3072);
    LD16S(hf[8], vl16, rWS, so3, 0); LD16S(hf[9], vl16, rWS, so3, 1024);
    const u32 so4 = kq == 3 ? so3 + 1024 : so3 + 2048;
    LD16S(hf[10], vl16, rWS, so4, 0);
    const u32 sq = WS_X1 + g * 65536 + h * 16384 + j * 128;
    LD8S(xq, vRow5, rWS, sq, 0);
  };
  auto p5_epi = [&](int h, v2u xx) {
    f32x4 y = *(LAS f32x4*)(L + h * 32768 + pr5);
#pragma unroll
    for (int q = 1; q < 4; ++q) y += *(LAS f32x4*)(L + h * 32768 + q * 8192 + pr5);
    const f32x4 gbv = *(LAS f32x4*)(L + gb5);
    const f32x4 xb = {blo(xx.x), bhi(xx.x), blo(xx.y), bhi(xx.y)};
    __builtin_amdgcn_raw_buffer_store_b64(pk4(xb + gbv * y), rOUT, vRow5, g * 65536 + h * 16384 + j * 128, 0);
  };
  f32x4 acc[4];
  auto p5_mma = [&](int s0, int s1) {
#pragma unroll
    for (int s = s0; s < s1; ++s)
#pragma unroll
      for (int n = 0; n < 4; ++n) acc[n] = mfma32(bw[s * 4 + n], hf[s], acc[n]);
  };
  swiglu_tile(accB, 0, 1);
  PIN(accB[1][0]); PIN(accB[1][1]); PIN(accB[2][0]); PIN(accB[2][1]);
  asm volatile("s_waitcnt vmcnt(1)" : "+v"(fl2)::"memory");
  if (__builtin_amdgcn_ballot_w64(fl2 != tag) & care5) poll_flags(rWS, vfl, flag_off(2, g, 0), care5, tag);
  p5_load(0);
  BAR();
  swiglu_tile(accB, 1, 1);
  b5_piece(IC<7>{}, IC<0>{}); b5_piece(IC<7>{}, IC<1>{});
  swiglu_tile(accB, 2, 1);
  b5_piece(IC<8>{}, IC<0>{}); b5_piece(IC<8>{}, IC<1>{});
  STAMP(19);
#pragma unroll
  for (int s = 0; s < 7; ++s) read_b5(s);
  asm volatile("s_waitcnt vmcnt(0)" : "+v"(hf[0]), "+v"(hf[1]), "+v"(hf[2]), "+v"(hf[3]), "+v"(hf[4]), "+v"(hf[5]), "+v"(hf[6]), "+v"(hf[7]), "+v"(hf[8]), "+v"(hf[9]), "+v"(hf[10]), "+v"(xq)::"memory");
  BAR();
  RAISE(flag_off(2, g, 1) + j * 128);
  b5_piece(IC<9>{}, IC<0>{}); b5_piece(IC<9>{}, IC<1>{}); b5_piece(IC<10>{}, IC<0>{}); b5_piece(IC<10>{}, IC<1>{});
  read_b5(7); read_b5(8);
  STAMP(20);
  acc[0] = z4; acc[1] = z4; acc[2] = z4; acc[3] = z4;
  p5_mma(0, 9);
  PIN(acc[0]); PIN(acc[1]); PIN(acc[2]); PIN(acc[3]);
  VMW(0);
  BAR();
  read_b5(9); read_b5(10);
  if (kq == 3) hf[10] = v4u{0u, 0u, 0u, 0u};
  p5_mma(9, 11);
#pragma unroll
  for (int n = 0; n < 4; ++n) *(LAS f32x4*)(L + ((n & 1) ? (pw5 ^ 64) : pw5) + (n >> 1) * 128) = acc[n];
  const v2u xqa = xq;
  poll_flags(rWS, vfl, flag_off(2, g, 1), care5, tag);
  p5_load(1);
  BAR();
  p5_epi(0, xqa);
  STAMP(21);
  acc[0] = z4; acc[1] = z4; acc[2] = z4; acc[3] = z4;
  asm volatile("s_waitcnt vmcnt(9)" : "+v"(hf[0]), "+v"(hf[1]), "+v"(hf[2]), "+v"(hf[3])::"memory");
  p5_mma(0, 4);
  asm volatile("s_waitcnt vmcnt(5)" : "+v"(hf[4]), "+v"(hf[5]), "+v"(hf[6]), "+v"(hf[7])::"memory");
  p5_mma(4, 8);
  asm volatile("s_waitcnt vmcnt(1)" : "+v"(hf[8]), "+v"(hf[9]), "+v"(hf[10]), "+v"(xq)::"memory");
  if (kq == 3) hf[10] = v4u{0u, 0u, 0u, 0u};
  p5_mma(8, 11);
#pragma unroll
  for (int n = 0; n < 4; ++n) *(LAS f32x4*)(L + 32768 + ((n & 1) ? (pw5 ^ 64) : pw5) + (n >> 1) * 128) = acc[n];
  BAR();
  p5_epi(1, xq);
  STAMP(22);
  STAMP_DUMP();
  if (b == 0 && w == 0) {
    for (u32 sp = 0; sp < SPIN_CAP; ++sp) {
      asm volatile("" ::: "memory");
      const v4u v = __builtin_amdgcn_raw_buffer_load_b128(rWS, vl16, WS_ALIVE, 16);
      if (__all(v.x == tag && v.y == tag && v.z == tag && v.w == tag)) break;
    }
    __builtin_amdgcn_raw_buffer_store_b32(tag, rWS, 0, WS_EPOCH, 16);
  }
}
}

extern "C" void kernel_launch(void* const* d_in, const int* in_sizes, int n_in, void* d_out, int out_size, void* d_ws, size_t ws_size,
                              hipStream_t stream) {
  Params p;
  p.x = (const u16*)d_in[0]; p.wqkv = (const u16*)d_in[1]; p.wo = (const u16*)d_in[2];
  p.w1 = (const u16*)d_in[4]; p.w3 = (const u16*)d_in[5]; p.w2 = (const u16*)d_in[6];
  p.c = (const u16*)d_in[7]; p.wmod = (const u16*)d_in[8];
  p.out = (u16*)d_out; p.ws = (char*)d_ws;
  (void)hipFuncSetAttribute((const void*)fused_block, hipFuncAttributeMaxDynamicSharedMemorySize, L_TOTAL);
  fused_block<<<256, 512, L_TOTAL, stream>>>(p);
}
```

```cpp
#include <hip/hip_runtime.h>
#include <stdint.h>

#define KDBG 0
#define NODMA 0
#define CODEWARM 1
#define L2WARM 1
#define XMAP 2

#define DI __device__ __forceinline__
#define GAS __attribute__((address_space(1)))
#define LAS __attribute__((address_space(3)))
typedef unsigned u32;
typedef unsigned short u16;
typedef unsigned long long u64;
typedef u32 v4u __attribute__((ext_vector_type(4)));
typedef u32 v2u __attribute__((ext_vector_type(2)));
typedef float f32x4 __attribute__((ext_vector_type(4)));
typedef float f32x2 __attribute__((ext_vector_type(2)));
typedef __bf16 bf16x8 __attribute__((ext_vector_type(8)));
typedef __bf16 bf16x2 __attribute__((ext_vector_type(2)));
typedef short s16x4 __attribute__((ext_vector_type(4)));
typedef __amdgpu_buffer_rsrc_t rsrc_t;
typedef GAS u32 gu32;
typedef GAS u64 gu64;
#define RLX_AGENT __ATOMIC_RELAXED, __HIP_MEMORY_SCOPE_AGENT

namespace {
constexpr int S_ = 2048, D_ = 512, F_ = 1376;
constexpr u32 WS_EPOCH = 0;
constexpr u32 WS_ALIVE = 4096;
constexpr u32 WS_FLAG = 32768;
constexpr u32 WS_STAT = 262144;
constexpr u32 WS_MODG = 524288;
constexpr u32 WS_MODF = 655360;
constexpr u32 WS_O = 1u << 20;
constexpr u32 WS_X1 = 3u << 20;
constexpr u32 WS_HH = 5u << 20;
constexpr u32 WS_RFLAG = 8192;
constexpr u32 WS_B4T = 11u << 20;
constexpr u32 WS_DBG = 16u << 20;
constexpr u32 WS_BYTES = 32u << 20;
constexpr int L_XY = 0;
constexpr int L_Z = 81920;
constexpr int L_MISC = 158720;
constexpr int L_TOTAL = 163840;
constexpr int L_RED = 73728;
constexpr int L_MODA = L_MISC;
constexpr int L_CNT = L_MISC + 4096;
constexpr int L_GAB = L_MISC + 4096 + 64;
constexpr int L_JUNK = L_MISC + 4096 + 64 + 512;
constexpr int L_STAMP = 156672;
constexpr u32 SPIN_CAP = 1u << 18;
constexpr u32 CODE_BYTES = 26112;

DI f32x4 mfma32(v4u a, v4u b, f32x4 c) {
  return __builtin_amdgcn_mfma_f32_16x16x32_bf16(__builtin_bit_cast(bf16x8, a), __builtin_bit_cast(bf16x8, b), c, 0, 0, 0);
}
DI f32x4 mfma16(v2u a, v2u b, f32x4 c) {
  return __builtin_amdgcn_mfma_f32_16x16x16bf16_1k(__builtin_bit_cast(s16x4, a), __builtin_bit_cast(s16x4, b), c, 0, 0, 0);
}
DI u32 pk(float lo, float hi) { f32x2 v = {lo, hi}; return __builtin_bit_cast(u32, __builtin_convertvector(v, bf16x2)); }
DI u32 pk2(f32x2 v) { return __builtin_bit_cast(u32, __builtin_convertvector(v, bf16x2)); }
DI v2u pk4(f32x4 v) { v2u r; r.x = pk(v.x, v.y); r.y = pk(v.z, v.w); return r; }
DI float asf(u32 u) { return __builtin_bit_cast(float, u); }
DI u32 asu(float f) { return __builtin_bit_cast(u32, f); }
DI float blo(u32 u) { return asf(u << 16); }
DI float bhi(u32 u) { return asf(u & 0xffff0000u); }
DI f32x2 unp(u32 u) { f32x2 r = {blo(u), bhi(u)}; return r; }
DI float dot2(u32 a, u32 b, float c) { return __builtin_amdgcn_fdot2_f32_bf16(__builtin_bit_cast(bf16x2, a), __builtin_bit_cast(bf16x2, b), c, false); }
DI v2u trd(LAS char* p) { return __builtin_bit_cast(v2u, __builtin_amdgcn_ds_read_tr16_b64_v4i16((LAS s16x4*)p)); }
DI v4u cat(v2u a, v2u b) { v4u r; r.x = a.x; r.y = a.y; r.z = b.x; r.w = b.y; return r; }
template <int MI, int SI> DI void dma(u32 mb, u32 voff, rsrc_t srd, u32 sb) {
  u32 t;
  asm volatile("s_add_u32 m0, %1, %c2\n\ts_add_u32 %0, %3, %c4\n\tbuffer_load_dwordx4 %5, %6, %0 offen lds"
               : "=&s"(t) : "s"(mb), "i"(MI), "s"(sb), "i"(SI), "v"(voff), "s"(srd) : "scc");
}
DI void touch(u32 voff, rsrc_t srd, u32 sb) {
  asm volatile("s_mov_b32 m0, %c0\n\ts_nop 0\n\tbuffer_load_dword %1, %2, %3 offen lds" ::"i"(L_JUNK), "v"(voff), "s"(srd), "s"(sb));
}
#define BAR() asm volatile("s_waitcnt lgkmcnt(0)\n\ts_barrier" ::: "memory")
#define VMW(n) asm volatile("s_waitcnt vmcnt(" #n ")" ::: "memory")
#define LGW() asm volatile("s_waitcnt lgkmcnt(0)" ::: "memory")
#define SCHED() __builtin_amdgcn_sched_barrier(0)
#define LD16(dst, voff, srd, soff, IMM) asm volatile("buffer_load_dwordx4 %0, %1, %2, %3 offen offset:" #IMM : "=v"(dst) : "v"(voff), "s"(srd), "s"(soff))
#define LD16S(dst, voff, srd, soff, IMM) asm volatile("buffer_load_dwordx4 %0, %1, %2, %3 offen offset:" #IMM " sc1" : "=v"(dst) : "v"(voff), "s"(srd), "s"(soff))
#define LD8(dst, voff, srd, soff, IMM) asm volatile("buffer_load_dwordx2 %0, %1, %2, %3 offen offset:" #IMM : "=v"(dst) : "v"(voff), "s"(srd), "s"(soff))
#define LD8S(dst, voff, srd, soff, IMM) asm volatile("buffer_load_dwordx2 %0, %1, %2, %3 offen offset:" #IMM " sc1" : "=v"(dst) : "v"(voff), "s"(srd), "s"(soff))
#define LD4(dst, voff, srd, soff) asm volatile("buffer_load_dword %0, %1, %2, %3 offen" : "=v"(dst) : "v"(voff), "s"(srd), "s"(soff))
#define LD4S(dst, voff, srd, soff) asm volatile("buffer_load_dword %0, %1, %2, %3 offen sc1" : "=v"(dst) : "v"(voff), "s"(srd), "s"(soff))

template <int CTRL> DI float dppf(float v) { return asf(__builtin_amdgcn_update_dpp(0, asu(v), CTRL, 0xf, 0xf, true)); }
DI float row16_sum(float v) { v += dppf<0x128>(v); v += dppf<0x124>(v); v += dppf<0x122>(v); v += dppf<0x121>(v); return v; }
DI float sw32_add(float a, float b) { auto r = __builtin_amdgcn_permlane32_swap(asu(a), asu(b), false, false); return asf(r[0]) + asf(r[1]); }
DI float sw16_add(float a, float b) { auto r = __builtin_amdgcn_permlane16_swap(asu(a), asu(b), false, false); return asf(r[0]) + asf(r[1]); }
DI float x16_max(float v) { auto r = __builtin_amdgcn_permlane16_swap(asu(v), asu(v), false, false); return fmaxf(asf(r[0]), asf(r[1])); }
DI float x32_max(float v) { auto r = __builtin_amdgcn_permlane32_swap(asu(v), asu(v), false, false); return fmaxf(asf(r[0]), asf(r[1])); }
DI int fresh_lane() { int l; asm volatile("v_mbcnt_lo_u32_b32 %0, -1, 0\n\tv_mbcnt_hi_u32_b32 %0, -1, %0" : "=v"(l)); return l; }
DI float rdl(float v, int l) { return asf(__builtin_amdgcn_readlane(asu(v), l)); }

struct Params {
  const u16 *x, *wqkv, *wo, *w1, *w3, *w2, *c, *wmod;
  u16* out;
  char* ws;
};

#define LOOPSEL 0
#define STAMP(i) do {} while (0)
#define KSTAMP(lp, i) do {} while (0)
#define STAMP_DUMP() do {} while (0)

DI u32 flag_off(int phase, int g, int h) { return WS_FLAG + ((phase * 32 + g) * 2 + h) * 1024; }
DI void poll_flags(rsrc_t rWS, u32 vfl, u32 foff, u64 care, u32 tag) {
  for (u32 sp = 0; sp < SPIN_CAP; ++sp) {
    asm volatile("" ::: "memory");
    const u32 v = __builtin_amdgcn_raw_buffer_load_b32(rWS, vfl, foff, 16);
    if (!(__builtin_amdgcn_ballot_w64(v != tag) & care)) break;
  }
  VMW(0);
}
DI void raise_flag(rsrc_t rWS, u32 foff, u32 vtag, u32 vzero, u32 m1) {
  asm volatile("s_mov_b32 exec_hi, 0\n\ts_mov_b32 exec_lo, %4\n\tbuffer_store_dword %0, %1, %2, %3 offen sc1\n\ts_mov_b64 exec, -1" ::"v"(vtag), "v"(vzero), "s"(rWS), "s"(foff), "s"(m1) : "memory");
}
DI void signal_flag(rsrc_t rWS, u32 cnt_addr, u32 foff, u32 tag) {
  u32 t, a = cnt_addr, one = 1u, vt = tag, vz = 0u;
  asm volatile("s_mov_b64 exec, 1\n\tds_add_rtn_u32 %0, %1, %2\n\ts_waitcnt lgkmcnt(0)\n\tv_cmp_eq_u32 vcc, 7, %0\n\ts_and_b64 exec, exec, vcc\n\t"
               "buffer_store_dword %3, %4, %5, %6 offen sc1\n\ts_mov_b64 exec, -1"
               : "=&v"(t) : "v"(a), "v"(one), "v"(vt), "v"(vz), "s"(rWS), "s"(foff) : "vcc", "scc", "memory");
}
DI void arrive(u32 cnt_addr) {
  u32 a = cnt_addr, one = 1u;
  asm volatile("s_waitcnt lgkmcnt(0)\n\ts_mov_b64 exec, 1\n\tds_add_u32 %0, %1\n\ts_mov_b64 exec, -1" ::"v"(a), "v"(one) : "memory");
}
DI void await8(u32 cnt_addr) {
  u32 a = cnt_addr, t, sv;
  asm volatile("1:\n\tds_read_b32 %0, %2\n\ts_waitcnt lgkmcnt(0)\n\tv_readfirstlane_b32 %1, %0\n\ts_nop 0\n\ts_cmp_lt_u32 %1, 8\n\ts_cbranch_scc1 1b"
               : "=&v"(t), "=&s"(sv) : "v"(a) : "scc", "memory");
}
#define PIN(x) asm volatile("" : "+v"(x))
DI float qp(float r) { PIN(r); return r; }
DI float q_shl(u32 a) { return qp(blo(a)); }
DI float q_hi(u32 a) { return qp(bhi(a)); }
DI float q_fma(float a, float b, float c) { return qp(__builtin_fmaf(a, b, c)); }
DI float q_fmas(float a, float b, float c) { return qp(__builtin_fmaf(a, b, c)); }
DI float q_mul(float a, float b) { return qp(a * b); }
DI float q_mulk(float a) { return qp(a * -1.4426950408889634f); }
DI float q_add1(float a) { return qp(a + 1.f); }
DI float q_exp(float a) { return qp(__builtin_amdgcn_exp2f(a)); }
DI float q_rcp(float a) { return qp(__builtin_amdgcn_rcpf(a)); }
DI u32 q_cvt(float a, float b) { u32 r = pk(a, b); PIN(r); return r; }
DI float q_mov(float a) { return qp(a); }
DI u32 opq(u32 v) { asm volatile("" : "+v"(v)); return v; }

DI float sel(u64 m, float a, float b) { return __builtin_amdgcn_inverse_ballot_w64(m) ? a : b; }
DI v2u attend(v2u kp, v2u vp, v2u kc, v2u vc, v2u q, u64 m0, u64 m1, u32 hm, bool noprev) {
  const f32x4 z = {0.f, 0.f, 0.f, 0.f};
  const f32x4 sp = mfma16(kp, q, z);
  const f32x4 sc = mfma16(kc, q, z);
  const float K2 = 0.25f * 1.4426950408889634f;
  const u64 par = 0xaaaaaaaaaaaaaaaaull;
  float p0 = sel(par, sp[1], sp[0]), p1 = sel(par, sp[3], sp[2]);
  if (noprev) { p0 = -INFINITY; p1 = -INFINITY; }
  const float t0 = sel(m0, sel(par, sc[1], sc[0]), p0), t1 = sel(m1, sel(par, sc[3], sc[2]), p1);
  const float m = x32_max(x16_max(fmaxf(t0, t1)));
  const float mk = -m * K2;
  const float e0 = __builtin_amdgcn_exp2f(__builtin_fmaf(t0, K2, mk)), e1 = __builtin_amdgcn_exp2f(__builtin_fmaf(t1, K2, mk));
  const u32 w0 = pk(e0, e0) & hm, w1 = pk(e1, e1) & hm;
  v4u pf;
  pf.x = __builtin_amdgcn_inverse_ballot_w64(m0) ? 0u : w0; pf.y = __builtin_amdgcn_inverse_ballot_w64(m1) ? 0u : w1;
  pf.z = __builtin_amdgcn_inverse_ballot_w64(m0) ? w0 : 0u; pf.w = __builtin_amdgcn_inverse_ballot_w64(m1) ? w1 : 0u;
  const f32x4 o = mfma32(cat(vp, vc), pf, z);
  float sum = e0 + e1;
  sum = sw16_add(sum, sum); sum = sw32_add(sum, sum);
  return pk4(o * __builtin_amdgcn_rcpf(sum));
}

template <int N> using IC = std::integral_constant<int, N>;
template <int I, int N, class F> DI void sfor(F&& f) { if constexpr (I < N) { f(IC<I>{}); sfor<I + 1, N>(f); } }

__global__ void __launch_bounds__(512, 2) fused_block(Params p) {
  extern __shared__ __attribute__((aligned(16))) char smem[];
  LAS char* L = (LAS char*)smem;
  const int tid = threadIdx.x, lane = tid & 63, w = __builtin_amdgcn_readfirstlane(tid >> 6);
  const int c16 = lane & 15, g4 = lane >> 4;
  const int b = blockIdx.x;
  int g, j;
  if (XMAP == 0) { g = (b & 7) * 4 + ((b >> 3) & 3); j = b >> 5; }
  else if (XMAP == 2) { const int x = b & 7, r = b >> 3; g = (x >> 1) * 8 + (r & 7); j = (x & 1) * 4 + (r >> 3); }
  else { const int x = b & 7, r = b >> 3; g = (x >> 2) * 16 + (r & 15); j = (x & 3) * 2 + (r >> 4); }
  const rsrc_t rX = __builtin_amdgcn_make_buffer_rsrc((void*)p.x, 0, S_ * D_ * 2, 0x00020000);
  const rsrc_t rQKV = __builtin_amdgcn_make_buffer_rsrc((void*)p.wqkv, 0, D_ * 3 * D_ * 2, 0x00020000);
  const rsrc_t rWO = __builtin_amdgcn_make_buffer_rsrc((void*)p.wo, 0, D_ * D_ * 2, 0x00020000);
  const rsrc_t rW1 = __builtin_amdgcn_make_buffer_rsrc((void*)p.w1, 0, D_ * F_ * 2, 0x00020000);
  const rsrc_t rW3 = __builtin_amdgcn_make_buffer_rsrc((void*)p.w3, 0, D_ * F_ * 2, 0x00020000);
  const rsrc_t rW2 = __builtin_amdgcn_make_buffer_rsrc((void*)p.w2, 0, F_ * D_ * 2, 0x00020000);
  const rsrc_t rC = __builtin_amdgcn_make_buffer_rsrc((void*)p.c, 0, D_ * 2, 0x00020000);
  const rsrc_t rMOD = __builtin_amdgcn_make_buffer_rsrc((void*)p.wmod, 0, D_ * 6 * D_ * 2, 0x00020000);
  const rsrc_t rWS = __builtin_amdgcn_make_buffer_rsrc((void*)p.ws, 0, WS_BYTES, 0x00020000);
  const rsrc_t rOUT = __builtin_amdgcn_make_buffer_rsrc((void*)p.out, 0, S_ * D_ * 2, 0x00020000);
  STAMP(0);

  u32 ep;
  { const u32 vz = 0; asm volatile("buffer_load_dword %0, %1, %2, 0 offen sc1" : "=v"(ep) : "v"(vz), "s"(rWS)); }
  u32 vl16 = lane * 16; PIN(vl16);
  const int bm = b < 192 ? b : b - 192, mq = bm / 48, cl = bm - 48 * mq;
  v4u wm[4]; v2u cv; u32 ep0;
  if (w < 4) {
    const u32 vm = (tid >> 3) * 24576 + (tid & 7) * 16, s0 = mq * (128 * 6144) + cl * 128, s1 = s0 + 6144, s2 = s0 + 12288, s3 = s0 + 18432;
    LD16(wm[0], vm, rMOD, s0, 0); LD16(wm[1], vm, rMOD, s1, 0); LD16(wm[2], vm, rMOD, s2, 0); LD16(wm[3], vm, rMOD, s3, 0);
    const u32 vc = (tid >> 3) * 8, scq = mq * 256;
    LD8(cv, vc, rC, scq, 0);
    const u32 vz = 0; asm volatile("buffer_load_dword %0, %1, %2, 0 offen sc1" : "=v"(ep0) : "v"(vz), "s"(rWS));
  }
  if (tid < 16) *(LAS u32*)(L + L_CNT + tid * 4) = 0u;
  if (w == 7) {
    u64 pc; asm volatile("s_getpc_b64 %0" : "=s"(pc));
    const rsrc_t rPC = __builtin_amdgcn_make_buffer_rsrc((void*)(pc & ~127ull), 0, CODE_BYTES, 0x00020000);
    touch(lane * 128 + ((b >> 3) & 3) * 8192, rPC, 0);
  }
  BAR();
  const int st_qd = lane >> 2, st_r = (st_qd & 3) + 4 * (st_qd >> 3);
  const int st_col = ((st_qd >> 2) & 1) * 64 + (((lane & 3) ^ ((w & 1) << 1)) << 4);
  const int rotp = g & 7;
  u32 vB1 = st_r * 3072 + st_col; PIN(vB1);
  const u32 mB1 = (w >> 2) * 12288 + (w & 3) * 1024, sB1 = (w >> 2) * 98304 + (w & 3) * 24576 + j * 128;
  auto stage_b1 = [&](auto PC, auto BC) {
    constexpr int pair = decltype(PC)::value, base = decltype(BC)::value;
    const u32 sp = ((pair + rotp) & 7) * 196608 + sB1;
    dma<base, 0>(mB1, vB1, rQKV, sp); dma<base + 4096, 1024>(mB1, vB1, rQKV, sp); dma<base + 8192, 2048>(mB1, vB1, rQKV, sp);
  };
  const int hd = w & 3, blk2 = w >> 2, kq = w & 3, mh = w >> 2;
  const int cs0 = j < 4 ? 22 * j : 88 + 21 * (j - 4);
  const int cend = cs0 + (j < 4 ? 22 : 21);
  STAMP(1);
  const int T0 = 12 * (g >> 4) + 3 * (w & 3), rcb = min(cs0 + T0, 168);
  if (w >= 4) {
    const int u = lane >> 2, row = (u & 3) | ((u >> 1) & 4) | ((u << 1) & 8);
    const u32 vo = row * 2752 + (lane & 3) * 16, so = (g & 15) * (32 * 2752) + rcb * 16, mb = L_Z + 49152 + (w & 3) * 4096;
    dma<0, 0>(mb, vo, rW1, so); dma<1024, 16 * 2752>(mb, vo, rW1, so); dma<2048, 0>(mb, vo, rW3, so); dma<3072, 16 * 2752>(mb, vo, rW3, so);
  } else {
    asm volatile("s_waitcnt vmcnt(0)" : "+v"(cv), "+v"(wm[0]), "+v"(wm[1]), "+v"(wm[2]), "+v"(wm[3]), "+v"(ep0)::"memory");
    const u32 tg = __builtin_amdgcn_readfirstlane(ep0) + 1u;
    const f32x4 cf = {blo(cv.x), bhi(cv.x), blo(cv.y), bhi(cv.y)};
    float sc[4];
#pragma unroll
    for (int r = 0; r < 4; ++r) sc[r] = cf[r] * __builtin_amdgcn_rcpf(1.f + __builtin_amdgcn_exp2f(cf[r] * -1.4426950408889634f));
    f32x2 v[4];
#pragma unroll
    for (int i = 0; i < 4; ++i) v[i] = (unp(wm[0][i]) * sc[0] + unp(wm[1][i]) * sc[1]) + (unp(wm[2][i]) * sc[2] + unp(wm[3][i]) * sc[3]);
    const float a0 = sw32_add(v[0].x, v[2].x), a1 = sw32_add(v[0].y, v[2].y), a2 = sw32_add(v[1].x, v[3].x), a3 = sw32_add(v[1].y, v[3].y);
    float t0 = sw16_add(a0, a2), t1 = sw16_add(a1, a3);
    t0 += dppf<0x128>(t0); t1 += dppf<0x128>(t1);
    f32x2 o = {t0, t1};
    *(LAS f32x2*)(L + L_RED + w * 256 + (lane & 7) * 32 + g4 * 8) = o;
    LGW();
    u32 old = 0;
    if (lane == 0) old = __hip_atomic_fetch_add((LAS u32*)(L + L_CNT + 28), 1u, __ATOMIC_RELAXED, __HIP_MEMORY_SCOPE_WORKGROUP);
    if (__builtin_amdgcn_readfirstlane(old) == 3u) {
      asm volatile("" ::: "memory");
      float s = 0.f;
#pragma unroll
      for (int i = 0; i < 4; ++i) s += *(LAS float*)(L + L_RED + i * 256 + lane * 4);
      v2u gr; gr.x = asu(s); gr.y = tg;
      if (b < 192) __builtin_amdgcn_raw_buffer_store_b64(gr, rWS, lane * 8, WS_MODG + (mq * 3072 + cl * 64) * 8, 16);
      __builtin_amdgcn_raw_buffer_store_b32(tg, rWS, 0, WS_ALIVE + b * 4, 16);
    }
  }
  STAMP(2);
  v4u xr[10];
  {
    const int r0 = 64 * g - 16 + w;
    const u32 s0 = (u32)max(r0, 0) * 1024u, s1 = (u32)max(r0 + 8, 0) * 1024u;
    LD16(xr[0], vl16, rX, s0, 0); LD16(xr[1], vl16, rX, s1, 0);
#pragma unroll
    for (int i = 2; i < 10; ++i) { const u32 so = (u32)(r0 + 8 * i) * 1024u; LD16(xr[i], vl16, rX, so, 0); }
  }
  stage_b1(IC<0>{}, IC<L_XY>{});
  int afo[4];
#pragma unroll
  for (int i = 0; i < 4; ++i) { afo[i] = blk2 * 32768 + c16 * 1024 + (((4 * i + g4) ^ c16) << 4); PIN(afo[i]); }
  const u64 am0 = __builtin_amdgcn_ballot_w64((c16 & 14) >= 4 * g4), am1 = __builtin_amdgcn_ballot_w64((c16 & 14) >= 4 * g4 + 2);
  u32 ahm = (lane & 1) ? 0xffff0000u : 0xffffu; PIN(ahm);
  u32 vOst = c16 * 16 + g4 * 256 + (hd & 1) * 8; PIN(vOst);
  int tB1 = g4 * 1024 + 64 * (c16 >> 2) + 8 * (lane & 3) + 32 * ((hd & 1) ^ (g4 & 1)) + 256 * (hd >> 1); PIN(tB1);
  int hw0 = w * 1024 + ((((lane - 8 * rotp) & 63) ^ w) << 4); PIN(hw0);
#define vfl ((opq(vl16) & 112u) << 3)
  STAMP(3);
  v4u bw[48];
  auto read_b1 = [&](int pair, int base, int off) {
#pragma unroll
    for (int kk = 0; kk < 2; ++kk)
#pragma unroll
      for (int ty = 0; ty < 3; ++ty) {
        LAS char* a = L + base + (off + kk * 12288 + ty * 4096);
        bw[(2 * pair + kk) * 3 + ty] = cat(trd(a), trd(a + 512));
      }
  };
  f32x2 xn[10][4];
  v4u ga_[4]; v2u pv;
  const u32 sMG = WS_MODG + w * 1024;
  {
    float u[10];
    sfor<0, 10>([&](auto I) {
      constexpr int i = decltype(I)::value;
      if constexpr (i == 0) asm volatile("s_waitcnt vmcnt(11)" : "+v"(xr[0]), "+v"(xr[1]), "+v"(ep)::"memory");
      if constexpr (i == 2) asm volatile("s_waitcnt vmcnt(12)" : "+v"(xr[2]), "+v"(xr[3])::"memory");
      if constexpr (i == 4) asm volatile("s_waitcnt vmcnt(13)" : "+v"(xr[4]), "+v"(xr[5])::"memory");
      if constexpr (i == 6) asm volatile("s_waitcnt vmcnt(14)" : "+v"(xr[6]), "+v"(xr[7])::"memory");
      if constexpr (i == 8) asm volatile("s_waitcnt vmcnt(15)" : "+v"(xr[8]), "+v"(xr[9])::"memory");
      float s = 0.f, q = 0.f;
#pragma unroll
      for (int e = 0; e < 4; ++e) { s = dot2(xr[i][e], 0x3f803f80u, s); q = dot2(xr[i][e], xr[i][e], q); }
      u[i] = sw32_add(s, q);
      PIN(u[i]);
      if constexpr (i == 1) stage_b1(IC<1>{}, IC<L_XY + 24576>{});
      if constexpr (i == 3) stage_b1(IC<2>{}, IC<L_XY + 49152>{});
      if constexpr (i == 5) stage_b1(IC<3>{}, IC<L_Z>{});
      if constexpr (i == 7) stage_b1(IC<4>{}, IC<L_Z + 24576>{});
    });
    float t[5];
#pragma unroll
    for (int i = 0; i < 5; ++i) t[i] = row16_sum(sw16_add(u[i], u[i + 5]));
    float v = t[0];
#pragma unroll
    for (int i = 1; i < 5; ++i) v = c16 == i ? t[i] : v;
    auto r = __builtin_amdgcn_permlane32_swap(asu(v), asu(v), false, false);
    const float mean = asf(r[0]) * (1.f / 512.f);
    const float var = fmaxf(asf(r[1]) * (1.f / 512.f) - mean * mean, 0.f);
    float rs = __builtin_amdgcn_rsqf(var + 1e-5f), nm = -mean * rs;
    PIN(rs); PIN(nm);
    STAMP(4);
    if (w >= 4) {
      LAS char* a0 = L + L_Z + 49152 + (w & 3) * 4096 + ((lane >> 1) & 1) * 2048 + 64 * ((c16 >> 2) + 4 * (g4 & 1) + 16 * (g4 >> 1)) + 8 * (lane & 1);
#pragma unroll
      for (int ct = 0; ct < 3; ++ct) {
        const int c = min(cs0 + T0 + ct - rcb, 3) * 16;
        const v4u f = cat(trd(a0 + c), trd(a0 + c + 512));
        const u32 sf = WS_B4T + ((j * 16 + (g & 15)) * 24 + T0 + ct) * 1024;
        asm volatile("buffer_store_dwordx4 %0, %1, %2, %3 offen sc1\n\ts_nop 1" ::"v"(f), "v"(vl16), "s"(rWS), "s"(sf) : "memory");
      }
    }
    { const u32 vo = ((lane >> 3) & 3) * 24576 + (lane & 7) * 8, so = WS_MODG + (1024 + 8 * b) * 8; LD8S(pv, vo, rWS, so, 0); }
    { const u32 s0 = sMG, s1 = sMG + 24576, s2 = sMG + 49152, s3 = sMG + 73728;
      LD16S(ga_[0], vl16, rWS, s0, 0); LD16S(ga_[1], vl16, rWS, s1, 0); LD16S(ga_[2], vl16, rWS, s2, 0); LD16S(ga_[3], vl16, rWS, s3, 0); }
    sfor<0, 10>([&](auto I) {
      constexpr int i = decltype(I)::value;
      const float a = rdl(rs, (i / 5) * 16 + i % 5), c = rdl(nm, (i / 5) * 16 + i % 5);
#pragma unroll
      for (int e = 0; e < 4; ++e) { xn[i][e] = unp(xr[i][e]) * a + c; PIN(xn[i][e]); }
    });
  }
  const u32 tag = __builtin_amdgcn_readfirstlane(ep) + 1u;
  u32 m1; asm("s_lshr_b32 %0, 1, %1" : "=s"(m1) : "s"(w) : "scc");
#define RAISE(fo) raise_flag(rWS, (fo), tag, vl16, m1)
#define SIGNAL(idx, fo) signal_flag(rWS, L_CNT + 4 * (idx), (fo), tag)
  STAMP(5);
  asm volatile("s_waitcnt vmcnt(0)" : "+v"(ga_[0]), "+v"(ga_[1]), "+v"(ga_[2]), "+v"(ga_[3]), "+v"(pv)::"memory");
  auto gsum = [&](v4u (&a)[4], f32x2& o) -> u32 {
    o.x = (asf(a[0].x) + asf(a[1].x)) + (asf(a[2].x) + asf(a[3].x)); o.y = (asf(a[0].z) + asf(a[1].z)) + (asf(a[2].z) + asf(a[3].z));
    return ((a[0].y ^ tag) | (a[0].w ^ tag) | (a[1].y ^ tag) | (a[1].w ^ tag)) | ((a[2].y ^ tag) | (a[2].w ^ tag) | (a[3].y ^ tag) | (a[3].w ^ tag));
  };
  auto gather = [&](u32 voff, u32 soff, f32x2& o) {
    for (u32 sp = 0; sp < SPIN_CAP; ++sp) {
      asm volatile("" ::: "memory");
      v4u a[4];
#pragma unroll
      for (int q = 0; q < 4; ++q) a[q] = __builtin_amdgcn_raw_buffer_load_b128(rWS, voff, soff + q * 24576, 16);
      if (!__builtin_amdgcn_ballot_w64(gsum(a, o) != 0u)) break;
    }
  };
  const float one = w >= 4 ? 1.f : 0.f;
  {
    f32x2 mA;
    if (__builtin_amdgcn_ballot_w64(gsum(ga_, mA) != 0u)) gather(vl16, sMG, mA);
    *(LAS f32x2*)(L + L_MODA + w * 512 + (vl16 >> 1)) = mA + one;
  }
  VMW(0); BAR();
  RAISE(WS_RFLAG + (j * 32 + g) * 4);
  if (w == 7) {
    const u32 vo = ((lane >> 3) & 3) * 24576 + (lane & 7) * 8, so = WS_MODG + (1024 + 8 * b) * 8;
    for (u32 sp = 0; sp < SPIN_CAP && __builtin_amdgcn_ballot_w64(pv.y != tag); ++sp) {
      asm volatile("" ::: "memory");
      pv = __builtin_amdgcn_raw_buffer_load_b64(rWS, vo, so, 16);
    }
    float sv = asf(pv.x);
    sv += dppf<0x128>(sv); sv = sw16_add(sv, sv);
    pv.x = asu(sv);
    __builtin_amdgcn_raw_buffer_store_b64(pv, rWS, (lane & 7) * 8, WS_MODF + (1024 + 8 * b) * 8, 16);
  }
  STAMP(6);
  int vma = L_MODA + 2 * vl16; PIN(vma);
  const f32x4 ms0 = *(LAS f32x4*)(L + vma), ms1 = *(LAS f32x4*)(L + vma + 16);
  const f32x4 ma0 = *(LAS f32x4*)(L + vma + 2048), ma1 = *(LAS f32x4*)(L + vma + 2064);
  int tz0 = tB1 + L_Z; PIN(tz0);
  { int t2 = tB1 + 49152; PIN(t2); read_b1(0, tB1, 0); read_b1(1, tB1, 24576); read_b1(2, t2, 0); read_b1(3, tz0, 0); read_b1(4, tz0, 24576); }
  BAR();
  STAMP(7);
  {
    int hw1 = (hw0 ^ 128) + 8192; PIN(hw1);
    sfor<0, 10>([&](auto I) {
      constexpr int i = decltype(I)::value;
      v4u o;
      o.x = pk2(xn[i][0] * ma0.xy + ms0.xy); o.y = pk2(xn[i][1] * ma0.zw + ms0.zw);
      o.z = pk2(xn[i][2] * ma1.xy + ms1.xy); o.w = pk2(xn[i][3] * ma1.zw + ms1.zw);
      if constexpr (i == 8) { hw0 += 65536; hw1 += 65536; }
      *(LAS v4u*)(L + ((i & 1) ? hw1 : hw0) + ((i >> 1) & 3) * 16384) = o;
      if constexpr (i == 0) stage_b1(IC<5>{}, IC<L_Z>{});
      if constexpr (i == 3) stage_b1(IC<6>{}, IC<L_Z + 24576>{});
      if constexpr (i == 6) stage_b1(IC<7>{}, IC<L_Z + 49152>{});
      SCHED();
    });
  }
  VMW(0);
  BAR();
  STAMP(8);

  const f32x4 z4 = {0.f, 0.f, 0.f, 0.f};
#define AFRAG(off, ks) (*(LAS v4u*)(L + afo[(ks) & 3] + ((off) + ((ks) >> 2) * 256)))
  const int ch0 = cs0 + 3 * w;
  constexpr int ND = NODMA ? 0 : 1;
  v4u b4[48];
  const u32 sB4 = WS_B4T + (j * 16 * 24 + 3 * w) * 1024;
  auto load_b4 = [&](auto NC) {
    constexpr int n = decltype(NC)::value, ks = n / 3, ct = n % 3;
    const u32 so = sB4 + ((ks + 2 * rotp) & 15) * 24576;
    v4u& r = b4[n];
    if constexpr (ct == 0) LD16S(r, vl16, rWS, so, 0); else if constexpr (ct == 1) LD16S(r, vl16, rWS, so, 1024); else LD16S(r, vl16, rWS, so, 2048);
  };
  auto landed_b4 = [&](auto AC, auto BC) {
#pragma unroll
    for (int i = decltype(AC)::value; i < decltype(BC)::value; ++i) { v4u& r = b4[i]; PIN(r); }
  };
  v4u gb_, gg_;
  u32 vgg, fl0, vRow, vB3, vB5;
  int pw, pr, t3;
  f32x4 aKh = z4, aVh = z4, aQ = z4, aK = z4, aV = z4, bQ = z4, bK = z4, bV = z4;
  {
    v4u ah = AFRAG(0, 0), a1 = AFRAG(16384, 0), a2 = AFRAG(32768, 0);
    int l3, c3, g3; f32x2 mB, mG;
    const u32 mB3 = L_Z + (w >> 2) * 4096 + (w & 3) * 1024, sB3 = ((w >> 2) * 32 + 4 * (w & 3)) * 1024 + j * 128;
    auto job = [&](auto NC) {
      constexpr int n = decltype(NC)::value;
      if constexpr (n == 3) { l3 = opq(vl16) >> 4; c3 = l3 & 15; g3 = l3 >> 4; PIN(c3); PIN(g3); vgg = (vl16 < 512 ? 8192 : 20480 - 512) + 512 * j + vl16; PIN(vgg); }
      if constexpr (n == 7) { vRow = (8 * w + (l3 >> 3)) * 1024 + (l3 & 7) * 16; PIN(vRow); }
      if constexpr (n == 8) { pr = (8 * w + (l3 >> 3)) * 256 + (((2 * (l3 & 7)) ^ ((l3 >> 3) & 7)) << 4); PIN(pr); }
      if constexpr (n == 10) { pw = kq * 16384 + (32 * mh + c3) * 256 + ((g3 ^ (c3 & 3)) << 4) + ((c3 & 4) << 4); PIN(pw); }
      if constexpr (n == 11) { t3 = L_Z + kq * 16384 + g3 * 1024 + 64 * (c3 >> 2) + 8 * (l3 & 3) + 32 * (g3 & 1); PIN(t3); }
      if constexpr (n == 13) { vB3 = ((l3 >> 4) & 1) * 64 + (((l3 & 3) ^ ((w & 1) << 1)) << 4); PIN(vB3); }
      if constexpr (n == 14) { vB5 = vB3 + (((l3 >> 2) & 3) + 4 * (l3 >> 5)) * 1024; PIN(vB5); vB3 += (16 * (l3 >> 5) + ((l3 >> 2) & 3)) * 1024; PIN(vB3); }
      if constexpr (n == 31) { mB.x = asf(gb_.x) + one; PIN(mB.x); mB.y = asf(gb_.z) + one; PIN(mB.y); *(LAS f32x2*)(L + L_MODA + w * 512 + (vl16 >> 1)) = mB; }
      if constexpr (n == 32) { mG.x = asf(gg_.x); mG.y = asf(gg_.z); *(LAS f32x2*)(L + L_GAB + (vl16 >> 1)) = mG; }
      if constexpr (n == 34) { fl0 = (gb_.y ^ tag) | (gb_.w ^ tag) | (gg_.y ^ tag) | (gg_.w ^ tag); PIN(fl0); }
      if constexpr (n == 12) {
        const u32 vr = (vl16 >> 2) & 124, sr = WS_RFLAG + j * 128; u32 t, cnt = SPIN_CAP;
        asm volatile("1:\n\tbuffer_load_dword %0, %2, %3, %4 offen sc1\n\ts_waitcnt vmcnt(0)\n\tv_cmp_ne_u32 vcc, %5, %0\n\ts_cbranch_vccz 2f\n\t"
                     "s_sub_u32 %1, %1, 1\n\ts_cbranch_scc0 1b\n\t2:" : "=&v"(t), "+s"(cnt) : "v"(vr), "s"(rWS), "s"(sr), "s"(tag) : "vcc", "scc", "memory");
        arrive(L_CNT + 8);
      }
      if constexpr (n == 14) { const u32 s0 = WS_MODF + 12288 + w * 1024; LD16S(gb_, vl16, rWS, s0, 0); }
      if constexpr (n == 16) { const u32 s0 = WS_MODF; LD16S(gg_, vgg, rWS, s0, 0); }
      if constexpr (n == 30) asm volatile("s_waitcnt vmcnt(10)" : "+v"(gb_), "+v"(gg_)::"memory");
      if constexpr (n >= 13 && (n & 1)) load_b4(IC<(n - 13) / 2>{});
      if constexpr (n == 22) await8(L_CNT + 8);
      if constexpr (n >= 22 && n < 46 && (n - 22) % 3 == 0) dma<((n - 22) / 3) * 8192, ((n - 22) / 3) * 65536>(mB3, vB3, rWO, sB3);
    };
    sfor<0, 16>([&](auto KS) {
      constexpr int ks = decltype(KS)::value;
      KSTAMP(1, ks);
      if constexpr (ks == 1) read_b1(5, tz0, 0);
      if constexpr (ks == 2) read_b1(6, tz0, 24576);
      if constexpr (ks == 3) { int tz2 = tz0 + 49152; PIN(tz2); read_b1(7, tz2, 0); }
      SCHED();
      v4u nh = ah, n1 = a1, n2 = a2;
      aKh = mfma32(bw[ks * 3 + 1], ah, aKh);
      aVh = mfma32(ah, bw[ks * 3 + 2], aVh);
      if constexpr (ks < 15) nh = AFRAG(0, ks + 1);
      job(IC<3 * ks>{});
      SCHED();
      aQ = mfma32(bw[ks * 3 + 0], a1, aQ);
      aK = mfma32(bw[ks * 3 + 1], a1, aK);
      aV = mfma32(a1, bw[ks * 3 + 2], aV);
      if constexpr (ks < 15) n1 = AFRAG(16384, ks + 1);
      job(IC<3 * ks + 1>{});
      SCHED();
      bQ = mfma32(bw[ks * 3 + 0], a2, bQ);
      bK = mfma32(bw[ks * 3 + 1], a2, bK);
      bV = mfma32(a2, bw[ks * 3 + 2], bV);
      if constexpr (ks < 15) n2 = AFRAG(32768, ks + 1);
      job(IC<3 * ks + 2>{});
      ah = nh; a1 = n1; a2 = n2;
      SCHED();
    });
  }
  STAMP(9); KSTAMP(1, 16);
  {
    const u32 sOst = WS_O + ((g * 4 + 2 * blk2) * 16 + 2 * j + (hd >> 1)) * 1024;
    const v2u k1p = pk4(aK), v1p = pk4(aV);
    const v2u o0 = attend(pk4(aKh), pk4(aVh), k1p, v1p, pk4(aQ), am0, am1, ahm, g == 0 && blk2 == 0);
    const v2u o1 = attend(k1p, v1p, pk4(bK), pk4(bV), pk4(bQ), am0, am1, ahm, false);
    __builtin_amdgcn_raw_buffer_store_b64(o0, rWS, vOst, sOst, 16);
    __builtin_amdgcn_raw_buffer_store_b64(o1, rWS, vOst, sOst + 16384, 16);
  }
  if (__builtin_amdgcn_ballot_w64(fl0 != 0u)) {
    for (u32 sp = 0; sp < SPIN_CAP; ++sp) {
      asm volatile("" ::: "memory");
      gb_ = __builtin_amdgcn_raw_buffer_load_b128(rWS, vl16, WS_MODF + 12288 + w * 1024, 16);
      gg_ = __builtin_amdgcn_raw_buffer_load_b128(rWS, vgg, WS_MODF, 16);
      if (!__builtin_amdgcn_ballot_w64(((gb_.y ^ tag) | (gb_.w ^ tag) | (gg_.y ^ tag) | (gg_.w ^ tag)) != 0u)) break;
    }
    const f32x2 mB = {asf(gb_.x) + one, asf(gb_.z) + one}, mG = {asf(gg_.x), asf(gg_.z)};
    *(LAS f32x2*)(L + L_MODA + w * 512 + (vl16 >> 1)) = mB;
    *(LAS f32x2*)(L + L_GAB + (vl16 >> 1)) = mG;
  }
  STAMP(10);
  VMW(0);
  landed_b4(IC<0>{}, IC<18>{});
  BAR();
  RAISE(flag_off(0, g, 0) + j * 128);
  STAMP(11);

  v4u xv;
  { const u32 sx = g * 65536 + j * 128; LD16(xv, vRow, rX, sx, 0); }
  v4u b3[16];
#pragma unroll
  for (int s = 0; s < 4; ++s)
#pragma unroll
    for (int n = 0; n < 4; ++n) { LAS char* a = L + ((n & 1) ? (t3 ^ 32) : t3) + (s * 4096 + 256 * (n >> 1)); b3[s * 4 + n] = cat(trd(a), trd(a + 512)); }
  sfor<18, 21>(load_b4);
  const u64 care2 = 0x0101010101010101ull * (3u << (2 * kq));
  poll_flags(rWS, vfl, flag_off(0, g, 0), care2, tag);
  PIN(xv);
  v4u af[8];
  {
    const u32 so = WS_O + ((g * 4 + 2 * mh) * 16 + 4 * kq) * 1024, so1 = so + 16384;
    LD16S(af[0], vl16, rWS, so, 0); LD16S(af[1], vl16, rWS, so, 1024); LD16S(af[2], vl16, rWS, so, 2048); LD16S(af[3], vl16, rWS, so, 3072);
    LD16S(af[4], vl16, rWS, so1, 0); LD16S(af[5], vl16, rWS, so1, 1024); LD16S(af[6], vl16, rWS, so1, 2048); LD16S(af[7], vl16, rWS, so1, 3072);
  }
  STAMP(12);
  sfor<21, 24>(load_b4);
  sfor<0, 2>([&](auto M) {
    constexpr int m = decltype(M)::value;
    asm volatile("s_waitcnt vmcnt(%c4)" : "+v"(af[4 * m]), "+v"(af[4 * m + 1]), "+v"(af[4 * m + 2]), "+v"(af[4 * m + 3]) : "i"(4 * (1 - m) + 3) : "memory");
    f32x4 acc[4] = {z4, z4, z4, z4};
#pragma unroll
    for (int s = 0; s < 4; ++s)
#pragma unroll
      for (int n = 0; n < 4; ++n) acc[n] = mfma32(b3[s * 4 + n], af[4 * m + s], acc[n]);
#pragma unroll
    for (int n = 0; n < 4; ++n) *(LAS f32x4*)(L + ((n & 1) ? (pw ^ 64) : pw) + (m * 4096 + (n >> 1) * 128)) = acc[n];
  });
  BAR();
  STAMP(13);
  sfor<24, 28>(load_b4);
  {
    f32x4 y0 = *(LAS f32x4*)(L + pr), y1 = *(LAS f32x4*)(L + (pr ^ 16));
#pragma unroll
    for (int q = 1; q < 4; ++q) { y0 += *(LAS f32x4*)(L + pr + q * 16384); y1 += *(LAS f32x4*)(L + (pr ^ 16) + q * 16384); }
    const int ga = L_GAB + ((vl16 & 112) << 1);
    const f32x4 g0 = *(LAS f32x4*)(L + ga), g1 = *(LAS f32x4*)(L + ga + 16);
    const f32x4 x0 = f32x4{blo(xv.x), bhi(xv.x), blo(xv.y), bhi(xv.y)} + g0 * y0, x1 = f32x4{blo(xv.z), bhi(xv.z), blo(xv.w), bhi(xv.w)} + g1 * y1;
    { const v4u o = cat(pk4(x0), pk4(x1)); const u32 so = WS_X1 + g * 65536 + j * 128;
      asm volatile("buffer_store_dwordx4 %0, %1, %2, %3 offen sc1\n\ts_nop 1" ::"v"(o), "v"(vRow), "s"(rWS), "s"(so) : "memory"); }
    const f32x4 sv = x0 + x1;
    float mean = (sv.x + sv.y) + (sv.z + sv.w);
    mean += dppf<0xB1>(mean); mean += dppf<0x4E>(mean); mean += dppf<0x141>(mean);
    mean *= 1.f / 64.f;
    const f32x4 d0 = x0 - mean, d1 = x1 - mean, dd = d0 * d0 + d1 * d1;
    float m2 = (dd.x + dd.y) + (dd.z + dd.w);
    m2 += dppf<0xB1>(m2); m2 += dppf<0x4E>(m2); m2 += dppf<0x141>(m2);
    v2u st; st.x = asu(mean); st.y = asu(m2);
    __builtin_amdgcn_raw_buffer_store_b64(st, rWS, (vRow >> 10) << 6, WS_STAT + g * 4096 + j * 8, 16);
  }
  STAMP(14);
  VMW(0);
  landed_b4(IC<18>{}, IC<28>{});
  BAR();
  RAISE(flag_off(1, g, 0) + j * 128);
  sfor<28, 34>(load_b4);
  STAMP(15);

  v4u x1v[4]; v2u stv;
  auto ln2_load = [&](int h) {
    const u32 so = WS_X1 + g * 65536 + (32 * (w >> 2) + 16 * h + 4 * (w & 3)) * 1024;
    LD16S(x1v[0], vl16, rWS, so, 0); LD16S(x1v[1], vl16, rWS, so, 1024); LD16S(x1v[2], vl16, rWS, so, 2048); LD16S(x1v[3], vl16, rWS, so, 3072);
    const u32 vs = (vl16 >> 1) & 255, ss = WS_STAT + g * 4096 + (32 * (w >> 2) + 16 * h + 4 * (w & 3)) * 64;
    LD8S(stv, vs, rWS, ss, 0);
  };
  float l2r, l2n;
  auto ln2_stats = [&]() {
    const float mj = asf(stv.x);
    float mean = mj;
    mean += dppf<0xB1>(mean); mean += dppf<0x4E>(mean); mean += dppf<0x141>(mean);
    mean *= 0.125f;
    const float dm = mj - mean;
    float m2 = asf(stv.y) + 64.f * dm * dm;
    m2 += dppf<0xB1>(m2); m2 += dppf<0x4E>(m2); m2 += dppf<0x141>(m2);
    l2r = __builtin_amdgcn_rsqf(m2 * (1.f / 512.f) + 1e-5f);
    l2n = -mean * l2r;
  };
  auto b5_base = [](int s) constexpr -> int {
    return s < 5 ? 65536 + s * 16384 : s < 7 ? (s - 5) * 16384 : s < 9 ? 32768 + (s - 7) * 16384 : 65536 + (s - 9) * 16384;
  };
  u32 vH;
  const u32 mB5 = (w >> 2) * 4096 + (w & 3) * 1024, sB5 = ((w >> 2) * 352 + 8 * (w & 3)) * 1024 + j * 128;
  auto b5_piece = [&](auto SC, auto QC) {
    constexpr int s = decltype(SC)::value, qq = decltype(QC)::value;
    if constexpr (NODMA) {}
    else if constexpr (s == 10 && qq == 1) dma<b5_base(s) + qq * 8192, (qq * 704 + 32 * s) * 1024>(mB5, vB5, rW2, sB5 - (w >> 2) * 32768);
    else dma<b5_base(s) + qq * 8192, (qq * 704 + 32 * s) * 1024>(mB5, vB5, rW2, sB5);
  };
  u32 hmask[3];
#pragma unroll
  for (int ct = 0; ct < 3; ++ct)
    asm("s_cmp_lt_u32 %1, %2\n\ts_cselect_b32 %0, -1, 0" : "=s"(hmask[ct]) : "s"((u32)(ch0 + ct)), "s"((u32)cend) : "scc");
  auto hh_store = [&](v2u o, int ct, int h) {
    const u32 so = WS_HH + ((g * 4 + h) * 172 + ch0 + ct) * 256;
    asm volatile("s_mov_b32 exec_lo, %4\n\ts_mov_b32 exec_hi, %4\n\tbuffer_store_dwordx2 %0, %1, %2, %3 offen sc1\n\ts_mov_b64 exec, -1"
                 ::"v"(o), "v"(vH), "s"(rWS), "s"(so), "s"(hmask[ct]) : "memory");
  };
  auto swiglu_tile = [&](f32x4 (&acc)[3][2], int ct, int h) {
    f32x4 G, U, R;
#pragma unroll
    for (int e = 0; e < 4; ++e) {
      auto r = __builtin_amdgcn_permlane32_swap(asu(acc[ct][0][e]), asu(acc[ct][1][e]), false, false);
      G[e] = asf(r[0]); U[e] = asf(r[1]);
    }
    const f32x4 T = G * -1.4426950408889634f;
#pragma unroll
    for (int e = 0; e < 4; ++e) R[e] = __builtin_amdgcn_rcpf(1.f + __builtin_amdgcn_exp2f(T[e]));
    hh_store(pk4((G * U) * R), ct, h);
  };
#pragma unroll
  for (int i = 0; i < 4; ++i) { afo[i] -= blk2 * 32768; PIN(afo[i]); }
  int vm2 = L_MODA + 2 * vl16; PIN(vm2);
  int h2w = w * 4096 + (((vl16 - 128 * rotp) & 1023) ^ ((4 * w & 15) << 4)); PIN(h2w);
  poll_flags(rWS, vfl, flag_off(1, g, 0), ~0ull, tag);
  landed_b4(IC<28>{}, IC<34>{});
  ln2_load(0);
  sfor<34, 40>(load_b4);
  {
    const f32x4 s0 = *(LAS f32x4*)(L + vm2), s1 = *(LAS f32x4*)(L + vm2 + 16), a0 = *(LAS f32x4*)(L + vm2 + 2048), a1 = *(LAS f32x4*)(L + vm2 + 2064);
    asm volatile("s_waitcnt vmcnt(%c5)" : "+v"(x1v[0]), "+v"(x1v[1]), "+v"(x1v[2]), "+v"(x1v[3]), "+v"(stv) : "i"(6) : "memory");
    ln2_stats();
#pragma unroll
    for (int i = 0; i < 4; ++i) {
      const float rs = rdl(l2r, 8 * i), nn = rdl(l2n, 8 * i);
      v4u o;
      o.x = pk2((unp(x1v[i].x) * rs + nn) * a0.xy + s0.xy); o.y = pk2((unp(x1v[i].y) * rs + nn) * a0.zw + s0.zw);
      o.z = pk2((unp(x1v[i].z) * rs + nn) * a1.xy + s1.xy); o.w = pk2((unp(x1v[i].w) * rs + nn) * a1.zw + s1.zw);
      *(LAS v4u*)(L + (h2w ^ (i << 4)) + i * 1024) = o;
    }
  }
  ln2_load(1);
  BAR();
  STAMP(16);
  f32x4 accA[3][2] = {{z4, z4}, {z4, z4}, {z4, z4}};
  {
    f32x4 cs, ca; float rsv[4], nnv, t[4]; v2u ov;
    auto ln_op = [&](auto NC) {
      constexpr int n = decltype(NC)::value, u = n / 16, k = n % 16, hf = u & 1, i = u >> 1;
      if constexpr (u < 8) {
        if constexpr (k == 0) {
          cs = *(LAS f32x4*)(L + vm2 + hf * 16); ca = *(LAS f32x4*)(L + vm2 + 2048 + hf * 16);
          if constexpr (hf == 0) nnv = q_mov(rdl(l2n, 8 * i));
        }
        else if constexpr (k == 1) t[0] = q_shl(x1v[i][2 * hf]);
        else if constexpr (k == 2) t[1] = q_hi(x1v[i][2 * hf]);
        else if constexpr (k == 3) t[2] = q_shl(x1v[i][2 * hf + 1]);
        else if constexpr (k == 4) t[3] = q_hi(x1v[i][2 * hf + 1]);
        else if constexpr (k < 9) t[k - 5] = q_fmas(t[k - 5], rsv[i], nnv);
        else if constexpr (k < 13) t[k - 9] = q_fma(t[k - 9], ca[k - 9], cs[k - 9]);
        else if constexpr (k == 13) ov.x = q_cvt(t[0], t[1]);
        else if constexpr (k == 14) ov.y = q_cvt(t[2], t[3]);
        else {
          *(LAS v2u*)(L + (h2w ^ (i << 4)) + (32768 + i * 1024 + hf * 8)) = ov;
        }
      }
    };
    v4u a0 = AFRAG(0, 0), a1 = AFRAG(16384, 0);
    sfor<0, 16>([&](auto KS) {
      constexpr int ks = decltype(KS)::value;
      KSTAMP(3, ks);
      v4u n0 = a0, n1 = a1;
      if constexpr (ks < 15) { n0 = AFRAG(0, ks + 1); n1 = AFRAG(16384, ks + 1); }
      if constexpr (ks == 3) {
        asm volatile("s_waitcnt vmcnt(%c5)" : "+v"(x1v[0]), "+v"(x1v[1]), "+v"(x1v[2]), "+v"(x1v[3]), "+v"(stv) : "i"(3 * ND) : "memory");
        landed_b4(IC<34>{}, IC<40>{});
      }
      if constexpr (ks == 13) { VMW(0); landed_b4(IC<40>{}, IC<48>{}); }
      SCHED();
      if constexpr (ks == 3) { ln2_stats(); PIN(l2r); PIN(l2n);
#pragma unroll
        for (int i = 0; i < 4; ++i) rsv[i] = rdl(l2r, 8 * i); }
      sfor<0, 6>([&](auto MC) {
        constexpr int m = decltype(MC)::value, ct = m >> 1;
        if constexpr (m & 1) accA[ct][1] = mfma32(b4[ks * 3 + ct], a1, accA[ct][1]); else accA[ct][0] = mfma32(b4[ks * 3 + ct], a0, accA[ct][0]);
        if constexpr (ks < 6 && m == 0) b5_piece(IC<ks / 2>{}, IC<ks & 1>{});
        if constexpr (ks >= 7 && ks < 11 && (m == 0 || m == 3)) load_b4(IC<40 + (ks - 7) * 2 + (m == 3)>{});
        if constexpr (ks >= 3 && ks < 7 && m == 3) b5_piece(IC<3 + (ks - 3) / 2>{}, IC<(ks - 3) & 1>{});
        if constexpr (ks >= 4) {
          constexpr int o = (ks - 4) * 15 + (m >> 1) * 5 + (m & 1) * 2;
          ln_op(IC<o>{}); ln_op(IC<o + 1>{});
          if constexpr (m & 1) ln_op(IC<o + 2>{});
          SCHED();
        }
      });
      a0 = n0; a1 = n1;
      SCHED();
    });
  }
  vH = (vl16 & 255) + ((vl16 >> 5) & 8) + (vl16 >> 9) * (2 * 172 * 256); PIN(vH);
  BAR();
  STAMP(17); KSTAMP(3, 16);
  f32x4 accB[3][2] = {{z4, z4}, {z4, z4}, {z4, z4}};
  u32 vRow5, fl2; int pr5, pw5, t5, t5z, t5y, gb5;
  auto read_b5 = [&](int s) {
    const int bs = b5_base(s);
    const int tb = bs < 65536 ? t5 : bs < 114688 ? t5z : t5y, off = bs < 65536 ? bs : bs < 114688 ? bs - 65536 : bs - 114688;
#pragma unroll
    for (int n = 0; n < 4; ++n) {
      LAS char* a = L + ((n & 1) ? (tb ^ 32) : tb) + (off + 256 * (n >> 1));
      bw[s * 4 + n] = cat(trd(a), trd(a + 512));
    }
  };
  const u64 care5 = 0x0101010101010101ull * (kq == 0 ? 0x03u : kq == 1 ? 0x0cu : kq == 2 ? 0x70u : 0xc0u);
  v4u hf[11]; v2u xq;
  auto p5_load = [&](int h) {
    const u32 so = WS_HH + ((g * 4 + 2 * mh + h) * 172 + 44 * kq) * 256;
    const u32 so2 = so + 4096, so3 = so + 8192;
    LD16S(hf[0], vl16, rWS, so, 0); LD16S(hf[1], vl16, rWS, so, 1024); LD16S(hf[2], vl16, rWS, so, 2048); LD16S(hf[3], vl16, rWS, so, 3072);
    LD16S(hf[4], vl16, rWS, so2, 0); LD16S(hf[5], vl16, rWS, so2, 1024); LD16S(hf[6], vl16, rWS, so2, 2048); LD16S(hf[7], vl16, rWS, so2, 3072);
    LD16S(hf[8], vl16, rWS, so3, 0); LD16S(hf[9], vl16, rWS, so3, 1024);
    const u32 so4 = kq == 3 ? so3 + 1024 : so3 + 2048;
    LD16S(hf[10], vl16, rWS, so4, 0);
    const u32 sq = WS_X1 + g * 65536 + h * 16384 + j * 128;
    LD8S(xq, vRow5, rWS, sq, 0);
  };
  auto p5_epi = [&](int h, v2u xx) {
    f32x4 y = *(LAS f32x4*)(L + h * 32768 + pr5);
#pragma unroll
    for (int q = 1; q < 4; ++q) y += *(LAS f32x4*)(L + h * 32768 + q * 8192 + pr5);
    const f32x4 gbv = *(LAS f32x4*)(L + gb5);
    const f32x4 xb = {blo(xx.x), bhi(xx.x), blo(xx.y), bhi(xx.y)};
    __builtin_amdgcn_raw_buffer_store_b64(pk4(xb + gbv * y), rOUT, vRow5, g * 65536 + h * 16384 + j * 128, 0);
  };
  f32x4 acc[4];
  auto p5_mma = [&](int s0, int s1) {
#pragma unroll
    for (int s = s0; s < s1; ++s)
#pragma unroll
      for (int n = 0; n < 4; ++n) acc[n] = mfma32(bw[s * 4 + n], hf[s], acc[n]);
  };
  {
    float G[4], U[4], T[4]; v2u ov;
    auto sw_op = [&](auto NC) {
      constexpr int n = decltype(NC)::value, ct = n / 22, k = n % 22;
      if constexpr (ct < 3) {
        if constexpr (k < 4) { auto r = __builtin_amdgcn_permlane32_swap(asu(accA[ct][0][k]), asu(accA[ct][1][k]), false, false); G[k] = asf(r[0]); U[k] = asf(r[1]); PIN(G[k]); PIN(U[k]); }
        else if constexpr (k < 6) { T[2 * (k - 4)] = q_mulk(G[2 * (k - 4)]); T[2 * (k - 4) + 1] = q_mulk(G[2 * (k - 4) + 1]); }
        else if constexpr (k < 10) T[k - 6] = q_exp(T[k - 6]);
        else if constexpr (k < 12) { T[2 * (k - 10)] = q_add1(T[2 * (k - 10)]); T[2 * (k - 10) + 1] = q_add1(T[2 * (k - 10) + 1]); }
        else if constexpr (k < 16) T[k - 12] = q_rcp(T[k - 12]);
        else if constexpr (k < 18) { G[2 * (k - 16)] = q_mul(G[2 * (k - 16)], U[2 * (k - 16)]); G[2 * (k - 16) + 1] = q_mul(G[2 * (k - 16) + 1], U[2 * (k - 16) + 1]); }
        else if constexpr (k < 20) { G[2 * (k - 18)] = q_mul(G[2 * (k - 18)], T[2 * (k - 18)]); G[2 * (k - 18) + 1] = q_mul(G[2 * (k - 18) + 1], T[2 * (k - 18) + 1]); }
        else if constexpr (k == 20) { ov.x = q_cvt(G[0], G[1]); ov.y = q_cvt(G[2], G[3]); }
        else hh_store(ov, ct, 0);
      }
    };
    v4u a0 = AFRAG(32768, 0), a1 = AFRAG(49152, 0);
    int l5, c5, g5;
    sfor<0, 16>([&](auto KS) {
      constexpr int ks = decltype(KS)::value;
      KSTAMP(4, ks);
      v4u n0 = a0, n1 = a1;
      if constexpr (ks > 0 && ks < 15) { n0 = AFRAG(32768, ks + 1); n1 = AFRAG(49152, ks + 1); }
      if constexpr (ks == 8) { VMW(0); SIGNAL(1, flag_off(2, g, 0) + j * 128); }
      if constexpr (ks >= 9 && ks < 14) read_b5(ks - 9);
      if constexpr (ks == 14) {
        const u32 fo = flag_off(2, g, 0), vf = vfl; u32 t, cnt = SPIN_CAP;
        asm volatile("1:\n\tbuffer_load_dword %0, %2, %3, %4 offen sc1\n\ts_waitcnt vmcnt(0)\n\tv_cmp_ne_u32 vcc, %5, %0\n\ts_and_b64 vcc, vcc, %6\n\ts_cbranch_scc0 2f\n\t"
                     "s_sub_u32 %1, %1, 1\n\ts_cbranch_scc0 1b\n\t2:" : "=&v"(t), "+s"(cnt) : "v"(vf), "s"(rWS), "s"(fo), "s"(tag), "s"(care5) : "vcc", "scc", "memory");
        p5_load(0);
      }
      SCHED();
      sfor<0, 6>([&](auto MC) {
        constexpr int m = decltype(MC)::value, ct = m >> 1, n = ks * 6 + m;
        if constexpr (m & 1) accB[ct][1] = mfma32(b4[ks * 3 + ct], a1, accB[ct][1]); else accB[ct][0] = mfma32(b4[ks * 3 + ct], a0, accB[ct][0]);
        if constexpr (n == 3) n0 = AFRAG(32768, 1);
        if constexpr (n == 5) n1 = AFRAG(49152, 1);
        if constexpr (n < 33) { sw_op(IC<2 * n>{}); sw_op(IC<2 * n + 1>{}); }
        if constexpr (n < 24 && n % 6 == 0) b5_piece(IC<5 + n / 12>{}, IC<(n / 6) & 1>{});
        if constexpr (n == 36) { l5 = opq(vl16) >> 4; c5 = l5 & 15; g5 = l5 >> 4; PIN(c5); PIN(g5); }
        if constexpr (n == 37) { t5 = kq * 4096 + g5 * 1024 + 64 * (c5 >> 2); PIN(t5); }
        if constexpr (n == 38) { t5 += 8 * (l5 & 3) + 32 * (g5 & 1); PIN(t5); }
        if constexpr (n == 39) { t5z = t5 + 65536; PIN(t5z); t5y = t5 + 114688; PIN(t5y); }
        if constexpr (n == 40) { vRow5 = (32 * (w >> 2) + 4 * (w & 3) + g5) * 1024 + c5 * 8; PIN(vRow5); }
        if constexpr (n == 42) { pr5 = (4 * w + g5) * 256 + ((c5 ^ ((4 * w + g5) & 7)) << 4); PIN(pr5); }
        if constexpr (n == 44) { pw5 = kq * 8192 + (16 * mh + c5) * 256 + ((c5 & 4) << 4); PIN(pw5); }
        if constexpr (n == 46) { pw5 += (g5 ^ (c5 & 3)) << 4; PIN(pw5); gb5 = L_GAB + 256 + c5 * 16; PIN(gb5); }
        SCHED();
      });
      a0 = n0; a1 = n1;
      SCHED();
    });
  }
  STAMP(18); KSTAMP(4, 16);
  swiglu_tile(accB, 0, 1);
  swiglu_tile(accB, 1, 1);
  swiglu_tile(accB, 2, 1);
  asm volatile("s_waitcnt vmcnt(0)" : "+v"(hf[0]), "+v"(hf[1]), "+v"(hf[2]), "+v"(hf[3]), "+v"(hf[4]), "+v"(hf[5]), "+v"(hf[6]), "+v"(hf[7]), "+v"(hf[8]), "+v"(hf[9]), "+v"(hf[10]), "+v"(xq)::"memory");
  BAR();
  RAISE(flag_off(2, g, 1) + j * 128);
  STAMP(19);
  read_b5(5); read_b5(6);
  acc[0] = z4; acc[1] = z4; acc[2] = z4; acc[3] = z4;
  sfor<0, 7>([&](auto SC) {
    constexpr int sx = decltype(SC)::value;
    if constexpr (sx < 4) { b5_piece(IC<7 + sx>{}, IC<0>{}); b5_piece(IC<7 + sx>{}, IC<1>{}); }
    p5_mma(sx, sx + 1);
    SCHED();
  });
  PIN(acc[0]); PIN(acc[1]); PIN(acc[2]); PIN(acc[3]);
  VMW(0);
  BAR();
  STAMP(20);
  read_b5(7); read_b5(8); read_b5(9); read_b5(10);
  if (kq == 3) hf[10] = v4u{0u, 0u, 0u, 0u};
  p5_mma(7, 11);
#pragma unroll
  for (int n = 0; n < 4; ++n) *(LAS f32x4*)(L + ((n & 1) ? (pw5 ^ 64) : pw5) + (n >> 1) * 128) = acc[n];
  const v2u xqa = xq;
  poll_flags(rWS, vfl, flag_off(2, g, 1), care5, tag);
  p5_load(1);
  BAR();
  p5_epi(0, xqa);
  STAMP(21);
  acc[0] = z4; acc[1] = z4; acc[2] = z4; acc[3] = z4;
  asm volatile("s_waitcnt vmcnt(9)" : "+v"(hf[0]), "+v"(hf[1]), "+v"(hf[2]), "+v"(hf[3])::"memory");
  p5_mma(0, 4);
  asm volatile("s_waitcnt vmcnt(5)" : "+v"(hf[4]), "+v"(hf[5]), "+v"(hf[6]), "+v"(hf[7])::"memory");
  p5_mma(4, 8);
  asm volatile("s_waitcnt vmcnt(1)" : "+v"(hf[8]), "+v"(hf[9]), "+v"(hf[10]), "+v"(xq)::"memory");
  if (kq == 3) hf[10] = v4u{0u, 0u, 0u, 0u};
  p5_mma(8, 11);
#pragma unroll
  for (int n = 0; n < 4; ++n) *(LAS f32x4*)(L + 32768 + ((n & 1) ? (pw5 ^ 64) : pw5) + (n >> 1) * 128) = acc[n];
  BAR();
  p5_epi(1, xq);
  STAMP(22);
  STAMP_DUMP();
  if (b == 0 && w == 0) {
    for (u32 sp = 0; sp < SPIN_CAP; ++sp) {
      asm volatile("" ::: "memory");
      const v4u v = __builtin_amdgcn_raw_buffer_load_b128(rWS, vl16, WS_ALIVE, 16);
      if (__all(v.x == tag && v.y == tag && v.z == tag && v.w == tag)) break;
    }
    __builtin_amdgcn_raw_buffer_store_b32(tag, rWS, 0, WS_EPOCH, 16);
  }
}
}

extern "C" void kernel_launch(void* const* d_in, const int* in_sizes, int n_in, void* d_out, int out_size, void* d_ws, size_t ws_size,
                              hipStream_t stream) {
  Params p;
  p.x = (const u16*)d_in[0]; p.wqkv = (const u16*)d_in[1]; p.wo = (const u16*)d_in[2];
  p.w1 = (const u16*)d_in[4]; p.w3 = (const u16*)d_in[5]; p.w2 = (const u16*)d_in[6];
  p.c = (const u16*)d_in[7]; p.wmod = (const u16*)d_in[8];
  p.out = (u16*)d_out; p.ws = (char*)d_ws;
  (void)hipFuncSetAttribute((const void*)fused_block, hipFuncAttributeMaxDynamicSharedMemorySize, L_TOTAL);
  fused_block<<<256, 512, L_TOTAL, stream>>>(p);
}
```

```cpp
#include <hip/hip_runtime.h>
#include <stdint.h>

#define KDBG 0
#define NODMA 0
#define CODEWARM 1
#define L2WARM 1
#define TLBWARM 0
#define XMAP 2

#define DI __device__ __forceinline__
#define GAS __attribute__((address_space(1)))
#define LAS __attribute__((address_space(3)))
typedef unsigned u32;
typedef unsigned short u16;
typedef unsigned long long u64;
typedef u32 v4u __attribute__((ext_vector_type(4)));
typedef u32 v2u __attribute__((ext_vector_type(2)));
typedef float f32x4 __attribute__((ext_vector_type(4)));
typedef float f32x2 __attribute__((ext_vector_type(2)));
typedef __bf16 bf16x8 __attribute__((ext_vector_type(8)));
typedef __bf16 bf16x2 __attribute__((ext_vector_type(2)));
typedef short s16x4 __attribute__((ext_vector_type(4)));
typedef __amdgpu_buffer_rsrc_t rsrc_t;
typedef GAS u32 gu32;
typedef GAS u64 gu64;
#define RLX_AGENT __ATOMIC_RELAXED, __HIP_MEMORY_SCOPE_AGENT

namespace {
constexpr int S_ = 2048, D_ = 512, F_ = 1376;
constexpr u32 WS_EPOCH = 0;
constexpr u32 WS_ALIVE = 4096;
constexpr u32 WS_FLAG = 32768;
constexpr u32 WS_STAT = 262144;
constexpr u32 WS_MODG = 524288;
constexpr u32 WS_MODF = 655360;
constexpr u32 WS_O = 1u << 20;
constexpr u32 WS_X1 = 3u << 20;
constexpr u32 WS_HH = 5u << 20;
constexpr u32 WS_RFLAG = 8192;
constexpr u32 WS_B4T = 11u << 20;
constexpr u32 WS_B5T = 14u << 20;
constexpr u32 WS_DBG = 16u << 20;
constexpr u32 WS_BYTES = 32u << 20;
constexpr int L_XY = 0;
constexpr int L_Z = 81920;
constexpr int L_MISC = 158720;
constexpr int L_TOTAL = 163840;
constexpr int L_RED = 73728;
constexpr int L_MODA = L_MISC;
constexpr int L_CNT = L_MISC + 4096;
constexpr int L_GAB = L_MISC + 4096 + 64;
constexpr int L_JUNK = L_MISC + 4096 + 64 + 512;
constexpr int L_STAMP = 156672;
constexpr u32 SPIN_CAP = 1u << 18;
constexpr u32 CODE_BYTES = 26112;

DI f32x4 mfma32(v4u a, v4u b, f32x4 c) {
  return __builtin_amdgcn_mfma_f32_16x16x32_bf16(__builtin_bit_cast(bf16x8, a), __builtin_bit_cast(bf16x8, b), c, 0, 0, 0);
}
DI f32x4 mfma16(v2u a, v2u b, f32x4 c) {
  return __builtin_amdgcn_mfma_f32_16x16x16bf16_1k(__builtin_bit_cast(s16x4, a), __builtin_bit_cast(s16x4, b), c, 0, 0, 0);
}
DI u32 pk(float lo, float hi) { f32x2 v = {lo, hi}; return __builtin_bit_cast(u32, __builtin_convertvector(v, bf16x2)); }
DI u32 pk2(f32x2 v) { return __builtin_bit_cast(u32, __builtin_convertvector(v, bf16x2)); }
DI v2u pk4(f32x4 v) { v2u r; r.x = pk(v.x, v.y); r.y = pk(v.z, v.w); return r; }
DI float asf(u32 u) { return __builtin_bit_cast(float, u); }
DI u32 asu(float f) { return __builtin_bit_cast(u32, f); }
DI float blo(u32 u) { return asf(u << 16); }
DI float bhi(u32 u) { return asf(u & 0xffff0000u); }
DI f32x2 unp(u32 u) { f32x2 r = {blo(u), bhi(u)}; return r; }
DI float dot2(u32 a, u32 b, float c) { return __builtin_amdgcn_fdot2_f32_bf16(__builtin_bit_cast(bf16x2, a), __builtin_bit_cast(bf16x2, b), c, false); }
DI v2u trd(LAS char* p) { return __builtin_bit_cast(v2u, __builtin_amdgcn_ds_read_tr16_b64_v4i16((LAS s16x4*)p)); }
DI v4u cat(v2u a, v2u b) { v4u r; r.x = a.x; r.y = a.y; r.z = b.x; r.w = b.y; return r; }
template <int MI, int SI> DI void dma(u32 mb, u32 voff, rsrc_t srd, u32 sb) {
  u32 t;
  asm volatile("s_add_u32 m0, %1, %c2\n\ts_add_u32 %0, %3, %c4\n\tbuffer_load_dwordx4 %5, %6, %0 offen lds"
               : "=&s"(t) : "s"(mb), "i"(MI), "s"(sb), "i"(SI), "v"(voff), "s"(srd) : "scc");
}
DI void touch(u32 voff, rsrc_t srd, u32 sb) {
  asm volatile("s_mov_b32 m0, %c0\n\ts_nop 0\n\tbuffer_load_dword %1, %2, %3 offen sc1 lds" ::"i"(L_JUNK), "v"(voff), "s"(srd), "s"(sb));
}
#define BAR() asm volatile("s_waitcnt lgkmcnt(0)\n\ts_barrier" ::: "memory")
#define VMW(n) asm volatile("s_waitcnt vmcnt(" #n ")" ::: "memory")
#define LGW() asm volatile("s_waitcnt lgkmcnt(0)" ::: "memory")
#define SCHED() __builtin_amdgcn_sched_barrier(0)
#define LD16(dst, voff, srd, soff, IMM) asm volatile("buffer_load_dwordx4 %0, %1, %2, %3 offen offset:" #IMM : "=v"(dst) : "v"(voff), "s"(srd), "s"(soff))
#define LD16S(dst, voff, srd, soff, IMM) asm volatile("buffer_load_dwordx4 %0, %1, %2, %3 offen offset:" #IMM " sc1" : "=v"(dst) : "v"(voff), "s"(srd), "s"(soff))
#define LD8(dst, voff, srd, soff, IMM) asm volatile("buffer_load_dwordx2 %0, %1, %2, %3 offen offset:" #IMM : "=v"(dst) : "v"(voff), "s"(srd), "s"(soff))
#define LD8S(dst, voff, srd, soff, IMM) asm volatile("buffer_load_dwordx2 %0, %1, %2, %3 offen offset:" #IMM " sc1" : "=v"(dst) : "v"(voff), "s"(srd), "s"(soff))
#define LD4(dst, voff, srd, soff) asm volatile("buffer_load_dword %0, %1, %2, %3 offen" : "=v"(dst) : "v"(voff), "s"(srd), "s"(soff))
#define LD4S(dst, voff, srd, soff) asm volatile("buffer_load_dword %0, %1, %2, %3 offen sc1" : "=v"(dst) : "v"(voff), "s"(srd), "s"(soff))

template <int IMM> DI void ld16s(v4u& r, u32 v, rsrc_t rs, u32 so) { asm volatile("buffer_load_dwordx4 %0, %1, %2, %3 offen offset:%c4 sc1" : "=v"(r) : "v"(v), "s"(rs), "s"(so), "i"(IMM)); }
template <int CTRL> DI float dppf(float v) { return asf(__builtin_amdgcn_update_dpp(0, asu(v), CTRL, 0xf, 0xf, true)); }
DI float row16_sum(float v) { v += dppf<0x128>(v); v += dppf<0x124>(v); v += dppf<0x122>(v); v += dppf<0x121>(v); return v; }
DI float sw32_add(float a, float b) { auto r = __builtin_amdgcn_permlane32_swap(asu(a), asu(b), false, false); return asf(r[0]) + asf(r[1]); }
DI float sw16_add(float a, float b) { auto r = __builtin_amdgcn_permlane16_swap(asu(a), asu(b), false, false); return asf(r[0]) + asf(r[1]); }
DI float x16_max(float v) { auto r = __builtin_amdgcn_permlane16_swap(asu(v), asu(v), false, false); return fmaxf(asf(r[0]), asf(r[1])); }
DI float x32_max(float v) { auto r = __builtin_amdgcn_permlane32_swap(asu(v), asu(v), false, false); return fmaxf(asf(r[0]), asf(r[1])); }
DI int fresh_lane() { int l; asm volatile("v_mbcnt_lo_u32_b32 %0, -1, 0\n\tv_mbcnt_hi_u32_b32 %0, -1, %0" : "=v"(l)); return l; }
DI float rdl(float v, int l) { return asf(__builtin_amdgcn_readlane(asu(v), l)); }

struct Params {
  const u16 *x, *wqkv, *wo, *w1, *w3, *w2, *c, *wmod;
  u16* out;
  char* ws;
};

#define LOOPSEL 0
#define STAMP(i) do {} while (0)
#define KSTAMP(lp, i) do {} while (0)
#define STAMP_DUMP() do {} while (0)

DI u32 flag_off(int phase, int g, int h) { return WS_FLAG + ((phase * 32 + g) * 2 + h) * 1024; }
DI void poll_flags(rsrc_t rWS, u32 vfl, u32 foff, u64 care, u32 tag) {
  for (u32 sp = 0; sp < SPIN_CAP; ++sp) {
    asm volatile("" ::: "memory");
    const u32 v = __builtin_amdgcn_raw_buffer_load_b32(rWS, vfl, foff, 16);
    if (!(__builtin_amdgcn_ballot_w64(v != tag) & care)) break;
  }
  VMW(0);
}
DI void raise_flag(rsrc_t rWS, u32 foff, u32 vtag, u32 vzero, u32 m1) {
  asm volatile("s_mov_b32 exec_hi, 0\n\ts_mov_b32 exec_lo, %4\n\tbuffer_store_dword %0, %1, %2, %3 offen sc1\n\ts_mov_b64 exec, -1" ::"v"(vtag), "v"(vzero), "s"(rWS), "s"(foff), "s"(m1) : "memory");
}
DI void signal_flag(rsrc_t rWS, u32 cnt_addr, u32 foff, u32 tag) {
  u32 t, a = cnt_addr, one = 1u, vt = tag, vz = 0u;
  asm volatile("s_mov_b64 exec, 1\n\tds_add_rtn_u32 %0, %1, %2\n\ts_waitcnt lgkmcnt(0)\n\tv_cmp_eq_u32 vcc, 7, %0\n\ts_and_b64 exec, exec, vcc\n\t"
               "buffer_store_dword %3, %4, %5, %6 offen sc1\n\ts_mov_b64 exec, -1"
               : "=&v"(t) : "v"(a), "v"(one), "v"(vt), "v"(vz), "s"(rWS), "s"(foff) : "vcc", "scc", "memory");
}
DI void arrive(u32 cnt_addr) {
  u32 a = cnt_addr, one = 1u;
  asm volatile("s_waitcnt lgkmcnt(0)\n\ts_mov_b64 exec, 1\n\tds_add_u32 %0, %1\n\ts_mov_b64 exec, -1" ::"v"(a), "v"(one) : "memory");
}
DI void await8(u32 cnt_addr) {
  u32 a = cnt_addr, t, sv;
  asm volatile("1:\n\tds_read_b32 %0, %2\n\ts_waitcnt lgkmcnt(0)\n\tv_readfirstlane_b32 %1, %0\n\ts_nop 0\n\ts_cmp_lt_u32 %1, 8\n\ts_cbranch_scc1 1b"
               : "=&v"(t), "=&s"(sv) : "v"(a) : "scc", "memory");
}
#define PIN(x) asm volatile("" : "+v"(x))
DI float qp(float r) { PIN(r); return r; }
DI float q_shl(u32 a) { return qp(blo(a)); }
DI float q_hi(u32 a) { return qp(bhi(a)); }
DI float q_fma(float a, float b, float c) { return qp(__builtin_fmaf(a, b, c)); }
DI float q_fmas(float a, float b, float c) { return qp(__builtin_fmaf(a, b, c)); }
DI float q_mul(float a, float b) { return qp(a * b); }
DI float q_mulk(float a) { return qp(a * -1.4426950408889634f); }
DI float q_add1(float a) { return qp(a + 1.f); }
DI float q_exp(float a) { return qp(__builtin_amdgcn_exp2f(a)); }
DI float q_rcp(float a) { return qp(__builtin_amdgcn_rcpf(a)); }
DI u32 q_cvt(float a, float b) { u32 r = pk(a, b); PIN(r); return r; }
DI float q_mov(float a) { return qp(a); }
DI u32 opq(u32 v) { asm volatile("" : "+v"(v)); return v; }

DI float sel(u64 m, float a, float b) { return __builtin_amdgcn_inverse_ballot_w64(m) ? a : b; }
DI v2u attend(v2u kp, v2u vp, v2u kc, v2u vc, v2u q, u64 m0, u64 m1, u32 hm, bool noprev) {
  const f32x4 z = {0.f, 0.f, 0.f, 0.f};
  const f32x4 sp = mfma16(kp, q, z);
  const f32x4 sc = mfma16(kc, q, z);
  const float K2 = 0.25f * 1.4426950408889634f;
  const u64 par = 0xaaaaaaaaaaaaaaaaull;
  float p0 = sel(par, sp[1], sp[0]), p1 = sel(par, sp[3], sp[2]);
  if (noprev) { p0 = -INFINITY; p1 = -INFINITY; }
  const float t0 = sel(m0, sel(par, sc[1], sc[0]), p0), t1 = sel(m1, sel(par, sc[3], sc[2]), p1);
  const float m = x32_max(x16_max(fmaxf(t0, t1)));
  const float mk = -m * K2;
  const float e0 = __builtin_amdgcn_exp2f(__builtin_fmaf(t0, K2, mk)), e1 = __builtin_amdgcn_exp2f(__builtin_fmaf(t1, K2, mk));
  const u32 w0 = pk(e0, e0) & hm, w1 = pk(e1, e1) & hm;
  v4u pf;
  pf.x = __builtin_amdgcn_inverse_ballot_w64(m0) ? 0u : w0; pf.y = __builtin_amdgcn_inverse_ballot_w64(m1) ? 0u : w1;
  pf.z = __builtin_amdgcn_inverse_ballot_w64(m0) ? w0 : 0u; pf.w = __builtin_amdgcn_inverse_ballot_w64(m1) ? w1 : 0u;
  const f32x4 o = mfma32(cat(vp, vc), pf, z);
  float sum = e0 + e1;
  sum = sw16_add(sum, sum); sum = sw32_add(sum, sum);
  return pk4(o * __builtin_amdgcn_rcpf(sum));
}

template <int N> using IC = std::integral_constant<int, N>;
template <int I, int N, class F> DI void sfor(F&& f) { if constexpr (I < N) { f(IC<I>{}); sfor<I + 1, N>(f); } }

__global__ void __launch_bounds__(512, 2) fused_block(Params p) {
  extern __shared__ __attribute__((aligned(16))) char smem[];
  LAS char* L = (LAS char*)smem;
  const int tid = threadIdx.x, lane = tid & 63, w = __builtin_amdgcn_readfirstlane(tid >> 6);
  const int c16 = lane & 15, g4 = lane >> 4;
  const int b = blockIdx.x;
  int g, j;
  if (XMAP == 0) { g = (b & 7) * 4 + ((b >> 3) & 3); j = b >> 5; }
  else if (XMAP == 2) { const int x = b & 7, r = b >> 3; g = (x >> 1) * 8 + (r & 7); j = (x & 1) * 4 + (r >> 3); }
  else { const int x = b & 7, r = b >> 3; g = (x >> 2) * 16 + (r & 15); j = (x & 3) * 2 + (r >> 4); }
  const rsrc_t rX = __builtin_amdgcn_make_buffer_rsrc((void*)p.x, 0, S_ * D_ * 2, 0x00020000);
  const rsrc_t rQKV = __builtin_amdgcn_make_buffer_rsrc((void*)p.wqkv, 0, D_ * 3 * D_ * 2, 0x00020000);
  const rsrc_t rWO = __builtin_amdgcn_make_buffer_rsrc((void*)p.wo, 0, D_ * D_ * 2, 0x00020000);
  const rsrc_t rW1 = __builtin_amdgcn_make_buffer_rsrc((void*)p.w1, 0, D_ * F_ * 2, 0x00020000);
  const rsrc_t rW3 = __builtin_amdgcn_make_buffer_rsrc((void*)p.w3, 0, D_ * F_ * 2, 0x00020000);
  const rsrc_t rW2 = __builtin_amdgcn_make_buffer_rsrc((void*)p.w2, 0, F_ * D_ * 2, 0x00020000);
  const rsrc_t rC = __builtin_amdgcn_make_buffer_rsrc((void*)p.c, 0, D_ * 2, 0x00020000);
  const rsrc_t rMOD = __builtin_amdgcn_make_buffer_rsrc((void*)p.wmod, 0, D_ * 6 * D_ * 2, 0x00020000);
  const rsrc_t rWS = __builtin_amdgcn_make_buffer_rsrc((void*)p.ws, 0, WS_BYTES, 0x00020000);
  const rsrc_t rOUT = __builtin_amdgcn_make_buffer_rsrc((void*)p.out, 0, S_ * D_ * 2, 0x00020000);
  STAMP(0);

  u32 ep;
  { const u32 vz = 0; asm volatile("buffer_load_dword %0, %1, %2, 0 offen sc1" : "=v"(ep) : "v"(vz), "s"(rWS)); }
  u32 vl16 = lane * 16; PIN(vl16);
  const int bm = b < 192 ? b : b - 192, mq = bm / 48, cl = bm - 48 * mq;
  v4u wm[4]; v2u cv; u32 ep0;
  if (w < 4) {
    const u32 vm = (tid >> 3) * 24576 + (tid & 7) * 16, s0 = mq * (128 * 6144) + cl * 128, s1 = s0 + 6144, s2 = s0 + 12288, s3 = s0 + 18432;
    LD16(wm[0], vm, rMOD, s0, 0); LD16(wm[1], vm, rMOD, s1, 0); LD16(wm[2], vm, rMOD, s2, 0); LD16(wm[3], vm, rMOD, s3, 0);
    const u32 vc = (tid >> 3) * 8, scq = mq * 256;
    LD8(cv, vc, rC, scq, 0);
    const u32 vz = 0; asm volatile("buffer_load_dword %0, %1, %2, 0 offen sc1" : "=v"(ep0) : "v"(vz), "s"(rWS));
  }
  if (tid < 16) *(LAS u32*)(L + L_CNT + tid * 4) = 0u;
  if (w == 7) {
    u64 pc; asm volatile("s_getpc_b64 %0" : "=s"(pc));
    const rsrc_t rPC = __builtin_amdgcn_make_buffer_rsrc((void*)(pc & ~127ull), 0, CODE_BYTES, 0x00020000);
    touch(lane * 128 + ((b >> 3) & 3) * 8192, rPC, 0);
  }
  BAR();
  const int st_qd = lane >> 2, st_r = (st_qd & 3) + 4 * (st_qd >> 3);
  const int st_col = ((st_qd >> 2) & 1) * 64 + (((lane & 3) ^ ((w & 1) << 1)) << 4);
  const int rotp = g & 7;
  u32 vB1 = st_r * 3072 + st_col; PIN(vB1);
  const u32 mB1 = (w >> 2) * 12288 + (w & 3) * 1024, sB1 = (w >> 2) * 98304 + (w & 3) * 24576 + j * 128;
  auto stage_b1 = [&](auto PC, auto BC) {
    constexpr int pair = decltype(PC)::value, base = decltype(BC)::value;
    const u32 sp = ((pair + rotp) & 7) * 196608 + sB1;
    dma<base, 0>(mB1, vB1, rQKV, sp); dma<base + 4096, 1024>(mB1, vB1, rQKV, sp); dma<base + 8192, 2048>(mB1, vB1, rQKV, sp);
  };
  const int hd = w & 3, blk2 = w >> 2, kq = w & 3, mh = w >> 2;
  const int cs0 = j < 4 ? 22 * j : 88 + 21 * (j - 4);
  const int cend = cs0 + (j < 4 ? 22 : 21);
  STAMP(1);
  const int T0 = 12 * (g >> 4) + 3 * (w & 3), rcb = min(cs0 + T0, 168);
  if (w < 4) {
    asm volatile("s_waitcnt vmcnt(0)" : "+v"(cv), "+v"(wm[0]), "+v"(wm[1]), "+v"(wm[2]), "+v"(wm[3]), "+v"(ep0)::"memory");
    const u32 tg = __builtin_amdgcn_readfirstlane(ep0) + 1u;
    const f32x4 cf = {blo(cv.x), bhi(cv.x), blo(cv.y), bhi(cv.y)};
    float sc[4];
#pragma unroll
    for (int r = 0; r < 4; ++r) sc[r] = cf[r] * __builtin_amdgcn_rcpf(1.f + __builtin_amdgcn_exp2f(cf[r] * -1.4426950408889634f));
    f32x2 v[4];
#pragma unroll
    for (int i = 0; i < 4; ++i) v[i] = (unp(wm[0][i]) * sc[0] + unp(wm[1][i]) * sc[1]) + (unp(wm[2][i]) * sc[2] + unp(wm[3][i]) * sc[3]);
    const float a0 = sw32_add(v[0].x, v[2].x), a1 = sw32_add(v[0].y, v[2].y), a2 = sw32_add(v[1].x, v[3].x), a3 = sw32_add(v[1].y, v[3].y);
    float t0 = sw16_add(a0, a2), t1 = sw16_add(a1, a3);
    t0 += dppf<0x128>(t0); t1 += dppf<0x128>(t1);
    f32x2 o = {t0, t1};
    *(LAS f32x2*)(L + L_RED + w * 256 + (lane & 7) * 32 + g4 * 8) = o;
    LGW();
    u32 old = 0;
    if (lane == 0) old = __hip_atomic_fetch_add((LAS u32*)(L + L_CNT + 28), 1u, __ATOMIC_RELAXED, __HIP_MEMORY_SCOPE_WORKGROUP);
    if (__builtin_amdgcn_readfirstlane(old) == 3u) {
      asm volatile("" ::: "memory");
      float s = 0.f;
#pragma unroll
      for (int i = 0; i < 4; ++i) s += *(LAS float*)(L + L_RED + i * 256 + lane * 4);
      v2u gr; gr.x = asu(s); gr.y = tg;
      if (b < 192) __builtin_amdgcn_raw_buffer_store_b64(gr, rWS, lane * 8, WS_MODG + (mq * 3072 + cl * 64) * 8, 16);
      __builtin_amdgcn_raw_buffer_store_b32(tg, rWS, 0, WS_ALIVE + b * 4, 16);
    }
  }
  STAMP(2);
  v4u xr[10];
  {
    const int r0 = 64 * g - 16 + w;
    const u32 s0 = (u32)max(r0, 0) * 1024u, s1 = (u32)max(r0 + 8, 0) * 1024u;
    LD16(xr[0], vl16, rX, s0, 0); LD16(xr[1], vl16, rX, s1, 0);
#pragma unroll
    for (int i = 2; i < 10; ++i) { const u32 so = (u32)(r0 + 8 * i) * 1024u; LD16(xr[i], vl16, rX, so, 0); }
  }
  stage_b1(IC<0>{}, IC<L_XY>{});
  int afo[4];
#pragma unroll
  for (int i = 0; i < 4; ++i) { afo[i] = blk2 * 32768 + c16 * 1024 + (((4 * i + g4) ^ c16) << 4); PIN(afo[i]); }
  const u64 am0 = __builtin_amdgcn_ballot_w64((c16 & 14) >= 4 * g4), am1 = __builtin_amdgcn_ballot_w64((c16 & 14) >= 4 * g4 + 2);
  u32 ahm = (lane & 1) ? 0xffff0000u : 0xffffu; PIN(ahm);
  u32 vOst = c16 * 16 + g4 * 256 + (hd & 1) * 8; PIN(vOst);
  int tB1 = g4 * 1024 + 64 * (c16 >> 2) + 8 * (lane & 3) + 32 * ((hd & 1) ^ (g4 & 1)) + 256 * (hd >> 1); PIN(tB1);
  int hw0 = w * 1024 + ((((lane - 8 * rotp) & 63) ^ w) << 4); PIN(hw0);
#define vfl ((opq(vl16) & 112u) << 3)
  STAMP(3);
  v4u bw[48];
  auto read_b1 = [&](int pair, int base, int off) {
#pragma unroll
    for (int kk = 0; kk < 2; ++kk)
#pragma unroll
      for (int ty = 0; ty < 3; ++ty) {
        LAS char* a = L + base + (off + kk * 12288 + ty * 4096);
        bw[(2 * pair + kk) * 3 + ty] = cat(trd(a), trd(a + 512));
      }
  };
  f32x2 xn[10][4];
  v4u ga_[4]; v2u pv;
  const u32 sMG = WS_MODG + w * 1024;
  {
    float u[10];
    sfor<0, 10>([&](auto I) {
      constexpr int i = decltype(I)::value;
      if constexpr (i == 0) asm volatile("s_waitcnt vmcnt(11)" : "+v"(xr[0]), "+v"(xr[1]), "+v"(ep)::"memory");
      if constexpr (i == 2) asm volatile("s_waitcnt vmcnt(12)" : "+v"(xr[2]), "+v"(xr[3])::"memory");
      if constexpr (i == 4) asm volatile("s_waitcnt vmcnt(13)" : "+v"(xr[4]), "+v"(xr[5])::"memory");
      if constexpr (i == 6) asm volatile("s_waitcnt vmcnt(14)" : "+v"(xr[6]), "+v"(xr[7])::"memory");
      if constexpr (i == 8) asm volatile("s_waitcnt vmcnt(15)" : "+v"(xr[8]), "+v"(xr[9])::"memory");
      float s = 0.f, q = 0.f;
#pragma unroll
      for (int e = 0; e < 4; ++e) { s = dot2(xr[i][e], 0x3f803f80u, s); q = dot2(xr[i][e], xr[i][e], q); }
      u[i] = sw32_add(s, q);
      PIN(u[i]);
      if constexpr (i == 1) stage_b1(IC<1>{}, IC<L_XY + 24576>{});
      if constexpr (i == 3) stage_b1(IC<2>{}, IC<L_XY + 49152>{});
      if constexpr (i == 5) stage_b1(IC<3>{}, IC<L_Z>{});
      if constexpr (i == 7) stage_b1(IC<4>{}, IC<L_Z + 24576>{});
    });
    float t[5];
#pragma unroll
    for (int i = 0; i < 5; ++i) t[i] = row16_sum(sw16_add(u[i], u[i + 5]));
    float v = t[0];
#pragma unroll
    for (int i = 1; i < 5; ++i) v = c16 == i ? t[i] : v;
    auto r = __builtin_amdgcn_permlane32_swap(asu(v), asu(v), false, false);
    const float mean = asf(r[0]) * (1.f / 512.f);
    const float var = fmaxf(asf(r[1]) * (1.f / 512.f) - mean * mean, 0.f);
    float rs = __builtin_amdgcn_rsqf(var + 1e-5f), nm = -mean * rs;
    PIN(rs); PIN(nm);
    STAMP(4);
    { const u32 vo = ((lane >> 3) & 3) * 24576 + (lane & 7) * 8, so = WS_MODG + (1024 + 8 * b) * 8; LD8S(pv, vo, rWS, so, 0); }
    { const u32 s0 = sMG, s1 = sMG + 24576, s2 = sMG + 49152, s3 = sMG + 73728;
      LD16S(ga_[0], vl16, rWS, s0, 0); LD16S(ga_[1], vl16, rWS, s1, 0); LD16S(ga_[2], vl16, rWS, s2, 0); LD16S(ga_[3], vl16, rWS, s3, 0); }
    if (w >= 4) {
      const int u = lane >> 2, row = (u & 3) | ((u >> 1) & 4) | ((u << 1) & 8);
      const u32 vo = row * 2752 + (lane & 3) * 16, so = (g & 15) * (32 * 2752) + rcb * 16, mb = L_Z + 49152 + (w & 3) * 4096;
      dma<0, 0>(mb, vo, rW1, so); dma<1024, 16 * 2752>(mb, vo, rW1, so); dma<2048, 0>(mb, vo, rW3, so); dma<3072, 16 * 2752>(mb, vo, rW3, so);
    } else {
      const int u = lane >> 1, row = (u & 3) | ((u >> 1) & 4) | ((u << 1) & 8) | (u & 16);
      const u32 vo = row * 1024 + (lane & 1) * 16, so = g * 32768 + j * 128 + w * 32, mb = L_Z + 65536 + w * 2048;
      dma<0, 0>(mb, vo, rW2, so); dma<1024, 0>(mb, vo, rW2, so + (g < 11 ? 32 * 32768 : 0));
    }
    sfor<0, 10>([&](auto I) {
      constexpr int i = decltype(I)::value;
      const float a = rdl(rs, (i / 5) * 16 + i % 5), c = rdl(nm, (i / 5) * 16 + i % 5);
#pragma unroll
      for (int e = 0; e < 4; ++e) { xn[i][e] = unp(xr[i][e]) * a + c; PIN(xn[i][e]); }
    });
  }
  const u32 tag = __builtin_amdgcn_readfirstlane(ep) + 1u;
  u32 m1; asm("s_lshr_b32 %0, 1, %1" : "=s"(m1) : "s"(w) : "scc");
#define RAISE(fo) raise_flag(rWS, (fo), tag, vl16, m1)
#define SIGNAL(idx, fo) signal_flag(rWS, L_CNT + 4 * (idx), (fo), tag)
  STAMP(5);
  asm volatile("s_waitcnt vmcnt(2)" : "+v"(ga_[0]), "+v"(ga_[1]), "+v"(ga_[2]), "+v"(ga_[3]), "+v"(pv)::"memory");
  auto gsum = [&](v4u (&a)[4], f32x2& o) -> u32 {
    o.x = (asf(a[0].x) + asf(a[1].x)) + (asf(a[2].x) + asf(a[3].x)); o.y = (asf(a[0].z) + asf(a[1].z)) + (asf(a[2].z) + asf(a[3].z));
    return ((a[0].y ^ tag) | (a[0].w ^ tag) | (a[1].y ^ tag) | (a[1].w ^ tag)) | ((a[2].y ^ tag) | (a[2].w ^ tag) | (a[3].y ^ tag) | (a[3].w ^ tag));
  };
  auto gather = [&](u32 voff, u32 soff, f32x2& o) {
    for (u32 sp = 0; sp < SPIN_CAP; ++sp) {
      asm volatile("" ::: "memory");
      v4u a[4];
#pragma unroll
      for (int q = 0; q < 4; ++q) a[q] = __builtin_amdgcn_raw_buffer_load_b128(rWS, voff, soff + q * 24576, 16);
      if (!__builtin_amdgcn_ballot_w64(gsum(a, o) != 0u)) break;
    }
  };
  const float one = w >= 4 ? 1.f : 0.f;
  {
    f32x2 mA;
    if (__builtin_amdgcn_ballot_w64(gsum(ga_, mA) != 0u)) gather(vl16, sMG, mA);
    *(LAS f32x2*)(L + L_MODA + w * 512 + (vl16 >> 1)) = mA + one;
  }
  BAR();
  if (w == 7) {
    const u32 vo = ((lane >> 3) & 3) * 24576 + (lane & 7) * 8, so = WS_MODG + (1024 + 8 * b) * 8;
    for (u32 sp = 0; sp < SPIN_CAP && __builtin_amdgcn_ballot_w64(pv.y != tag); ++sp) {
      asm volatile("" ::: "memory");
      pv = __builtin_amdgcn_raw_buffer_load_b64(rWS, vo, so, 16);
    }
    float sv = asf(pv.x);
    sv += dppf<0x128>(sv); sv = sw16_add(sv, sv);
    pv.x = asu(sv);
    __builtin_amdgcn_raw_buffer_store_b64(pv, rWS, (lane & 7) * 8, WS_MODF + (1024 + 8 * b) * 8, 16);
  }
  STAMP(6);
  int vma = L_MODA + 2 * vl16; PIN(vma);
  const f32x4 ms0 = *(LAS f32x4*)(L + vma), ms1 = *(LAS f32x4*)(L + vma + 16);
  const f32x4 ma0 = *(LAS f32x4*)(L + vma + 2048), ma1 = *(LAS f32x4*)(L + vma + 2064);
  int tz0 = tB1 + L_Z; PIN(tz0);
  { int t2 = tB1 + 49152; PIN(t2); read_b1(0, tB1, 0); read_b1(1, tB1, 24576); read_b1(2, t2, 0); read_b1(3, tz0, 0); read_b1(4, tz0, 24576); }
  {
    VMW(0);
    if (w >= 4) {
      LAS char* a0 = L + L_Z + 49152 + (w & 3) * 4096 + ((lane >> 1) & 1) * 2048 + 64 * ((c16 >> 2) + 4 * (g4 & 1) + 16 * (g4 >> 1)) + 8 * (lane & 1);
#pragma unroll
      for (int ct = 0; ct < 3; ++ct) {
        const int c = min(cs0 + T0 + ct - rcb, 3) * 16;
        const v4u f = cat(trd(a0 + c), trd(a0 + c + 512));
        const u32 sf = WS_B4T + ((j * 16 + (g & 15)) * 24 + T0 + ct) * 1024;
        asm volatile("buffer_store_dwordx4 %0, %1, %2, %3 offen sc1\n\ts_nop 1" ::"v"(f), "v"(vl16), "s"(rWS), "s"(sf) : "memory");
      }
    }
    else {
      LAS char* a0 = L + L_Z + 65536 + w * 2048 + 32 * ((c16 >> 2) + 4 * (g4 & 1) + 16 * (g4 >> 1)) + 8 * (lane & 3);
#pragma unroll
      for (int i = 0; i < 2; ++i) {
        const v4u f = cat(trd(a0 + i * 1024), trd(a0 + i * 1024 + 256));
        const u32 sf = WS_B5T + ((j * 43 + g + (i && g < 11 ? 32 : 0)) * 4 + w) * 1024;
        asm volatile("buffer_store_dwordx4 %0, %1, %2, %3 offen sc1\n\ts_nop 1" ::"v"(f), "v"(vl16), "s"(rWS), "s"(sf) : "memory");
      }
    }
  }
  BAR();
  STAMP(7);
  {
    int hw1 = (hw0 ^ 128) + 8192; PIN(hw1);
    sfor<0, 10>([&](auto I) {
      constexpr int i = decltype(I)::value;
      v4u o;
      o.x = pk2(xn[i][0] * ma0.xy + ms0.xy); o.y = pk2(xn[i][1] * ma0.zw + ms0.zw);
      o.z = pk2(xn[i][2] * ma1.xy + ms1.xy); o.w = pk2(xn[i][3] * ma1.zw + ms1.zw);
      if constexpr (i == 8) { hw0 += 65536; hw1 += 65536; }
      *(LAS v4u*)(L + ((i & 1) ? hw1 : hw0) + ((i >> 1) & 3) * 16384) = o;
      if constexpr (i == 0) stage_b1(IC<5>{}, IC<L_Z>{});
      if constexpr (i == 3) stage_b1(IC<6>{}, IC<L_Z + 24576>{});
      if constexpr (i == 6) stage_b1(IC<7>{}, IC<L_Z + 49152>{});
      SCHED();
    });
  }
  VMW(0);
  BAR();
  RAISE(WS_RFLAG + (j * 32 + g) * 4);
  u32 flr;
  STAMP(8);

  const f32x4 z4 = {0.f, 0.f, 0.f, 0.f};
#define AFRAG(off, ks) (*(LAS v4u*)(L + afo[(ks) & 3] + ((off) + ((ks) >> 2) * 256)))
  const int ch0 = cs0 + 3 * w;
  constexpr int ND = NODMA ? 0 : 1;
  v4u b4[48];
  const u32 sB4 = WS_B4T + (j * 16 * 24 + 3 * w) * 1024;
  auto load_b4 = [&](auto NC) {
    constexpr int n = decltype(NC)::value, ks = n / 3, ct = n % 3;
    const u32 so = sB4 + ((ks + 2 * rotp) & 15) * 24576;
    v4u& r = b4[n];
    if constexpr (ct == 0) LD16S(r, vl16, rWS, so, 0); else if constexpr (ct == 1) LD16S(r, vl16, rWS, so, 1024); else LD16S(r, vl16, rWS, so, 2048);
  };
  auto landed_b4 = [&](auto AC, auto BC) {
#pragma unroll
    for (int i = decltype(AC)::value; i < decltype(BC)::value; ++i) { v4u& r = b4[i]; PIN(r); }
  };
  v4u gb_, gg_;
  u32 vgg, fl0, vRow, vB3;
  int pw, pr, t3;
  f32x4 aKh = z4, aVh = z4, aQ = z4, aK = z4, aV = z4, bQ = z4, bK = z4, bV = z4;
  {
    v4u ah = AFRAG(0, 0), a1 = AFRAG(16384, 0), a2 = AFRAG(32768, 0);
    int l3, c3, g3; f32x2 mB, mG;
    const u32 mB3 = L_Z + (w >> 2) * 4096 + (w & 3) * 1024, sB3 = ((w >> 2) * 32 + 4 * (w & 3)) * 1024 + j * 128;
    auto job = [&](auto NC) {
      constexpr int n = decltype(NC)::value;
      if constexpr (n == 3) { l3 = opq(vl16) >> 4; c3 = l3 & 15; g3 = l3 >> 4; PIN(c3); PIN(g3); vgg = (vl16 < 512 ? 8192 : 20480 - 512) + 512 * j + vl16; PIN(vgg); }
      if constexpr (n == 7) { vRow = (8 * w + (l3 >> 3)) * 1024 + (l3 & 7) * 16; PIN(vRow); }
      if constexpr (n == 8) { pr = (8 * w + (l3 >> 3)) * 256 + (((2 * (l3 & 7)) ^ ((l3 >> 3) & 7)) << 4); PIN(pr); }
      if constexpr (n == 10) { pw = kq * 16384 + (32 * mh + c3) * 256 + ((g3 ^ (c3 & 3)) << 4) + ((c3 & 4) << 4); PIN(pw); }
      if constexpr (n == 11) { t3 = L_Z + kq * 16384 + g3 * 1024 + 64 * (c3 >> 2) + 8 * (l3 & 3) + 32 * (g3 & 1); PIN(t3); }
      if constexpr (n == 13) { vB3 = ((l3 >> 4) & 1) * 64 + (((l3 & 3) ^ ((w & 1) << 1)) << 4); PIN(vB3); }
      if constexpr (n == 14) { vB3 += (16 * (l3 >> 5) + ((l3 >> 2) & 3)) * 1024; PIN(vB3); }
      if constexpr (n == 31) { mB.x = asf(gb_.x) + one; PIN(mB.x); mB.y = asf(gb_.z) + one; PIN(mB.y); *(LAS f32x2*)(L + L_MODA + w * 512 + (vl16 >> 1)) = mB; }
      if constexpr (n == 32) { mG.x = asf(gg_.x); mG.y = asf(gg_.z); *(LAS f32x2*)(L + L_GAB + (vl16 >> 1)) = mG; }
      if constexpr (n == 34) { fl0 = (gb_.y ^ tag) | (gb_.w ^ tag) | (gg_.y ^ tag) | (gg_.w ^ tag); PIN(fl0); }
      if constexpr (n == 6) { const u32 s0 = WS_MODF + 12288 + w * 1024; LD16S(gb_, vl16, rWS, s0, 0); }
      if constexpr (n == 9) { const u32 s0 = WS_MODF; LD16S(gg_, vgg, rWS, s0, 0); }
      if constexpr (n == 12) arrive(L_CNT + 8);
      if constexpr (n == 15) await8(L_CNT + 8);
      if constexpr (n >= 15 && n < 31 && (n & 1)) dma<((n - 15) / 2) * 8192, ((n - 15) / 2) * 65536>(mB3, vB3, rWO, sB3);
      if constexpr (n == 24) { const u32 vr = (vl16 >> 2) & 124, sr = WS_RFLAG + j * 128; LD4S(flr, vr, rWS, sr); }
      if constexpr (n == 30) {
        asm volatile("s_waitcnt vmcnt(3)" : "+v"(gb_), "+v"(gg_), "+v"(flr)::"memory");
        const u32 vr = (vl16 >> 2) & 124, sr = WS_RFLAG + j * 128; u32 cnt = SPIN_CAP;
        asm volatile("1:\n\tv_cmp_ne_u32 vcc, %5, %0\n\ts_cbranch_vccz 2f\n\tbuffer_load_dword %0, %2, %3, %4 offen sc1\n\ts_waitcnt vmcnt(0)\n\t"
                     "s_sub_u32 %1, %1, 1\n\ts_cbranch_scc0 1b\n\t2:" : "+v"(flr), "+s"(cnt) : "v"(vr), "s"(rWS), "s"(sr), "s"(tag) : "vcc", "scc", "memory");
      }
      if constexpr (n >= 31 && (n & 1)) load_b4(IC<(n - 31) / 2>{});
      if constexpr (n == 46) load_b4(IC<9>{});
    };
    sfor<0, 16>([&](auto KS) {
      constexpr int ks = decltype(KS)::value;
      KSTAMP(1, ks);
      if constexpr (ks == 1) read_b1(5, tz0, 0);
      if constexpr (ks == 2) read_b1(6, tz0, 24576);
      if constexpr (ks == 3) { int tz2 = tz0 + 49152; PIN(tz2); read_b1(7, tz2, 0); }
      SCHED();
      v4u nh = ah, n1 = a1, n2 = a2;
      aKh = mfma32(bw[ks * 3 + 1], ah, aKh);
      aVh = mfma32(ah, bw[ks * 3 + 2], aVh);
      if constexpr (ks < 15) nh = AFRAG(0, ks + 1);
      job(IC<3 * ks>{});
      SCHED();
      aQ = mfma32(bw[ks * 3 + 0], a1, aQ);
      aK = mfma32(bw[ks * 3 + 1], a1, aK);
      aV = mfma32(a1, bw[ks * 3 + 2], aV);
      if constexpr (ks < 15) n1 = AFRAG(16384, ks + 1);
      job(IC<3 * ks + 1>{});
      SCHED();
      bQ = mfma32(bw[ks * 3 + 0], a2, bQ);
      bK = mfma32(bw[ks * 3 + 1], a2, bK);
      bV = mfma32(a2, bw[ks * 3 + 2], bV);
      if constexpr (ks < 15) n2 = AFRAG(32768, ks + 1);
      job(IC<3 * ks + 2>{});
      ah = nh; a1 = n1; a2 = n2;
      SCHED();
    });
  }
  STAMP(9); KSTAMP(1, 16);
  {
    const u32 sOst = WS_O + ((g * 4 + 2 * blk2) * 16 + 2 * j + (hd >> 1)) * 1024;
    const v2u k1p = pk4(aK), v1p = pk4(aV);
    const v2u o0 = attend(pk4(aKh), pk4(aVh), k1p, v1p, pk4(aQ), am0, am1, ahm, g == 0 && blk2 == 0);
    const v2u o1 = attend(k1p, v1p, pk4(bK), pk4(bV), pk4(bQ), am0, am1, ahm, false);
    __builtin_amdgcn_raw_buffer_store_b64(o0, rWS, vOst, sOst, 16);
    __builtin_amdgcn_raw_buffer_store_b64(o1, rWS, vOst, sOst + 16384, 16);
  }
  if (__builtin_amdgcn_ballot_w64(fl0 != 0u)) {
    for (u32 sp = 0; sp < SPIN_CAP; ++sp) {
      asm volatile("" ::: "memory");
      gb_ = __builtin_amdgcn_raw_buffer_load_b128(rWS, vl16, WS_MODF + 12288 + w * 1024, 16);
      gg_ = __builtin_amdgcn_raw_buffer_load_b128(rWS, vgg, WS_MODF, 16);
      if (!__builtin_amdgcn_ballot_w64(((gb_.y ^ tag) | (gb_.w ^ tag) | (gg_.y ^ tag) | (gg_.w ^ tag)) != 0u)) break;
    }
    const f32x2 mB = {asf(gb_.x) + one, asf(gb_.z) + one}, mG = {asf(gg_.x), asf(gg_.z)};
    *(LAS f32x2*)(L + L_MODA + w * 512 + (vl16 >> 1)) = mB;
    *(LAS f32x2*)(L + L_GAB + (vl16 >> 1)) = mG;
  }
  STAMP(10);
  VMW(0);
  landed_b4(IC<0>{}, IC<10>{});
  BAR();
  RAISE(flag_off(0, g, 0) + j * 128);
  STAMP(11);

  v4u xv;
  { const u32 sx = g * 65536 + j * 128; LD16(xv, vRow, rX, sx, 0); }
  v4u b3[16];
#pragma unroll
  for (int s = 0; s < 4; ++s)
#pragma unroll
    for (int n = 0; n < 4; ++n) { LAS char* a = L + ((n & 1) ? (t3 ^ 32) : t3) + (s * 4096 + 256 * (n >> 1)); b3[s * 4 + n] = cat(trd(a), trd(a + 512)); }
  sfor<10, 15>(load_b4);
  const u64 care2 = 0x0101010101010101ull * (3u << (2 * kq));
  poll_flags(rWS, vfl, flag_off(0, g, 0), care2, tag);
  PIN(xv);
  v4u af[8];
  {
    const u32 so = WS_O + ((g * 4 + 2 * mh) * 16 + 4 * kq) * 1024, so1 = so + 16384;
    LD16S(af[0], vl16, rWS, so, 0); LD16S(af[1], vl16, rWS, so, 1024); LD16S(af[2], vl16, rWS, so, 2048); LD16S(af[3], vl16, rWS, so, 3072);
    LD16S(af[4], vl16, rWS, so1, 0); LD16S(af[5], vl16, rWS, so1, 1024); LD16S(af[6], vl16, rWS, so1, 2048); LD16S(af[7], vl16, rWS, so1, 3072);
  }
  STAMP(12);
  sfor<15, 18>(load_b4);
  sfor<0, 2>([&](auto M) {
    constexpr int m = decltype(M)::value;
    asm volatile("s_waitcnt vmcnt(%c4)" : "+v"(af[4 * m]), "+v"(af[4 * m + 1]), "+v"(af[4 * m + 2]), "+v"(af[4 * m + 3]) : "i"(m == 0 ? 7 : 5) : "memory");
    f32x4 acc[4] = {z4, z4, z4, z4};
    sfor<0, 4>([&](auto SC) {
      constexpr int sx = decltype(SC)::value;
#pragma unroll
      for (int n = 0; n < 4; ++n) acc[n] = mfma32(b3[sx * 4 + n], af[4 * m + sx], acc[n]);
      if constexpr (sx & 1) load_b4(IC<18 + 2 * m + (sx >> 1)>{});
      SCHED();
    });
#pragma unroll
    for (int n = 0; n < 4; ++n) *(LAS f32x4*)(L + ((n & 1) ? (pw ^ 64) : pw) + (m * 4096 + (n >> 1) * 128)) = acc[n];
  });
  BAR();
  STAMP(13);
  {
    f32x4 y0 = *(LAS f32x4*)(L + pr), y1 = *(LAS f32x4*)(L + (pr ^ 16));
#pragma unroll
    for (int q = 1; q < 4; ++q) { y0 += *(LAS f32x4*)(L + pr + q * 16384); y1 += *(LAS f32x4*)(L + (pr ^ 16) + q * 16384); }
    const int ga = L_GAB + ((vl16 & 112) << 1);
    const f32x4 g0 = *(LAS f32x4*)(L + ga), g1 = *(LAS f32x4*)(L + ga + 16);
    const f32x4 x0 = f32x4{blo(xv.x), bhi(xv.x), blo(xv.y), bhi(xv.y)} + g0 * y0, x1 = f32x4{blo(xv.z), bhi(xv.z), blo(xv.w), bhi(xv.w)} + g1 * y1;
    { const v4u o = cat(pk4(x0), pk4(x1)); const u32 so = WS_X1 + g * 65536 + j * 128;
      asm volatile("buffer_store_dwordx4 %0, %1, %2, %3 offen sc1\n\ts_nop 1" ::"v"(o), "v"(vRow), "s"(rWS), "s"(so) : "memory"); }
    const f32x4 sv = x0 + x1;
    float mean = (sv.x + sv.y) + (sv.z + sv.w);
    mean += dppf<0xB1>(mean); mean += dppf<0x4E>(mean); mean += dppf<0x141>(mean);
    mean *= 1.f / 64.f;
    const f32x4 d0 = x0 - mean, d1 = x1 - mean, dd = d0 * d0 + d1 * d1;
    float m2 = (dd.x + dd.y) + (dd.z + dd.w);
    m2 += dppf<0xB1>(m2); m2 += dppf<0x4E>(m2); m2 += dppf<0x141>(m2);
    v2u st; st.x = asu(mean); st.y = asu(m2);
    __builtin_amdgcn_raw_buffer_store_b64(st, rWS, (vRow >> 10) << 6, WS_STAT + g * 4096 + j * 8, 16);
  }
  STAMP(14);
  VMW(0);
  landed_b4(IC<10>{}, IC<22>{});
  BAR();
  RAISE(flag_off(1, g, 0) + j * 128);
  sfor<22, 27>(load_b4);
  STAMP(15);

  v4u x1v[4]; v2u stv;
  auto ln2_load = [&](int h) {
    const u32 so = WS_X1 + g * 65536 + (32 * (w >> 2) + 16 * h + 4 * (w & 3)) * 1024;
    LD16S(x1v[0], vl16, rWS, so, 0); LD16S(x1v[1], vl16, rWS, so, 1024); LD16S(x1v[2], vl16, rWS, so, 2048); LD16S(x1v[3], vl16, rWS, so, 3072);
    const u32 vs = (vl16 >> 1) & 255, ss = WS_STAT + g * 4096 + (32 * (w >> 2) + 16 * h + 4 * (w & 3)) * 64;
    LD8S(stv, vs, rWS, ss, 0);
  };
  float l2r, l2n;
  auto ln2_stats = [&]() {
    const float mj = asf(stv.x);
    float mean = mj;
    mean += dppf<0xB1>(mean); mean += dppf<0x4E>(mean); mean += dppf<0x141>(mean);
    mean *= 0.125f;
    const float dm = mj - mean;
    float m2 = asf(stv.y) + 64.f * dm * dm;
    m2 += dppf<0xB1>(m2); m2 += dppf<0x4E>(m2); m2 += dppf<0x141>(m2);
    l2r = __builtin_amdgcn_rsqf(m2 * (1.f / 512.f) + 1e-5f);
    l2n = -mean * l2r;
  };
  u32 vH;
  u32 hmask[3];
#pragma unroll
  for (int ct = 0; ct < 3; ++ct)
    asm("s_cmp_lt_u32 %1, %2\n\ts_cselect_b32 %0, -1, 0" : "=s"(hmask[ct]) : "s"((u32)(ch0 + ct)), "s"((u32)cend) : "scc");
  auto hh_store = [&](v2u o, int ct, int h) {
    const u32 so = WS_HH + ((g * 4 + h) * 172 + ch0 + ct) * 256;
    asm volatile("s_mov_b32 exec_lo, %4\n\ts_mov_b32 exec_hi, %4\n\tbuffer_store_dwordx2 %0, %1, %2, %3 offen sc1\n\ts_mov_b64 exec, -1"
                 ::"v"(o), "v"(vH), "s"(rWS), "s"(so), "s"(hmask[ct]) : "memory");
  };
  auto swiglu_tile = [&](f32x4 (&acc)[3][2], int ct, int h) {
    f32x4 G, U, R;
#pragma unroll
    for (int e = 0; e < 4; ++e) {
      auto r = __builtin_amdgcn_permlane32_swap(asu(acc[ct][0][e]), asu(acc[ct][1][e]), false, false);
      G[e] = asf(r[0]); U[e] = asf(r[1]);
    }
    const f32x4 T = G * -1.4426950408889634f;
#pragma unroll
    for (int e = 0; e < 4; ++e) R[e] = __builtin_amdgcn_rcpf(1.f + __builtin_amdgcn_exp2f(T[e]));
    hh_store(pk4((G * U) * R), ct, h);
  };
#pragma unroll
  for (int i = 0; i < 4; ++i) { afo[i] -= blk2 * 32768; PIN(afo[i]); }
  int vm2 = L_MODA + 2 * vl16; PIN(vm2);
  int h2w = w * 4096 + (((vl16 - 128 * rotp) & 1023) ^ ((4 * w & 15) << 4)); PIN(h2w);
  poll_flags(rWS, vfl, flag_off(1, g, 0), ~0ull, tag);
  landed_b4(IC<22>{}, IC<27>{});
  ln2_load(0);
  sfor<27, 31>(load_b4);
  {
    const f32x4 s0 = *(LAS f32x4*)(L + vm2), s1 = *(LAS f32x4*)(L + vm2 + 16), a0 = *(LAS f32x4*)(L + vm2 + 2048), a1 = *(LAS f32x4*)(L + vm2 + 2064);
    asm volatile("s_waitcnt vmcnt(%c5)" : "+v"(x1v[0]), "+v"(x1v[1]), "+v"(x1v[2]), "+v"(x1v[3]), "+v"(stv) : "i"(4) : "memory");
    ln2_stats();
#pragma unroll
    for (int i = 0; i < 4; ++i) {
      const float rs = rdl(l2r, 8 * i), nn = rdl(l2n, 8 * i);
      v4u o;
      o.x = pk2((unp(x1v[i].x) * rs + nn) * a0.xy + s0.xy); o.y = pk2((unp(x1v[i].y) * rs + nn) * a0.zw + s0.zw);
      o.z = pk2((unp(x1v[i].z) * rs + nn) * a1.xy + s1.xy); o.w = pk2((unp(x1v[i].w) * rs + nn) * a1.zw + s1.zw);
      *(LAS v4u*)(L + (h2w ^ (i << 4)) + i * 1024) = o;
    }
  }
  ln2_load(1);
  BAR();
  STAMP(16);
  f32x4 accA[3][2] = {{z4, z4}, {z4, z4}, {z4, z4}};
  {
    f32x4 cs, ca; float rsv[4], nnv, t[4]; v2u ov;
    auto ln_op = [&](auto NC) {
      constexpr int n = decltype(NC)::value, u = n / 16, k = n % 16, hf = u & 1, i = u >> 1;
      if constexpr (u < 8) {
        if constexpr (k == 0) {
          cs = *(LAS f32x4*)(L + vm2 + hf * 16); ca = *(LAS f32x4*)(L + vm2 + 2048 + hf * 16);
          if constexpr (hf == 0) nnv = q_mov(rdl(l2n, 8 * i));
        }
        else if constexpr (k == 1) t[0] = q_shl(x1v[i][2 * hf]);
        else if constexpr (k == 2) t[1] = q_hi(x1v[i][2 * hf]);
        else if constexpr (k == 3) t[2] = q_shl(x1v[i][2 * hf + 1]);
        else if constexpr (k == 4) t[3] = q_hi(x1v[i][2 * hf + 1]);
        else if constexpr (k < 9) t[k - 5] = q_fmas(t[k - 5], rsv[i], nnv);
        else if constexpr (k < 13) t[k - 9] = q_fma(t[k - 9], ca[k - 9], cs[k - 9]);
        else if constexpr (k == 13) ov.x = q_cvt(t[0], t[1]);
        else if constexpr (k == 14) ov.y = q_cvt(t[2], t[3]);
        else {
          *(LAS v2u*)(L + (h2w ^ (i << 4)) + (32768 + i * 1024 + hf * 8)) = ov;
        }
      }
    };
    v4u a0 = AFRAG(0, 0), a1 = AFRAG(16384, 0);
    sfor<0, 16>([&](auto KS) {
      constexpr int ks = decltype(KS)::value;
      KSTAMP(3, ks);
      v4u n0 = a0, n1 = a1;
      if constexpr (ks < 15) { n0 = AFRAG(0, ks + 1); n1 = AFRAG(16384, ks + 1); }
      if constexpr (ks == 3) {
        asm volatile("s_waitcnt vmcnt(5)" : "+v"(x1v[0]), "+v"(x1v[1]), "+v"(x1v[2]), "+v"(x1v[3]), "+v"(stv)::"memory");
        landed_b4(IC<27>{}, IC<31>{});
      }
      if constexpr (ks == 10) { VMW(6); landed_b4(IC<31>{}, IC<40>{}); }
      if constexpr (ks == 13) { VMW(2); landed_b4(IC<40>{}, IC<46>{}); }
      if constexpr (ks == 15) { VMW(0); landed_b4(IC<46>{}, IC<48>{}); }
      SCHED();
      if constexpr (ks == 3) { ln2_stats(); PIN(l2r); PIN(l2n);
#pragma unroll
        for (int i = 0; i < 4; ++i) rsv[i] = rdl(l2r, 8 * i); }
      sfor<0, 6>([&](auto MC) {
        constexpr int m = decltype(MC)::value, ct = m >> 1;
        if constexpr (m & 1) accA[ct][1] = mfma32(b4[ks * 3 + ct], a1, accA[ct][1]); else accA[ct][0] = mfma32(b4[ks * 3 + ct], a0, accA[ct][0]);
        if constexpr ((ks * 6 + m) % 4 == 0 && 31 + (ks * 6 + m) / 4 < 48) load_b4(IC<31 + (ks * 6 + m) / 4>{});
        if constexpr (ks >= 4) {
          constexpr int o = (ks - 4) * 15 + (m >> 1) * 5 + (m & 1) * 2;
          ln_op(IC<o>{}); ln_op(IC<o + 1>{});
          if constexpr (m & 1) ln_op(IC<o + 2>{});
          SCHED();
        }
      });
      a0 = n0; a1 = n1;
      SCHED();
    });
  }
  vH = (vl16 & 255) + ((vl16 >> 5) & 8) + (vl16 >> 9) * (2 * 172 * 256); PIN(vH);
  BAR();
  STAMP(17); KSTAMP(3, 16);
  f32x4 accB[3][2] = {{z4, z4}, {z4, z4}, {z4, z4}};
  u32 vRow5; int pr5, pw5, gb5;
  const int kb5 = 5 * w + min(w, 3);
  u64 care5 = 0x0101010101010101ull * ((0x80c06030180c0603ull >> (8 * w)) & 255u);
  asm volatile("" : "+s"(care5));
  v4u b5[24], hA[12], hB[12]; v2u xqA, xqB;
  const u32 sB5 = WS_B5T + (j * 43 + kb5) * 4096, d5 = (w + 5) >> 3;
  auto load_b5 = [&](auto NC) {
    constexpr int n = decltype(NC)::value, sx = n / 4, t = n % 4;
    const u32 so = sB5 + (sx == 5 ? (5 - d5) * 4096 : sx * 4096);
    ld16s<t * 1024>(b5[n], vl16, rWS, so);
  };
  auto load_h = [&](auto NC, v4u (&hf)[12], int h) {
    constexpr int n = decltype(NC)::value, sx = n / 2, m = n % 2;
    const u32 so = WS_HH + ((g * 4 + 2 * m + h) * 172 + 4 * kb5) * 256 + (sx == 5 ? (5 - d5) * 1024 : sx * 1024);
    ld16s<0>(hf[n], vl16, rWS, so);
  };
  auto poll5 = [&](int h) {
    const u32 fo = flag_off(2, g, h), vf = vfl; u32 t, cnt = SPIN_CAP;
    asm volatile("1:\n\tbuffer_load_dword %0, %2, %3, %4 offen sc1\n\ts_waitcnt vmcnt(0)\n\tv_cmp_ne_u32 vcc, %5, %0\n\ts_and_b64 vcc, vcc, %6\n\ts_cbranch_scc0 2f\n\t"
                 "s_sub_u32 %1, %1, 1\n\ts_cbranch_scc0 1b\n\t2:" : "=&v"(t), "+s"(cnt) : "v"(vf), "s"(rWS), "s"(fo), "s"(tag), "s"(care5) : "vcc", "scc", "memory");
  };
  {
    float G[4], U[4], T[4]; v2u ov;
    auto sw_op = [&](auto NC) {
      constexpr int n = decltype(NC)::value, ct = n / 22, k = n % 22;
      if constexpr (ct < 3) {
        if constexpr (k < 4) { auto r = __builtin_amdgcn_permlane32_swap(asu(accA[ct][0][k]), asu(accA[ct][1][k]), false, false); G[k] = asf(r[0]); U[k] = asf(r[1]); PIN(G[k]); PIN(U[k]); }
        else if constexpr (k < 6) { T[2 * (k - 4)] = q_mulk(G[2 * (k - 4)]); T[2 * (k - 4) + 1] = q_mulk(G[2 * (k - 4) + 1]); }
        else if constexpr (k < 10) T[k - 6] = q_exp(T[k - 6]);
        else if constexpr (k < 12) { T[2 * (k - 10)] = q_add1(T[2 * (k - 10)]); T[2 * (k - 10) + 1] = q_add1(T[2 * (k - 10) + 1]); }
        else if constexpr (k < 16) T[k - 12] = q_rcp(T[k - 12]);
        else if constexpr (k < 18) { G[2 * (k - 16)] = q_mul(G[2 * (k - 16)], U[2 * (k - 16)]); G[2 * (k - 16) + 1] = q_mul(G[2 * (k - 16) + 1], U[2 * (k - 16) + 1]); }
        else if constexpr (k < 20) { G[2 * (k - 18)] = q_mul(G[2 * (k - 18)], T[2 * (k - 18)]); G[2 * (k - 18) + 1] = q_mul(G[2 * (k - 18) + 1], T[2 * (k - 18) + 1]); }
        else if constexpr (k == 20) { ov.x = q_cvt(G[0], G[1]); ov.y = q_cvt(G[2], G[3]); }
        else hh_store(ov, ct, 0);
      }
    };
    v4u a0 = AFRAG(32768, 0), a1 = AFRAG(49152, 0);
    int l5, c5, g5;
    sfor<0, 16>([&](auto KS) {
      constexpr int ks = decltype(KS)::value;
      KSTAMP(4, ks);
      v4u n0 = a0, n1 = a1;
      if constexpr (ks > 0 && ks < 15) { n0 = AFRAG(32768, ks + 1); n1 = AFRAG(49152, ks + 1); }
      if constexpr (ks == 8) { VMW(0); SIGNAL(1, flag_off(2, g, 0) + j * 128); }
      if constexpr (ks == 12) poll5(0);
      SCHED();
      sfor<0, 6>([&](auto MC) {
        constexpr int m = decltype(MC)::value, ct = m >> 1, n = ks * 6 + m;
        if constexpr (m & 1) accB[ct][1] = mfma32(b4[ks * 3 + ct], a1, accB[ct][1]); else accB[ct][0] = mfma32(b4[ks * 3 + ct], a0, accB[ct][0]);
        if constexpr (n == 3) n0 = AFRAG(32768, 1);
        if constexpr (n == 5) n1 = AFRAG(49152, 1);
        if constexpr (n < 33) { sw_op(IC<2 * n>{}); sw_op(IC<2 * n + 1>{}); }
        if constexpr (n >= 12 && n < 40 && n % 4 == 0) load_b5(IC<(n - 12) / 4>{});
        if constexpr (n >= 48 && n < 63 && n % 3 == 0) load_b5(IC<7 + (n - 48) / 3>{});
        if constexpr (n >= 72 && n % 2 == 0) load_h(IC<(n - 72) / 2>{}, hA, 0);
        if constexpr (n == 38) { l5 = opq(vl16) >> 4; c5 = l5 & 15; g5 = l5 >> 4; PIN(c5); PIN(g5); }
        if constexpr (n == 40) { vRow5 = (32 * (w >> 2) + 4 * (w & 3) + g5) * 1024 + c5 * 8; PIN(vRow5); }
        if constexpr (n == 42) { pr5 = (4 * w + g5) * 256 + ((c5 ^ ((4 * w + g5) & 7)) << 4); PIN(pr5); }
        if constexpr (n == 44) { pw5 = w * 8192 + c5 * 256 + ((c5 & 4) << 4); PIN(pw5); }
        if constexpr (n == 46) { pw5 += (g5 ^ (c5 & 3)) << 4; PIN(pw5); gb5 = L_GAB + 256 + c5 * 16; PIN(gb5); }
        SCHED();
      });
      a0 = n0; a1 = n1;
      SCHED();
    });
  }
  STAMP(18); KSTAMP(4, 16);
  PIN(accB[0][0]); PIN(accB[0][1]); PIN(accB[1][0]); PIN(accB[1][1]); PIN(accB[2][0]); PIN(accB[2][1]);
  swiglu_tile(accB, 0, 1);
  swiglu_tile(accB, 1, 1);
  swiglu_tile(accB, 2, 1);
  VMW(0);
  BAR();
  RAISE(flag_off(2, g, 1) + j * 128);
  STAMP(19);
#pragma unroll
  for (int i = 0; i < 12; ++i) PIN(hA[i]);
#pragma unroll
  for (int i = 0; i < 12; ++i) PIN(b5[i]);
  sfor<12, 18>(load_b5);
  auto p5_half = [&](auto HC, v4u (&hf)[12]) {
    constexpr int h = decltype(HC)::value;
    f32x4 acc[2][4] = {{z4, z4, z4, z4}, {z4, z4, z4, z4}};
    sfor<0, 6>([&](auto SC) {
      constexpr int sx = decltype(SC)::value;
      if constexpr (h == 0 && sx == 1) sfor<18, 21>(load_b5);
      if constexpr (h == 0 && sx == 2) { sfor<21, 24>(load_b5);
        const u32 sq = WS_X1 + g * 65536 + j * 128, sq1 = sq + 16384, vr5 = vRow5; const rsrc_t rr = rWS; LD8S(xqA, vr5, rr, sq, 0); LD8S(xqB, vr5, rr, sq1, 0); }
      if constexpr (h == 0 && sx == 3) {
        VMW(8);
#pragma unroll
        for (int i = 12; i < 18; ++i) PIN(b5[i]);
      }
      if constexpr (h == 0 && sx == 4) {
        poll5(1);
#pragma unroll
        for (int i = 18; i < 24; ++i) PIN(b5[i]);
        PIN(xqA); PIN(xqB);
      }
      if constexpr (h == 1 && sx == 0) asm volatile("s_waitcnt vmcnt(9)" : "+v"(hf[0]), "+v"(hf[1]), "+v"(hf[2]), "+v"(hf[3])::"memory");
      if constexpr (h == 1 && sx == 2) asm volatile("s_waitcnt vmcnt(5)" : "+v"(hf[4]), "+v"(hf[5]), "+v"(hf[6]), "+v"(hf[7])::"memory");
      if constexpr (h == 1 && sx == 4) asm volatile("s_waitcnt vmcnt(1)" : "+v"(hf[8]), "+v"(hf[9]), "+v"(hf[10]), "+v"(hf[11])::"memory");
      if constexpr (sx == 5) { if (w >= 3) { hf[10] = v4u{0u, 0u, 0u, 0u}; hf[11] = v4u{0u, 0u, 0u, 0u}; } }
#pragma unroll
      for (int m = 0; m < 2; ++m) {
#pragma unroll
        for (int n = 0; n < 4; ++n) acc[m][n] = mfma32(b5[sx * 4 + n], hf[2 * sx + m], acc[m][n]);
        if constexpr (h == 0 && sx >= 4) { if (m == 0) { load_h(IC<4 * (sx - 4)>{}, hB, 1); load_h(IC<4 * (sx - 4) + 1>{}, hB, 1); } else { load_h(IC<4 * (sx - 4) + 2>{}, hB, 1); load_h(IC<4 * (sx - 4) + 3>{}, hB, 1); } }
        SCHED();
      }
    });
    const int pwh = h ? opq(pw5 + 65536) : pw5;
#pragma unroll
    for (int m = 0; m < 2; ++m)
#pragma unroll
      for (int n = 0; n < 4; ++n) *(LAS f32x4*)(L + ((n & 1) ? (pwh ^ 64) : pwh) + (m * 4096 + (n >> 1) * 128)) = acc[m][n];
    if constexpr (h == 0) sfor<8, 12>([&](auto I) { load_h(I, hB, 1); });
    BAR();
    const int prh = h ? opq(pr5 + 65536) : pr5;
    f32x4 y = *(LAS f32x4*)(L + prh);
#pragma unroll
    for (int q = 1; q < 8; ++q) y += *(LAS f32x4*)(L + prh + q * 8192);
    const f32x4 gbv = *(LAS f32x4*)(L + gb5);
    const v2u xx = h ? xqB : xqA;
    const f32x4 xb = {blo(xx.x), bhi(xx.x), blo(xx.y), bhi(xx.y)};
    __builtin_amdgcn_raw_buffer_store_b64(pk4(xb + gbv * y), rOUT, vRow5, g * 65536 + h * 16384 + j * 128, 0);
  };
  STAMP(20);
  p5_half(IC<0>{}, hA);
  STAMP(21);
  p5_half(IC<1>{}, hB);
  STAMP(22);
  STAMP_DUMP();
  if (b == 0 && w == 0) {
    for (u32 sp = 0; sp < SPIN_CAP; ++sp) {
      asm volatile("" ::: "memory");
      const v4u v = __builtin_amdgcn_raw_buffer_load_b128(rWS, vl16, WS_ALIVE, 16);
      if (__all(v.x == tag && v.y == tag && v.z == tag && v.w == tag)) break;
    }
    __builtin_amdgcn_raw_buffer_store_b32(tag, rWS, 0, WS_EPOCH, 16);
  }
}
}

extern "C" void kernel_launch(void* const* d_in, const int* in_sizes, int n_in, void* d_out, int out_size, void* d_ws, size_t ws_size,
                              hipStream_t stream) {
  Params p;
  p.x = (const u16*)d_in[0]; p.wqkv = (const u16*)d_in[1]; p.wo = (const u16*)d_in[2];
  p.w1 = (const u16*)d_in[4]; p.w3 = (const u16*)d_in[5]; p.w2 = (const u16*)d_in[6];
  p.c = (const u16*)d_in[7]; p.wmod = (const u16*)d_in[8];
  p.out = (u16*)d_out; p.ws = (char*)d_ws;
  (void)hipFuncSetAttribute((const void*)fused_block, hipFuncAttributeMaxDynamicSharedMemorySize, L_TOTAL);
  fused_block<<<256, 512, L_TOTAL, stream>>>(p);
}
```

```cpp
#include <hip/hip_runtime.h>
#include <stdint.h>

#define KDBG 0
#define NODMA 0
#define CODEWARM 1
#define L2WARM 1
#define TLBWARM 0
#define XMAP 2

#define DI __device__ __forceinline__
#define GAS __attribute__((address_space(1)))
#define LAS __attribute__((address_space(3)))
typedef unsigned u32;
typedef unsigned short u16;
typedef unsigned long long u64;
typedef u32 v4u __attribute__((ext_vector_type(4)));
typedef u32 v2u __attribute__((ext_vector_type(2)));
typedef float f32x4 __attribute__((ext_vector_type(4)));
typedef float f32x2 __attribute__((ext_vector_type(2)));
typedef __bf16 bf16x8 __attribute__((ext_vector_type(8)));
typedef __bf16 bf16x2 __attribute__((ext_vector_type(2)));
typedef short s16x4 __attribute__((ext_vector_type(4)));
typedef __amdgpu_buffer_rsrc_t rsrc_t;
typedef GAS u32 gu32;
typedef GAS u64 gu64;
#define RLX_AGENT __ATOMIC_RELAXED, __HIP_MEMORY_SCOPE_AGENT

namespace {
constexpr int S_ = 2048, D_ = 512, F_ = 1376;
constexpr u32 WS_EPOCH = 0;
constexpr u32 WS_ALIVE = 4096;
constexpr u32 WS_FLAG = 32768;
constexpr u32 WS_STAT = 262144;
constexpr u32 WS_MODG = 524288;
constexpr u32 WS_MODF = 655360;
constexpr u32 WS_O = 1u << 20;
constexpr u32 WS_X1 = 3u << 20;
constexpr u32 WS_HH = 5u << 20;
constexpr u32 WS_RFLAG = 8192;
constexpr u32 WS_B4T = 11u << 20;
constexpr u32 WS_B5T = 14u << 20;
constexpr u32 WS_DBG = 16u << 20;
constexpr u32 WS_BYTES = 32u << 20;
constexpr int L_XY = 0;
constexpr int L_Z = 81920;
constexpr int L_MISC = 158720;
constexpr int L_TOTAL = 163840;
constexpr int L_RED = 73728;
constexpr int L_MODA = L_MISC;
constexpr int L_CNT = L_MISC + 4096;
constexpr int L_GAB = L_MISC + 4096 + 64;
constexpr int L_JUNK = L_MISC + 4096 + 64 + 512;
constexpr int L_STAMP = 156672;
constexpr u32 SPIN_CAP = 1u << 18;
constexpr u32 CODE_BYTES = 26112;

DI f32x4 mfma32(v4u a, v4u b, f32x4 c) {
  return __builtin_amdgcn_mfma_f32_16x16x32_bf16(__builtin_bit_cast(bf16x8, a), __builtin_bit_cast(bf16x8, b), c, 0, 0, 0);
}
DI f32x4 mfma16(v2u a, v2u b, f32x4 c) {
  return __builtin_amdgcn_mfma_f32_16x16x16bf16_1k(__builtin_bit_cast(s16x4, a), __builtin_bit_cast(s16x4, b), c, 0, 0, 0);
}
DI u32 pk(float lo, float hi) { f32x2 v = {lo, hi}; return __builtin_bit_cast(u32, __builtin_convertvector(v, bf16x2)); }
DI u32 pk2(f32x2 v) { return __builtin_bit_cast(u32, __builtin_convertvector(v, bf16x2)); }
DI v2u pk4(f32x4 v) { v2u r; r.x = pk(v.x, v.y); r.y = pk(v.z, v.w); return r; }
DI float asf(u32 u) { return __builtin_bit_cast(float, u); }
DI u32 asu(float f) { return __builtin_bit_cast(u32, f); }
DI float blo(u32 u) { return asf(u << 16); }
DI float bhi(u32 u) { return asf(u & 0xffff0000u); }
DI f32x2 unp(u32 u) { f32x2 r = {blo(u), bhi(u)}; return r; }
DI float dot2(u32 a, u32 b, float c) { return __builtin_amdgcn_fdot2_f32_bf16(__builtin_bit_cast(bf16x2, a), __builtin_bit_cast(bf16x2, b), c, false); }
DI v2u trd(LAS char* p) { return __builtin_bit_cast(v2u, __builtin_amdgcn_ds_read_tr16_b64_v4i16((LAS s16x4*)p)); }
DI v4u cat(v2u a, v2u b) { v4u r; r.x = a.x; r.y = a.y; r.z = b.x; r.w = b.y; return r; }
template <int MI, int SI> DI void dma(u32 mb, u32 voff, rsrc_t srd, u32 sb) {
  u32 t;
  asm volatile("s_add_u32 m0, %1, %c2\n\ts_add_u32 %0, %3, %c4\n\tbuffer_load_dwordx4 %5, %6, %0 offen lds"
               : "=&s"(t) : "s"(mb), "i"(MI), "s"(sb), "i"(SI), "v"(voff), "s"(srd) : "scc");
}
DI void touch(u32 voff, rsrc_t srd, u32 sb) {
  asm volatile("s_mov_b32 m0, %c0\n\ts_nop 0\n\tbuffer_load_dword %1, %2, %3 offen sc1 lds" ::"i"(L_JUNK), "v"(voff), "s"(srd), "s"(sb));
}
#define BAR() asm volatile("s_waitcnt lgkmcnt(0)\n\ts_barrier" ::: "memory")
#define VMW(n) asm volatile("s_waitcnt vmcnt(" #n ")" ::: "memory")
#define LGW() asm volatile("s_waitcnt lgkmcnt(0)" ::: "memory")
#define SCHED() __builtin_amdgcn_sched_barrier(0)
#define LD16(dst, voff, srd, soff, IMM) asm volatile("buffer_load_dwordx4 %0, %1, %2, %3 offen offset:" #IMM : "=v"(dst) : "v"(voff), "s"(srd), "s"(soff))
#define LD16S(dst, voff, srd, soff, IMM) asm volatile("buffer_load_dwordx4 %0, %1, %2, %3 offen offset:" #IMM " sc1" : "=v"(dst) : "v"(voff), "s"(srd), "s"(soff))
#define LD8(dst, voff, srd, soff, IMM) asm volatile("buffer_load_dwordx2 %0, %1, %2, %3 offen offset:" #IMM : "=v"(dst) : "v"(voff), "s"(srd), "s"(soff))
#define LD8S(dst, voff, srd, soff, IMM) asm volatile("buffer_load_dwordx2 %0, %1, %2, %3 offen offset:" #IMM " sc1" : "=v"(dst) : "v"(voff), "s"(srd), "s"(soff))
#define LD4(dst, voff, srd, soff) asm volatile("buffer_load_dword %0, %1, %2, %3 offen" : "=v"(dst) : "v"(voff), "s"(srd), "s"(soff))
#define LD4S(dst, voff, srd, soff) asm volatile("buffer_load_dword %0, %1, %2, %3 offen sc1" : "=v"(dst) : "v"(voff), "s"(srd), "s"(soff))

template <int IMM> DI void ld16s(v4u& r, u32 v, rsrc_t rs, u32 so) { asm volatile("buffer_load_dwordx4 %0, %1, %2, %3 offen offset:%c4 sc1" : "=v"(r) : "v"(v), "s"(rs), "s"(so), "i"(IMM)); }
template <int CTRL> DI float dppf(float v) { return asf(__builtin_amdgcn_update_dpp(0, asu(v), CTRL, 0xf, 0xf, true)); }
DI float row16_sum(float v) { v += dppf<0x128>(v); v += dppf<0x124>(v); v += dppf<0x122>(v); v += dppf<0x121>(v); return v; }
DI float sw32_add(float a, float b) { auto r = __builtin_amdgcn_permlane32_swap(asu(a), asu(b), false, false); return asf(r[0]) + asf(r[1]); }
DI float sw16_add(float a, float b) { auto r = __builtin_amdgcn_permlane16_swap(asu(a), asu(b), false, false); return asf(r[0]) + asf(r[1]); }
DI float x16_max(float v) { auto r = __builtin_amdgcn_permlane16_swap(asu(v), asu(v), false, false); return fmaxf(asf(r[0]), asf(r[1])); }
DI float x32_max(float v) { auto r = __builtin_amdgcn_permlane32_swap(asu(v), asu(v), false, false); return fmaxf(asf(r[0]), asf(r[1])); }
DI int fresh_lane() { int l; asm volatile("v_mbcnt_lo_u32_b32 %0, -1, 0\n\tv_mbcnt_hi_u32_b32 %0, -1, %0" : "=v"(l)); return l; }
DI float rdl(float v, int l) { return asf(__builtin_amdgcn_readlane(asu(v), l)); }

struct Params {
  const u16 *x, *wqkv, *wo, *w1, *w3, *w2, *c, *wmod;
  u16* out;
  char* ws;
};

#define LOOPSEL 0
#define STAMP(i) do {} while (0)
#define KSTAMP(lp, i) do {} while (0)
#define STAMP_DUMP() do {} while (0)

DI u32 flag_off(int phase, int g, int h) { return WS_FLAG + ((phase * 32 + g) * 2 + h) * 1024; }
DI void poll_flags(rsrc_t rWS, u32 vfl, u32 foff, u64 care, u32 tag) {
  for (u32 sp = 0; sp < SPIN_CAP; ++sp) {
    asm volatile("" ::: "memory");
    const u32 v = __builtin_amdgcn_raw_buffer_load_b32(rWS, vfl, foff, 16);
    if (!(__builtin_amdgcn_ballot_w64(v != tag) & care)) break;
  }
  VMW(0);
}
DI void raise_flag(rsrc_t rWS, u32 foff, u32 vtag, u32 vzero, u32 m1) {
  asm volatile("s_mov_b32 exec_hi, 0\n\ts_mov_b32 exec_lo, %4\n\tbuffer_store_dword %0, %1, %2, %3 offen sc1\n\ts_mov_b64 exec, -1" ::"v"(vtag), "v"(vzero), "s"(rWS), "s"(foff), "s"(m1) : "memory");
}
DI void signal_flag(rsrc_t rWS, u32 cnt_addr, u32 foff, u32 tag) {
  u32 t, a = cnt_addr, one = 1u, vt = tag, vz = 0u;
  asm volatile("s_mov_b64 exec, 1\n\tds_add_rtn_u32 %0, %1, %2\n\ts_waitcnt lgkmcnt(0)\n\tv_cmp_eq_u32 vcc, 7, %0\n\ts_and_b64 exec, exec, vcc\n\t"
               "buffer_store_dword %3, %4, %5, %6 offen sc1\n\ts_mov_b64 exec, -1"
               : "=&v"(t) : "v"(a), "v"(one), "v"(vt), "v"(vz), "s"(rWS), "s"(foff) : "vcc", "scc", "memory");
}
DI void arrive(u32 cnt_addr) {
  u32 a = cnt_addr, one = 1u;
  asm volatile("s_waitcnt lgkmcnt(0)\n\ts_mov_b64 exec, 1\n\tds_add_u32 %0, %1\n\ts_mov_b64 exec, -1" ::"v"(a), "v"(one) : "memory");
}
DI void await8(u32 cnt_addr) {
  u32 a = cnt_addr, t, sv;
  asm volatile("1:\n\tds_read_b32 %0, %2\n\ts_waitcnt lgkmcnt(0)\n\tv_readfirstlane_b32 %1, %0\n\ts_nop 0\n\ts_cmp_lt_u32 %1, 8\n\ts_cbranch_scc1 1b"
               : "=&v"(t), "=&s"(sv) : "v"(a) : "scc", "memory");
}
#define PIN(x) asm volatile("" : "+v"(x))
DI float qp(float r) { PIN(r); return r; }
DI float q_shl(u32 a) { return qp(blo(a)); }
DI float q_hi(u32 a) { return qp(bhi(a)); }
DI float q_fma(float a, float b, float c) { return qp(__builtin_fmaf(a, b, c)); }
DI float q_fmas(float a, float b, float c) { return qp(__builtin_fmaf(a, b, c)); }
DI float q_mul(float a, float b) { return qp(a * b); }
DI float q_mulk(float a) { return qp(a * -1.4426950408889634f); }
DI float q_add1(float a) { return qp(a + 1.f); }
DI float q_exp(float a) { return qp(__builtin_amdgcn_exp2f(a)); }
DI float q_rcp(float a) { return qp(__builtin_amdgcn_rcpf(a)); }
DI u32 q_cvt(float a, float b) { u32 r = pk(a, b); PIN(r); return r; }
DI float q_mov(float a) { return qp(a); }
DI u32 opq(u32 v) { asm volatile("" : "+v"(v)); return v; }

DI float sel(u64 m, float a, float b) { return __builtin_amdgcn_inverse_ballot_w64(m) ? a : b; }
DI v2u attend(v2u kp, v2u vp, v2u kc, v2u vc, v2u q, u64 m0, u64 m1, u32 hm, bool noprev) {
  const f32x4 z = {0.f, 0.f, 0.f, 0.f};
  const f32x4 sp = mfma16(kp, q, z);
  const f32x4 sc = mfma16(kc, q, z);
  const float K2 = 0.25f * 1.4426950408889634f;
  const u64 par = 0xaaaaaaaaaaaaaaaaull;
  float p0 = sel(par, sp[1], sp[0]), p1 = sel(par, sp[3], sp[2]);
  if (noprev) { p0 = -INFINITY; p1 = -INFINITY; }
  const float t0 = sel(m0, sel(par, sc[1], sc[0]), p0), t1 = sel(m1, sel(par, sc[3], sc[2]), p1);
  const float m = x32_max(x16_max(fmaxf(t0, t1)));
  const float mk = -m * K2;
  const float e0 = __builtin_amdgcn_exp2f(__builtin_fmaf(t0, K2, mk)), e1 = __builtin_amdgcn_exp2f(__builtin_fmaf(t1, K2, mk));
  const u32 w0 = pk(e0, e0) & hm, w1 = pk(e1, e1) & hm;
  v4u pf;
  pf.x = __builtin_amdgcn_inverse_ballot_w64(m0) ? 0u : w0; pf.y = __builtin_amdgcn_inverse_ballot_w64(m1) ? 0u : w1;
  pf.z = __builtin_amdgcn_inverse_ballot_w64(m0) ? w0 : 0u; pf.w = __builtin_amdgcn_inverse_ballot_w64(m1) ? w1 : 0u;
  const f32x4 o = mfma32(cat(vp, vc), pf, z);
  float sum = e0 + e1;
  sum = sw16_add(sum, sum); sum = sw32_add(sum, sum);
  return pk4(o * __builtin_amdgcn_rcpf(sum));
}

template <int N> using IC = std::integral_constant<int, N>;
template <int I, int N, class F> DI void sfor(F&& f) { if constexpr (I < N) { f(IC<I>{}); sfor<I + 1, N>(f); } }

__global__ void __launch_bounds__(512, 2) fused_block(Params p) {
  extern __shared__ __attribute__((aligned(16))) char smem[];
  LAS char* L = (LAS char*)smem;
  const int tid = threadIdx.x, lane = tid & 63, w = __builtin_amdgcn_readfirstlane(tid >> 6);
  const int c16 = lane & 15, g4 = lane >> 4;
  const int b = blockIdx.x;
  int g, j;
  if (XMAP == 0) { g = (b & 7) * 4 + ((b >> 3) & 3); j = b >> 5; }
  else if (XMAP == 2) { const int x = b & 7, r = b >> 3; g = (x >> 1) * 8 + (r & 7); j = (x & 1) * 4 + (r >> 3); }
  else { const int x = b & 7, r = b >> 3; g = (x >> 2) * 16 + (r & 15); j = (x & 3) * 2 + (r >> 4); }
  const rsrc_t rX = __builtin_amdgcn_make_buffer_rsrc((void*)p.x, 0, S_ * D_ * 2, 0x00020000);
  const rsrc_t rQKV = __builtin_amdgcn_make_buffer_rsrc((void*)p.wqkv, 0, D_ * 3 * D_ * 2, 0x00020000);
  const rsrc_t rWO = __builtin_amdgcn_make_buffer_rsrc((void*)p.wo, 0, D_ * D_ * 2, 0x00020000);
  const rsrc_t rW1 = __builtin_amdgcn_make_buffer_rsrc((void*)p.w1, 0, D_ * F_ * 2, 0x00020000);
  const rsrc_t rW3 = __builtin_amdgcn_make_buffer_rsrc((void*)p.w3, 0, D_ * F_ * 2, 0x00020000);
  const rsrc_t rW2 = __builtin_amdgcn_make_buffer_rsrc((void*)p.w2, 0, F_ * D_ * 2, 0x00020000);
  const rsrc_t rC = __builtin_amdgcn_make_buffer_rsrc((void*)p.c, 0, D_ * 2, 0x00020000);
  const rsrc_t rMOD = __builtin_amdgcn_make_buffer_rsrc((void*)p.wmod, 0, D_ * 6 * D_ * 2, 0x00020000);
  const rsrc_t rWS = __builtin_amdgcn_make_buffer_rsrc((void*)p.ws, 0, WS_BYTES, 0x00020000);
  const rsrc_t rOUT = __builtin_amdgcn_make_buffer_rsrc((void*)p.out, 0, S_ * D_ * 2, 0x00020000);
  STAMP(0);

  u32 ep;
  { const u32 vz = 0; asm volatile("buffer_load_dword %0, %1, %2, 0 offen sc1" : "=v"(ep) : "v"(vz), "s"(rWS)); }
  u32 vl16 = lane * 16; PIN(vl16);
  const int bm = b < 192 ? b : b - 192, mq = bm / 48, cl = bm - 48 * mq;
  v4u wm[4]; v2u cv; u32 ep0;
  if (w < 4) {
    const u32 vm = (tid >> 3) * 24576 + (tid & 7) * 16, s0 = mq * (128 * 6144) + cl * 128, s1 = s0 + 6144, s2 = s0 + 12288, s3 = s0 + 18432;
    LD16(wm[0], vm, rMOD, s0, 0); LD16(wm[1], vm, rMOD, s1, 0); LD16(wm[2], vm, rMOD, s2, 0); LD16(wm[3], vm, rMOD, s3, 0);
    const u32 vc = (tid >> 3) * 8, scq = mq * 256;
    LD8(cv, vc, rC, scq, 0);
    const u32 vz = 0; asm volatile("buffer_load_dword %0, %1, %2, 0 offen sc1" : "=v"(ep0) : "v"(vz), "s"(rWS));
  }
  if (tid < 16) *(LAS u32*)(L + L_CNT + tid * 4) = 0u;
  if (w == 7) {
    u64 pc; asm volatile("s_getpc_b64 %0" : "=s"(pc));
    const rsrc_t rPC = __builtin_amdgcn_make_buffer_rsrc((void*)(pc & ~127ull), 0, CODE_BYTES, 0x00020000);
    touch(lane * 128 + ((b >> 3) & 3) * 8192, rPC, 0);
  }
  BAR();
  const int st_qd = lane >> 2, st_r = (st_qd & 3) + 4 * (st_qd >> 3);
  const int st_col = ((st_qd >> 2) & 1) * 64 + (((lane & 3) ^ ((w & 1) << 1)) << 4);
  const int rotp = g & 7;
  u32 vB1 = st_r * 3072 + st_col; PIN(vB1);
  const u32 mB1 = (w >> 2) * 12288 + (w & 3) * 1024, sB1 = (w >> 2) * 98304 + (w & 3) * 24576 + j * 128;
  auto stage_b1 = [&](auto PC, auto BC) {
    constexpr int pair = decltype(PC)::value, base = decltype(BC)::value;
    const u32 sp = ((pair + rotp) & 7) * 196608 + sB1;
    dma<base, 0>(mB1, vB1, rQKV, sp); dma<base + 4096, 1024>(mB1, vB1, rQKV, sp); dma<base + 8192, 2048>(mB1, vB1, rQKV, sp);
  };
  const int hd = w & 3, blk2 = w >> 2, kq = w & 3, mh = w >> 2;
  const int cs0 = j < 4 ? 22 * j : 88 + 21 * (j - 4);
  const int cend = cs0 + (j < 4 ? 22 : 21);
  STAMP(1);
  const int T0 = 12 * (g >> 4) + 3 * (w & 3), rcb = min(cs0 + T0, 168);
  if (w < 4) {
    asm volatile("s_waitcnt vmcnt(0)" : "+v"(cv), "+v"(wm[0]), "+v"(wm[1]), "+v"(wm[2]), "+v"(wm[3]), "+v"(ep0)::"memory");
    const u32 tg = __builtin_amdgcn_readfirstlane(ep0) + 1u;
    const f32x4 cf = {blo(cv.x), bhi(cv.x), blo(cv.y), bhi(cv.y)};
    float sc[4];
#pragma unroll
    for (int r = 0; r < 4; ++r) sc[r] = cf[r] * __builtin_amdgcn_rcpf(1.f + __builtin_amdgcn_exp2f(cf[r] * -1.4426950408889634f));
    f32x2 v[4];
#pragma unroll
    for (int i = 0; i < 4; ++i) v[i] = (unp(wm[0][i]) * sc[0] + unp(wm[1][i]) * sc[1]) + (unp(wm[2][i]) * sc[2] + unp(wm[3][i]) * sc[3]);
    const float a0 = sw32_add(v[0].x, v[2].x), a1 = sw32_add(v[0].y, v[2].y), a2 = sw32_add(v[1].x, v[3].x), a3 = sw32_add(v[1].y, v[3].y);
    float t0 = sw16_add(a0, a2), t1 = sw16_add(a1, a3);
    t0 += dppf<0x128>(t0); t1 += dppf<0x128>(t1);
    f32x2 o = {t0, t1};
    *(LAS f32x2*)(L + L_RED + w * 256 + (lane & 7) * 32 + g4 * 8) = o;
    LGW();
    u32 old = 0;
    if (lane == 0) old = __hip_atomic_fetch_add((LAS u32*)(L + L_CNT + 28), 1u, __ATOMIC_RELAXED, __HIP_MEMORY_SCOPE_WORKGROUP);
    if (__builtin_amdgcn_readfirstlane(old) == 3u) {
      asm volatile("" ::: "memory");
      float s = 0.f;
#pragma unroll
      for (int i = 0; i < 4; ++i) s += *(LAS float*)(L + L_RED + i * 256 + lane * 4);
      v2u gr; gr.x = asu(s); gr.y = tg;
      if (b < 192) __builtin_amdgcn_raw_buffer_store_b64(gr, rWS, lane * 8, WS_MODG + (mq * 3072 + cl * 64) * 8, 16);
      __builtin_amdgcn_raw_buffer_store_b32(tg, rWS, 0, WS_ALIVE + b * 4, 16);
    }
  }
  STAMP(2);
  v4u xr[10];
  {
    const int r0 = 64 * g - 16 + w;
    const u32 s0 = (u32)max(r0, 0) * 1024u, s1 = (u32)max(r0 + 8, 0) * 1024u;
    LD16(xr[0], vl16, rX, s0, 0); LD16(xr[1], vl16, rX, s1, 0);
#pragma unroll
    for (int i = 2; i < 10; ++i) { const u32 so = (u32)(r0 + 8 * i) * 1024u; LD16(xr[i], vl16, rX, so, 0); }
  }
  stage_b1(IC<0>{}, IC<L_XY>{});
  int afo[4];
#pragma unroll
  for (int i = 0; i < 4; ++i) { afo[i] = blk2 * 32768 + c16 * 1024 + (((4 * i + g4) ^ c16) << 4); PIN(afo[i]); }
  const u64 am0 = __builtin_amdgcn_ballot_w64((c16 & 14) >= 4 * g4), am1 = __builtin_amdgcn_ballot_w64((c16 & 14) >= 4 * g4 + 2);
  u32 ahm = (lane & 1) ? 0xffff0000u : 0xffffu; PIN(ahm);
  u32 vOst = c16 * 16 + g4 * 256 + (hd & 1) * 8; PIN(vOst);
  int tB1 = g4 * 1024 + 64 * (c16 >> 2) + 8 * (lane & 3) + 32 * ((hd & 1) ^ (g4 & 1)) + 256 * (hd >> 1); PIN(tB1);
  int hw0 = w * 1024 + ((((lane - 8 * rotp) & 63) ^ w) << 4); PIN(hw0);
#define vfl ((opq(vl16) & 112u) << 3)
  STAMP(3);
  v4u bw[48];
  auto read_b1 = [&](int pair, int base, int off) {
#pragma unroll
    for (int kk = 0; kk < 2; ++kk)
#pragma unroll
      for (int ty = 0; ty < 3; ++ty) {
        LAS char* a = L + base + (off + kk * 12288 + ty * 4096);
        bw[(2 * pair + kk) * 3 + ty] = cat(trd(a), trd(a + 512));
      }
  };
  f32x2 xn[10][4];
  v4u ga_[4]; v2u pv;
  const u32 sMG = WS_MODG + w * 1024;
  {
    float u[10];
    sfor<0, 10>([&](auto I) {
      constexpr int i = decltype(I)::value;
      if constexpr (i == 0) asm volatile("s_waitcnt vmcnt(11)" : "+v"(xr[0]), "+v"(xr[1]), "+v"(ep)::"memory");
      if constexpr (i == 2) asm volatile("s_waitcnt vmcnt(12)" : "+v"(xr[2]), "+v"(xr[3])::"memory");
      if constexpr (i == 4) asm volatile("s_waitcnt vmcnt(13)" : "+v"(xr[4]), "+v"(xr[5])::"memory");
      if constexpr (i == 6) asm volatile("s_waitcnt vmcnt(14)" : "+v"(xr[6]), "+v"(xr[7])::"memory");
      if constexpr (i == 8) asm volatile("s_waitcnt vmcnt(15)" : "+v"(xr[8]), "+v"(xr[9])::"memory");
      float s = 0.f, q = 0.f;
#pragma unroll
      for (int e = 0; e < 4; ++e) { s = dot2(xr[i][e], 0x3f803f80u, s); q = dot2(xr[i][e], xr[i][e], q); }
      u[i] = sw32_add(s, q);
      PIN(u[i]);
      if constexpr (i == 1) stage_b1(IC<1>{}, IC<L_XY + 24576>{});
      if constexpr (i == 3) stage_b1(IC<2>{}, IC<L_XY + 49152>{});
      if constexpr (i == 5) stage_b1(IC<3>{}, IC<L_Z>{});
      if constexpr (i == 7) stage_b1(IC<4>{}, IC<L_Z + 24576>{});
    });
    float t[5];
#pragma unroll
    for (int i = 0; i < 5; ++i) t[i] = row16_sum(sw16_add(u[i], u[i + 5]));
    float v = t[0];
#pragma unroll
    for (int i = 1; i < 5; ++i) v = c16 == i ? t[i] : v;
    auto r = __builtin_amdgcn_permlane32_swap(asu(v), asu(v), false, false);
    const float mean = asf(r[0]) * (1.f / 512.f);
    const float var = fmaxf(asf(r[1]) * (1.f / 512.f) - mean * mean, 0.f);
    float rs = __builtin_amdgcn_rsqf(var + 1e-5f), nm = -mean * rs;
    PIN(rs); PIN(nm);
    STAMP(4);
    { const u32 vo = ((lane >> 3) & 3) * 24576 + (lane & 7) * 8, so = WS_MODG + (1024 + 8 * b) * 8; LD8S(pv, vo, rWS, so, 0); }
    { const u32 s0 = sMG, s1 = sMG + 24576, s2 = sMG + 49152, s3 = sMG + 73728;
      LD16S(ga_[0], vl16, rWS, s0, 0); LD16S(ga_[1], vl16, rWS, s1, 0); LD16S(ga_[2], vl16, rWS, s2, 0); LD16S(ga_[3], vl16, rWS, s3, 0); }
    if (w >= 4) {
      const int u = lane >> 2, row = (u & 3) | ((u >> 1) & 4) | ((u << 1) & 8);
      const u32 vo = row * 2752 + (lane & 3) * 16, so = (g & 15) * (32 * 2752) + rcb * 16, mb = L_Z + 49152 + (w & 3) * 4096;
      dma<0, 0>(mb, vo, rW1, so); dma<1024, 16 * 2752>(mb, vo, rW1, so); dma<2048, 0>(mb, vo, rW3, so); dma<3072, 16 * 2752>(mb, vo, rW3, so);
    } else {
      const int u = lane >> 1, row = (u & 3) | ((u >> 1) & 4) | ((u << 1) & 8) | (u & 16);
      const u32 vo = row * 1024 + (lane & 1) * 16, so = g * 32768 + j * 128 + w * 32, mb = L_Z + 65536 + w * 2048;
      dma<0, 0>(mb, vo, rW2, so); dma<1024, 0>(mb, vo, rW2, so + (g < 11 ? 32 * 32768 : 0));
    }
    sfor<0, 10>([&](auto I) {
      constexpr int i = decltype(I)::value;
      const float a = rdl(rs, (i / 5) * 16 + i % 5), c = rdl(nm, (i / 5) * 16 + i % 5);
#pragma unroll
      for (int e = 0; e < 4; ++e) { xn[i][e] = unp(xr[i][e]) * a + c; PIN(xn[i][e]); }
    });
  }
  const u32 tag = __builtin_amdgcn_readfirstlane(ep) + 1u;
  u32 m1; asm("s_lshr_b32 %0, 1, %1" : "=s"(m1) : "s"(w) : "scc");
#define RAISE(fo) raise_flag(rWS, (fo), tag, vl16, m1)
#define SIGNAL(idx, fo) signal_flag(rWS, L_CNT + 4 * (idx), (fo), tag)
  STAMP(5);
  asm volatile("s_waitcnt vmcnt(2)" : "+v"(ga_[0]), "+v"(ga_[1]), "+v"(ga_[2]), "+v"(ga_[3]), "+v"(pv)::"memory");
  auto gsum = [&](v4u (&a)[4], f32x2& o) -> u32 {
    o.x = (asf(a[0].x) + asf(a[1].x)) + (asf(a[2].x) + asf(a[3].x)); o.y = (asf(a[0].z) + asf(a[1].z)) + (asf(a[2].z) + asf(a[3].z));
    return ((a[0].y ^ tag) | (a[0].w ^ tag) | (a[1].y ^ tag) | (a[1].w ^ tag)) | ((a[2].y ^ tag) | (a[2].w ^ tag) | (a[3].y ^ tag) | (a[3].w ^ tag));
  };
  auto gather = [&](u32 voff, u32 soff, f32x2& o) {
    for (u32 sp = 0; sp < SPIN_CAP; ++sp) {
      asm volatile("" ::: "memory");
      v4u a[4];
#pragma unroll
      for (int q = 0; q < 4; ++q) a[q] = __builtin_amdgcn_raw_buffer_load_b128(rWS, voff, soff + q * 24576, 16);
      if (!__builtin_amdgcn_ballot_w64(gsum(a, o) != 0u)) break;
    }
  };
  const float one = w >= 4 ? 1.f : 0.f;
  {
    f32x2 mA;
    if (__builtin_amdgcn_ballot_w64(gsum(ga_, mA) != 0u)) gather(vl16, sMG, mA);
    *(LAS f32x2*)(L + L_MODA + w * 512 + (vl16 >> 1)) = mA + one;
  }
  BAR();
  if (w == 7) {
    const u32 vo = ((lane >> 3) & 3) * 24576 + (lane & 7) * 8, so = WS_MODG + (1024 + 8 * b) * 8;
    for (u32 sp = 0; sp < SPIN_CAP && __builtin_amdgcn_ballot_w64(pv.y != tag); ++sp) {
      asm volatile("" ::: "memory");
      pv = __builtin_amdgcn_raw_buffer_load_b64(rWS, vo, so, 16);
    }
    float sv = asf(pv.x);
    sv += dppf<0x128>(sv); sv = sw16_add(sv, sv);
    pv.x = asu(sv);
    __builtin_amdgcn_raw_buffer_store_b64(pv, rWS, (lane & 7) * 8, WS_MODF + (1024 + 8 * b) * 8, 16);
  }
  STAMP(6);
  int vma = L_MODA + 2 * vl16; PIN(vma);
  const f32x4 ms0 = *(LAS f32x4*)(L + vma), ms1 = *(LAS f32x4*)(L + vma + 16);
  const f32x4 ma0 = *(LAS f32x4*)(L + vma + 2048), ma1 = *(LAS f32x4*)(L + vma + 2064);
  int tz0 = tB1 + L_Z; PIN(tz0);
  { int t2 = tB1 + 49152; PIN(t2); read_b1(0, tB1, 0); read_b1(1, tB1, 24576); read_b1(2, t2, 0); read_b1(3, tz0, 0); read_b1(4, tz0, 24576); }
  {
    VMW(0);
    if (w >= 4) {
      LAS char* a0 = L + L_Z + 49152 + (w & 3) * 4096 + ((lane >> 1) & 1) * 2048 + 64 * ((c16 >> 2) + 4 * (g4 & 1) + 16 * (g4 >> 1)) + 8 * (lane & 1);
#pragma unroll
      for (int ct = 0; ct < 3; ++ct) {
        const int c = min(cs0 + T0 + ct - rcb, 3) * 16;
        const v4u f = cat(trd(a0 + c), trd(a0 + c + 512));
        const u32 sf = WS_B4T + ((j * 16 + (g & 15)) * 24 + T0 + ct) * 1024;
        asm volatile("buffer_store_dwordx4 %0, %1, %2, %3 offen sc1\n\ts_nop 1" ::"v"(f), "v"(vl16), "s"(rWS), "s"(sf) : "memory");
      }
    }
    else {
      LAS char* a0 = L + L_Z + 65536 + w * 2048 + 32 * ((c16 >> 2) + 4 * (g4 & 1) + 16 * (g4 >> 1)) + 8 * (lane & 3);
#pragma unroll
      for (int i = 0; i < 2; ++i) {
        const v4u f = cat(trd(a0 + i * 1024), trd(a0 + i * 1024 + 256));
        const u32 sf = WS_B5T + ((j * 43 + g + (i && g < 11 ? 32 : 0)) * 4 + w) * 1024;
        asm volatile("buffer_store_dwordx4 %0, %1, %2, %3 offen sc1\n\ts_nop 1" ::"v"(f), "v"(vl16), "s"(rWS), "s"(sf) : "memory");
      }
    }
  }
  BAR();
  STAMP(7);
  {
    int hw1 = (hw0 ^ 128) + 8192; PIN(hw1);
    sfor<0, 10>([&](auto I) {
      constexpr int i = decltype(I)::value;
      v4u o;
      o.x = pk2(xn[i][0] * ma0.xy + ms0.xy); o.y = pk2(xn[i][1] * ma0.zw + ms0.zw);
      o.z = pk2(xn[i][2] * ma1.xy + ms1.xy); o.w = pk2(xn[i][3] * ma1.zw + ms1.zw);
      if constexpr (i == 8) { hw0 += 65536; hw1 += 65536; }
      *(LAS v4u*)(L + ((i & 1) ? hw1 : hw0) + ((i >> 1) & 3) * 16384) = o;
      if constexpr (i == 0) stage_b1(IC<5>{}, IC<L_Z>{});
      if constexpr (i == 3) stage_b1(IC<6>{}, IC<L_Z + 24576>{});
      if constexpr (i == 6) stage_b1(IC<7>{}, IC<L_Z + 49152>{});
      SCHED();
    });
  }
  VMW(0);
  BAR();
  RAISE(WS_RFLAG + (j * 32 + g) * 4);
  u32 flr;
  STAMP(8);

  const f32x4 z4 = {0.f, 0.f, 0.f, 0.f};
#define AFRAG(off, ks) (*(LAS v4u*)(L + afo[(ks) & 3] + ((off) + ((ks) >> 2) * 256)))
  const int ch0 = cs0 + 3 * w;
  constexpr int ND = NODMA ? 0 : 1;
  v4u b4[48];
  const u32 sB4 = WS_B4T + (j * 16 * 24 + 3 * w) * 1024;
  auto load_b4 = [&](auto NC) {
    constexpr int n = decltype(NC)::value, ks = n / 3, ct = n % 3;
    const u32 so = sB4 + ((ks + 2 * rotp) & 15) * 24576;
    v4u& r = b4[n];
    if constexpr (ct == 0) LD16S(r, vl16, rWS, so, 0); else if constexpr (ct == 1) LD16S(r, vl16, rWS, so, 1024); else LD16S(r, vl16, rWS, so, 2048);
  };
  auto landed_b4 = [&](auto AC, auto BC) {
#pragma unroll
    for (int i = decltype(AC)::value; i < decltype(BC)::value; ++i) { v4u& r = b4[i]; PIN(r); }
  };
  v4u gb_, gg_;
  u32 vgg, fl0, vRow, vB3;
  int pw, pr, t3;
  f32x4 aKh = z4, aVh = z4, aQ = z4, aK = z4, aV = z4, bQ = z4, bK = z4, bV = z4;
  {
    v4u ah = AFRAG(0, 0), a1 = AFRAG(16384, 0), a2 = AFRAG(32768, 0);
    int l3, c3, g3; f32x2 mB, mG;
    const u32 mB3 = L_Z + (w >> 2) * 4096 + (w & 3) * 1024, sB3 = ((w >> 2) * 32 + 4 * (w & 3)) * 1024 + j * 128;
    auto job = [&](auto NC) {
      constexpr int n = decltype(NC)::value;
      if constexpr (n == 3) { l3 = opq(vl16) >> 4; c3 = l3 & 15; g3 = l3 >> 4; PIN(c3); PIN(g3); vgg = (vl16 < 512 ? 8192 : 20480 - 512) + 512 * j + vl16; PIN(vgg); }
      if constexpr (n == 7) { vRow = (8 * w + (l3 >> 3)) * 1024 + (l3 & 7) * 16; PIN(vRow); }
      if constexpr (n == 8) { pr = (8 * w + (l3 >> 3)) * 256 + (((2 * (l3 & 7)) ^ ((l3 >> 3) & 7)) << 4); PIN(pr); }
      if constexpr (n == 10) { pw = kq * 16384 + (32 * mh + c3) * 256 + ((g3 ^ (c3 & 3)) << 4) + ((c3 & 4) << 4); PIN(pw); }
      if constexpr (n == 11) { t3 = L_Z + kq * 16384 + g3 * 1024 + 64 * (c3 >> 2) + 8 * (l3 & 3) + 32 * (g3 & 1); PIN(t3); }
      if constexpr (n == 13) { vB3 = ((l3 >> 4) & 1) * 64 + (((l3 & 3) ^ ((w & 1) << 1)) << 4); PIN(vB3); }
      if constexpr (n == 14) { vB3 += (16 * (l3 >> 5) + ((l3 >> 2) & 3)) * 1024; PIN(vB3); }
      if constexpr (n == 31) { mB.x = asf(gb_.x) + one; PIN(mB.x); mB.y = asf(gb_.z) + one; PIN(mB.y); *(LAS f32x2*)(L + L_MODA + w * 512 + (vl16 >> 1)) = mB; }
      if constexpr (n == 32) { mG.x = asf(gg_.x); mG.y = asf(gg_.z); *(LAS f32x2*)(L + L_GAB + (vl16 >> 1)) = mG; }
      if constexpr (n == 34) { fl0 = (gb_.y ^ tag) | (gb_.w ^ tag) | (gg_.y ^ tag) | (gg_.w ^ tag); PIN(fl0); }
      if constexpr (n == 6) { const u32 s0 = WS_MODF + 12288 + w * 1024; LD16S(gb_, vl16, rWS, s0, 0); }
      if constexpr (n == 9) { const u32 s0 = WS_MODF; LD16S(gg_, vgg, rWS, s0, 0); }
      if constexpr (n == 12) arrive(L_CNT + 8);
      if constexpr (n == 15) await8(L_CNT + 8);
      if constexpr (n >= 15 && n < 31 && (n & 1)) dma<((n - 15) / 2) * 8192, ((n - 15) / 2) * 65536>(mB3, vB3, rWO, sB3);
      if constexpr (n == 24) { const u32 vr = (vl16 >> 2) & 124, sr = WS_RFLAG + j * 128; LD4S(flr, vr, rWS, sr); }
      if constexpr (n == 30) {
        asm volatile("s_waitcnt vmcnt(3)" : "+v"(gb_), "+v"(gg_), "+v"(flr)::"memory");
        const u32 vr = (vl16 >> 2) & 124, sr = WS_RFLAG + j * 128; u32 cnt = SPIN_CAP;
        asm volatile("1:\n\tv_cmp_ne_u32 vcc, %5, %0\n\ts_cbranch_vccz 2f\n\tbuffer_load_dword %0, %2, %3, %4 offen sc1\n\ts_waitcnt vmcnt(0)\n\t"
                     "s_sub_u32 %1, %1, 1\n\ts_cbranch_scc0 1b\n\t2:" : "+v"(flr), "+s"(cnt) : "v"(vr), "s"(rWS), "s"(sr), "s"(tag) : "vcc", "scc", "memory");
      }
      if constexpr (n >= 31 && (n & 1)) load_b4(IC<(n - 31) / 2>{});
      if constexpr (n == 46) load_b4(IC<9>{});
    };
    sfor<0, 16>([&](auto KS) {
      constexpr int ks = decltype(KS)::value;
      KSTAMP(1, ks);
      if constexpr (ks == 1) read_b1(5, tz0, 0);
      if constexpr (ks == 2) read_b1(6, tz0, 24576);
      if constexpr (ks == 3) { int tz2 = tz0 + 49152; PIN(tz2); read_b1(7, tz2, 0); }
      SCHED();
      v4u nh = ah, n1 = a1, n2 = a2;
      aKh = mfma32(bw[ks * 3 + 1], ah, aKh);
      aVh = mfma32(ah, bw[ks * 3 + 2], aVh);
      if constexpr (ks < 15) nh = AFRAG(0, ks + 1);
      job(IC<3 * ks>{});
      SCHED();
      aQ = mfma32(bw[ks * 3 + 0], a1, aQ);
      aK = mfma32(bw[ks * 3 + 1], a1, aK);
      aV = mfma32(a1, bw[ks * 3 + 2], aV);
      if constexpr (ks < 15) n1 = AFRAG(16384, ks + 1);
      job(IC<3 * ks + 1>{});
      SCHED();
      bQ = mfma32(bw[ks * 3 + 0], a2, bQ);
      bK = mfma32(bw[ks * 3 + 1], a2, bK);
      bV = mfma32(a2, bw[ks * 3 + 2], bV);
      if constexpr (ks < 15) n2 = AFRAG(32768, ks + 1);
      job(IC<3 * ks + 2>{});
      ah = nh; a1 = n1; a2 = n2;
      SCHED();
    });
  }
  STAMP(9); KSTAMP(1, 16);
  {
    const u32 sOst = WS_O + ((g * 4 + 2 * blk2) * 16 + 2 * j + (hd >> 1)) * 1024;
    const v2u k1p = pk4(aK), v1p = pk4(aV);
    const v2u o0 = attend(pk4(aKh), pk4(aVh), k1p, v1p, pk4(aQ), am0, am1, ahm, g == 0 && blk2 == 0);
    const v2u o1 = attend(k1p, v1p, pk4(bK), pk4(bV), pk4(bQ), am0, am1, ahm, false);
    __builtin_amdgcn_raw_buffer_store_b64(o0, rWS, vOst, sOst, 16);
    __builtin_amdgcn_raw_buffer_store_b64(o1, rWS, vOst, sOst + 16384, 16);
  }
  if (__builtin_amdgcn_ballot_w64(fl0 != 0u)) {
    for (u32 sp = 0; sp < SPIN_CAP; ++sp) {
      asm volatile("" ::: "memory");
      gb_ = __builtin_amdgcn_raw_buffer_load_b128(rWS, vl16, WS_MODF + 12288 + w * 1024, 16);
      gg_ = __builtin_amdgcn_raw_buffer_load_b128(rWS, vgg, WS_MODF, 16);
      if (!__builtin_amdgcn_ballot_w64(((gb_.y ^ tag) | (gb_.w ^ tag) | (gg_.y ^ tag) | (gg_.w ^ tag)) != 0u)) break;
    }
    const f32x2 mB = {asf(gb_.x) + one, asf(gb_.z) + one}, mG = {asf(gg_.x), asf(gg_.z)};
    *(LAS f32x2*)(L + L_MODA + w * 512 + (vl16 >> 1)) = mB;
    *(LAS f32x2*)(L + L_GAB + (vl16 >> 1)) = mG;
  }
  STAMP(10);
  VMW(0);
  landed_b4(IC<0>{}, IC<10>{});
  BAR();
  RAISE(flag_off(0, g, 0) + j * 128);
  STAMP(11);

  v4u xv;
  { const u32 sx = g * 65536 + j * 128; LD16(xv, vRow, rX, sx, 0); }
  v4u b3[16];
#pragma unroll
  for (int s = 0; s < 4; ++s)
#pragma unroll
    for (int n = 0; n < 4; ++n) { LAS char* a = L + ((n & 1) ? (t3 ^ 32) : t3) + (s * 4096 + 256 * (n >> 1)); b3[s * 4 + n] = cat(trd(a), trd(a + 512)); }
  sfor<10, 15>(load_b4);
  const u64 care2 = 0x0101010101010101ull * (3u << (2 * kq));
  poll_flags(rWS, vfl, flag_off(0, g, 0), care2, tag);
  PIN(xv);
  v4u af[8];
  {
    const u32 so = WS_O + ((g * 4 + 2 * mh) * 16 + 4 * kq) * 1024, so1 = so + 16384;
    LD16S(af[0], vl16, rWS, so, 0); LD16S(af[1], vl16, rWS, so, 1024); LD16S(af[2], vl16, rWS, so, 2048); LD16S(af[3], vl16, rWS, so, 3072);
    LD16S(af[4], vl16, rWS, so1, 0); LD16S(af[5], vl16, rWS, so1, 1024); LD16S(af[6], vl16, rWS, so1, 2048); LD16S(af[7], vl16, rWS, so1, 3072);
  }
  STAMP(12);
  sfor<15, 18>(load_b4);
  sfor<0, 2>([&](auto M) {
    constexpr int m = decltype(M)::value;
    asm volatile("s_waitcnt vmcnt(%c4)" : "+v"(af[4 * m]), "+v"(af[4 * m + 1]), "+v"(af[4 * m + 2]), "+v"(af[4 * m + 3]) : "i"(m == 0 ? 7 : 5) : "memory");
    f32x4 acc[4] = {z4, z4, z4, z4};
    sfor<0, 4>([&](auto SC) {
      constexpr int sx = decltype(SC)::value;
#pragma unroll
      for (int n = 0; n < 4; ++n) acc[n] = mfma32(b3[sx * 4 + n], af[4 * m + sx], acc[n]);
      if constexpr (sx & 1) load_b4(IC<18 + 2 * m + (sx >> 1)>{});
      SCHED();
    });
#pragma unroll
    for (int n = 0; n < 4; ++n) *(LAS f32x4*)(L + ((n & 1) ? (pw ^ 64) : pw) + (m * 4096 + (n >> 1) * 128)) = acc[n];
  });
  BAR();
  STAMP(13);
  {
    f32x4 y0 = *(LAS f32x4*)(L + pr), y1 = *(LAS f32x4*)(L + (pr ^ 16));
#pragma unroll
    for (int q = 1; q < 4; ++q) { y0 += *(LAS f32x4*)(L + pr + q * 16384); y1 += *(LAS f32x4*)(L + (pr ^ 16) + q * 16384); }
    const int ga = L_GAB + ((vl16 & 112) << 1);
    const f32x4 g0 = *(LAS f32x4*)(L + ga), g1 = *(LAS f32x4*)(L + ga + 16);
    const f32x4 x0 = f32x4{blo(xv.x), bhi(xv.x), blo(xv.y), bhi(xv.y)} + g0 * y0, x1 = f32x4{blo(xv.z), bhi(xv.z), blo(xv.w), bhi(xv.w)} + g1 * y1;
    { const v4u o = cat(pk4(x0), pk4(x1)); const u32 so = WS_X1 + g * 65536 + j * 128;
      asm volatile("buffer_store_dwordx4 %0, %1, %2, %3 offen sc1\n\ts_nop 1" ::"v"(o), "v"(vRow), "s"(rWS), "s"(so) : "memory"); }
    const f32x4 sv = x0 + x1;
    float mean = (sv.x + sv.y) + (sv.z + sv.w);
    mean += dppf<0xB1>(mean); mean += dppf<0x4E>(mean); mean += dppf<0x141>(mean);
    mean *= 1.f / 64.f;
    const f32x4 d0 = x0 - mean, d1 = x1 - mean, dd = d0 * d0 + d1 * d1;
    float m2 = (dd.x + dd.y) + (dd.z + dd.w);
    m2 += dppf<0xB1>(m2); m2 += dppf<0x4E>(m2); m2 += dppf<0x141>(m2);
    v2u st; st.x = asu(mean); st.y = asu(m2);
    __builtin_amdgcn_raw_buffer_store_b64(st, rWS, (vRow >> 10) << 6, WS_STAT + g * 4096 + j * 8, 16);
  }
  STAMP(14);
  VMW(0);
  landed_b4(IC<10>{}, IC<22>{});
  BAR();
  RAISE(flag_off(1, g, 0) + j * 128);
  sfor<22, 27>(load_b4);
  STAMP(15);

  v4u x1v[4]; v2u stv;
  auto ln2_load = [&](int h) {
    const u32 so = WS_X1 + g * 65536 + (32 * (w >> 2) + 16 * h + 4 * (w & 3)) * 1024;
    LD16S(x1v[0], vl16, rWS, so, 0); LD16S(x1v[1], vl16, rWS, so, 1024); LD16S(x1v[2], vl16, rWS, so, 2048); LD16S(x1v[3], vl16, rWS, so, 3072);
    const u32 vs = (vl16 >> 1) & 255, ss = WS_STAT + g * 4096 + (32 * (w >> 2) + 16 * h + 4 * (w & 3)) * 64;
    LD8S(stv, vs, rWS, ss, 0);
  };
  float l2r, l2n;
  auto ln2_stats = [&]() {
    const float mj = asf(stv.x);
    float mean = mj;
    mean += dppf<0xB1>(mean); mean += dppf<0x4E>(mean); mean += dppf<0x141>(mean);
    mean *= 0.125f;
    const float dm = mj - mean;
    float m2 = asf(stv.y) + 64.f * dm * dm;
    m2 += dppf<0xB1>(m2); m2 += dppf<0x4E>(m2); m2 += dppf<0x141>(m2);
    l2r = __builtin_amdgcn_rsqf(m2 * (1.f / 512.f) + 1e-5f);
    l2n = -mean * l2r;
  };
  u32 vH;
  u32 hmask[3];
#pragma unroll
  for (int ct = 0; ct < 3; ++ct)
    asm("s_cmp_lt_u32 %1, %2\n\ts_cselect_b32 %0, -1, 0" : "=s"(hmask[ct]) : "s"((u32)(ch0 + ct)), "s"((u32)cend) : "scc");
  auto hh_store = [&](v2u o, int ct, int h) {
    const u32 so = WS_HH + ((g * 4 + h) * 172 + ch0 + ct) * 256;
    asm volatile("s_mov_b32 exec_lo, %4\n\ts_mov_b32 exec_hi, %4\n\tbuffer_store_dwordx2 %0, %1, %2, %3 offen sc1\n\ts_mov_b64 exec, -1"
                 ::"v"(o), "v"(vH), "s"(rWS), "s"(so), "s"(hmask[ct]) : "memory");
  };
  auto swiglu_tile = [&](f32x4 (&acc)[3][2], int ct, int h) {
    f32x4 G, U, R;
#pragma unroll
    for (int e = 0; e < 4; ++e) {
      auto r = __builtin_amdgcn_permlane32_swap(asu(acc[ct][0][e]), asu(acc[ct][1][e]), false, false);
      G[e] = asf(r[0]); U[e] = asf(r[1]);
    }
    const f32x4 T = G * -1.4426950408889634f;
#pragma unroll
    for (int e = 0; e < 4; ++e) R[e] = __builtin_amdgcn_rcpf(1.f + __builtin_amdgcn_exp2f(T[e]));
    hh_store(pk4((G * U) * R), ct, h);
  };
#pragma unroll
  for (int i = 0; i < 4; ++i) { afo[i] -= blk2 * 32768; PIN(afo[i]); }
  int vm2 = L_MODA + 2 * vl16; PIN(vm2);
  int h2w = w * 4096 + (((vl16 - 128 * rotp) & 1023) ^ ((4 * w & 15) << 4)); PIN(h2w);
  poll_flags(rWS, vfl, flag_off(1, g, 0), ~0ull, tag);
  landed_b4(IC<22>{}, IC<27>{});
  ln2_load(0);
  sfor<27, 31>(load_b4);
  {
    const f32x4 s0 = *(LAS f32x4*)(L + vm2), s1 = *(LAS f32x4*)(L + vm2 + 16), a0 = *(LAS f32x4*)(L + vm2 + 2048), a1 = *(LAS f32x4*)(L + vm2 + 2064);
    asm volatile("s_waitcnt vmcnt(%c5)" : "+v"(x1v[0]), "+v"(x1v[1]), "+v"(x1v[2]), "+v"(x1v[3]), "+v"(stv) : "i"(4) : "memory");
    ln2_stats();
#pragma unroll
    for (int i = 0; i < 4; ++i) {
      const float rs = rdl(l2r, 8 * i), nn = rdl(l2n, 8 * i);
      v4u o;
      o.x = pk2((unp(x1v[i].x) * rs + nn) * a0.xy + s0.xy); o.y = pk2((unp(x1v[i].y) * rs + nn) * a0.zw + s0.zw);
      o.z = pk2((unp(x1v[i].z) * rs + nn) * a1.xy + s1.xy); o.w = pk2((unp(x1v[i].w) * rs + nn) * a1.zw + s1.zw);
      *(LAS v4u*)(L + (h2w ^ (i << 4)) + i * 1024) = o;
    }
  }
  ln2_load(1);
  BAR();
  STAMP(16);
  f32x4 accA[3][2] = {{z4, z4}, {z4, z4}, {z4, z4}};
  {
    f32x4 cs, ca; float rsv[4], nnv, t[4]; v2u ov;
    auto ln_op = [&](auto NC) {
      constexpr int n = decltype(NC)::value, u = n / 16, k = n % 16, hf = u & 1, i = u >> 1;
      if constexpr (u < 8) {
        if constexpr (k == 0) {
          cs = *(LAS f32x4*)(L + vm2 + hf * 16); ca = *(LAS f32x4*)(L + vm2 + 2048 + hf * 16);
          if constexpr (hf == 0) nnv = q_mov(rdl(l2n, 8 * i));
        }
        else if constexpr (k == 1) t[0] = q_shl(x1v[i][2 * hf]);
        else if constexpr (k == 2) t[1] = q_hi(x1v[i][2 * hf]);
        else if constexpr (k == 3) t[2] = q_shl(x1v[i][2 * hf + 1]);
        else if constexpr (k == 4) t[3] = q_hi(x1v[i][2 * hf + 1]);
        else if constexpr (k < 9) t[k - 5] = q_fmas(t[k - 5], rsv[i], nnv);
        else if constexpr (k < 13) t[k - 9] = q_fma(t[k - 9], ca[k - 9], cs[k - 9]);
        else if constexpr (k == 13) ov.x = q_cvt(t[0], t[1]);
        else if constexpr (k == 14) ov.y = q_cvt(t[2], t[3]);
        else {
          *(LAS v2u*)(L + (h2w ^ (i << 4)) + (32768 + i * 1024 + hf * 8)) = ov;
        }
      }
    };
    v4u a0 = AFRAG(0, 0), a1 = AFRAG(16384, 0);
    sfor<0, 16>([&](auto KS) {
      constexpr int ks = decltype(KS)::value;
      KSTAMP(3, ks);
      v4u n0 = a0, n1 = a1;
      if constexpr (ks < 15) { n0 = AFRAG(0, ks + 1); n1 = AFRAG(16384, ks + 1); }
      if constexpr (ks == 3) {
        asm volatile("s_waitcnt vmcnt(5)" : "+v"(x1v[0]), "+v"(x1v[1]), "+v"(x1v[2]), "+v"(x1v[3]), "+v"(stv)::"memory");
        landed_b4(IC<27>{}, IC<31>{});
      }
      if constexpr (ks == 10) { VMW(6); landed_b4(IC<31>{}, IC<40>{}); }
      if constexpr (ks == 13) { VMW(2); landed_b4(IC<40>{}, IC<46>{}); }
      if constexpr (ks == 15) { VMW(0); landed_b4(IC<46>{}, IC<48>{}); }
      SCHED();
      if constexpr (ks == 3) { ln2_stats(); PIN(l2r); PIN(l2n);
#pragma unroll
        for (int i = 0; i < 4; ++i) rsv[i] = rdl(l2r, 8 * i); }
      sfor<0, 6>([&](auto MC) {
        constexpr int m = decltype(MC)::value, ct = m >> 1;
        if constexpr (m & 1) accA[ct][1] = mfma32(b4[ks * 3 + ct], a1, accA[ct][1]); else accA[ct][0] = mfma32(b4[ks * 3 + ct], a0, accA[ct][0]);
        if constexpr ((ks * 6 + m) % 4 == 0 && 31 + (ks * 6 + m) / 4 < 48) load_b4(IC<31 + (ks * 6 + m) / 4>{});
        if constexpr (ks >= 4) {
          constexpr int o = (ks - 4) * 15 + (m >> 1) * 5 + (m & 1) * 2;
          ln_op(IC<o>{}); ln_op(IC<o + 1>{});
          if constexpr (m & 1) ln_op(IC<o + 2>{});
          SCHED();
        }
      });
      a0 = n0; a1 = n1;
      SCHED();
    });
  }
  vH = (vl16 & 255) + ((vl16 >> 5) & 8) + (vl16 >> 9) * (2 * 172 * 256); PIN(vH);
  BAR();
  STAMP(17); KSTAMP(3, 16);
  f32x4 accB[3][2] = {{z4, z4}, {z4, z4}, {z4, z4}};
  u32 vRow5; int pr5, pw5, gb5;
  const int kb5 = 5 * w + min(w, 3);
  u64 care5 = 0x0101010101010101ull * ((0x80c06030180c0603ull >> (8 * w)) & 255u);
  asm volatile("" : "+s"(care5));
  v4u b5[24], hA[12], hB[12]; v2u xqA, xqB;
  const u32 sB5 = WS_B5T + (j * 43 + kb5) * 4096, d5 = (w + 5) >> 3;
  auto load_b5 = [&](auto NC) {
    constexpr int n = decltype(NC)::value, sx = n / 4, t = n % 4;
    const u32 so = sB5 + (sx == 5 ? (5 - d5) * 4096 : sx * 4096);
    ld16s<t * 1024>(b5[n], vl16, rWS, so);
  };
  auto load_h = [&](auto NC, v4u (&hf)[12], int h) {
    constexpr int n = decltype(NC)::value, sx = n / 2, m = n % 2;
    const u32 so = WS_HH + ((g * 4 + 2 * m + h) * 172 + 4 * kb5) * 256 + (sx == 5 ? (5 - d5) * 1024 : sx * 1024);
    ld16s<0>(hf[n], vl16, rWS, so);
  };
  auto poll5 = [&](int h) {
    const u32 fo = flag_off(2, g, h), vf = vfl; u32 t, cnt = SPIN_CAP;
    asm volatile("1:\n\tbuffer_load_dword %0, %2, %3, %4 offen sc1\n\ts_waitcnt vmcnt(0)\n\tv_cmp_ne_u32 vcc, %5, %0\n\ts_and_b64 vcc, vcc, %6\n\ts_cbranch_scc0 2f\n\t"
                 "s_sub_u32 %1, %1, 1\n\ts_cbranch_scc0 1b\n\t2:" : "=&v"(t), "+s"(cnt) : "v"(vf), "s"(rWS), "s"(fo), "s"(tag), "s"(care5) : "vcc", "scc", "memory");
  };
  {
    float G[4], U[4], T[4]; v2u ov;
    auto sw_op = [&](auto NC) {
      constexpr int n = decltype(NC)::value, ct = n / 22, k = n % 22;
      if constexpr (ct < 3) {
        if constexpr (k < 4) { auto r = __builtin_amdgcn_permlane32_swap(asu(accA[ct][0][k]), asu(accA[ct][1][k]), false, false); G[k] = asf(r[0]); U[k] = asf(r[1]); PIN(G[k]); PIN(U[k]); }
        else if constexpr (k < 6) { T[2 * (k - 4)] = q_mulk(G[2 * (k - 4)]); T[2 * (k - 4) + 1] = q_mulk(G[2 * (k - 4) + 1]); }
        else if constexpr (k < 10) T[k - 6] = q_exp(T[k - 6]);
        else if constexpr (k < 12) { T[2 * (k - 10)] = q_add1(T[2 * (k - 10)]); T[2 * (k - 10) + 1] = q_add1(T[2 * (k - 10) + 1]); }
        else if constexpr (k < 16) T[k - 12] = q_rcp(T[k - 12]);
        else if constexpr (k < 18) { G[2 * (k - 16)] = q_mul(G[2 * (k - 16)], U[2 * (k - 16)]); G[2 * (k - 16) + 1] = q_mul(G[2 * (k - 16) + 1], U[2 * (k - 16) + 1]); }
        else if constexpr (k < 20) { G[2 * (k - 18)] = q_mul(G[2 * (k - 18)], T[2 * (k - 18)]); G[2 * (k - 18) + 1] = q_mul(G[2 * (k - 18) + 1], T[2 * (k - 18) + 1]); }
        else if constexpr (k == 20) { ov.x = q_cvt(G[0], G[1]); ov.y = q_cvt(G[2], G[3]); }
        else hh_store(ov, ct, 0);
      }
    };
    v4u a0 = AFRAG(32768, 0), a1 = AFRAG(49152, 0);
    int l5, c5, g5;
    sfor<0, 16>([&](auto KS) {
      constexpr int ks = decltype(KS)::value;
      KSTAMP(4, ks);
      v4u n0 = a0, n1 = a1;
      if constexpr (ks > 0 && ks < 15) { n0 = AFRAG(32768, ks + 1); n1 = AFRAG(49152, ks + 1); }
      if constexpr (ks == 8) { VMW(0); SIGNAL(1, flag_off(2, g, 0) + j * 128); }
      if constexpr (ks == 12) poll5(0);
      SCHED();
      sfor<0, 6>([&](auto MC) {
        constexpr int m = decltype(MC)::value, ct = m >> 1, n = ks * 6 + m;
        if constexpr (m & 1) accB[ct][1] = mfma32(b4[ks * 3 + ct], a1, accB[ct][1]); else accB[ct][0] = mfma32(b4[ks * 3 + ct], a0, accB[ct][0]);
        if constexpr (n == 3) n0 = AFRAG(32768, 1);
        if constexpr (n == 5) n1 = AFRAG(49152, 1);
        if constexpr (n < 33) { sw_op(IC<2 * n>{}); sw_op(IC<2 * n + 1>{}); }
        if constexpr (n >= 12 && n < 40 && n % 4 == 0) load_b5(IC<(n - 12) / 4>{});
        if constexpr (n >= 48 && n < 63 && n % 3 == 0) load_b5(IC<7 + (n - 48) / 3>{});
        if constexpr (n >= 72 && n % 2 == 0) load_h(IC<(n - 72) / 2>{}, hA, 0);
        if constexpr (n == 38) { l5 = opq(vl16) >> 4; c5 = l5 & 15; g5 = l5 >> 4; PIN(c5); PIN(g5); }
        if constexpr (n == 40) { vRow5 = (32 * (w >> 2) + 4 * (w & 3) + g5) * 1024 + c5 * 8; PIN(vRow5); }
        if constexpr (n == 42) { pr5 = (4 * w + g5) * 256 + ((c5 ^ ((4 * w + g5) & 7)) << 4); PIN(pr5); }
        if constexpr (n == 44) { pw5 = w * 8192 + c5 * 256 + ((c5 & 4) << 4); PIN(pw5); }
        if constexpr (n == 46) { pw5 += (g5 ^ (c5 & 3)) << 4; PIN(pw5); gb5 = L_GAB + 256 + c5 * 16; PIN(gb5); }
        SCHED();
      });
      a0 = n0; a1 = n1;
      SCHED();
    });
  }
  STAMP(18); KSTAMP(4, 16);
  PIN(accB[0][0]); PIN(accB[0][1]); PIN(accB[1][0]); PIN(accB[1][1]); PIN(accB[2][0]); PIN(accB[2][1]);
  swiglu_tile(accB, 0, 1);
  swiglu_tile(accB, 1, 1);
  swiglu_tile(accB, 2, 1);
  VMW(0);
  BAR();
  RAISE(flag_off(2, g, 1) + j * 128);
  STAMP(19);
#pragma unroll
  for (int i = 0; i < 12; ++i) PIN(hA[i]);
#pragma unroll
  for (int i = 0; i < 12; ++i) PIN(b5[i]);
  sfor<12, 18>(load_b5);
  auto p5_half = [&](auto HC, v4u (&hf)[12]) {
    constexpr int h = decltype(HC)::value;
    f32x4 acc[2][4] = {{z4, z4, z4, z4}, {z4, z4, z4, z4}};
    sfor<0, 6>([&](auto SC) {
      constexpr int sx = decltype(SC)::value;
      if constexpr (h == 0 && sx == 1) sfor<18, 21>(load_b5);
      if constexpr (h == 0 && sx == 2) { sfor<21, 24>(load_b5);
        const u32 sq = WS_X1 + g * 65536 + j * 128, sq1 = sq + 16384, vr5 = vRow5; const rsrc_t rr = rWS; LD8S(xqA, vr5, rr, sq, 0); LD8S(xqB, vr5, rr, sq1, 0); }
      if constexpr (h == 0 && sx == 3) {
        VMW(8);
#pragma unroll
        for (int i = 12; i < 18; ++i) PIN(b5[i]);
      }
      if constexpr (h == 0 && sx == 4) {
        poll5(1);
#pragma unroll
        for (int i = 18; i < 24; ++i) PIN(b5[i]);
        PIN(xqA); PIN(xqB);
      }
      if constexpr (h == 1 && sx == 0) asm volatile("s_waitcnt vmcnt(9)" : "+v"(hf[0]), "+v"(hf[1]), "+v"(hf[2]), "+v"(hf[3])::"memory");
      if constexpr (h == 1 && sx == 2) asm volatile("s_waitcnt vmcnt(5)" : "+v"(hf[4]), "+v"(hf[5]), "+v"(hf[6]), "+v"(hf[7])::"memory");
      if constexpr (h == 1 && sx == 4) asm volatile("s_waitcnt vmcnt(1)" : "+v"(hf[8]), "+v"(hf[9]), "+v"(hf[10]), "+v"(hf[11])::"memory");
      if constexpr (sx == 5) { if (w >= 3) { hf[10] = v4u{0u, 0u, 0u, 0u}; hf[11] = v4u{0u, 0u, 0u, 0u}; } }
#pragma unroll
      for (int m = 0; m < 2; ++m) {
#pragma unroll
        for (int n = 0; n < 4; ++n) acc[m][n] = mfma32(b5[sx * 4 + n], hf[2 * sx + m], acc[m][n]);
        if constexpr (h == 0 && sx >= 4) { if (m == 0) { load_h(IC<4 * (sx - 4)>{}, hB, 1); load_h(IC<4 * (sx - 4) + 1>{}, hB, 1); } else { load_h(IC<4 * (sx - 4) + 2>{}, hB, 1); load_h(IC<4 * (sx - 4) + 3>{}, hB, 1); } }
        SCHED();
      }
    });
    const int pwh = h ? opq(pw5 + 65536) : pw5;
#pragma unroll
    for (int m = 0; m < 2; ++m)
#pragma unroll
      for (int n = 0; n < 4; ++n) *(LAS f32x4*)(L + ((n & 1) ? (pwh ^ 64) : pwh) + (m * 4096 + (n >> 1) * 128)) = acc[m][n];
    if constexpr (h == 0) sfor<8, 12>([&](auto I) { load_h(I, hB, 1); });
    BAR();
    const int prh = h ? opq(pr5 + 65536) : pr5;
    f32x4 y = *(LAS f32x4*)(L + prh);
#pragma unroll
    for (int q = 1; q < 8; ++q) y += *(LAS f32x4*)(L + prh + q * 8192);
    const f32x4 gbv = *(LAS f32x4*)(L + gb5);
    const v2u xx = h ? xqB : xqA;
    const f32x4 xb = {blo(xx.x), bhi(xx.x), blo(xx.y), bhi(xx.y)};
    __builtin_amdgcn_raw_buffer_store_b64(pk4(xb + gbv * y), rOUT, vRow5, g * 65536 + h * 16384 + j * 128, 0);
  };
  STAMP(20);
  p5_half(IC<0>{}, hA);
  STAMP(21);
  p5_half(IC<1>{}, hB);
  STAMP(22);
  STAMP_DUMP();
  if (b == 0 && w == 0) {
    for (u32 sp = 0; sp < SPIN_CAP; ++sp) {
      asm volatile("" ::: "memory");
      const v4u v = __builtin_amdgcn_raw_buffer_load_b128(rWS, vl16, WS_ALIVE, 16);
      if (__all(v.x == tag && v.y == tag && v.z == tag && v.w == tag)) break;
    }
    __builtin_amdgcn_raw_buffer_store_b32(tag, rWS, 0, WS_EPOCH, 16);
  }
}

__global__ void __launch_bounds__(256) pre_ws(char* ws, u32* sink) {
  const size_t tid = (size_t)blockIdx.x * 256 + threadIdx.x, nt = (size_t)gridDim.x * 256;
  v4u a = {0, 0, 0, 0};
  for (size_t k = tid; k < (16u << 20) / 16; k += nt) a ^= ((const v4u*)ws)[k];
  if ((a.x ^ a.y ^ a.z ^ a.w) == 0x12345678u) *sink = 1;
}
}

extern "C" void kernel_launch(void* const* d_in, const int* in_sizes, int n_in, void* d_out, int out_size, void* d_ws, size_t ws_size,
                              hipStream_t stream) {
  Params p;
  p.x = (const u16*)d_in[0]; p.wqkv = (const u16*)d_in[1]; p.wo = (const u16*)d_in[2];
  p.w1 = (const u16*)d_in[4]; p.w3 = (const u16*)d_in[5]; p.w2 = (const u16*)d_in[6];
  p.c = (const u16*)d_in[7]; p.wmod = (const u16*)d_in[8];
  p.out = (u16*)d_out; p.ws = (char*)d_ws;
  (void)hipFuncSetAttribute((const void*)fused_block, hipFuncAttributeMaxDynamicSharedMemorySize, L_TOTAL);
  pre_ws<<<1024, 256, 0, stream>>>(p.ws, (u32*)(p.ws + (20u << 20)));
  fused_block<<<256, 512, L_TOTAL, stream>>>(p);
}
```

```cpp
#include <hip/hip_runtime.h>
#include <stdint.h>

#define KDBG 0
#define NODMA 0
#define CODEWARM 0
#define L2WARM 1
#define XMAP 2

#define DI __device__ __forceinline__
#define GAS __attribute__((address_space(1)))
#define LAS __attribute__((address_space(3)))
typedef unsigned u32;
typedef unsigned short u16;
typedef unsigned long long u64;
typedef u32 v4u __attribute__((ext_vector_type(4)));
typedef u32 v2u __attribute__((ext_vector_type(2)));
typedef float f32x4 __attribute__((ext_vector_type(4)));
typedef float f32x2 __attribute__((ext_vector_type(2)));
typedef __bf16 bf16x8 __attribute__((ext_vector_type(8)));
typedef __bf16 bf16x2 __attribute__((ext_vector_type(2)));
typedef short s16x4 __attribute__((ext_vector_type(4)));
typedef __amdgpu_buffer_rsrc_t rsrc_t;
typedef GAS u32 gu32;
typedef GAS u64 gu64;
#define RLX_AGENT __ATOMIC_RELAXED, __HIP_MEMORY_SCOPE_AGENT

namespace {
constexpr int S_ = 2048, D_ = 512, F_ = 1376;
constexpr u32 WS_EPOCH = 0;
constexpr u32 WS_ALIVE = 4096;
constexpr u32 WS_FLAG = 32768;
constexpr u32 WS_STAT = 262144;
constexpr u32 WS_MODG = 524288;
constexpr u32 WS_MODF = 655360;
constexpr u32 WS_O = 1u << 20;
constexpr u32 WS_X1 = 3u << 20;
constexpr u32 WS_HH = 5u << 20;
constexpr u32 WS_RFLAG = 8192;
constexpr u32 WS_B4T = 11u << 20;
constexpr u32 WS_B5T = 14u << 20;
constexpr u32 WS_B3T = (15u << 20) + (512u << 10);
constexpr u32 WS_DBG = 16u << 20;
constexpr u32 WS_BYTES = 32u << 20;
constexpr int L_XY = 0;
constexpr int L_Z = 81920;
constexpr int L_MISC = 158720;
constexpr int L_TOTAL = 163840;
constexpr int L_RED = 73728;
constexpr int L_MODA = L_MISC;
constexpr int L_CNT = L_MISC + 4096;
constexpr int L_GAB = L_MISC + 4096 + 64;
constexpr int L_JUNK = L_MISC + 4096 + 64 + 512;
constexpr int L_STAMP = 156672;
constexpr u32 SPIN_CAP = 1u << 18;
constexpr u32 CODE_BYTES = 26112;

DI f32x4 mfma32(v4u a, v4u b, f32x4 c) {
  return __builtin_amdgcn_mfma_f32_16x16x32_bf16(__builtin_bit_cast(bf16x8, a), __builtin_bit_cast(bf16x8, b), c, 0, 0, 0);
}
DI f32x4 mfma16(v2u a, v2u b, f32x4 c) {
  return __builtin_amdgcn_mfma_f32_16x16x16bf16_1k(__builtin_bit_cast(s16x4, a), __builtin_bit_cast(s16x4, b), c, 0, 0, 0);
}
DI u32 pk(float lo, float hi) { f32x2 v = {lo, hi}; return __builtin_bit_cast(u32, __builtin_convertvector(v, bf16x2)); }
DI u32 pk2(f32x2 v) { return __builtin_bit_cast(u32, __builtin_convertvector(v, bf16x2)); }
DI v2u pk4(f32x4 v) { v2u r; r.x = pk(v.x, v.y); r.y = pk(v.z, v.w); return r; }
DI float asf(u32 u) { return __builtin_bit_cast(float, u); }
DI u32 asu(float f) { return __builtin_bit_cast(u32, f); }
DI float blo(u32 u) { return asf(u << 16); }
DI float bhi(u32 u) { return asf(u & 0xffff0000u); }
DI f32x2 unp(u32 u) { f32x2 r = {blo(u), bhi(u)}; return r; }
DI float dot2(u32 a, u32 b, float c) { return __builtin_amdgcn_fdot2_f32_bf16(__builtin_bit_cast(bf16x2, a), __builtin_bit_cast(bf16x2, b), c, false); }
DI v2u trd(LAS char* p) { return __builtin_bit_cast(v2u, __builtin_amdgcn_ds_read_tr16_b64_v4i16((LAS s16x4*)p)); }
DI v4u cat(v2u a, v2u b) { v4u r; r.x = a.x; r.y = a.y; r.z = b.x; r.w = b.y; return r; }
template <int MI, int SI> DI void dma(u32 mb, u32 voff, rsrc_t srd, u32 sb) {
  u32 t;
  asm volatile("s_add_u32 m0, %1, %c2\n\ts_add_u32 %0, %3, %c4\n\tbuffer_load_dwordx4 %5, %6, %0 offen lds"
               : "=&s"(t) : "s"(mb), "i"(MI), "s"(sb), "i"(SI), "v"(voff), "s"(srd) : "scc");
}
DI void touch(u32 voff, rsrc_t srd, u32 sb) {
  asm volatile("s_mov_b32 m0, %c0\n\ts_nop 0\n\tbuffer_load_dword %1, %2, %3 offen lds" ::"i"(L_JUNK), "v"(voff), "s"(srd), "s"(sb));
}
#define BAR() asm volatile("s_waitcnt lgkmcnt(0)\n\ts_barrier" ::: "memory")
#define VMW(n) asm volatile("s_waitcnt vmcnt(" #n ")" ::: "memory")
#define LGW() asm volatile("s_waitcnt lgkmcnt(0)" ::: "memory")
#define SCHED() __builtin_amdgcn_sched_barrier(0)
#define LD16(dst, voff, srd, soff, IMM) asm volatile("buffer_load_dwordx4 %0, %1, %2, %3 offen offset:" #IMM : "=v"(dst) : "v"(voff), "s"(srd), "s"(soff))
#define LD16S(dst, voff, srd, soff, IMM) asm volatile("buffer_load_dwordx4 %0, %1, %2, %3 offen offset:" #IMM " sc1" : "=v"(dst) : "v"(voff), "s"(srd), "s"(soff))
#define LD8(dst, voff, srd, soff, IMM) asm volatile("buffer_load_dwordx2 %0, %1, %2, %3 offen offset:" #IMM : "=v"(dst) : "v"(voff), "s"(srd), "s"(soff))
#define LD8S(dst, voff, srd, soff, IMM) asm volatile("buffer_load_dwordx2 %0, %1, %2, %3 offen offset:" #IMM " sc1" : "=v"(dst) : "v"(voff), "s"(srd), "s"(soff))
#define LD4(dst, voff, srd, soff) asm volatile("buffer_load_dword %0, %1, %2, %3 offen" : "=v"(dst) : "v"(voff), "s"(srd), "s"(soff))
#define LD4S(dst, voff, srd, soff) asm volatile("buffer_load_dword %0, %1, %2, %3 offen sc1" : "=v"(dst) : "v"(voff), "s"(srd), "s"(soff))

template <int IMM> DI void ld16s(v4u& r, u32 v, rsrc_t rs, u32 so) { asm volatile("buffer_load_dwordx4 %0, %1, %2, %3 offen offset:%c4 sc1" : "=v"(r) : "v"(v), "s"(rs), "s"(so), "i"(IMM)); }
template <int CTRL> DI float dppf(float v) { return asf(__builtin_amdgcn_update_dpp(0, asu(v), CTRL, 0xf, 0xf, true)); }
DI float row16_sum(float v) { v += dppf<0x128>(v); v += dppf<0x124>(v); v += dppf<0x122>(v); v += dppf<0x121>(v); return v; }
DI float sw32_add(float a, float b) { auto r = __builtin_amdgcn_permlane32_swap(asu(a), asu(b), false, false); return asf(r[0]) + asf(r[1]); }
DI float sw16_add(float a, float b) { auto r = __builtin_amdgcn_permlane16_swap(asu(a), asu(b), false, false); return asf(r[0]) + asf(r[1]); }
DI float x16_max(float v) { auto r = __builtin_amdgcn_permlane16_swap(asu(v), asu(v), false, false); return fmaxf(asf(r[0]), asf(r[1])); }
DI float x32_max(float v) { auto r = __builtin_amdgcn_permlane32_swap(asu(v), asu(v), false, false); return fmaxf(asf(r[0]), asf(r[1])); }
DI int fresh_lane() { int l; asm volatile("v_mbcnt_lo_u32_b32 %0, -1, 0\n\tv_mbcnt_hi_u32_b32 %0, -1, %0" : "=v"(l)); return l; }
DI float rdl(float v, int l) { return asf(__builtin_amdgcn_readlane(asu(v), l)); }

struct Params {
  const u16 *x, *wqkv, *wo, *w1, *w3, *w2, *c, *wmod;
  u16* out;
  char* ws;
};

#define LOOPSEL 0
#define STAMP(i) do {} while (0)
#define KSTAMP(lp, i) do {} while (0)
#define STAMP_DUMP() do {} while (0)

DI u32 flag_off(int phase, int g, int h) { return WS_FLAG + ((phase * 32 + g) * 2 + h) * 1024; }
DI void poll_flags(rsrc_t rWS, u32 vfl, u32 foff, u64 care, u32 tag) {
  for (u32 sp = 0; sp < SPIN_CAP; ++sp) {
    asm volatile("" ::: "memory");
    const u32 v = __builtin_amdgcn_raw_buffer_load_b32(rWS, vfl, foff, 16);
    if (!(__builtin_amdgcn_ballot_w64(v != tag) & care)) break;
  }
  VMW(0);
}
DI void raise_flag(rsrc_t rWS, u32 foff, u32 vtag, u32 vzero, u32 m1) {
  asm volatile("s_mov_b32 exec_hi, 0\n\ts_mov_b32 exec_lo, %4\n\tbuffer_store_dword %0, %1, %2, %3 offen sc1\n\ts_mov_b64 exec, -1" ::"v"(vtag), "v"(vzero), "s"(rWS), "s"(foff), "s"(m1) : "memory");
}
DI void signal_flag(rsrc_t rWS, u32 cnt_addr, u32 foff, u32 tag) {
  u32 t, a = cnt_addr, one = 1u, vt = tag, vz = 0u;
  asm volatile("s_mov_b64 exec, 1\n\tds_add_rtn_u32 %0, %1, %2\n\ts_waitcnt lgkmcnt(0)\n\tv_cmp_eq_u32 vcc, 7, %0\n\ts_and_b64 exec, exec, vcc\n\t"
               "buffer_store_dword %3, %4, %5, %6 offen sc1\n\ts_mov_b64 exec, -1"
               : "=&v"(t) : "v"(a), "v"(one), "v"(vt), "v"(vz), "s"(rWS), "s"(foff) : "vcc", "scc", "memory");
}
DI void arrive(u32 cnt_addr) {
  u32 a = cnt_addr, one = 1u;
  asm volatile("s_waitcnt lgkmcnt(0)\n\ts_mov_b64 exec, 1\n\tds_add_u32 %0, %1\n\ts_mov_b64 exec, -1" ::"v"(a), "v"(one) : "memory");
}
DI void await8(u32 cnt_addr) {
  u32 a = cnt_addr, t, sv;
  asm volatile("1:\n\tds_read_b32 %0, %2\n\ts_waitcnt lgkmcnt(0)\n\tv_readfirstlane_b32 %1, %0\n\ts_nop 0\n\ts_cmp_lt_u32 %1, 8\n\ts_cbranch_scc1 1b"
               : "=&v"(t), "=&s"(sv) : "v"(a) : "scc", "memory");
}
#define PIN(x) asm volatile("" : "+v"(x))
DI float qp(float r) { PIN(r); return r; }
DI float q_shl(u32 a) { return qp(blo(a)); }
DI float q_hi(u32 a) { return qp(bhi(a)); }
DI float q_fma(float a, float b, float c) { return qp(__builtin_fmaf(a, b, c)); }
DI float q_fmas(float a, float b, float c) { return qp(__builtin_fmaf(a, b, c)); }
DI float q_mul(float a, float b) { return qp(a * b); }
DI float q_mulk(float a) { return qp(a * -1.4426950408889634f); }
DI float q_add1(float a) { return qp(a + 1.f); }
DI float q_exp(float a) { return qp(__builtin_amdgcn_exp2f(a)); }
DI float q_rcp(float a) { return qp(__builtin_amdgcn_rcpf(a)); }
DI u32 q_cvt(float a, float b) { u32 r = pk(a, b); PIN(r); return r; }
DI float q_mov(float a) { return qp(a); }
DI u32 opq(u32 v) { asm volatile("" : "+v"(v)); return v; }

DI float sel(u64 m, float a, float b) { return __builtin_amdgcn_inverse_ballot_w64(m) ? a : b; }
DI v2u attend(v2u kp, v2u vp, v2u kc, v2u vc, v2u q, u64 m0, u64 m1, u32 hm, bool noprev) {
  const f32x4 z = {0.f, 0.f, 0.f, 0.f};
  const f32x4 sp = mfma16(kp, q, z);
  const f32x4 sc = mfma16(kc, q, z);
  const float K2 = 0.25f * 1.4426950408889634f;
  const u64 par = 0xaaaaaaaaaaaaaaaaull;
  float p0 = sel(par, sp[1], sp[0]), p1 = sel(par, sp[3], sp[2]);
  if (noprev) { p0 = -INFINITY; p1 = -INFINITY; }
  const float t0 = sel(m0, sel(par, sc[1], sc[0]), p0), t1 = sel(m1, sel(par, sc[3], sc[2]), p1);
  const float m = x32_max(x16_max(fmaxf(t0, t1)));
  const float mk = -m * K2;
  const float e0 = __builtin_amdgcn_exp2f(__builtin_fmaf(t0, K2, mk)), e1 = __builtin_amdgcn_exp2f(__builtin_fmaf(t1, K2, mk));
  const u32 w0 = pk(e0, e0) & hm, w1 = pk(e1, e1) & hm;
  v4u pf;
  pf.x = __builtin_amdgcn_inverse_ballot_w64(m0) ? 0u : w0; pf.y = __builtin_amdgcn_inverse_ballot_w64(m1) ? 0u : w1;
  pf.z = __builtin_amdgcn_inverse_ballot_w64(m0) ? w0 : 0u; pf.w = __builtin_amdgcn_inverse_ballot_w64(m1) ? w1 : 0u;
  const f32x4 o = mfma32(cat(vp, vc), pf, z);
  float sum = e0 + e1;
  sum = sw16_add(sum, sum); sum = sw32_add(sum, sum);
  return pk4(o * __builtin_amdgcn_rcpf(sum));
}

template <int N> using IC = std::integral_constant<int, N>;
template <int I, int N, class F> DI void sfor(F&& f) { if constexpr (I < N) { f(IC<I>{}); sfor<I + 1, N>(f); } }

__global__ void __launch_bounds__(512, 2) fused_block(Params p) {
  extern __shared__ __attribute__((aligned(16))) char smem[];
  LAS char* L = (LAS char*)smem;
  const int tid = threadIdx.x, lane = tid & 63, w = __builtin_amdgcn_readfirstlane(tid >> 6);
  const int c16 = lane & 15, g4 = lane >> 4;
  const int b = blockIdx.x;
  int g, j;
  if (XMAP == 0) { g = (b & 7) * 4 + ((b >> 3) & 3); j = b >> 5; }
  else if (XMAP == 2) { const int x = b & 7, r = b >> 3; g = (x >> 1) * 8 + (r & 7); j = (x & 1) * 4 + (r >> 3); }
  else { const int x = b & 7, r = b >> 3; g = (x >> 2) * 16 + (r & 15); j = (x & 3) * 2 + (r >> 4); }
  const rsrc_t rX = __builtin_amdgcn_make_buffer_rsrc((void*)p.x, 0, S_ * D_ * 2, 0x00020000);
  const rsrc_t rQKV = __builtin_amdgcn_make_buffer_rsrc((void*)p.wqkv, 0, D_ * 3 * D_ * 2, 0x00020000);
  const rsrc_t rWO = __builtin_amdgcn_make_buffer_rsrc((void*)p.wo, 0, D_ * D_ * 2, 0x00020000);
  const rsrc_t rW1 = __builtin_amdgcn_make_buffer_rsrc((void*)p.w1, 0, D_ * F_ * 2, 0x00020000);
  const rsrc_t rW3 = __builtin_amdgcn_make_buffer_rsrc((void*)p.w3, 0, D_ * F_ * 2, 0x00020000);
  const rsrc_t rW2 = __builtin_amdgcn_make_buffer_rsrc((void*)p.w2, 0, F_ * D_ * 2, 0x00020000);
  const rsrc_t rC = __builtin_amdgcn_make_buffer_rsrc((void*)p.c, 0, D_ * 2, 0x00020000);
  const rsrc_t rMOD = __builtin_amdgcn_make_buffer_rsrc((void*)p.wmod, 0, D_ * 6 * D_ * 2, 0x00020000);
  const rsrc_t rWS = __builtin_amdgcn_make_buffer_rsrc((void*)p.ws, 0, WS_BYTES, 0x00020000);
  const rsrc_t rOUT = __builtin_amdgcn_make_buffer_rsrc((void*)p.out, 0, S_ * D_ * 2, 0x00020000);
  STAMP(0);

  u32 ep;
  { const u32 vz = 0; asm volatile("buffer_load_dword %0, %1, %2, 0 offen sc1" : "=v"(ep) : "v"(vz), "s"(rWS)); }
  u32 vl16 = lane * 16; PIN(vl16);
  const int bm = b < 192 ? b : b - 192, mq = bm / 48, cl = bm - 48 * mq;
  v4u wm[4]; v2u cv; u32 ep0;
  if (w < 4) {
    const u32 vm = (tid >> 3) * 24576 + (tid & 7) * 16, s0 = mq * (128 * 6144) + cl * 128, s1 = s0 + 6144, s2 = s0 + 12288, s3 = s0 + 18432;
    LD16(wm[0], vm, rMOD, s0, 0); LD16(wm[1], vm, rMOD, s1, 0); LD16(wm[2], vm, rMOD, s2, 0); LD16(wm[3], vm, rMOD, s3, 0);
    const u32 vc = (tid >> 3) * 8, scq = mq * 256;
    LD8(cv, vc, rC, scq, 0);
    const u32 vz = 0; asm volatile("buffer_load_dword %0, %1, %2, 0 offen sc1" : "=v"(ep0) : "v"(vz), "s"(rWS));
  }
  if (tid < 16) *(LAS u32*)(L + L_CNT + tid * 4) = 0u;
  BAR();
  const int st_qd = lane >> 2, st_r = (st_qd & 3) + 4 * (st_qd >> 3);
  const int st_col = ((st_qd >> 2) & 1) * 64 + (((lane & 3) ^ ((w & 1) << 1)) << 4);
  const int rotp = g & 7;
  u32 vB1 = st_r * 3072 + st_col; PIN(vB1);
  const u32 mB1 = (w >> 2) * 12288 + (w & 3) * 1024, sB1 = (w >> 2) * 98304 + (w & 3) * 24576 + j * 128;
  auto stage_b1 = [&](auto PC, auto BC) {
    constexpr int pair = decltype(PC)::value, base = decltype(BC)::value;
    const u32 sp = ((pair + rotp) & 7) * 196608 + sB1;
    dma<base, 0>(mB1, vB1, rQKV, sp); dma<base + 4096, 1024>(mB1, vB1, rQKV, sp); dma<base + 8192, 2048>(mB1, vB1, rQKV, sp);
  };
  auto b1_piece = [&](auto PC, auto BC, auto TC) {
    constexpr int pair = decltype(PC)::value, base = decltype(BC)::value, ty = decltype(TC)::value;
    dma<base + ty * 4096, ty * 1024>(mB1, vB1, rQKV, ((pair + rotp) & 7) * 196608 + sB1);
  };
  const int hd = w & 3, blk2 = w >> 2, kq = w & 3, mh = w >> 2;
  const int cs0 = j < 4 ? 22 * j : 88 + 21 * (j - 4);
  const int cend = cs0 + (j < 4 ? 22 : 21);
  STAMP(1);
  const int T0 = 12 * (g >> 4) + 3 * (w & 3), rcb = min(cs0 + T0, 168);
  if (w < 4) {
    asm volatile("s_waitcnt vmcnt(0)" : "+v"(cv), "+v"(wm[0]), "+v"(wm[1]), "+v"(wm[2]), "+v"(wm[3]), "+v"(ep0)::"memory");
    const u32 tg = __builtin_amdgcn_readfirstlane(ep0) + 1u;
    const f32x4 cf = {blo(cv.x), bhi(cv.x), blo(cv.y), bhi(cv.y)};
    float sc[4];
#pragma unroll
    for (int r = 0; r < 4; ++r) sc[r] = cf[r] * __builtin_amdgcn_rcpf(1.f + __builtin_amdgcn_exp2f(cf[r] * -1.4426950408889634f));
    f32x2 v[4];
#pragma unroll
    for (int i = 0; i < 4; ++i) v[i] = (unp(wm[0][i]) * sc[0] + unp(wm[1][i]) * sc[1]) + (unp(wm[2][i]) * sc[2] + unp(wm[3][i]) * sc[3]);
    const float a0 = sw32_add(v[0].x, v[2].x), a1 = sw32_add(v[0].y, v[2].y), a2 = sw32_add(v[1].x, v[3].x), a3 = sw32_add(v[1].y, v[3].y);
    float t0 = sw16_add(a0, a2), t1 = sw16_add(a1, a3);
    t0 += dppf<0x128>(t0); t1 += dppf<0x128>(t1);
    f32x2 o = {t0, t1};
    *(LAS f32x2*)(L + L_RED + w * 256 + (lane & 7) * 32 + g4 * 8) = o;
    LGW();
    u32 old = 0;
    if (lane == 0) old = __hip_atomic_fetch_add((LAS u32*)(L + L_CNT + 28), 1u, __ATOMIC_RELAXED, __HIP_MEMORY_SCOPE_WORKGROUP);
    if (__builtin_amdgcn_readfirstlane(old) == 3u) {
      asm volatile("" ::: "memory");
      float s = 0.f;
#pragma unroll
      for (int i = 0; i < 4; ++i) s += *(LAS float*)(L + L_RED + i * 256 + lane * 4);
      v2u gr; gr.x = asu(s); gr.y = tg;
      if (b < 192) __builtin_amdgcn_raw_buffer_store_b64(gr, rWS, lane * 8, WS_MODG + (mq * 3072 + cl * 64) * 8, 16);
      __builtin_amdgcn_raw_buffer_store_b32(tg, rWS, 0, WS_ALIVE + b * 4, 16);
    }
  }
  STAMP(2);
  v4u xr[10];
  {
    const int r0 = 64 * g - 16 + w;
    const u32 s0 = (u32)max(r0, 0) * 1024u, s1 = (u32)max(r0 + 8, 0) * 1024u;
    LD16(xr[0], vl16, rX, s0, 0); LD16(xr[1], vl16, rX, s1, 0);
#pragma unroll
    for (int i = 2; i < 10; ++i) { const u32 so = (u32)(r0 + 8 * i) * 1024u; LD16(xr[i], vl16, rX, so, 0); }
  }
  stage_b1(IC<0>{}, IC<L_XY>{});
  int afo[4];
#pragma unroll
  for (int i = 0; i < 4; ++i) { afo[i] = blk2 * 32768 + c16 * 1024 + (((4 * i + g4) ^ c16) << 4); PIN(afo[i]); }
  const u64 am0 = __builtin_amdgcn_ballot_w64((c16 & 14) >= 4 * g4), am1 = __builtin_amdgcn_ballot_w64((c16 & 14) >= 4 * g4 + 2);
  u32 ahm = (lane & 1) ? 0xffff0000u : 0xffffu; PIN(ahm);
  u32 vOst = c16 * 16 + g4 * 256 + (hd & 1) * 8; PIN(vOst);
  int tB1 = g4 * 1024 + 64 * (c16 >> 2) + 8 * (lane & 3) + 32 * ((hd & 1) ^ (g4 & 1)) + 256 * (hd >> 1); PIN(tB1);
  int hw0 = w * 1024 + ((((lane - 8 * rotp) & 63) ^ w) << 4); PIN(hw0);
#define vfl ((opq(vl16) & 112u) << 3)
  STAMP(3);
  v4u bw[48];
  auto read_b1 = [&](int pair, int base, int off) {
#pragma unroll
    for (int kk = 0; kk < 2; ++kk)
#pragma unroll
      for (int ty = 0; ty < 3; ++ty) {
        LAS char* a = L + base + (off + kk * 12288 + ty * 4096);
        bw[(2 * pair + kk) * 3 + ty] = cat(trd(a), trd(a + 512));
      }
  };
  f32x2 xn[10][4];
  v4u ga_[4]; v2u pv;
  const u32 sMG = WS_MODG + w * 1024;
  {
    float u[10];
    sfor<0, 10>([&](auto I) {
      constexpr int i = decltype(I)::value;
      if constexpr (i == 0) asm volatile("s_waitcnt vmcnt(11)" : "+v"(xr[0]), "+v"(xr[1]), "+v"(ep)::"memory");
      if constexpr (i == 2) asm volatile("s_waitcnt vmcnt(9)" : "+v"(xr[2]), "+v"(xr[3])::"memory");
      if constexpr (i == 4) asm volatile("s_waitcnt vmcnt(10)" : "+v"(xr[4]), "+v"(xr[5])::"memory");
      if constexpr (i == 6) asm volatile("s_waitcnt vmcnt(8)" : "+v"(xr[6]), "+v"(xr[7])::"memory");
      if constexpr (i == 8) asm volatile("s_waitcnt vmcnt(9)" : "+v"(xr[8]), "+v"(xr[9])::"memory");
      float s = 0.f, q = 0.f;
#pragma unroll
      for (int e = 0; e < 4; ++e) { s = dot2(xr[i][e], 0x3f803f80u, s); q = dot2(xr[i][e], xr[i][e], q); }
      u[i] = sw32_add(s, q);
      PIN(u[i]);
      if constexpr (i == 3) stage_b1(IC<1>{}, IC<L_XY + 24576>{});
      if constexpr (i == 7) stage_b1(IC<2>{}, IC<L_XY + 49152>{});
      if constexpr (i == 9) {
        const u32 s0 = sMG, s1 = sMG + 24576, s2 = sMG + 49152, s3 = sMG + 73728;
        ld16s<0>(ga_[0], vl16, rWS, s0); ld16s<0>(ga_[1], vl16, rWS, s1); ld16s<0>(ga_[2], vl16, rWS, s2); ld16s<0>(ga_[3], vl16, rWS, s3);
      }
    });
    float t[5];
#pragma unroll
    for (int i = 0; i < 5; ++i) t[i] = row16_sum(sw16_add(u[i], u[i + 5]));
    float v = t[0];
#pragma unroll
    for (int i = 1; i < 5; ++i) v = c16 == i ? t[i] : v;
    auto r = __builtin_amdgcn_permlane32_swap(asu(v), asu(v), false, false);
    const float mean = asf(r[0]) * (1.f / 512.f);
    const float var = fmaxf(asf(r[1]) * (1.f / 512.f) - mean * mean, 0.f);
    float rs = __builtin_amdgcn_rsqf(var + 1e-5f), nm = -mean * rs;
    PIN(rs); PIN(nm);
    STAMP(4);
    { const u32 vo = ((lane >> 3) & 3) * 24576 + (lane & 7) * 8, so = WS_MODG + (1024 + 8 * b) * 8; LD8S(pv, vo, rWS, so, 0); }
    if (w >= 4) {
      const int u = lane >> 2, row = (u & 3) | ((u >> 1) & 4) | ((u << 1) & 8);
      const u32 vo = row * 2752 + (lane & 3) * 16, so = (g & 15) * (32 * 2752) + rcb * 16, mb = L_Z + 49152 + (w & 3) * 4096;
      dma<0, 0>(mb, vo, rW1, so); dma<1024, 16 * 2752>(mb, vo, rW1, so); dma<2048, 0>(mb, vo, rW3, so); dma<3072, 16 * 2752>(mb, vo, rW3, so);
    } else {
      const int u = lane >> 1, row = (u & 3) | ((u >> 1) & 4) | ((u << 1) & 8) | (u & 16);
      const u32 vo = row * 1024 + (lane & 1) * 16, so = g * 32768 + j * 128 + w * 32, mb = L_Z + 65536 + w * 2048;
      dma<0, 0>(mb, vo, rW2, so); dma<1024, 0>(mb, vo, rW2, so + (g < 11 ? 32 * 32768 : 0));
      const int rwo = (u & 7) | ((u >> 1) & 8) | ((u << 1) & 16);
      dma<0, 0>(76800 + w * 1024, rwo * 1024 + (lane & 1) * 16, rWO, (g & 15) * 32768 + j * 128 + w * 32);
    }
    sfor<0, 10>([&](auto I) {
      constexpr int i = decltype(I)::value;
      if constexpr (i == 2) stage_b1(IC<3>{}, IC<L_Z>{});
      if constexpr (i == 6) stage_b1(IC<4>{}, IC<L_Z + 24576>{});
      const float a = rdl(rs, (i / 5) * 16 + i % 5), c = rdl(nm, (i / 5) * 16 + i % 5);
#pragma unroll
      for (int e = 0; e < 4; ++e) { xn[i][e] = unp(xr[i][e]) * a + c; PIN(xn[i][e]); }
    });
  }
  const u32 tag = __builtin_amdgcn_readfirstlane(ep) + 1u;
  u32 m1; asm("s_lshr_b32 %0, 1, %1" : "=s"(m1) : "s"(w) : "scc");
#define RAISE(fo) raise_flag(rWS, (fo), tag, vl16, m1)
#define SIGNAL(idx, fo) signal_flag(rWS, L_CNT + 4 * (idx), (fo), tag)
  STAMP(5);
  asm volatile("s_waitcnt vmcnt(9)" : "+v"(ga_[0]), "+v"(ga_[1]), "+v"(ga_[2]), "+v"(ga_[3]), "+v"(pv)::"memory");
  auto gsum = [&](v4u (&a)[4], f32x2& o) -> u32 {
    o.x = (asf(a[0].x) + asf(a[1].x)) + (asf(a[2].x) + asf(a[3].x)); o.y = (asf(a[0].z) + asf(a[1].z)) + (asf(a[2].z) + asf(a[3].z));
    return ((a[0].y ^ tag) | (a[0].w ^ tag) | (a[1].y ^ tag) | (a[1].w ^ tag)) | ((a[2].y ^ tag) | (a[2].w ^ tag) | (a[3].y ^ tag) | (a[3].w ^ tag));
  };
  auto gather = [&](u32 voff, u32 soff, f32x2& o) {
    for (u32 sp = 0; sp < SPIN_CAP; ++sp) {
      asm volatile("" ::: "memory");
      v4u a[4];
#pragma unroll
      for (int q = 0; q < 4; ++q) a[q] = __builtin_amdgcn_raw_buffer_load_b128(rWS, voff, soff + q * 24576, 16);
      if (!__builtin_amdgcn_ballot_w64(gsum(a, o) != 0u)) break;
    }
  };
  const float one = w >= 4 ? 1.f : 0.f;
  {
    f32x2 mA;
    if (__builtin_amdgcn_ballot_w64(gsum(ga_, mA) != 0u)) gather(vl16, sMG, mA);
    *(LAS f32x2*)(L + L_MODA + w * 512 + (vl16 >> 1)) = mA + one;
  }
  BAR();
  if (w == 7) {
    const u32 vo = ((lane >> 3) & 3) * 24576 + (lane & 7) * 8, so = WS_MODG + (1024 + 8 * b) * 8;
    for (u32 sp = 0; sp < SPIN_CAP && __builtin_amdgcn_ballot_w64(pv.y != tag); ++sp) {
      asm volatile("" ::: "memory");
      pv = __builtin_amdgcn_raw_buffer_load_b64(rWS, vo, so, 16);
    }
    float sv = asf(pv.x);
    sv += dppf<0x128>(sv); sv = sw16_add(sv, sv);
    pv.x = asu(sv);
    __builtin_amdgcn_raw_buffer_store_b64(pv, rWS, (lane & 7) * 8, WS_MODF + (1024 + 8 * b) * 8, 16);
  }
  STAMP(6);
  int vma = L_MODA + 2 * vl16; PIN(vma);
  const f32x4 ms0 = *(LAS f32x4*)(L + vma), ms1 = *(LAS f32x4*)(L + vma + 16);
  const f32x4 ma0 = *(LAS f32x4*)(L + vma + 2048), ma1 = *(LAS f32x4*)(L + vma + 2064);
  int tz0 = tB1 + L_Z; PIN(tz0);
  { int t2 = tB1 + 49152; PIN(t2); read_b1(0, tB1, 0); read_b1(1, tB1, 24576); read_b1(2, t2, 0); }
  {
    VMW(6);
    if (w >= 4) {
      LAS char* a0 = L + L_Z + 49152 + (w & 3) * 4096 + ((lane >> 1) & 1) * 2048 + 64 * ((c16 >> 2) + 4 * (g4 & 1) + 16 * (g4 >> 1)) + 8 * (lane & 1);
#pragma unroll
      for (int ct = 0; ct < 3; ++ct) {
        const int c = min(cs0 + T0 + ct - rcb, 3) * 16;
        const v4u f = cat(trd(a0 + c), trd(a0 + c + 512));
        const u32 sf = WS_B4T + ((j * 16 + (g & 15)) * 24 + T0 + ct) * 1024;
        asm volatile("buffer_store_dwordx4 %0, %1, %2, %3 offen sc1\n\ts_nop 1" ::"v"(f), "v"(vl16), "s"(rWS), "s"(sf) : "memory");
      }
    }
    else {
      LAS char* a0 = L + L_Z + 65536 + w * 2048 + 32 * ((c16 >> 2) + 4 * (g4 & 1) + 16 * (g4 >> 1)) + 8 * (lane & 3);
#pragma unroll
      for (int i = 0; i < 2; ++i) {
        const v4u f = cat(trd(a0 + i * 1024), trd(a0 + i * 1024 + 256));
        const u32 sf = WS_B5T + ((j * 43 + g + (i && g < 11 ? 32 : 0)) * 4 + w) * 1024;
        asm volatile("buffer_store_dwordx4 %0, %1, %2, %3 offen sc1\n\ts_nop 1" ::"v"(f), "v"(vl16), "s"(rWS), "s"(sf) : "memory");
      }
      LAS char* a1 = L + 76800 + w * 1024 + 32 * ((c16 >> 2) + 4 * (g4 & 1) + 16 * (g4 >> 1)) + 8 * (lane & 3);
      const v4u f = cat(trd(a1), trd(a1 + 256));
      const u32 sf = WS_B3T + ((j * 16 + (g & 15)) * 4 + w) * 1024;
      asm volatile("buffer_store_dwordx4 %0, %1, %2, %3 offen sc1\n\ts_nop 1" ::"v"(f), "v"(vl16), "s"(rWS), "s"(sf) : "memory");
    }
  }
  BAR();
  STAMP(7);
  {
    int hw1 = (hw0 ^ 128) + 8192; PIN(hw1);
    sfor<0, 10>([&](auto I) {
      constexpr int i = decltype(I)::value;
      v4u o;
      o.x = pk2(xn[i][0] * ma0.xy + ms0.xy); o.y = pk2(xn[i][1] * ma0.zw + ms0.zw);
      o.z = pk2(xn[i][2] * ma1.xy + ms1.xy); o.w = pk2(xn[i][3] * ma1.zw + ms1.zw);
      if constexpr (i == 8) { hw0 += 65536; hw1 += 65536; }
      *(LAS v4u*)(L + ((i & 1) ? hw1 : hw0) + ((i >> 1) & 3) * 16384) = o;
      SCHED();
    });
  }
  VMW(0);
  BAR();
  RAISE(WS_RFLAG + (j * 32 + g) * 4);
  u32 flr;
  STAMP(8);

  const f32x4 z4 = {0.f, 0.f, 0.f, 0.f};
#define AFRAG(off, ks) (*(LAS v4u*)(L + afo[(ks) & 3] + ((off) + ((ks) >> 2) * 256)))
  const int ch0 = cs0 + 3 * w;
  constexpr int ND = NODMA ? 0 : 1;
  v4u b4[48];
  const u32 sB4 = WS_B4T + (j * 16 * 24 + 3 * w) * 1024;
  auto load_b4 = [&](auto NC) {
    constexpr int n = decltype(NC)::value, ks = n / 3, ct = n % 3;
    const u32 so = sB4 + ((ks + 2 * rotp) & 15) * 24576;
    v4u& r = b4[n];
    if constexpr (ct == 0) LD16S(r, vl16, rWS, so, 0); else if constexpr (ct == 1) LD16S(r, vl16, rWS, so, 1024); else LD16S(r, vl16, rWS, so, 2048);
  };
  auto landed_b4 = [&](auto AC, auto BC) {
#pragma unroll
    for (int i = decltype(AC)::value; i < decltype(BC)::value; ++i) { v4u& r = b4[i]; PIN(r); }
  };
  v4u gb_, gg_;
  u32 vgg, fl0, vRow;
  int pw, pr;
  f32x4 aKh = z4, aVh = z4, aQ = z4, aK = z4, aV = z4, bQ = z4, bK = z4, bV = z4;
  {
    v4u ah = AFRAG(0, 0), a1 = AFRAG(16384, 0), a2 = AFRAG(32768, 0);
    int l3, c3, g3; f32x2 mB, mG;
    auto job = [&](auto NC) {
      constexpr int n = decltype(NC)::value;
      if constexpr (n == 3) { l3 = opq(vl16) >> 4; c3 = l3 & 15; g3 = l3 >> 4; PIN(c3); PIN(g3); vgg = (vl16 < 512 ? 8192 : 20480 - 512) + 512 * j + vl16; PIN(vgg); }
      if constexpr (n == 7) { vRow = (8 * w + (l3 >> 3)) * 1024 + (l3 & 7) * 16; PIN(vRow); }
      if constexpr (n == 8) { pr = (8 * w + (l3 >> 3)) * 256 + (((2 * (l3 & 7)) ^ ((l3 >> 3) & 7)) << 4); PIN(pr); }
      if constexpr (n == 10) { pw = kq * 16384 + (32 * mh + c3) * 256 + ((g3 ^ (c3 & 3)) << 4) + ((c3 & 4) << 4); PIN(pw); }
      if constexpr (n == 31) { mB.x = asf(gb_.x) + one; PIN(mB.x); mB.y = asf(gb_.z) + one; PIN(mB.y); *(LAS f32x2*)(L + L_MODA + w * 512 + (vl16 >> 1)) = mB; }
      if constexpr (n == 32) { mG.x = asf(gg_.x); mG.y = asf(gg_.z); *(LAS f32x2*)(L + L_GAB + (vl16 >> 1)) = mG; }
      if constexpr (n == 34) { fl0 = (gb_.y ^ tag) | (gb_.w ^ tag) | (gg_.y ^ tag) | (gg_.w ^ tag); PIN(fl0); }
      if constexpr (n == 7) { const u32 s0 = WS_MODF + 12288 + w * 1024; LD16S(gb_, vl16, rWS, s0, 0); }
      if constexpr (n == 9) { const u32 s0 = WS_MODF; LD16S(gg_, vgg, rWS, s0, 0); }
      if constexpr (n < 6 && !(n & 1)) b1_piece(IC<5>{}, IC<L_Z + 49152>{}, IC<n / 2>{});
      if constexpr (n == 6) arrive(L_CNT + 8);
      if constexpr (n == 10) await8(L_CNT + 8);
      if constexpr (n >= 10 && n < 22 && !(n & 1)) b1_piece(IC<6 + (n - 10) / 6>{}, IC<L_Z + ((n - 10) / 6) * 24576>{}, IC<((n - 10) / 2) % 3>{});
      if constexpr (n == 21) { const u32 vr = (vl16 >> 2) & 124, sr = WS_RFLAG + j * 128; LD4S(flr, vr, rWS, sr); }
      if constexpr (n == 26) {
        asm volatile("s_waitcnt vmcnt(0)" : "+v"(gb_), "+v"(gg_), "+v"(flr)::"memory");
        arrive(L_CNT + 12);
        const u32 vr = (vl16 >> 2) & 124, sr = WS_RFLAG + j * 128; u32 cnt = SPIN_CAP;
        asm volatile("1:\n\tv_cmp_ne_u32 vcc, %5, %0\n\ts_cbranch_vccz 2f\n\tbuffer_load_dword %0, %2, %3, %4 offen sc1\n\ts_waitcnt vmcnt(0)\n\t"
                     "s_sub_u32 %1, %1, 1\n\ts_cbranch_scc0 1b\n\t2:" : "+v"(flr), "+s"(cnt) : "v"(vr), "s"(rWS), "s"(sr), "s"(tag) : "vcc", "scc", "memory");
      }
      if constexpr (n == 28) { await8(L_CNT + 12); int tz2 = tz0 + 49152; PIN(tz2); read_b1(5, tz2, 0); }
      if constexpr (n == 32) read_b1(6, tz0, 0);
      if constexpr (n == 37) read_b1(7, tz0, 24576);
      if constexpr (n >= 27 && n < 47 && (n & 1)) load_b4(IC<(n - 27) / 2>{});
    };
    sfor<0, 16>([&](auto KS) {
      constexpr int ks = decltype(KS)::value;
      KSTAMP(1, ks);
      if constexpr (ks == 0) read_b1(3, tz0, 0);
      if constexpr (ks == 1) read_b1(4, tz0, 24576);
      SCHED();
      v4u nh = ah, n1 = a1, n2 = a2;
      aKh = mfma32(bw[ks * 3 + 1], ah, aKh);
      aVh = mfma32(ah, bw[ks * 3 + 2], aVh);
      if constexpr (ks < 15) nh = AFRAG(0, ks + 1);
      job(IC<3 * ks>{});
      SCHED();
      aQ = mfma32(bw[ks * 3 + 0], a1, aQ);
      aK = mfma32(bw[ks * 3 + 1], a1, aK);
      aV = mfma32(a1, bw[ks * 3 + 2], aV);
      if constexpr (ks < 15) n1 = AFRAG(16384, ks + 1);
      job(IC<3 * ks + 1>{});
      SCHED();
      bQ = mfma32(bw[ks * 3 + 0], a2, bQ);
      bK = mfma32(bw[ks * 3 + 1], a2, bK);
      bV = mfma32(a2, bw[ks * 3 + 2], bV);
      if constexpr (ks < 15) n2 = AFRAG(32768, ks + 1);
      job(IC<3 * ks + 2>{});
      ah = nh; a1 = n1; a2 = n2;
      SCHED();
    });
  }
  STAMP(9); KSTAMP(1, 16);
  v4u b3[16];
  auto load_b3 = [&](auto NC) { constexpr int n = decltype(NC)::value; ld16s<(n & 3) * 1024>(b3[n], vl16, rWS, WS_B3T + (j * 16 + 4 * kq + (n >> 2)) * 4096); };
  {
    const u32 sOst = WS_O + ((g * 4 + 2 * blk2) * 16 + 2 * j + (hd >> 1)) * 1024;
    const v2u k1p = pk4(aK), v1p = pk4(aV);
    sfor<0, 6>(load_b3);
    const v2u o0 = attend(pk4(aKh), pk4(aVh), k1p, v1p, pk4(aQ), am0, am1, ahm, g == 0 && blk2 == 0);
    const v2u o1 = attend(k1p, v1p, pk4(bK), pk4(bV), pk4(bQ), am0, am1, ahm, false);
    __builtin_amdgcn_raw_buffer_store_b64(o0, rWS, vOst, sOst, 16);
    __builtin_amdgcn_raw_buffer_store_b64(o1, rWS, vOst, sOst + 16384, 16);
  }
  if (__builtin_amdgcn_ballot_w64(fl0 != 0u)) {
    for (u32 sp = 0; sp < SPIN_CAP; ++sp) {
      asm volatile("" ::: "memory");
      gb_ = __builtin_amdgcn_raw_buffer_load_b128(rWS, vl16, WS_MODF + 12288 + w * 1024, 16);
      gg_ = __builtin_amdgcn_raw_buffer_load_b128(rWS, vgg, WS_MODF, 16);
      if (!__builtin_amdgcn_ballot_w64(((gb_.y ^ tag) | (gb_.w ^ tag) | (gg_.y ^ tag) | (gg_.w ^ tag)) != 0u)) break;
    }
    const f32x2 mB = {asf(gb_.x) + one, asf(gb_.z) + one}, mG = {asf(gg_.x), asf(gg_.z)};
    *(LAS f32x2*)(L + L_MODA + w * 512 + (vl16 >> 1)) = mB;
    *(LAS f32x2*)(L + L_GAB + (vl16 >> 1)) = mG;
  }
  STAMP(10);
  VMW(0);
  landed_b4(IC<0>{}, IC<10>{});
  BAR();
  RAISE(flag_off(0, g, 0) + j * 128);
  STAMP(11);

  v4u xv;
  { const u32 sx = g * 65536 + j * 128; LD16(xv, vRow, rX, sx, 0); }
  sfor<6, 12>(load_b3);
  const u64 care2 = 0x0101010101010101ull * (3u << (2 * kq));
  poll_flags(rWS, vfl, flag_off(0, g, 0), care2, tag);
  PIN(xv);
  sfor<12, 16>(load_b3);
  v4u af[8];
  {
    const u32 so = WS_O + ((g * 4 + 2 * mh) * 16 + 4 * kq) * 1024, so1 = so + 16384;
    LD16S(af[0], vl16, rWS, so, 0); LD16S(af[1], vl16, rWS, so, 1024); LD16S(af[2], vl16, rWS, so, 2048); LD16S(af[3], vl16, rWS, so, 3072);
    LD16S(af[4], vl16, rWS, so1, 0); LD16S(af[5], vl16, rWS, so1, 1024); LD16S(af[6], vl16, rWS, so1, 2048); LD16S(af[7], vl16, rWS, so1, 3072);
  }
  STAMP(12);
  sfor<0, 2>([&](auto M) {
    constexpr int m = decltype(M)::value;
    asm volatile("s_waitcnt vmcnt(%c4)" : "+v"(af[4 * m]), "+v"(af[4 * m + 1]), "+v"(af[4 * m + 2]), "+v"(af[4 * m + 3]) : "i"(m == 0 ? 4 : 2) : "memory");
    if constexpr (m == 0) {
#pragma unroll
      for (int i = 0; i < 16; ++i) PIN(b3[i]);
    }
    f32x4 acc[4] = {z4, z4, z4, z4};
    sfor<0, 4>([&](auto SC) {
      constexpr int sx = decltype(SC)::value;
#pragma unroll
      for (int n = 0; n < 4; ++n) acc[n] = mfma32(b3[sx * 4 + n], af[4 * m + sx], acc[n]);
      if constexpr (sx & 1) load_b4(IC<10 + 2 * m + (sx >> 1)>{});
      SCHED();
    });
#pragma unroll
    for (int n = 0; n < 4; ++n) *(LAS f32x4*)(L + ((n & 1) ? (pw ^ 64) : pw) + (m * 4096 + (n >> 1) * 128)) = acc[n];
  });
  BAR();
  STAMP(13);
  sfor<14, 18>(load_b4);
  {
    f32x4 y0 = *(LAS f32x4*)(L + pr), y1 = *(LAS f32x4*)(L + (pr ^ 16));
#pragma unroll
    for (int q = 1; q < 4; ++q) { y0 += *(LAS f32x4*)(L + pr + q * 16384); y1 += *(LAS f32x4*)(L + (pr ^ 16) + q * 16384); }
    const int ga = L_GAB + ((vl16 & 112) << 1);
    const f32x4 g0 = *(LAS f32x4*)(L + ga), g1 = *(LAS f32x4*)(L + ga + 16);
    const f32x4 x0 = f32x4{blo(xv.x), bhi(xv.x), blo(xv.y), bhi(xv.y)} + g0 * y0, x1 = f32x4{blo(xv.z), bhi(xv.z), blo(xv.w), bhi(xv.w)} + g1 * y1;
    { const v4u o = cat(pk4(x0), pk4(x1)); const u32 so = WS_X1 + g * 65536 + j * 128;
      asm volatile("buffer_store_dwordx4 %0, %1, %2, %3 offen sc1\n\ts_nop 1" ::"v"(o), "v"(vRow), "s"(rWS), "s"(so) : "memory"); }
    const f32x4 sv = x0 + x1;
    float mean = (sv.x + sv.y) + (sv.z + sv.w);
    mean += dppf<0xB1>(mean); mean += dppf<0x4E>(mean); mean += dppf<0x141>(mean);
    mean *= 1.f / 64.f;
    const f32x4 d0 = x0 - mean, d1 = x1 - mean, dd = d0 * d0 + d1 * d1;
    float m2 = (dd.x + dd.y) + (dd.z + dd.w);
    m2 += dppf<0xB1>(m2); m2 += dppf<0x4E>(m2); m2 += dppf<0x141>(m2);
    v2u st; st.x = asu(mean); st.y = asu(m2);
    __builtin_amdgcn_raw_buffer_store_b64(st, rWS, (vRow >> 10) << 6, WS_STAT + g * 4096 + j * 8, 16);
  }
  STAMP(14);
  VMW(0);
  landed_b4(IC<10>{}, IC<18>{});
  BAR();
  RAISE(flag_off(1, g, 0) + j * 128);
  sfor<18, 24>(load_b4);
  STAMP(15);

  v4u x1v[4]; v2u stv;
  auto ln2_load = [&](int h) {
    const u32 so = WS_X1 + g * 65536 + (32 * (w >> 2) + 16 * h + 4 * (w & 3)) * 1024;
    LD16S(x1v[0], vl16, rWS, so, 0); LD16S(x1v[1], vl16, rWS, so, 1024); LD16S(x1v[2], vl16, rWS, so, 2048); LD16S(x1v[3], vl16, rWS, so, 3072);
    const u32 vs = (vl16 >> 1) & 255, ss = WS_STAT + g * 4096 + (32 * (w >> 2) + 16 * h + 4 * (w & 3)) * 64;
    LD8S(stv, vs, rWS, ss, 0);
  };
  float l2r, l2n;
  auto ln2_stats = [&]() {
    const float mj = asf(stv.x);
    float mean = mj;
    mean += dppf<0xB1>(mean); mean += dppf<0x4E>(mean); mean += dppf<0x141>(mean);
    mean *= 0.125f;
    const float dm = mj - mean;
    float m2 = asf(stv.y) + 64.f * dm * dm;
    m2 += dppf<0xB1>(m2); m2 += dppf<0x4E>(m2); m2 += dppf<0x141>(m2);
    l2r = __builtin_amdgcn_rsqf(m2 * (1.f / 512.f) + 1e-5f);
    l2n = -mean * l2r;
  };
  u32 vH;
  u32 hmask[3];
#pragma unroll
  for (int ct = 0; ct < 3; ++ct)
    asm("s_cmp_lt_u32 %1, %2\n\ts_cselect_b32 %0, -1, 0" : "=s"(hmask[ct]) : "s"((u32)(ch0 + ct)), "s"((u32)cend) : "scc");
  auto hh_store = [&](v2u o, int ct, int h) {
    const u32 so = WS_HH + ((g * 4 + h) * 172 + ch0 + ct) * 256;
    asm volatile("s_mov_b32 exec_lo, %4\n\ts_mov_b32 exec_hi, %4\n\tbuffer_store_dwordx2 %0, %1, %2, %3 offen sc1\n\ts_mov_b64 exec, -1"
                 ::"v"(o), "v"(vH), "s"(rWS), "s"(so), "s"(hmask[ct]) : "memory");
  };
  auto swiglu_tile = [&](f32x4 (&acc)[3][2], int ct, int h) {
    f32x4 G, U, R;
#pragma unroll
    for (int e = 0; e < 4; ++e) {
      auto r = __builtin_amdgcn_permlane32_swap(asu(acc[ct][0][e]), asu(acc[ct][1][e]), false, false);
      G[e] = asf(r[0]); U[e] = asf(r[1]);
    }
    const f32x4 T = G * -1.4426950408889634f;
#pragma unroll
    for (int e = 0; e < 4; ++e) R[e] = __builtin_amdgcn_rcpf(1.f + __builtin_amdgcn_exp2f(T[e]));
    hh_store(pk4((G * U) * R), ct, h);
  };
#pragma unroll
  for (int i = 0; i < 4; ++i) { afo[i] -= blk2 * 32768; PIN(afo[i]); }
  int vm2 = L_MODA + 2 * vl16; PIN(vm2);
  int h2w = w * 4096 + (((vl16 - 128 * rotp) & 1023) ^ ((4 * w & 15) << 4)); PIN(h2w);
  poll_flags(rWS, vfl, flag_off(1, g, 0), ~0ull, tag);
  landed_b4(IC<18>{}, IC<24>{});
  ln2_load(0);
  sfor<24, 28>(load_b4);
  {
    const f32x4 s0 = *(LAS f32x4*)(L + vm2), s1 = *(LAS f32x4*)(L + vm2 + 16), a0 = *(LAS f32x4*)(L + vm2 + 2048), a1 = *(LAS f32x4*)(L + vm2 + 2064);
    asm volatile("s_waitcnt vmcnt(%c5)" : "+v"(x1v[0]), "+v"(x1v[1]), "+v"(x1v[2]), "+v"(x1v[3]), "+v"(stv) : "i"(4) : "memory");
    ln2_stats();
#pragma unroll
    for (int i = 0; i < 4; ++i) {
      const float rs = rdl(l2r, 8 * i), nn = rdl(l2n, 8 * i);
      v4u o;
      o.x = pk2((unp(x1v[i].x) * rs + nn) * a0.xy + s0.xy); o.y = pk2((unp(x1v[i].y) * rs + nn) * a0.zw + s0.zw);
      o.z = pk2((unp(x1v[i].z) * rs + nn) * a1.xy + s1.xy); o.w = pk2((unp(x1v[i].w) * rs + nn) * a1.zw + s1.zw);
      *(LAS v4u*)(L + (h2w ^ (i << 4)) + i * 1024) = o;
    }
  }
  ln2_load(1);
  BAR();
  STAMP(16);
  f32x4 accA[3][2] = {{z4, z4}, {z4, z4}, {z4, z4}};
  {
    f32x4 cs, ca; float rsv[4], nnv, t[4]; v2u ov;
    auto ln_op = [&](auto NC) {
      constexpr int n = decltype(NC)::value, u = n / 16, k = n % 16, hf = u & 1, i = u >> 1;
      if constexpr (u < 8) {
        if constexpr (k == 0) {
          cs = *(LAS f32x4*)(L + vm2 + hf * 16); ca = *(LAS f32x4*)(L + vm2 + 2048 + hf * 16);
          if constexpr (hf == 0) nnv = q_mov(rdl(l2n, 8 * i));
        }
        else if constexpr (k == 1) t[0] = q_shl(x1v[i][2 * hf]);
        else if constexpr (k == 2) t[1] = q_hi(x1v[i][2 * hf]);
        else if constexpr (k == 3) t[2] = q_shl(x1v[i][2 * hf + 1]);
        else if constexpr (k == 4) t[3] = q_hi(x1v[i][2 * hf + 1]);
        else if constexpr (k < 9) t[k - 5] = q_fmas(t[k - 5], rsv[i], nnv);
        else if constexpr (k < 13) t[k - 9] = q_fma(t[k - 9], ca[k - 9], cs[k - 9]);
        else if constexpr (k == 13) ov.x = q_cvt(t[0], t[1]);
        else if constexpr (k == 14) ov.y = q_cvt(t[2], t[3]);
        else {
          *(LAS v2u*)(L + (h2w ^ (i << 4)) + (32768 + i * 1024 + hf * 8)) = ov;
        }
      }
    };
    v4u a0 = AFRAG(0, 0), a1 = AFRAG(16384, 0);
    sfor<0, 16>([&](auto KS) {
      constexpr int ks = decltype(KS)::value;
      KSTAMP(3, ks);
      v4u n0 = a0, n1 = a1;
      if constexpr (ks < 15) { n0 = AFRAG(0, ks + 1); n1 = AFRAG(16384, ks + 1); }
      if constexpr (ks == 3) {
        asm volatile("s_waitcnt vmcnt(6)" : "+v"(x1v[0]), "+v"(x1v[1]), "+v"(x1v[2]), "+v"(x1v[3]), "+v"(stv)::"memory");
        landed_b4(IC<24>{}, IC<28>{});
      }
      if constexpr (ks == 9) { VMW(4); landed_b4(IC<28>{}, IC<39>{}); }
      if constexpr (ks == 13) { VMW(2); landed_b4(IC<39>{}, IC<46>{}); }
      if constexpr (ks == 15) { VMW(0); landed_b4(IC<46>{}, IC<48>{}); }
      SCHED();
      if constexpr (ks == 3) { ln2_stats(); PIN(l2r); PIN(l2n);
#pragma unroll
        for (int i = 0; i < 4; ++i) rsv[i] = rdl(l2r, 8 * i); }
      sfor<0, 6>([&](auto MC) {
        constexpr int m = decltype(MC)::value, ct = m >> 1;
        if constexpr (m & 1) accA[ct][1] = mfma32(b4[ks * 3 + ct], a1, accA[ct][1]); else accA[ct][0] = mfma32(b4[ks * 3 + ct], a0, accA[ct][0]);
        if constexpr (ks < 6 && m % 3 == 0) load_b4(IC<28 + ks * 2 + m / 3>{});
        if constexpr (ks >= 6 && ks < 9 && m == 0) load_b4(IC<40 + (ks - 6)>{});
        if constexpr (ks >= 9 && ks < 12 && (m == 0 || (m == 3 && ks < 11))) load_b4(IC<43 + (ks - 9) * 2 + (m == 3)>{});
        if constexpr (ks >= 4) {
          constexpr int o = (ks - 4) * 15 + (m >> 1) * 5 + (m & 1) * 2;
          ln_op(IC<o>{}); ln_op(IC<o + 1>{});
          if constexpr (m & 1) ln_op(IC<o + 2>{});
          SCHED();
        }
      });
      a0 = n0; a1 = n1;
      SCHED();
    });
  }
  vH = (vl16 & 255) + ((vl16 >> 5) & 8) + (vl16 >> 9) * (2 * 172 * 256); PIN(vH);
  BAR();
  STAMP(17); KSTAMP(3, 16);
  f32x4 accB[3][2] = {{z4, z4}, {z4, z4}, {z4, z4}};
  u32 vRow5; int pr5, pw5, gb5;
  const int kb5 = 5 * w + min(w, 3);
  u64 care5 = 0x0101010101010101ull * ((0x80c06030180c0603ull >> (8 * w)) & 255u);
  asm volatile("" : "+s"(care5));
  v4u b5[24], hA[12], hB[12]; v2u xqA, xqB;
  const u32 sB5 = WS_B5T + (j * 43 + kb5) * 4096, d5 = (w + 5) >> 3;
  auto load_b5 = [&](auto NC) {
    constexpr int n = decltype(NC)::value, sx = n / 4, t = n % 4;
    const u32 so = sB5 + (sx == 5 ? (5 - d5) * 4096 : sx * 4096);
    ld16s<t * 1024>(b5[n], vl16, rWS, so);
  };
  auto load_h = [&](auto NC, v4u (&hf)[12], int h) {
    constexpr int n = decltype(NC)::value, sx = n / 2, m = n % 2;
    const u32 so = WS_HH + ((g * 4 + 2 * m + h) * 172 + 4 * kb5) * 256 + (sx == 5 ? (5 - d5) * 1024 : sx * 1024);
    ld16s<0>(hf[n], vl16, rWS, so);
  };
  auto poll5 = [&](int h) {
    const u32 fo = flag_off(2, g, h), vf = vfl; u32 t, cnt = SPIN_CAP;
    asm volatile("1:\n\tbuffer_load_dword %0, %2, %3, %4 offen sc1\n\ts_waitcnt vmcnt(0)\n\tv_cmp_ne_u32 vcc, %5, %0\n\ts_and_b64 vcc, vcc, %6\n\ts_cbranch_scc0 2f\n\t"
                 "s_sub_u32 %1, %1, 1\n\ts_cbranch_scc0 1b\n\t2:" : "=&v"(t), "+s"(cnt) : "v"(vf), "s"(rWS), "s"(fo), "s"(tag), "s"(care5) : "vcc", "scc", "memory");
  };
  {
    float G[4], U[4], T[4]; v2u ov;
    auto sw_op = [&](auto NC) {
      constexpr int n = decltype(NC)::value, ct = n / 22, k = n % 22;
      if constexpr (ct < 3) {
        if constexpr (k < 4) { auto r = __builtin_amdgcn_permlane32_swap(asu(accA[ct][0][k]), asu(accA[ct][1][k]), false, false); G[k] = asf(r[0]); U[k] = asf(r[1]); PIN(G[k]); PIN(U[k]); }
        else if constexpr (k < 6) { T[2 * (k - 4)] = q_mulk(G[2 * (k - 4)]); T[2 * (k - 4) + 1] = q_mulk(G[2 * (k - 4) + 1]); }
        else if constexpr (k < 10) T[k - 6] = q_exp(T[k - 6]);
        else if constexpr (k < 12) { T[2 * (k - 10)] = q_add1(T[2 * (k - 10)]); T[2 * (k - 10) + 1] = q_add1(T[2 * (k - 10) + 1]); }
        else if constexpr (k < 16) T[k - 12] = q_rcp(T[k - 12]);
        else if constexpr (k < 18) { G[2 * (k - 16)] = q_mul(G[2 * (k - 16)], U[2 * (k - 16)]); G[2 * (k - 16) + 1] = q_mul(G[2 * (k - 16) + 1], U[2 * (k - 16) + 1]); }
        else if constexpr (k < 20) { G[2 * (k - 18)] = q_mul(G[2 * (k - 18)], T[2 * (k - 18)]); G[2 * (k - 18) + 1] = q_mul(G[2 * (k - 18) + 1], T[2 * (k - 18) + 1]); }
        else if constexpr (k == 20) { ov.x = q_cvt(G[0], G[1]); ov.y = q_cvt(G[2], G[3]); }
        else hh_store(ov, ct, 0);
      }
    };
    v4u a0 = AFRAG(32768, 0), a1 = AFRAG(49152, 0);
    int l5, c5, g5;
    sfor<0, 16>([&](auto KS) {
      constexpr int ks = decltype(KS)::value;
      KSTAMP(4, ks);
      v4u n0 = a0, n1 = a1;
      if constexpr (ks > 0 && ks < 15) { n0 = AFRAG(32768, ks + 1); n1 = AFRAG(49152, ks + 1); }
      if constexpr (ks == 7) { VMW(0); SIGNAL(1, flag_off(2, g, 0) + j * 128); }
      if constexpr (ks == 14) poll5(0);
      SCHED();
      sfor<0, 6>([&](auto MC) {
        constexpr int m = decltype(MC)::value, ct = m >> 1, n = ks * 6 + m;
        if constexpr (m & 1) accB[ct][1] = mfma32(b4[ks * 3 + ct], a1, accB[ct][1]); else accB[ct][0] = mfma32(b4[ks * 3 + ct], a0, accB[ct][0]);
        if constexpr (n == 3) n0 = AFRAG(32768, 1);
        if constexpr (n == 5) n1 = AFRAG(49152, 1);
        if constexpr (n < 33) { sw_op(IC<2 * n>{}); sw_op(IC<2 * n + 1>{}); }
        if constexpr (n >= 12 && n < 32 && n % 4 == 0) load_b5(IC<(n - 12) / 4>{});
        if constexpr (n >= 42 && n < 75 && n % 3 == 0) load_b5(IC<5 + (n - 42) / 3>{});
        if constexpr (n >= 84 && n % 2 == 0) load_h(IC<(n - 84) / 2>{}, hA, 0);
        if constexpr (n == 38) { l5 = opq(vl16) >> 4; c5 = l5 & 15; g5 = l5 >> 4; PIN(c5); PIN(g5); }
        if constexpr (n == 40) { vRow5 = (32 * (w >> 2) + 4 * (w & 3) + g5) * 1024 + c5 * 8; PIN(vRow5); }
        if constexpr (n == 42) { pr5 = (4 * w + g5) * 256 + ((c5 ^ ((4 * w + g5) & 7)) << 4); PIN(pr5); }
        if constexpr (n == 44) { pw5 = w * 8192 + c5 * 256 + ((c5 & 4) << 4); PIN(pw5); }
        if constexpr (n == 46) { pw5 += (g5 ^ (c5 & 3)) << 4; PIN(pw5); gb5 = L_GAB + 256 + c5 * 16; PIN(gb5); }
        SCHED();
      });
      a0 = n0; a1 = n1;
      SCHED();
    });
  }
  STAMP(18); KSTAMP(4, 16);
  PIN(accB[0][0]); PIN(accB[0][1]); PIN(accB[1][0]); PIN(accB[1][1]); PIN(accB[2][0]); PIN(accB[2][1]);
  sfor<6, 9>([&](auto I) { load_h(I, hA, 0); });
  swiglu_tile(accB, 0, 1);
  sfor<9, 12>([&](auto I) { load_h(I, hA, 0); });
  swiglu_tile(accB, 1, 1);
  swiglu_tile(accB, 2, 1);
  VMW(0);
  BAR();
  RAISE(flag_off(2, g, 1) + j * 128);
  STAMP(19);
#pragma unroll
  for (int i = 0; i < 12; ++i) PIN(hA[i]);
#pragma unroll
  for (int i = 0; i < 16; ++i) PIN(b5[i]);
  sfor<16, 20>(load_b5);
  auto p5_half = [&](auto HC, v4u (&hf)[12]) {
    constexpr int h = decltype(HC)::value;
    f32x4 acc[2][4] = {{z4, z4, z4, z4}, {z4, z4, z4, z4}};
    sfor<0, 6>([&](auto SC) {
      constexpr int sx = decltype(SC)::value;
      if constexpr (h == 0 && sx == 1) { sfor<20, 24>(load_b5);
        const u32 sq = WS_X1 + g * 65536 + j * 128, sq1 = sq + 16384, vr5 = vRow5; const rsrc_t rr = rWS; LD8S(xqA, vr5, rr, sq, 0); LD8S(xqB, vr5, rr, sq1, 0); }
      if constexpr (h == 0 && sx == 4) {
        poll5(1);
#pragma unroll
        for (int i = 16; i < 24; ++i) PIN(b5[i]);
        PIN(xqA); PIN(xqB);
      }
      if constexpr (h == 1 && sx == 0) asm volatile("s_waitcnt vmcnt(9)" : "+v"(hf[0]), "+v"(hf[1]), "+v"(hf[2]), "+v"(hf[3])::"memory");
      if constexpr (h == 1 && sx == 2) asm volatile("s_waitcnt vmcnt(5)" : "+v"(hf[4]), "+v"(hf[5]), "+v"(hf[6]), "+v"(hf[7])::"memory");
      if constexpr (h == 1 && sx == 4) asm volatile("s_waitcnt vmcnt(1)" : "+v"(hf[8]), "+v"(hf[9]), "+v"(hf[10]), "+v"(hf[11])::"memory");
      if constexpr (sx == 5) { if (w >= 3) { hf[10] = v4u{0u, 0u, 0u, 0u}; hf[11] = v4u{0u, 0u, 0u, 0u}; } }
#pragma unroll
      for (int m = 0; m < 2; ++m) {
#pragma unroll
        for (int n = 0; n < 4; ++n) acc[m][n] = mfma32(b5[sx * 4 + n], hf[2 * sx + m], acc[m][n]);
        if constexpr (h == 0 && sx >= 4) { if (m == 0) { load_h(IC<4 * (sx - 4)>{}, hB, 1); load_h(IC<4 * (sx - 4) + 1>{}, hB, 1); } else { load_h(IC<4 * (sx - 4) + 2>{}, hB, 1); load_h(IC<4 * (sx - 4) + 3>{}, hB, 1); } }
        SCHED();
      }
    });
    const int pwh = h ? opq(pw5 + 65536) : pw5;
#pragma unroll
    for (int m = 0; m < 2; ++m)
#pragma unroll
      for (int n = 0; n < 4; ++n) *(LAS f32x4*)(L + ((n & 1) ? (pwh ^ 64) : pwh) + (m * 4096 + (n >> 1) * 128)) = acc[m][n];
    if constexpr (h == 0) sfor<8, 12>([&](auto I) { load_h(I, hB, 1); });
    BAR();
    const int prh = h ? opq(pr5 + 65536) : pr5;
    f32x4 y = *(LAS f32x4*)(L + prh);
#pragma unroll
    for (int q = 1; q < 8; ++q) y += *(LAS f32x4*)(L + prh + q * 8192);
    const f32x4 gbv = *(LAS f32x4*)(L + gb5);
    const v2u xx = h ? xqB : xqA;
    const f32x4 xb = {blo(xx.x), bhi(xx.x), blo(xx.y), bhi(xx.y)};
    __builtin_amdgcn_raw_buffer_store_b64(pk4(xb + gbv * y), rOUT, vRow5, g * 65536 + h * 16384 + j * 128, 0);
  };
  STAMP(20);
  p5_half(IC<0>{}, hA);
  STAMP(21);
  p5_half(IC<1>{}, hB);
  STAMP(22);
  STAMP_DUMP();
  if (b == 0 && w == 0) {
    for (u32 sp = 0; sp < SPIN_CAP; ++sp) {
      asm volatile("" ::: "memory");
      const v4u v = __builtin_amdgcn_raw_buffer_load_b128(rWS, vl16, WS_ALIVE, 16);
      if (__all(v.x == tag && v.y == tag && v.z == tag && v.w == tag)) break;
    }
    __builtin_amdgcn_raw_buffer_store_b32(tag, rWS, 0, WS_EPOCH, 16);
  }
}
}

extern "C" void kernel_launch(void* const* d_in, const int* in_sizes, int n_in, void* d_out, int out_size, void* d_ws, size_t ws_size,
                              hipStream_t stream) {
  Params p;
  p.x = (const u16*)d_in[0]; p.wqkv = (const u16*)d_in[1]; p.wo = (const u16*)d_in[2];
  p.w1 = (const u16*)d_in[4]; p.w3 = (const u16*)d_in[5]; p.w2 = (const u16*)d_in[6];
  p.c = (const u16*)d_in[7]; p.wmod = (const u16*)d_in[8];
  p.out = (u16*)d_out; p.ws = (char*)d_ws;
  (void)hipFuncSetAttribute((const void*)fused_block, hipFuncAttributeMaxDynamicSharedMemorySize, L_TOTAL);
  fused_block<<<256, 512, L_TOTAL, stream>>>(p);
}
```

```cpp
#include <hip/hip_runtime.h>
#include <stdint.h>

#define KDBG 0
#define NODMA 0
#define CODEWARM 0
#define L2WARM 1
#define EARLYT 1
#define XMAP 2

#define DI __device__ __forceinline__
#define GAS __attribute__((address_space(1)))
#define LAS __attribute__((address_space(3)))
typedef unsigned u32;
typedef unsigned short u16;
typedef unsigned long long u64;
typedef u32 v4u __attribute__((ext_vector_type(4)));
typedef u32 v2u __attribute__((ext_vector_type(2)));
typedef float f32x4 __attribute__((ext_vector_type(4)));
typedef float f32x2 __attribute__((ext_vector_type(2)));
typedef __bf16 bf16x8 __attribute__((ext_vector_type(8)));
typedef __bf16 bf16x2 __attribute__((ext_vector_type(2)));
typedef short s16x4 __attribute__((ext_vector_type(4)));
typedef __amdgpu_buffer_rsrc_t rsrc_t;
typedef GAS u32 gu32;
typedef GAS u64 gu64;
#define RLX_AGENT __ATOMIC_RELAXED, __HIP_MEMORY_SCOPE_AGENT

namespace {
constexpr int S_ = 2048, D_ = 512, F_ = 1376;
constexpr u32 WS_EPOCH = 0;
constexpr u32 WS_ALIVE = 4096;
constexpr u32 WS_FLAG = 32768;
constexpr u32 WS_STAT = 262144;
constexpr u32 WS_MODG = 524288;
constexpr u32 WS_MODF = 655360;
constexpr u32 WS_O = 1u << 20;
constexpr u32 WS_X1 = 3u << 20;
constexpr u32 WS_HH = 5u << 20;
constexpr u32 WS_RFLAG = 8192;
constexpr u32 WS_B4T = 11u << 20;
constexpr u32 WS_B5T = 14u << 20;
constexpr u32 WS_B3T = (15u << 20) + (512u << 10);
constexpr u32 WS_DBG = 16u << 20;
constexpr u32 WS_BYTES = 32u << 20;
constexpr int L_XY = 0;
constexpr int L_Z = 81920;
constexpr int L_MISC = 158720;
constexpr int L_TOTAL = 163840;
constexpr int L_RED = 73728;
constexpr int L_MODA = L_MISC;
constexpr int L_CNT = L_MISC + 4096;
constexpr int L_GAB = L_MISC + 4096 + 64;
constexpr int L_JUNK = L_MISC + 4096 + 64 + 512;
constexpr int L_STAMP = 156672;
constexpr u32 SPIN_CAP = 1u << 18;
constexpr u32 CODE_BYTES = 26112;

DI f32x4 mfma32(v4u a, v4u b, f32x4 c) {
  return __builtin_amdgcn_mfma_f32_16x16x32_bf16(__builtin_bit_cast(bf16x8, a), __builtin_bit_cast(bf16x8, b), c, 0, 0, 0);
}
DI f32x4 mfma16(v2u a, v2u b, f32x4 c) {
  return __builtin_amdgcn_mfma_f32_16x16x16bf16_1k(__builtin_bit_cast(s16x4, a), __builtin_bit_cast(s16x4, b), c, 0, 0, 0);
}
DI u32 pk(float lo, float hi) { f32x2 v = {lo, hi}; return __builtin_bit_cast(u32, __builtin_convertvector(v, bf16x2)); }
DI u32 pk2(f32x2 v) { return __builtin_bit_cast(u32, __builtin_convertvector(v, bf16x2)); }
DI v2u pk4(f32x4 v) { v2u r; r.x = pk(v.x, v.y); r.y = pk(v.z, v.w); return r; }
DI float asf(u32 u) { return __builtin_bit_cast(float, u); }
DI u32 asu(float f) { return __builtin_bit_cast(u32, f); }
DI float blo(u32 u) { return asf(u << 16); }
DI float bhi(u32 u) { return asf(u & 0xffff0000u); }
DI f32x2 unp(u32 u) { f32x2 r = {blo(u), bhi(u)}; return r; }
DI float dot2(u32 a, u32 b, float c) { return __builtin_amdgcn_fdot2_f32_bf16(__builtin_bit_cast(bf16x2, a), __builtin_bit_cast(bf16x2, b), c, false); }
DI v2u trd(LAS char* p) { return __builtin_bit_cast(v2u, __builtin_amdgcn_ds_read_tr16_b64_v4i16((LAS s16x4*)p)); }
DI v4u cat(v2u a, v2u b) { v4u r; r.x = a.x; r.y = a.y; r.z = b.x; r.w = b.y; return r; }
template <int MI, int SI> DI void dma(u32 mb, u32 voff, rsrc_t srd, u32 sb) {
  u32 t;
  asm volatile("s_add_u32 m0, %1, %c2\n\ts_add_u32 %0, %3, %c4\n\tbuffer_load_dwordx4 %5, %6, %0 offen lds"
               : "=&s"(t) : "s"(mb), "i"(MI), "s"(sb), "i"(SI), "v"(voff), "s"(srd) : "scc");
}
DI void touch(u32 voff, rsrc_t srd, u32 sb) {
  asm volatile("s_mov_b32 m0, %c0\n\ts_nop 0\n\tbuffer_load_dword %1, %2, %3 offen lds" ::"i"(L_JUNK), "v"(voff), "s"(srd), "s"(sb));
}
#define BAR() asm volatile("s_waitcnt lgkmcnt(0)\n\ts_barrier" ::: "memory")
#define VMW(n) asm volatile("s_waitcnt vmcnt(" #n ")" ::: "memory")
#define LGW() asm volatile("s_waitcnt lgkmcnt(0)" ::: "memory")
#define SCHED() __builtin_amdgcn_sched_barrier(0)
#define LD16(dst, voff, srd, soff, IMM) asm volatile("buffer_load_dwordx4 %0, %1, %2, %3 offen offset:" #IMM : "=v"(dst) : "v"(voff), "s"(srd), "s"(soff))
#define LD16S(dst, voff, srd, soff, IMM) asm volatile("buffer_load_dwordx4 %0, %1, %2, %3 offen offset:" #IMM " sc1" : "=v"(dst) : "v"(voff), "s"(srd), "s"(soff))
#define LD8(dst, voff, srd, soff, IMM) asm volatile("buffer_load_dwordx2 %0, %1, %2, %3 offen offset:" #IMM : "=v"(dst) : "v"(voff), "s"(srd), "s"(soff))
#define LD8S(dst, voff, srd, soff, IMM) asm volatile("buffer_load_dwordx2 %0, %1, %2, %3 offen offset:" #IMM " sc1" : "=v"(dst) : "v"(voff), "s"(srd), "s"(soff))
#define LD4(dst, voff, srd, soff) asm volatile("buffer_load_dword %0, %1, %2, %3 offen" : "=v"(dst) : "v"(voff), "s"(srd), "s"(soff))
#define LD4S(dst, voff, srd, soff) asm volatile("buffer_load_dword %0, %1, %2, %3 offen sc1" : "=v"(dst) : "v"(voff), "s"(srd), "s"(soff))

template <int IMM> DI void ld16s(v4u& r, u32 v, rsrc_t rs, u32 so) { asm volatile("buffer_load_dwordx4 %0, %1, %2, %3 offen offset:%c4 sc1" : "=v"(r) : "v"(v), "s"(rs), "s"(so), "i"(IMM)); }
template <int CTRL> DI float dppf(float v) { return asf(__builtin_amdgcn_update_dpp(0, asu(v), CTRL, 0xf, 0xf, true)); }
DI float row16_sum(float v) { v += dppf<0x128>(v); v += dppf<0x124>(v); v += dppf<0x122>(v); v += dppf<0x121>(v); return v; }
DI float sw32_add(float a, float b) { auto r = __builtin_amdgcn_permlane32_swap(asu(a), asu(b), false, false); return asf(r[0]) + asf(r[1]); }
DI float sw16_add(float a, float b) { auto r = __builtin_amdgcn_permlane16_swap(asu(a), asu(b), false, false); return asf(r[0]) + asf(r[1]); }
DI float x16_max(float v) { auto r = __builtin_amdgcn_permlane16_swap(asu(v), asu(v), false, false); return fmaxf(asf(r[0]), asf(r[1])); }
DI float x32_max(float v) { auto r = __builtin_amdgcn_permlane32_swap(asu(v), asu(v), false, false); return fmaxf(asf(r[0]), asf(r[1])); }
DI int fresh_lane() { int l; asm volatile("v_mbcnt_lo_u32_b32 %0, -1, 0\n\tv_mbcnt_hi_u32_b32 %0, -1, %0" : "=v"(l)); return l; }
DI float rdl(float v, int l) { return asf(__builtin_amdgcn_readlane(asu(v), l)); }

struct Params {
  const u16 *x, *wqkv, *wo, *w1, *w3, *w2, *c, *wmod;
  u16* out;
  char* ws;
};

#define LOOPSEL 0
#define STAMP(i) do {} while (0)
#define KSTAMP(lp, i) do {} while (0)
#define STAMP_DUMP() do {} while (0)

DI u32 flag_off(int phase, int g, int h) { return WS_FLAG + ((phase * 32 + g) * 2 + h) * 1024; }
DI void poll_flags(rsrc_t rWS, u32 vfl, u32 foff, u64 care, u32 tag) {
  for (u32 sp = 0; sp < SPIN_CAP; ++sp) {
    asm volatile("" ::: "memory");
    const u32 v = __builtin_amdgcn_raw_buffer_load_b32(rWS, vfl, foff, 16);
    if (!(__builtin_amdgcn_ballot_w64(v != tag) & care)) break;
  }
  VMW(0);
}
DI void raise_flag(rsrc_t rWS, u32 foff, u32 vtag, u32 vzero, u32 m1) {
  asm volatile("s_mov_b32 exec_hi, 0\n\ts_mov_b32 exec_lo, %4\n\tbuffer_store_dword %0, %1, %2, %3 offen sc1\n\ts_mov_b64 exec, -1" ::"v"(vtag), "v"(vzero), "s"(rWS), "s"(foff), "s"(m1) : "memory");
}
DI void signal_flag(rsrc_t rWS, u32 cnt_addr, u32 foff, u32 tag) {
  u32 t, a = cnt_addr, one = 1u, vt = tag, vz = 0u;
  asm volatile("s_mov_b64 exec, 1\n\tds_add_rtn_u32 %0, %1, %2\n\ts_waitcnt lgkmcnt(0)\n\tv_cmp_eq_u32 vcc, 7, %0\n\ts_and_b64 exec, exec, vcc\n\t"
               "buffer_store_dword %3, %4, %5, %6 offen sc1\n\ts_mov_b64 exec, -1"
               : "=&v"(t) : "v"(a), "v"(one), "v"(vt), "v"(vz), "s"(rWS), "s"(foff) : "vcc", "scc", "memory");
}
DI void arrive(u32 cnt_addr) {
  u32 a = cnt_addr, one = 1u;
  asm volatile("s_waitcnt lgkmcnt(0)\n\ts_mov_b64 exec, 1\n\tds_add_u32 %0, %1\n\ts_mov_b64 exec, -1" ::"v"(a), "v"(one) : "memory");
}
DI void await8(u32 cnt_addr) {
  u32 a = cnt_addr, t, sv;
  asm volatile("1:\n\tds_read_b32 %0, %2\n\ts_waitcnt lgkmcnt(0)\n\tv_readfirstlane_b32 %1, %0\n\ts_nop 0\n\ts_cmp_lt_u32 %1, 8\n\ts_cbranch_scc1 1b"
               : "=&v"(t), "=&s"(sv) : "v"(a) : "scc", "memory");
}
#define PIN(x) asm volatile("" : "+v"(x))
DI float qp(float r) { PIN(r); return r; }
DI float q_shl(u32 a) { return qp(blo(a)); }
DI float q_hi(u32 a) { return qp(bhi(a)); }
DI float q_fma(float a, float b, float c) { return qp(__builtin_fmaf(a, b, c)); }
DI float q_fmas(float a, float b, float c) { return qp(__builtin_fmaf(a, b, c)); }
DI float q_mul(float a, float b) { return qp(a * b); }
DI float q_mulk(float a) { return qp(a * -1.4426950408889634f); }
DI float q_add1(float a) { return qp(a + 1.f); }
DI float q_exp(float a) { return qp(__builtin_amdgcn_exp2f(a)); }
DI float q_rcp(float a) { return qp(__builtin_amdgcn_rcpf(a)); }
DI u32 q_cvt(float a, float b) { u32 r = pk(a, b); PIN(r); return r; }
DI float q_mov(float a) { return qp(a); }
DI u32 opq(u32 v) { asm volatile("" : "+v"(v)); return v; }

DI float sel(u64 m, float a, float b) { return __builtin_amdgcn_inverse_ballot_w64(m) ? a : b; }
DI v2u attend(v2u kp, v2u vp, v2u kc, v2u vc, v2u q, u64 m0, u64 m1, u32 hm, bool noprev) {
  const f32x4 z = {0.f, 0.f, 0.f, 0.f};
  const f32x4 sp = mfma16(kp, q, z);
  const f32x4 sc = mfma16(kc, q, z);
  const float K2 = 0.25f * 1.4426950408889634f;
  const u64 par = 0xaaaaaaaaaaaaaaaaull;
  float p0 = sel(par, sp[1], sp[0]), p1 = sel(par, sp[3], sp[2]);
  if (noprev) { p0 = -INFINITY; p1 = -INFINITY; }
  const float t0 = sel(m0, sel(par, sc[1], sc[0]), p0), t1 = sel(m1, sel(par, sc[3], sc[2]), p1);
  const float m = x32_max(x16_max(fmaxf(t0, t1)));
  const float mk = -m * K2;
  const float e0 = __builtin_amdgcn_exp2f(__builtin_fmaf(t0, K2, mk)), e1 = __builtin_amdgcn_exp2f(__builtin_fmaf(t1, K2, mk));
  const u32 w0 = pk(e0, e0) & hm, w1 = pk(e1, e1) & hm;
  v4u pf;
  pf.x = __builtin_amdgcn_inverse_ballot_w64(m0) ? 0u : w0; pf.y = __builtin_amdgcn_inverse_ballot_w64(m1) ? 0u : w1;
  pf.z = __builtin_amdgcn_inverse_ballot_w64(m0) ? w0 : 0u; pf.w = __builtin_amdgcn_inverse_ballot_w64(m1) ? w1 : 0u;
  const f32x4 o = mfma32(cat(vp, vc), pf, z);
  float sum = e0 + e1;
  sum = sw16_add(sum, sum); sum = sw32_add(sum, sum);
  return pk4(o * __builtin_amdgcn_rcpf(sum));
}

template <int N> using IC = std::integral_constant<int, N>;
template <int I, int N, class F> DI void sfor(F&& f) { if constexpr (I < N) { f(IC<I>{}); sfor<I + 1, N>(f); } }

__global__ void __launch_bounds__(512, 2) fused_block(Params p) {
  extern __shared__ __attribute__((aligned(16))) char smem[];
  LAS char* L = (LAS char*)smem;
  const int tid = threadIdx.x, lane = tid & 63, w = __builtin_amdgcn_readfirstlane(tid >> 6);
  const int c16 = lane & 15, g4 = lane >> 4;
  const int b = blockIdx.x;
  int g, j;
  if (XMAP == 0) { g = (b & 7) * 4 + ((b >> 3) & 3); j = b >> 5; }
  else if (XMAP == 2) { const int x = b & 7, r = b >> 3; g = (x >> 1) * 8 + (r & 7); j = (x & 1) * 4 + (r >> 3); }
  else { const int x = b & 7, r = b >> 3; g = (x >> 2) * 16 + (r & 15); j = (x & 3) * 2 + (r >> 4); }
  const rsrc_t rX = __builtin_amdgcn_make_buffer_rsrc((void*)p.x, 0, S_ * D_ * 2, 0x00020000);
  const rsrc_t rQKV = __builtin_amdgcn_make_buffer_rsrc((void*)p.wqkv, 0, D_ * 3 * D_ * 2, 0x00020000);
  const rsrc_t rWO = __builtin_amdgcn_make_buffer_rsrc((void*)p.wo, 0, D_ * D_ * 2, 0x00020000);
  const rsrc_t rW1 = __builtin_amdgcn_make_buffer_rsrc((void*)p.w1, 0, D_ * F_ * 2, 0x00020000);
  const rsrc_t rW3 = __builtin_amdgcn_make_buffer_rsrc((void*)p.w3, 0, D_ * F_ * 2, 0x00020000);
  const rsrc_t rW2 = __builtin_amdgcn_make_buffer_rsrc((void*)p.w2, 0, F_ * D_ * 2, 0x00020000);
  const rsrc_t rC = __builtin_amdgcn_make_buffer_rsrc((void*)p.c, 0, D_ * 2, 0x00020000);
  const rsrc_t rMOD = __builtin_amdgcn_make_buffer_rsrc((void*)p.wmod, 0, D_ * 6 * D_ * 2, 0x00020000);
  const rsrc_t rWS = __builtin_amdgcn_make_buffer_rsrc((void*)p.ws, 0, WS_BYTES, 0x00020000);
  const rsrc_t rOUT = __builtin_amdgcn_make_buffer_rsrc((void*)p.out, 0, S_ * D_ * 2, 0x00020000);
  STAMP(0);

  u32 ep;
  { const u32 vz = 0; asm volatile("buffer_load_dword %0, %1, %2, 0 offen sc1" : "=v"(ep) : "v"(vz), "s"(rWS)); }
  u32 vl16 = lane * 16; PIN(vl16);
  const int bm = b < 192 ? b : b - 192, mq = bm / 48, cl = bm - 48 * mq;
  v4u wm[4]; v2u cv; u32 ep0;
  if (w < 4) {
    const u32 vm = (tid >> 3) * 24576 + (tid & 7) * 16, s0 = mq * (128 * 6144) + cl * 128, s1 = s0 + 6144, s2 = s0 + 12288, s3 = s0 + 18432;
    LD16(wm[0], vm, rMOD, s0, 0); LD16(wm[1], vm, rMOD, s1, 0); LD16(wm[2], vm, rMOD, s2, 0); LD16(wm[3], vm, rMOD, s3, 0);
    const u32 vc = (tid >> 3) * 8, scq = mq * 256;
    LD8(cv, vc, rC, scq, 0);
    const u32 vz = 0; asm volatile("buffer_load_dword %0, %1, %2, 0 offen sc1" : "=v"(ep0) : "v"(vz), "s"(rWS));
  }
  if (tid < 16) *(LAS u32*)(L + L_CNT + tid * 4) = 0u;
  BAR();
  const int st_qd = lane >> 2, st_r = (st_qd & 3) + 4 * (st_qd >> 3);
  const int st_col = ((st_qd >> 2) & 1) * 64 + (((lane & 3) ^ ((w & 1) << 1)) << 4);
  const int rotp = g & 7;
  u32 vB1 = st_r * 3072 + st_col; PIN(vB1);
  const u32 mB1 = (w >> 2) * 12288 + (w & 3) * 1024, sB1 = (w >> 2) * 98304 + (w & 3) * 24576 + j * 128;
  auto stage_b1 = [&](auto PC, auto BC) {
    constexpr int pair = decltype(PC)::value, base = decltype(BC)::value;
    const u32 sp = ((pair + rotp) & 7) * 196608 + sB1;
    dma<base, 0>(mB1, vB1, rQKV, sp); dma<base + 4096, 1024>(mB1, vB1, rQKV, sp); dma<base + 8192, 2048>(mB1, vB1, rQKV, sp);
  };
  auto b1_piece = [&](auto PC, auto BC, auto TC) {
    constexpr int pair = decltype(PC)::value, base = decltype(BC)::value, ty = decltype(TC)::value;
    dma<base + ty * 4096, ty * 1024>(mB1, vB1, rQKV, ((pair + rotp) & 7) * 196608 + sB1);
  };
  const int hd = w & 3, blk2 = w >> 2, kq = w & 3, mh = w >> 2;
  const int cs0 = j < 4 ? 22 * j : 88 + 21 * (j - 4);
  const int cend = cs0 + (j < 4 ? 22 : 21);
  STAMP(1);
  const int T0 = 12 * (g >> 4) + 3 * (w & 3), rcb = min(cs0 + T0, 168);
  if (w == 4) touch((lane >> 1) * 2752 + (lane & 1) * 128, rW1, (g & 15) * (32 * 2752) + min(cs0 + 12 * (g >> 4), 160) * 16);
  if (w == 5) touch((lane >> 1) * 2752 + (lane & 1) * 128, rW3, (g & 15) * (32 * 2752) + min(cs0 + 12 * (g >> 4), 160) * 16);
  if (w == 6) touch((lane & 31) * 1024 + (lane >> 5) * (g < 11 ? 32 * 32768 : 0), rW2, g * 32768 + j * 128);
  if (w == 7) touch((lane & 31) * 1024, rWO, (g & 15) * 32768 + j * 128);
  if (w < 4) {
    asm volatile("s_waitcnt vmcnt(0)" : "+v"(cv), "+v"(wm[0]), "+v"(wm[1]), "+v"(wm[2]), "+v"(wm[3]), "+v"(ep0)::"memory");
    const u32 tg = __builtin_amdgcn_readfirstlane(ep0) + 1u;
    const f32x4 cf = {blo(cv.x), bhi(cv.x), blo(cv.y), bhi(cv.y)};
    float sc[4];
#pragma unroll
    for (int r = 0; r < 4; ++r) sc[r] = cf[r] * __builtin_amdgcn_rcpf(1.f + __builtin_amdgcn_exp2f(cf[r] * -1.4426950408889634f));
    f32x2 v[4];
#pragma unroll
    for (int i = 0; i < 4; ++i) v[i] = (unp(wm[0][i]) * sc[0] + unp(wm[1][i]) * sc[1]) + (unp(wm[2][i]) * sc[2] + unp(wm[3][i]) * sc[3]);
    const float a0 = sw32_add(v[0].x, v[2].x), a1 = sw32_add(v[0].y, v[2].y), a2 = sw32_add(v[1].x, v[3].x), a3 = sw32_add(v[1].y, v[3].y);
    float t0 = sw16_add(a0, a2), t1 = sw16_add(a1, a3);
    t0 += dppf<0x128>(t0); t1 += dppf<0x128>(t1);
    f32x2 o = {t0, t1};
    *(LAS f32x2*)(L + L_RED + w * 256 + (lane & 7) * 32 + g4 * 8) = o;
    LGW();
    u32 old = 0;
    if (lane == 0) old = __hip_atomic_fetch_add((LAS u32*)(L + L_CNT + 28), 1u, __ATOMIC_RELAXED, __HIP_MEMORY_SCOPE_WORKGROUP);
    if (__builtin_amdgcn_readfirstlane(old) == 3u) {
      asm volatile("" ::: "memory");
      float s = 0.f;
#pragma unroll
      for (int i = 0; i < 4; ++i) s += *(LAS float*)(L + L_RED + i * 256 + lane * 4);
      v2u gr; gr.x = asu(s); gr.y = tg;
      if (b < 192) __builtin_amdgcn_raw_buffer_store_b64(gr, rWS, lane * 8, WS_MODG + (mq * 3072 + cl * 64) * 8, 16);
      __builtin_amdgcn_raw_buffer_store_b32(tg, rWS, 0, WS_ALIVE + b * 4, 16);
    }
  }
  STAMP(2);
  v4u xr[10];
  {
    const int r0 = 64 * g - 16 + w;
    const u32 s0 = (u32)max(r0, 0) * 1024u, s1 = (u32)max(r0 + 8, 0) * 1024u;
    LD16(xr[0], vl16, rX, s0, 0); LD16(xr[1], vl16, rX, s1, 0);
#pragma unroll
    for (int i = 2; i < 10; ++i) { const u32 so = (u32)(r0 + 8 * i) * 1024u; LD16(xr[i], vl16, rX, so, 0); }
  }
  stage_b1(IC<0>{}, IC<L_XY>{});
  int afo[4];
#pragma unroll
  for (int i = 0; i < 4; ++i) { afo[i] = blk2 * 32768 + c16 * 1024 + (((4 * i + g4) ^ c16) << 4); PIN(afo[i]); }
  const u64 am0 = __builtin_amdgcn_ballot_w64((c16 & 14) >= 4 * g4), am1 = __builtin_amdgcn_ballot_w64((c16 & 14) >= 4 * g4 + 2);
  u32 ahm = (lane & 1) ? 0xffff0000u : 0xffffu; PIN(ahm);
  u32 vOst = c16 * 16 + g4 * 256 + (hd & 1) * 8; PIN(vOst);
  int tB1 = g4 * 1024 + 64 * (c16 >> 2) + 8 * (lane & 3) + 32 * ((hd & 1) ^ (g4 & 1)) + 256 * (hd >> 1); PIN(tB1);
  int hw0 = w * 1024 + ((((lane - 8 * rotp) & 63) ^ w) << 4); PIN(hw0);
#define vfl ((opq(vl16) & 112u) << 3)
  STAMP(3);
  v4u bw[48];
  auto read_b1 = [&](int pair, int base, int off) {
#pragma unroll
    for (int kk = 0; kk < 2; ++kk)
#pragma unroll
      for (int ty = 0; ty < 3; ++ty) {
        LAS char* a = L + base + (off + kk * 12288 + ty * 4096);
        bw[(2 * pair + kk) * 3 + ty] = cat(trd(a), trd(a + 512));
      }
  };
  f32x2 xn[10][4];
  v4u ga_[4]; v2u pv;
  const u32 sMG = WS_MODG + w * 1024;
  {
    float u[10];
    sfor<0, 10>([&](auto I) {
      constexpr int i = decltype(I)::value;
      if constexpr (i == 0) asm volatile("s_waitcnt vmcnt(11)" : "+v"(xr[0]), "+v"(xr[1]), "+v"(ep)::"memory");
      if constexpr (i == 2) asm volatile("s_waitcnt vmcnt(9)" : "+v"(xr[2]), "+v"(xr[3])::"memory");
      if constexpr (i == 4) asm volatile("s_waitcnt vmcnt(10)" : "+v"(xr[4]), "+v"(xr[5])::"memory");
      if constexpr (i == 6) asm volatile("s_waitcnt vmcnt(8)" : "+v"(xr[6]), "+v"(xr[7])::"memory");
      if constexpr (i == 8) asm volatile("s_waitcnt vmcnt(9)" : "+v"(xr[8]), "+v"(xr[9])::"memory");
      float s = 0.f, q = 0.f;
#pragma unroll
      for (int e = 0; e < 4; ++e) { s = dot2(xr[i][e], 0x3f803f80u, s); q = dot2(xr[i][e], xr[i][e], q); }
      u[i] = sw32_add(s, q);
      PIN(u[i]);
      if constexpr (i == 3) stage_b1(IC<1>{}, IC<L_XY + 24576>{});
      if constexpr (i == 7) stage_b1(IC<2>{}, IC<L_XY + 49152>{});
      if constexpr (i == 9) {
        const u32 s0 = sMG, s1 = sMG + 24576, s2 = sMG + 49152, s3 = sMG + 73728;
        ld16s<0>(ga_[0], vl16, rWS, s0); ld16s<0>(ga_[1], vl16, rWS, s1); ld16s<0>(ga_[2], vl16, rWS, s2); ld16s<0>(ga_[3], vl16, rWS, s3);
      }
    });
    float t[5];
#pragma unroll
    for (int i = 0; i < 5; ++i) t[i] = row16_sum(sw16_add(u[i], u[i + 5]));
    float v = t[0];
#pragma unroll
    for (int i = 1; i < 5; ++i) v = c16 == i ? t[i] : v;
    auto r = __builtin_amdgcn_permlane32_swap(asu(v), asu(v), false, false);
    const float mean = asf(r[0]) * (1.f / 512.f);
    const float var = fmaxf(asf(r[1]) * (1.f / 512.f) - mean * mean, 0.f);
    float rs = __builtin_amdgcn_rsqf(var + 1e-5f), nm = -mean * rs;
    PIN(rs); PIN(nm);
    STAMP(4);
    { const u32 vo = ((lane >> 3) & 3) * 24576 + (lane & 7) * 8, so = WS_MODG + (1024 + 8 * b) * 8; LD8S(pv, vo, rWS, so, 0); }
    if (w >= 4) {
      const int u = lane >> 2, row = (u & 3) | ((u >> 1) & 4) | ((u << 1) & 8);
      const u32 vo = row * 2752 + (lane & 3) * 16, so = (g & 15) * (32 * 2752) + rcb * 16, mb = L_Z + 49152 + (w & 3) * 4096;
      dma<0, 0>(mb, vo, rW1, so); dma<1024, 16 * 2752>(mb, vo, rW1, so); dma<2048, 0>(mb, vo, rW3, so); dma<3072, 16 * 2752>(mb, vo, rW3, so);
    } else {
      const int u = lane >> 1, row = (u & 3) | ((u >> 1) & 4) | ((u << 1) & 8) | (u & 16);
      const u32 vo = row * 1024 + (lane & 1) * 16, so = g * 32768 + j * 128 + w * 32, mb = L_Z + 65536 + w * 2048;
      dma<0, 0>(mb, vo, rW2, so); dma<1024, 0>(mb, vo, rW2, so + (g < 11 ? 32 * 32768 : 0));
      const int rwo = (u & 7) | ((u >> 1) & 8) | ((u << 1) & 16);
      dma<0, 0>(76800 + w * 1024, rwo * 1024 + (lane & 1) * 16, rWO, (g & 15) * 32768 + j * 128 + w * 32);
    }
    sfor<0, 10>([&](auto I) {
      constexpr int i = decltype(I)::value;
      if constexpr (i == 2) stage_b1(IC<3>{}, IC<L_Z>{});
      if constexpr (i == 6) stage_b1(IC<4>{}, IC<L_Z + 24576>{});
      const float a = rdl(rs, (i / 5) * 16 + i % 5), c = rdl(nm, (i / 5) * 16 + i % 5);
#pragma unroll
      for (int e = 0; e < 4; ++e) { xn[i][e] = unp(xr[i][e]) * a + c; PIN(xn[i][e]); }
    });
  }
  const u32 tag = __builtin_amdgcn_readfirstlane(ep) + 1u;
  u32 m1; asm("s_lshr_b32 %0, 1, %1" : "=s"(m1) : "s"(w) : "scc");
#define RAISE(fo) raise_flag(rWS, (fo), tag, vl16, m1)
#define SIGNAL(idx, fo) signal_flag(rWS, L_CNT + 4 * (idx), (fo), tag)
  STAMP(5);
  asm volatile("s_waitcnt vmcnt(9)" : "+v"(ga_[0]), "+v"(ga_[1]), "+v"(ga_[2]), "+v"(ga_[3]), "+v"(pv)::"memory");
  auto gsum = [&](v4u (&a)[4], f32x2& o) -> u32 {
    o.x = (asf(a[0].x) + asf(a[1].x)) + (asf(a[2].x) + asf(a[3].x)); o.y = (asf(a[0].z) + asf(a[1].z)) + (asf(a[2].z) + asf(a[3].z));
    return ((a[0].y ^ tag) | (a[0].w ^ tag) | (a[1].y ^ tag) | (a[1].w ^ tag)) | ((a[2].y ^ tag) | (a[2].w ^ tag) | (a[3].y ^ tag) | (a[3].w ^ tag));
  };
  auto gather = [&](u32 voff, u32 soff, f32x2& o) {
    for (u32 sp = 0; sp < SPIN_CAP; ++sp) {
      asm volatile("" ::: "memory");
      v4u a[4];
#pragma unroll
      for (int q = 0; q < 4; ++q) a[q] = __builtin_amdgcn_raw_buffer_load_b128(rWS, voff, soff + q * 24576, 16);
      if (!__builtin_amdgcn_ballot_w64(gsum(a, o) != 0u)) break;
    }
  };
  const float one = w >= 4 ? 1.f : 0.f;
  {
    f32x2 mA;
    if (__builtin_amdgcn_ballot_w64(gsum(ga_, mA) != 0u)) gather(vl16, sMG, mA);
    *(LAS f32x2*)(L + L_MODA + w * 512 + (vl16 >> 1)) = mA + one;
  }
  BAR();
  if (w == 7) {
    const u32 vo = ((lane >> 3) & 3) * 24576 + (lane & 7) * 8, so = WS_MODG + (1024 + 8 * b) * 8;
    for (u32 sp = 0; sp < SPIN_CAP && __builtin_amdgcn_ballot_w64(pv.y != tag); ++sp) {
      asm volatile("" ::: "memory");
      pv = __builtin_amdgcn_raw_buffer_load_b64(rWS, vo, so, 16);
    }
    float sv = asf(pv.x);
    sv += dppf<0x128>(sv); sv = sw16_add(sv, sv);
    pv.x = asu(sv);
    __builtin_amdgcn_raw_buffer_store_b64(pv, rWS, (lane & 7) * 8, WS_MODF + (1024 + 8 * b) * 8, 16);
  }
  STAMP(6);
  int vma = L_MODA + 2 * vl16; PIN(vma);
  const f32x4 ms0 = *(LAS f32x4*)(L + vma), ms1 = *(LAS f32x4*)(L + vma + 16);
  const f32x4 ma0 = *(LAS f32x4*)(L + vma + 2048), ma1 = *(LAS f32x4*)(L + vma + 2064);
  int tz0 = tB1 + L_Z; PIN(tz0);
  { int t2 = tB1 + 49152; PIN(t2); read_b1(0, tB1, 0); read_b1(1, tB1, 24576); read_b1(2, t2, 0); }
  {
    VMW(6);
    if (w >= 4) {
      LAS char* a0 = L + L_Z + 49152 + (w & 3) * 4096 + ((lane >> 1) & 1) * 2048 + 64 * ((c16 >> 2) + 4 * (g4 & 1) + 16 * (g4 >> 1)) + 8 * (lane & 1);
#pragma unroll
      for (int ct = 0; ct < 3; ++ct) {
        const int c = min(cs0 + T0 + ct - rcb, 3) * 16;
        const v4u f = cat(trd(a0 + c), trd(a0 + c + 512));
        const u32 sf = WS_B4T + ((j * 16 + (g & 15)) * 24 + T0 + ct) * 1024;
        asm volatile("buffer_store_dwordx4 %0, %1, %2, %3 offen sc1\n\ts_nop 1" ::"v"(f), "v"(vl16), "s"(rWS), "s"(sf) : "memory");
      }
    }
    else {
      LAS char* a0 = L + L_Z + 65536 + w * 2048 + 32 * ((c16 >> 2) + 4 * (g4 & 1) + 16 * (g4 >> 1)) + 8 * (lane & 3);
#pragma unroll
      for (int i = 0; i < 2; ++i) {
        const v4u f = cat(trd(a0 + i * 1024), trd(a0 + i * 1024 + 256));
        const u32 sf = WS_B5T + ((j * 43 + g + (i && g < 11 ? 32 : 0)) * 4 + w) * 1024;
        asm volatile("buffer_store_dwordx4 %0, %1, %2, %3 offen sc1\n\ts_nop 1" ::"v"(f), "v"(vl16), "s"(rWS), "s"(sf) : "memory");
      }
      LAS char* a1 = L + 76800 + w * 1024 + 32 * ((c16 >> 2) + 4 * (g4 & 1) + 16 * (g4 >> 1)) + 8 * (lane & 3);
      const v4u f = cat(trd(a1), trd(a1 + 256));
      const u32 sf = WS_B3T + ((j * 16 + (g & 15)) * 4 + w) * 1024;
      asm volatile("buffer_store_dwordx4 %0, %1, %2, %3 offen sc1\n\ts_nop 1" ::"v"(f), "v"(vl16), "s"(rWS), "s"(sf) : "memory");
    }
  }
  BAR();
  STAMP(7);
  {
    int hw1 = (hw0 ^ 128) + 8192; PIN(hw1);
    sfor<0, 10>([&](auto I) {
      constexpr int i = decltype(I)::value;
      v4u o;
      o.x = pk2(xn[i][0] * ma0.xy + ms0.xy); o.y = pk2(xn[i][1] * ma0.zw + ms0.zw);
      o.z = pk2(xn[i][2] * ma1.xy + ms1.xy); o.w = pk2(xn[i][3] * ma1.zw + ms1.zw);
      if constexpr (i == 8) { hw0 += 65536; hw1 += 65536; }
      *(LAS v4u*)(L + ((i & 1) ? hw1 : hw0) + ((i >> 1) & 3) * 16384) = o;
      SCHED();
    });
  }
  VMW(0);
  BAR();
  RAISE(WS_RFLAG + (j * 32 + g) * 4);
  u32 flr;
  STAMP(8);

  const f32x4 z4 = {0.f, 0.f, 0.f, 0.f};
#define AFRAG(off, ks) (*(LAS v4u*)(L + afo[(ks) & 3] + ((off) + ((ks) >> 2) * 256)))
  const int ch0 = cs0 + 3 * w;
  constexpr int ND = NODMA ? 0 : 1;
  v4u b4[48];
  const u32 sB4 = WS_B4T + (j * 16 * 24 + 3 * w) * 1024;
  auto load_b4 = [&](auto NC) {
    constexpr int n = decltype(NC)::value, ks = n / 3, ct = n % 3;
    const u32 so = sB4 + ((ks + 2 * rotp) & 15) * 24576;
    v4u& r = b4[n];
    if constexpr (ct == 0) LD16S(r, vl16, rWS, so, 0); else if constexpr (ct == 1) LD16S(r, vl16, rWS, so, 1024); else LD16S(r, vl16, rWS, so, 2048);
  };
  auto landed_b4 = [&](auto AC, auto BC) {
#pragma unroll
    for (int i = decltype(AC)::value; i < decltype(BC)::value; ++i) { v4u& r = b4[i]; PIN(r); }
  };
  v4u gb_, gg_;
  u32 vgg, fl0, vRow;
  int pw, pr;
  f32x4 aKh = z4, aVh = z4, aQ = z4, aK = z4, aV = z4, bQ = z4, bK = z4, bV = z4;
  {
    v4u ah = AFRAG(0, 0), a1 = AFRAG(16384, 0), a2 = AFRAG(32768, 0);
    int l3, c3, g3; f32x2 mB, mG;
    auto job = [&](auto NC) {
      constexpr int n = decltype(NC)::value;
      if constexpr (n == 3) { l3 = opq(vl16) >> 4; c3 = l3 & 15; g3 = l3 >> 4; PIN(c3); PIN(g3); vgg = (vl16 < 512 ? 8192 : 20480 - 512) + 512 * j + vl16; PIN(vgg); }
      if constexpr (n == 7) { vRow = (8 * w + (l3 >> 3)) * 1024 + (l3 & 7) * 16; PIN(vRow); }
      if constexpr (n == 8) { pr = (8 * w + (l3 >> 3)) * 256 + (((2 * (l3 & 7)) ^ ((l3 >> 3) & 7)) << 4); PIN(pr); }
      if constexpr (n == 10) { pw = kq * 16384 + (32 * mh + c3) * 256 + ((g3 ^ (c3 & 3)) << 4) + ((c3 & 4) << 4); PIN(pw); }
      if constexpr (n == 31) { mB.x = asf(gb_.x) + one; PIN(mB.x); mB.y = asf(gb_.z) + one; PIN(mB.y); *(LAS f32x2*)(L + L_MODA + w * 512 + (vl16 >> 1)) = mB; }
      if constexpr (n == 32) { mG.x = asf(gg_.x); mG.y = asf(gg_.z); *(LAS f32x2*)(L + L_GAB + (vl16 >> 1)) = mG; }
      if constexpr (n == 34) { fl0 = (gb_.y ^ tag) | (gb_.w ^ tag) | (gg_.y ^ tag) | (gg_.w ^ tag); PIN(fl0); }
      if constexpr (n == 7) { const u32 s0 = WS_MODF + 12288 + w * 1024; LD16S(gb_, vl16, rWS, s0, 0); }
      if constexpr (n == 9) { const u32 s0 = WS_MODF; LD16S(gg_, vgg, rWS, s0, 0); }
      if constexpr (n < 6 && !(n & 1)) b1_piece(IC<5>{}, IC<L_Z + 49152>{}, IC<n / 2>{});
      if constexpr (n == 6) arrive(L_CNT + 8);
      if constexpr (n == 10) await8(L_CNT + 8);
      if constexpr (n >= 10 && n < 22 && !(n & 1)) b1_piece(IC<6 + (n - 10) / 6>{}, IC<L_Z + ((n - 10) / 6) * 24576>{}, IC<((n - 10) / 2) % 3>{});
      if constexpr (n == 21) { const u32 vr = (vl16 >> 2) & 124, sr = WS_RFLAG + j * 128; LD4S(flr, vr, rWS, sr); }
      if constexpr (n == 26) {
        asm volatile("s_waitcnt vmcnt(0)" : "+v"(gb_), "+v"(gg_), "+v"(flr)::"memory");
        arrive(L_CNT + 12);
        const u32 vr = (vl16 >> 2) & 124, sr = WS_RFLAG + j * 128; u32 cnt = SPIN_CAP;
        asm volatile("1:\n\tv_cmp_ne_u32 vcc, %5, %0\n\ts_cbranch_vccz 2f\n\tbuffer_load_dword %0, %2, %3, %4 offen sc1\n\ts_waitcnt vmcnt(0)\n\t"
                     "s_sub_u32 %1, %1, 1\n\ts_cbranch_scc0 1b\n\t2:" : "+v"(flr), "+s"(cnt) : "v"(vr), "s"(rWS), "s"(sr), "s"(tag) : "vcc", "scc", "memory");
      }
      if constexpr (n == 28) { await8(L_CNT + 12); int tz2 = tz0 + 49152; PIN(tz2); read_b1(5, tz2, 0); }
      if constexpr (n == 32) read_b1(6, tz0, 0);
      if constexpr (n == 37) read_b1(7, tz0, 24576);
      if constexpr (n >= 27 && n < 47 && (n & 1)) load_b4(IC<(n - 27) / 2>{});
    };
    sfor<0, 16>([&](auto KS) {
      constexpr int ks = decltype(KS)::value;
      KSTAMP(1, ks);
      if constexpr (ks == 0) read_b1(3, tz0, 0);
      if constexpr (ks == 1) read_b1(4, tz0, 24576);
      SCHED();
      v4u nh = ah, n1 = a1, n2 = a2;
      aKh = mfma32(bw[ks * 3 + 1], ah, aKh);
      aVh = mfma32(ah, bw[ks * 3 + 2], aVh);
      if constexpr (ks < 15) nh = AFRAG(0, ks + 1);
      job(IC<3 * ks>{});
      SCHED();
      aQ = mfma32(bw[ks * 3 + 0], a1, aQ);
      aK = mfma32(bw[ks * 3 + 1], a1, aK);
      aV = mfma32(a1, bw[ks * 3 + 2], aV);
      if constexpr (ks < 15) n1 = AFRAG(16384, ks + 1);
      job(IC<3 * ks + 1>{});
      SCHED();
      bQ = mfma32(bw[ks * 3 + 0], a2, bQ);
      bK = mfma32(bw[ks * 3 + 1], a2, bK);
      bV = mfma32(a2, bw[ks * 3 + 2], bV);
      if constexpr (ks < 15) n2 = AFRAG(32768, ks + 1);
      job(IC<3 * ks + 2>{});
      ah = nh; a1 = n1; a2 = n2;
      SCHED();
    });
  }
  STAMP(9); KSTAMP(1, 16);
  v4u b3[16];
  auto load_b3 = [&](auto NC) { constexpr int n = decltype(NC)::value; ld16s<(n & 3) * 1024>(b3[n], vl16, rWS, WS_B3T + (j * 16 + 4 * kq + (n >> 2)) * 4096); };
  {
    const u32 sOst = WS_O + ((g * 4 + 2 * blk2) * 16 + 2 * j + (hd >> 1)) * 1024;
    const v2u k1p = pk4(aK), v1p = pk4(aV);
    sfor<0, 6>(load_b3);
    const v2u o0 = attend(pk4(aKh), pk4(aVh), k1p, v1p, pk4(aQ), am0, am1, ahm, g == 0 && blk2 == 0);
    const v2u o1 = attend(k1p, v1p, pk4(bK), pk4(bV), pk4(bQ), am0, am1, ahm, false);
    __builtin_amdgcn_raw_buffer_store_b64(o0, rWS, vOst, sOst, 16);
    __builtin_amdgcn_raw_buffer_store_b64(o1, rWS, vOst, sOst + 16384, 16);
  }
  if (__builtin_amdgcn_ballot_w64(fl0 != 0u)) {
    for (u32 sp = 0; sp < SPIN_CAP; ++sp) {
      asm volatile("" ::: "memory");
      gb_ = __builtin_amdgcn_raw_buffer_load_b128(rWS, vl16, WS_MODF + 12288 + w * 1024, 16);
      gg_ = __builtin_amdgcn_raw_buffer_load_b128(rWS, vgg, WS_MODF, 16);
      if (!__builtin_amdgcn_ballot_w64(((gb_.y ^ tag) | (gb_.w ^ tag) | (gg_.y ^ tag) | (gg_.w ^ tag)) != 0u)) break;
    }
    const f32x2 mB = {asf(gb_.x) + one, asf(gb_.z) + one}, mG = {asf(gg_.x), asf(gg_.z)};
    *(LAS f32x2*)(L + L_MODA + w * 512 + (vl16 >> 1)) = mB;
    *(LAS f32x2*)(L + L_GAB + (vl16 >> 1)) = mG;
  }
  STAMP(10);
  VMW(0);
  landed_b4(IC<0>{}, IC<10>{});
  BAR();
  RAISE(flag_off(0, g, 0) + j * 128);
  STAMP(11);

  v4u xv;
  { const u32 sx = g * 65536 + j * 128; LD16(xv, vRow, rX, sx, 0); }
  sfor<6, 12>(load_b3);
  const u64 care2 = 0x0101010101010101ull * (3u << (2 * kq));
  poll_flags(rWS, vfl, flag_off(0, g, 0), care2, tag);
  PIN(xv);
  sfor<12, 16>(load_b3);
  v4u af[8];
  {
    const u32 so = WS_O + ((g * 4 + 2 * mh) * 16 + 4 * kq) * 1024, so1 = so + 16384;
    LD16S(af[0], vl16, rWS, so, 0); LD16S(af[1], vl16, rWS, so, 1024); LD16S(af[2], vl16, rWS, so, 2048); LD16S(af[3], vl16, rWS, so, 3072);
    LD16S(af[4], vl16, rWS, so1, 0); LD16S(af[5], vl16, rWS, so1, 1024); LD16S(af[6], vl16, rWS, so1, 2048); LD16S(af[7], vl16, rWS, so1, 3072);
  }
  STAMP(12);
  sfor<0, 2>([&](auto M) {
    constexpr int m = decltype(M)::value;
    asm volatile("s_waitcnt vmcnt(%c4)" : "+v"(af[4 * m]), "+v"(af[4 * m + 1]), "+v"(af[4 * m + 2]), "+v"(af[4 * m + 3]) : "i"(m == 0 ? 4 : 2) : "memory");
    if constexpr (m == 0) {
#pragma unroll
      for (int i = 0; i < 16; ++i) PIN(b3[i]);
    }
    f32x4 acc[4] = {z4, z4, z4, z4};
    sfor<0, 4>([&](auto SC) {
      constexpr int sx = decltype(SC)::value;
#pragma unroll
      for (int n = 0; n < 4; ++n) acc[n] = mfma32(b3[sx * 4 + n], af[4 * m + sx], acc[n]);
      if constexpr (sx & 1) load_b4(IC<10 + 2 * m + (sx >> 1)>{});
      SCHED();
    });
#pragma unroll
    for (int n = 0; n < 4; ++n) *(LAS f32x4*)(L + ((n & 1) ? (pw ^ 64) : pw) + (m * 4096 + (n >> 1) * 128)) = acc[n];
  });
  BAR();
  STAMP(13);
  sfor<14, 18>(load_b4);
  {
    f32x4 y0 = *(LAS f32x4*)(L + pr), y1 = *(LAS f32x4*)(L + (pr ^ 16));
#pragma unroll
    for (int q = 1; q < 4; ++q) { y0 += *(LAS f32x4*)(L + pr + q * 16384); y1 += *(LAS f32x4*)(L + (pr ^ 16) + q * 16384); }
    const int ga = L_GAB + ((vl16 & 112) << 1);
    const f32x4 g0 = *(LAS f32x4*)(L + ga), g1 = *(LAS f32x4*)(L + ga + 16);
    const f32x4 x0 = f32x4{blo(xv.x), bhi(xv.x), blo(xv.y), bhi(xv.y)} + g0 * y0, x1 = f32x4{blo(xv.z), bhi(xv.z), blo(xv.w), bhi(xv.w)} + g1 * y1;
    { const v4u o = cat(pk4(x0), pk4(x1)); const u32 so = WS_X1 + g * 65536 + j * 128;
      asm volatile("buffer_store_dwordx4 %0, %1, %2, %3 offen sc1\n\ts_nop 1" ::"v"(o), "v"(vRow), "s"(rWS), "s"(so) : "memory"); }
    const f32x4 sv = x0 + x1;
    float mean = (sv.x + sv.y) + (sv.z + sv.w);
    mean += dppf<0xB1>(mean); mean += dppf<0x4E>(mean); mean += dppf<0x141>(mean);
    mean *= 1.f / 64.f;
    const f32x4 d0 = x0 - mean, d1 = x1 - mean, dd = d0 * d0 + d1 * d1;
    float m2 = (dd.x + dd.y) + (dd.z + dd.w);
    m2 += dppf<0xB1>(m2); m2 += dppf<0x4E>(m2); m2 += dppf<0x141>(m2);
    v2u st; st.x = asu(mean); st.y = asu(m2);
    __builtin_amdgcn_raw_buffer_store_b64(st, rWS, (vRow >> 10) << 6, WS_STAT + g * 4096 + j * 8, 16);
  }
  STAMP(14);
  VMW(0);
  landed_b4(IC<10>{}, IC<18>{});
  BAR();
  RAISE(flag_off(1, g, 0) + j * 128);
  sfor<18, 24>(load_b4);
  STAMP(15);

  v4u x1v[4]; v2u stv;
  auto ln2_load = [&](int h) {
    const u32 so = WS_X1 + g * 65536 + (32 * (w >> 2) + 16 * h + 4 * (w & 3)) * 1024;
    LD16S(x1v[0], vl16, rWS, so, 0); LD16S(x1v[1], vl16, rWS, so, 1024); LD16S(x1v[2], vl16, rWS, so, 2048); LD16S(x1v[3], vl16, rWS, so, 3072);
    const u32 vs = (vl16 >> 1) & 255, ss = WS_STAT + g * 4096 + (32 * (w >> 2) + 16 * h + 4 * (w & 3)) * 64;
    LD8S(stv, vs, rWS, ss, 0);
  };
  float l2r, l2n;
  auto ln2_stats = [&]() {
    const float mj = asf(stv.x);
    float mean = mj;
    mean += dppf<0xB1>(mean); mean += dppf<0x4E>(mean); mean += dppf<0x141>(mean);
    mean *= 0.125f;
    const float dm = mj - mean;
    float m2 = asf(stv.y) + 64.f * dm * dm;
    m2 += dppf<0xB1>(m2); m2 += dppf<0x4E>(m2); m2 += dppf<0x141>(m2);
    l2r = __builtin_amdgcn_rsqf(m2 * (1.f / 512.f) + 1e-5f);
    l2n = -mean * l2r;
  };
  u32 vH;
  u32 hmask[3];
#pragma unroll
  for (int ct = 0; ct < 3; ++ct)
    asm("s_cmp_lt_u32 %1, %2\n\ts_cselect_b32 %0, -1, 0" : "=s"(hmask[ct]) : "s"((u32)(ch0 + ct)), "s"((u32)cend) : "scc");
  auto hh_store = [&](v2u o, int ct, int h) {
    const u32 so = WS_HH + ((g * 4 + h) * 172 + ch0 + ct) * 256;
    asm volatile("s_mov_b32 exec_lo, %4\n\ts_mov_b32 exec_hi, %4\n\tbuffer_store_dwordx2 %0, %1, %2, %3 offen sc1\n\ts_mov_b64 exec, -1"
                 ::"v"(o), "v"(vH), "s"(rWS), "s"(so), "s"(hmask[ct]) : "memory");
  };
  auto swiglu_tile = [&](f32x4 (&acc)[3][2], int ct, int h) {
    f32x4 G, U, R;
#pragma unroll
    for (int e = 0; e < 4; ++e) {
      auto r = __builtin_amdgcn_permlane32_swap(asu(acc[ct][0][e]), asu(acc[ct][1][e]), false, false);
      G[e] = asf(r[0]); U[e] = asf(r[1]);
    }
    const f32x4 T = G * -1.4426950408889634f;
#pragma unroll
    for (int e = 0; e < 4; ++e) R[e] = __builtin_amdgcn_rcpf(1.f + __builtin_amdgcn_exp2f(T[e]));
    hh_store(pk4((G * U) * R), ct, h);
  };
#pragma unroll
  for (int i = 0; i < 4; ++i) { afo[i] -= blk2 * 32768; PIN(afo[i]); }
  int vm2 = L_MODA + 2 * vl16; PIN(vm2);
  int h2w = w * 4096 + (((vl16 - 128 * rotp) & 1023) ^ ((4 * w & 15) << 4)); PIN(h2w);
  poll_flags(rWS, vfl, flag_off(1, g, 0), ~0ull, tag);
  landed_b4(IC<18>{}, IC<24>{});
  ln2_load(0);
  sfor<24, 28>(load_b4);
  {
    const f32x4 s0 = *(LAS f32x4*)(L + vm2), s1 = *(LAS f32x4*)(L + vm2 + 16), a0 = *(LAS f32x4*)(L + vm2 + 2048), a1 = *(LAS f32x4*)(L + vm2 + 2064);
    asm volatile("s_waitcnt vmcnt(%c5)" : "+v"(x1v[0]), "+v"(x1v[1]), "+v"(x1v[2]), "+v"(x1v[3]), "+v"(stv) : "i"(4) : "memory");
    ln2_stats();
#pragma unroll
    for (int i = 0; i < 4; ++i) {
      const float rs = rdl(l2r, 8 * i), nn = rdl(l2n, 8 * i);
      v4u o;
      o.x = pk2((unp(x1v[i].x) * rs + nn) * a0.xy + s0.xy); o.y = pk2((unp(x1v[i].y) * rs + nn) * a0.zw + s0.zw);
      o.z = pk2((unp(x1v[i].z) * rs + nn) * a1.xy + s1.xy); o.w = pk2((unp(x1v[i].w) * rs + nn) * a1.zw + s1.zw);
      *(LAS v4u*)(L + (h2w ^ (i << 4)) + i * 1024) = o;
    }
  }
  ln2_load(1);
  BAR();
  STAMP(16);
  f32x4 accA[3][2] = {{z4, z4}, {z4, z4}, {z4, z4}};
  {
    f32x4 cs, ca; float rsv[4], nnv, t[4]; v2u ov;
    auto ln_op = [&](auto NC) {
      constexpr int n = decltype(NC)::value, u = n / 16, k = n % 16, hf = u & 1, i = u >> 1;
      if constexpr (u < 8) {
        if constexpr (k == 0) {
          cs = *(LAS f32x4*)(L + vm2 + hf * 16); ca = *(LAS f32x4*)(L + vm2 + 2048 + hf * 16);
          if constexpr (hf == 0) nnv = q_mov(rdl(l2n, 8 * i));
        }
        else if constexpr (k == 1) t[0] = q_shl(x1v[i][2 * hf]);
        else if constexpr (k == 2) t[1] = q_hi(x1v[i][2 * hf]);
        else if constexpr (k == 3) t[2] = q_shl(x1v[i][2 * hf + 1]);
        else if constexpr (k == 4) t[3] = q_hi(x1v[i][2 * hf + 1]);
        else if constexpr (k < 9) t[k - 5] = q_fmas(t[k - 5], rsv[i], nnv);
        else if constexpr (k < 13) t[k - 9] = q_fma(t[k - 9], ca[k - 9], cs[k - 9]);
        else if constexpr (k == 13) ov.x = q_cvt(t[0], t[1]);
        else if constexpr (k == 14) ov.y = q_cvt(t[2], t[3]);
        else {
          *(LAS v2u*)(L + (h2w ^ (i << 4)) + (32768 + i * 1024 + hf * 8)) = ov;
        }
      }
    };
    v4u a0 = AFRAG(0, 0), a1 = AFRAG(16384, 0);
    sfor<0, 16>([&](auto KS) {
      constexpr int ks = decltype(KS)::value;
      KSTAMP(3, ks);
      v4u n0 = a0, n1 = a1;
      if constexpr (ks < 15) { n0 = AFRAG(0, ks + 1); n1 = AFRAG(16384, ks + 1); }
      if constexpr (ks == 3) {
        asm volatile("s_waitcnt vmcnt(6)" : "+v"(x1v[0]), "+v"(x1v[1]), "+v"(x1v[2]), "+v"(x1v[3]), "+v"(stv)::"memory");
        landed_b4(IC<24>{}, IC<28>{});
      }
      if constexpr (ks == 9) { VMW(4); landed_b4(IC<28>{}, IC<39>{}); }
      if constexpr (ks == 13) { VMW(2); landed_b4(IC<39>{}, IC<46>{}); }
      if constexpr (ks == 15) { VMW(0); landed_b4(IC<46>{}, IC<48>{}); }
      SCHED();
      if constexpr (ks == 3) { ln2_stats(); PIN(l2r); PIN(l2n);
#pragma unroll
        for (int i = 0; i < 4; ++i) rsv[i] = rdl(l2r, 8 * i); }
      sfor<0, 6>([&](auto MC) {
        constexpr int m = decltype(MC)::value, ct = m >> 1;
        if constexpr (m & 1) accA[ct][1] = mfma32(b4[ks * 3 + ct], a1, accA[ct][1]); else accA[ct][0] = mfma32(b4[ks * 3 + ct], a0, accA[ct][0]);
        if constexpr (ks < 6 && m % 3 == 0) load_b4(IC<28 + ks * 2 + m / 3>{});
        if constexpr (ks >= 6 && ks < 9 && m == 0) load_b4(IC<40 + (ks - 6)>{});
        if constexpr (ks >= 9 && ks < 12 && (m == 0 || (m == 3 && ks < 11))) load_b4(IC<43 + (ks - 9) * 2 + (m == 3)>{});
        if constexpr (ks >= 4) {
          constexpr int o = (ks - 4) * 15 + (m >> 1) * 5 + (m & 1) * 2;
          ln_op(IC<o>{}); ln_op(IC<o + 1>{});
          if constexpr (m & 1) ln_op(IC<o + 2>{});
          SCHED();
        }
      });
      a0 = n0; a1 = n1;
      SCHED();
    });
  }
  vH = (vl16 & 255) + ((vl16 >> 5) & 8) + (vl16 >> 9) * (2 * 172 * 256); PIN(vH);
  BAR();
  STAMP(17); KSTAMP(3, 16);
  f32x4 accB[3][2] = {{z4, z4}, {z4, z4}, {z4, z4}};
  u32 vRow5; int pr5, pw5, gb5;
  const int kb5 = 5 * w + min(w, 3);
  u64 care5 = 0x0101010101010101ull * ((0x80c06030180c0603ull >> (8 * w)) & 255u);
  asm volatile("" : "+s"(care5));
  v4u b5[24], hA[12], hB[12]; v2u xqA, xqB;
  const u32 sB5 = WS_B5T + (j * 43 + kb5) * 4096, d5 = (w + 5) >> 3;
  auto load_b5 = [&](auto NC) {
    constexpr int n = decltype(NC)::value, sx = n / 4, t = n % 4;
    const u32 so = sB5 + (sx == 5 ? (5 - d5) * 4096 : sx * 4096);
    ld16s<t * 1024>(b5[n], vl16, rWS, so);
  };
  auto load_h = [&](auto NC, v4u (&hf)[12], int h) {
    constexpr int n = decltype(NC)::value, sx = n / 2, m = n % 2;
    const u32 so = WS_HH + ((g * 4 + 2 * m + h) * 172 + 4 * kb5) * 256 + (sx == 5 ? (5 - d5) * 1024 : sx * 1024);
    ld16s<0>(hf[n], vl16, rWS, so);
  };
  auto poll5 = [&](int h) {
    const u32 fo = flag_off(2, g, h), vf = vfl; u32 t, cnt = SPIN_CAP;
    asm volatile("1:\n\tbuffer_load_dword %0, %2, %3, %4 offen sc1\n\ts_waitcnt vmcnt(0)\n\tv_cmp_ne_u32 vcc, %5, %0\n\ts_and_b64 vcc, vcc, %6\n\ts_cbranch_scc0 2f\n\t"
                 "s_sub_u32 %1, %1, 1\n\ts_cbranch_scc0 1b\n\t2:" : "=&v"(t), "+s"(cnt) : "v"(vf), "s"(rWS), "s"(fo), "s"(tag), "s"(care5) : "vcc", "scc", "memory");
  };
  {
    float G[4], U[4], T[4]; v2u ov;
    auto sw_op = [&](auto NC) {
      constexpr int n = decltype(NC)::value, ct = n / 22, k = n % 22;
      if constexpr (ct < 3) {
        if constexpr (k < 4) { auto r = __builtin_amdgcn_permlane32_swap(asu(accA[ct][0][k]), asu(accA[ct][1][k]), false, false); G[k] = asf(r[0]); U[k] = asf(r[1]); PIN(G[k]); PIN(U[k]); }
        else if constexpr (k < 6) { T[2 * (k - 4)] = q_mulk(G[2 * (k - 4)]); T[2 * (k - 4) + 1] = q_mulk(G[2 * (k - 4) + 1]); }
        else if constexpr (k < 10) T[k - 6] = q_exp(T[k - 6]);
        else if constexpr (k < 12) { T[2 * (k - 10)] = q_add1(T[2 * (k - 10)]); T[2 * (k - 10) + 1] = q_add1(T[2 * (k - 10) + 1]); }
        else if constexpr (k < 16) T[k - 12] = q_rcp(T[k - 12]);
        else if constexpr (k < 18) { G[2 * (k - 16)] = q_mul(G[2 * (k - 16)], U[2 * (k - 16)]); G[2 * (k - 16) + 1] = q_mul(G[2 * (k - 16) + 1], U[2 * (k - 16) + 1]); }
        else if constexpr (k < 20) { G[2 * (k - 18)] = q_mul(G[2 * (k - 18)], T[2 * (k - 18)]); G[2 * (k - 18) + 1] = q_mul(G[2 * (k - 18) + 1], T[2 * (k - 18) + 1]); }
        else if constexpr (k == 20) { ov.x = q_cvt(G[0], G[1]); ov.y = q_cvt(G[2], G[3]); }
        else hh_store(ov, ct, 0);
      }
    };
    v4u a0 = AFRAG(32768, 0), a1 = AFRAG(49152, 0);
    int l5, c5, g5;
    sfor<0, 16>([&](auto KS) {
      constexpr int ks = decltype(KS)::value;
      KSTAMP(4, ks);
      v4u n0 = a0, n1 = a1;
      if constexpr (ks > 0 && ks < 15) { n0 = AFRAG(32768, ks + 1); n1 = AFRAG(49152, ks + 1); }
      if constexpr (ks == 7) { VMW(0); SIGNAL(1, flag_off(2, g, 0) + j * 128); }
      if constexpr (ks == 14) poll5(0);
      SCHED();
      sfor<0, 6>([&](auto MC) {
        constexpr int m = decltype(MC)::value, ct = m >> 1, n = ks * 6 + m;
        if constexpr (m & 1) accB[ct][1] = mfma32(b4[ks * 3 + ct], a1, accB[ct][1]); else accB[ct][0] = mfma32(b4[ks * 3 + ct], a0, accB[ct][0]);
        if constexpr (n == 3) n0 = AFRAG(32768, 1);
        if constexpr (n == 5) n1 = AFRAG(49152, 1);
        if constexpr (n < 33) { sw_op(IC<2 * n>{}); sw_op(IC<2 * n + 1>{}); }
        if constexpr (n >= 12 && n < 32 && n % 4 == 0) load_b5(IC<(n - 12) / 4>{});
        if constexpr (n >= 42 && n < 75 && n % 3 == 0) load_b5(IC<5 + (n - 42) / 3>{});
        if constexpr (n >= 84 && n % 2 == 0) load_h(IC<(n - 84) / 2>{}, hA, 0);
        if constexpr (n == 38) { l5 = opq(vl16) >> 4; c5 = l5 & 15; g5 = l5 >> 4; PIN(c5); PIN(g5); }
        if constexpr (n == 40) { vRow5 = (32 * (w >> 2) + 4 * (w & 3) + g5) * 1024 + c5 * 8; PIN(vRow5); }
        if constexpr (n == 42) { pr5 = (4 * w + g5) * 256 + ((c5 ^ ((4 * w + g5) & 7)) << 4); PIN(pr5); }
        if constexpr (n == 44) { pw5 = w * 8192 + c5 * 256 + ((c5 & 4) << 4); PIN(pw5); }
        if constexpr (n == 46) { pw5 += (g5 ^ (c5 & 3)) << 4; PIN(pw5); gb5 = L_GAB + 256 + c5 * 16; PIN(gb5); }
        SCHED();
      });
      a0 = n0; a1 = n1;
      SCHED();
    });
  }
  STAMP(18); KSTAMP(4, 16);
  PIN(accB[0][0]); PIN(accB[0][1]); PIN(accB[1][0]); PIN(accB[1][1]); PIN(accB[2][0]); PIN(accB[2][1]);
  sfor<6, 9>([&](auto I) { load_h(I, hA, 0); });
  swiglu_tile(accB, 0, 1);
  sfor<9, 12>([&](auto I) { load_h(I, hA, 0); });
  swiglu_tile(accB, 1, 1);
  swiglu_tile(accB, 2, 1);
  VMW(0);
  BAR();
  RAISE(flag_off(2, g, 1) + j * 128);
  STAMP(19);
#pragma unroll
  for (int i = 0; i < 12; ++i) PIN(hA[i]);
#pragma unroll
  for (int i = 0; i < 16; ++i) PIN(b5[i]);
  sfor<16, 20>(load_b5);
  auto p5_half = [&](auto HC, v4u (&hf)[12]) {
    constexpr int h = decltype(HC)::value;
    f32x4 acc[2][4] = {{z4, z4, z4, z4}, {z4, z4, z4, z4}};
    sfor<0, 6>([&](auto SC) {
      constexpr int sx = decltype(SC)::value;
      if constexpr (h == 0 && sx == 1) { sfor<20, 24>(load_b5);
        const u32 sq = WS_X1 + g * 65536 + j * 128, sq1 = sq + 16384, vr5 = vRow5; const rsrc_t rr = rWS; LD8S(xqA, vr5, rr, sq, 0); LD8S(xqB, vr5, rr, sq1, 0); }
      if constexpr (h == 0 && sx == 4) {
        poll5(1);
#pragma unroll
        for (int i = 16; i < 24; ++i) PIN(b5[i]);
        PIN(xqA); PIN(xqB);
      }
      if constexpr (h == 1 && sx == 0) asm volatile("s_waitcnt vmcnt(9)" : "+v"(hf[0]), "+v"(hf[1]), "+v"(hf[2]), "+v"(hf[3])::"memory");
      if constexpr (h == 1 && sx == 2) asm volatile("s_waitcnt vmcnt(5)" : "+v"(hf[4]), "+v"(hf[5]), "+v"(hf[6]), "+v"(hf[7])::"memory");
      if constexpr (h == 1 && sx == 4) asm volatile("s_waitcnt vmcnt(1)" : "+v"(hf[8]), "+v"(hf[9]), "+v"(hf[10]), "+v"(hf[11])::"memory");
      if constexpr (sx == 5) { if (w >= 3) { hf[10] = v4u{0u, 0u, 0u, 0u}; hf[11] = v4u{0u, 0u, 0u, 0u}; } }
#pragma unroll
      for (int m = 0; m < 2; ++m) {
#pragma unroll
        for (int n = 0; n < 4; ++n) acc[m][n] = mfma32(b5[sx * 4 + n], hf[2 * sx + m], acc[m][n]);
        if constexpr (h == 0 && sx >= 4) { if (m == 0) { load_h(IC<4 * (sx - 4)>{}, hB, 1); load_h(IC<4 * (sx - 4) + 1>{}, hB, 1); } else { load_h(IC<4 * (sx - 4) + 2>{}, hB, 1); load_h(IC<4 * (sx - 4) + 3>{}, hB, 1); } }
        SCHED();
      }
    });
    const int pwh = h ? opq(pw5 + 65536) : pw5;
#pragma unroll
    for (int m = 0; m < 2; ++m)
#pragma unroll
      for (int n = 0; n < 4; ++n) *(LAS f32x4*)(L + ((n & 1) ? (pwh ^ 64) : pwh) + (m * 4096 + (n >> 1) * 128)) = acc[m][n];
    if constexpr (h == 0) sfor<8, 12>([&](auto I) { load_h(I, hB, 1); });
    BAR();
    const int prh = h ? opq(pr5 + 65536) : pr5;
    f32x4 y = *(LAS f32x4*)(L + prh);
#pragma unroll
    for (int q = 1; q < 8; ++q) y += *(LAS f32x4*)(L + prh + q * 8192);
    const f32x4 gbv = *(LAS f32x4*)(L + gb5);
    const v2u xx = h ? xqB : xqA;
    const f32x4 xb = {blo(xx.x), bhi(xx.x), blo(xx.y), bhi(xx.y)};
    __builtin_amdgcn_raw_buffer_store_b64(pk4(xb + gbv * y), rOUT, vRow5, g * 65536 + h * 16384 + j * 128, 0);
  };
  STAMP(20);
  p5_half(IC<0>{}, hA);
  STAMP(21);
  p5_half(IC<1>{}, hB);
  STAMP(22);
  STAMP_DUMP();
  if (b == 0 && w == 0) {
    for (u32 sp = 0; sp < SPIN_CAP; ++sp) {
      asm volatile("" ::: "memory");
      const v4u v = __builtin_amdgcn_raw_buffer_load_b128(rWS, vl16, WS_ALIVE, 16);
      if (__all(v.x == tag && v.y == tag && v.z == tag && v.w == tag)) break;
    }
    __builtin_amdgcn_raw_buffer_store_b32(tag, rWS, 0, WS_EPOCH, 16);
  }
}
}

extern "C" void kernel_launch(void* const* d_in, const int* in_sizes, int n_in, void* d_out, int out_size, void* d_ws, size_t ws_size,
                              hipStream_t stream) {
  Params p;
  p.x = (const u16*)d_in[0]; p.wqkv = (const u16*)d_in[1]; p.wo = (const u16*)d_in[2];
  p.w1 = (const u16*)d_in[4]; p.w3 = (const u16*)d_in[5]; p.w2 = (const u16*)d_in[6];
  p.c = (const u16*)d_in[7]; p.wmod = (const u16*)d_in[8];
  p.out = (u16*)d_out; p.ws = (char*)d_ws;
  (void)hipFuncSetAttribute((const void*)fused_block, hipFuncAttributeMaxDynamicSharedMemorySize, L_TOTAL);
  fused_block<<<256, 512, L_TOTAL, stream>>>(p);
}
```

```cpp
#define SEG_A 19
#define SEG_B 22
#include <hip/hip_runtime.h>
#include <stdint.h>

#define KDBG 0
#define NODMA 0
#define CODEWARM 0
#define L2WARM 1
#define EARLYT 1
#define XMAP 2

#define DI __device__ __forceinline__
#define GAS __attribute__((address_space(1)))
#define LAS __attribute__((address_space(3)))
typedef unsigned u32;
typedef unsigned short u16;
typedef unsigned long long u64;
typedef u32 v4u __attribute__((ext_vector_type(4)));
typedef u32 v2u __attribute__((ext_vector_type(2)));
typedef float f32x4 __attribute__((ext_vector_type(4)));
typedef float f32x2 __attribute__((ext_vector_type(2)));
typedef __bf16 bf16x8 __attribute__((ext_vector_type(8)));
typedef __bf16 bf16x2 __attribute__((ext_vector_type(2)));
typedef short s16x4 __attribute__((ext_vector_type(4)));
typedef __amdgpu_buffer_rsrc_t rsrc_t;
typedef GAS u32 gu32;
typedef GAS u64 gu64;
#define RLX_AGENT __ATOMIC_RELAXED, __HIP_MEMORY_SCOPE_AGENT

namespace {
constexpr int S_ = 2048, D_ = 512, F_ = 1376;
constexpr u32 WS_EPOCH = 0;
constexpr u32 WS_ALIVE = 4096;
constexpr u32 WS_FLAG = 32768;
constexpr u32 WS_STAT = 262144;
constexpr u32 WS_MODG = 524288;
constexpr u32 WS_MODF = 655360;
constexpr u32 WS_O = 1u << 20;
constexpr u32 WS_X1 = 3u << 20;
constexpr u32 WS_HH = 5u << 20;
constexpr u32 WS_RFLAG = 8192;
constexpr u32 WS_B4T = 11u << 20;
constexpr u32 WS_B5T = 14u << 20;
constexpr u32 WS_B3T = (15u << 20) + (512u << 10);
constexpr u32 WS_DBG = 16u << 20;
constexpr u32 WS_BYTES = 32u << 20;
constexpr int L_XY = 0;
constexpr int L_Z = 81920;
constexpr int L_MISC = 158720;
constexpr int L_TOTAL = 163840;
constexpr int L_RED = 73728;
constexpr int L_MODA = L_MISC;
constexpr int L_CNT = L_MISC + 4096;
constexpr int L_GAB = L_MISC + 4096 + 64;
constexpr int L_JUNK = L_MISC + 4096 + 64 + 512;
constexpr int L_STAMP = 156672;
constexpr u32 SPIN_CAP = 1u << 18;
constexpr u32 CODE_BYTES = 26112;

DI f32x4 mfma32(v4u a, v4u b, f32x4 c) {
  return __builtin_amdgcn_mfma_f32_16x16x32_bf16(__builtin_bit_cast(bf16x8, a), __builtin_bit_cast(bf16x8, b), c, 0, 0, 0);
}
DI f32x4 mfma16(v2u a, v2u b, f32x4 c) {
  return __builtin_amdgcn_mfma_f32_16x16x16bf16_1k(__builtin_bit_cast(s16x4, a), __builtin_bit_cast(s16x4, b), c, 0, 0, 0);
}
DI u32 pk(float lo, float hi) { f32x2 v = {lo, hi}; return __builtin_bit_cast(u32, __builtin_convertvector(v, bf16x2)); }
DI u32 pk2(f32x2 v) { return __builtin_bit_cast(u32, __builtin_convertvector(v, bf16x2)); }
DI v2u pk4(f32x4 v) { v2u r; r.x = pk(v.x, v.y); r.y = pk(v.z, v.w); return r; }
DI float asf(u32 u) { return __builtin_bit_cast(float, u); }
DI u32 asu(float f) { return __builtin_bit_cast(u32, f); }
DI float blo(u32 u) { return asf(u << 16); }
DI float bhi(u32 u) { return asf(u & 0xffff0000u); }
DI f32x2 unp(u32 u) { f32x2 r = {blo(u), bhi(u)}; return r; }
DI float dot2(u32 a, u32 b, float c) { return __builtin_amdgcn_fdot2_f32_bf16(__builtin_bit_cast(bf16x2, a), __builtin_bit_cast(bf16x2, b), c, false); }
DI v2u trd(LAS char* p) { return __builtin_bit_cast(v2u, __builtin_amdgcn_ds_read_tr16_b64_v4i16((LAS s16x4*)p)); }
DI v4u cat(v2u a, v2u b) { v4u r; r.x = a.x; r.y = a.y; r.z = b.x; r.w = b.y; return r; }
template <int MI, int SI> DI void dma(u32 mb, u32 voff, rsrc_t srd, u32 sb) {
  u32 t;
  asm volatile("s_add_u32 m0, %1, %c2\n\ts_add_u32 %0, %3, %c4\n\tbuffer_load_dwordx4 %5, %6, %0 offen lds"
               : "=&s"(t) : "s"(mb), "i"(MI), "s"(sb), "i"(SI), "v"(voff), "s"(srd) : "scc");
}
DI void touch(u32 voff, rsrc_t srd, u32 sb) {
  asm volatile("s_mov_b32 m0, %c0\n\ts_nop 0\n\tbuffer_load_dword %1, %2, %3 offen lds" ::"i"(L_JUNK), "v"(voff), "s"(srd), "s"(sb));
}
#define BAR() asm volatile("s_waitcnt lgkmcnt(0)\n\ts_barrier" ::: "memory")
#define VMW(n) asm volatile("s_waitcnt vmcnt(" #n ")" ::: "memory")
#define LGW() asm volatile("s_waitcnt lgkmcnt(0)" ::: "memory")
#define SCHED() __builtin_amdgcn_sched_barrier(0)
#define LD16(dst, voff, srd, soff, IMM) asm volatile("buffer_load_dwordx4 %0, %1, %2, %3 offen offset:" #IMM : "=v"(dst) : "v"(voff), "s"(srd), "s"(soff))
#define LD16S(dst, voff, srd, soff, IMM) asm volatile("buffer_load_dwordx4 %0, %1, %2, %3 offen offset:" #IMM " sc1" : "=v"(dst) : "v"(voff), "s"(srd), "s"(soff))
#define LD8(dst, voff, srd, soff, IMM) asm volatile("buffer_load_dwordx2 %0, %1, %2, %3 offen offset:" #IMM : "=v"(dst) : "v"(voff), "s"(srd), "s"(soff))
#define LD8S(dst, voff, srd, soff, IMM) asm volatile("buffer_load_dwordx2 %0, %1, %2, %3 offen offset:" #IMM " sc1" : "=v"(dst) : "v"(voff), "s"(srd), "s"(soff))
#define LD4(dst, voff, srd, soff) asm volatile("buffer_load_dword %0, %1, %2, %3 offen" : "=v"(dst) : "v"(voff), "s"(srd), "s"(soff))
#define LD4S(dst, voff, srd, soff) asm volatile("buffer_load_dword %0, %1, %2, %3 offen sc1" : "=v"(dst) : "v"(voff), "s"(srd), "s"(soff))

template <int IMM> DI void ld16s(v4u& r, u32 v, rsrc_t rs, u32 so) { asm volatile("buffer_load_dwordx4 %0, %1, %2, %3 offen offset:%c4 sc1" : "=v"(r) : "v"(v), "s"(rs), "s"(so), "i"(IMM)); }
template <int CTRL> DI float dppf(float v) { return asf(__builtin_amdgcn_update_dpp(0, asu(v), CTRL, 0xf, 0xf, true)); }
DI float row16_sum(float v) { v += dppf<0x128>(v); v += dppf<0x124>(v); v += dppf<0x122>(v); v += dppf<0x121>(v); return v; }
DI float sw32_add(float a, float b) { auto r = __builtin_amdgcn_permlane32_swap(asu(a), asu(b), false, false); return asf(r[0]) + asf(r[1]); }
DI float sw16_add(float a, float b) { auto r = __builtin_amdgcn_permlane16_swap(asu(a), asu(b), false, false); return asf(r[0]) + asf(r[1]); }
DI float x16_max(float v) { auto r = __builtin_amdgcn_permlane16_swap(asu(v), asu(v), false, false); return fmaxf(asf(r[0]), asf(r[1])); }
DI float x32_max(float v) { auto r = __builtin_amdgcn_permlane32_swap(asu(v), asu(v), false, false); return fmaxf(asf(r[0]), asf(r[1])); }
DI int fresh_lane() { int l; asm volatile("v_mbcnt_lo_u32_b32 %0, -1, 0\n\tv_mbcnt_hi_u32_b32 %0, -1, %0" : "=v"(l)); return l; }
DI float rdl(float v, int l) { return asf(__builtin_amdgcn_readlane(asu(v), l)); }

struct Params {
  const u16 *x, *wqkv, *wo, *w1, *w3, *w2, *c, *wmod;
  u16* out;
  char* ws;
};

#define LOOPSEL 0
#define STAMP(i) do { if constexpr ((i) == SEG_A) asm volatile("s_memrealtime %0\n\ts_waitcnt lgkmcnt(0)" : "=s"(tA_)::"memory"); \
    if constexpr ((i) == SEG_B) asm volatile("s_memrealtime %0\n\ts_waitcnt lgkmcnt(0)" : "=s"(tB_)::"memory"); } while (0)
#define KSTAMP(lp, i) do {} while (0)
#define STAMP_DUMP() do {} while (0)

DI u32 flag_off(int phase, int g, int h) { return WS_FLAG + ((phase * 32 + g) * 2 + h) * 1024; }
DI void poll_flags(rsrc_t rWS, u32 vfl, u32 foff, u64 care, u32 tag) {
  for (u32 sp = 0; sp < SPIN_CAP; ++sp) {
    asm volatile("" ::: "memory");
    const u32 v = __builtin_amdgcn_raw_buffer_load_b32(rWS, vfl, foff, 16);
    if (!(__builtin_amdgcn_ballot_w64(v != tag) & care)) break;
  }
  VMW(0);
}
DI void raise_flag(rsrc_t rWS, u32 foff, u32 vtag, u32 vzero, u32 m1) {
  asm volatile("s_mov_b32 exec_hi, 0\n\ts_mov_b32 exec_lo, %4\n\tbuffer_store_dword %0, %1, %2, %3 offen sc1\n\ts_mov_b64 exec, -1" ::"v"(vtag), "v"(vzero), "s"(rWS), "s"(foff), "s"(m1) : "memory");
}
DI void signal_flag(rsrc_t rWS, u32 cnt_addr, u32 foff, u32 tag) {
  u32 t, a = cnt_addr, one = 1u, vt = tag, vz = 0u;
  asm volatile("s_mov_b64 exec, 1\n\tds_add_rtn_u32 %0, %1, %2\n\ts_waitcnt lgkmcnt(0)\n\tv_cmp_eq_u32 vcc, 7, %0\n\ts_and_b64 exec, exec, vcc\n\t"
               "buffer_store_dword %3, %4, %5, %6 offen sc1\n\ts_mov_b64 exec, -1"
               : "=&v"(t) : "v"(a), "v"(one), "v"(vt), "v"(vz), "s"(rWS), "s"(foff) : "vcc", "scc", "memory");
}
DI void arrive(u32 cnt_addr) {
  u32 a = cnt_addr, one = 1u;
  asm volatile("s_waitcnt lgkmcnt(0)\n\ts_mov_b64 exec, 1\n\tds_add_u32 %0, %1\n\ts_mov_b64 exec, -1" ::"v"(a), "v"(one) : "memory");
}
DI void await8(u32 cnt_addr) {
  u32 a = cnt_addr, t, sv;
  asm volatile("1:\n\tds_read_b32 %0, %2\n\ts_waitcnt lgkmcnt(0)\n\tv_readfirstlane_b32 %1, %0\n\ts_nop 0\n\ts_cmp_lt_u32 %1, 8\n\ts_cbranch_scc1 1b"
               : "=&v"(t), "=&s"(sv) : "v"(a) : "scc", "memory");
}
#define PIN(x) asm volatile("" : "+v"(x))
DI float qp(float r) { PIN(r); return r; }
DI float q_shl(u32 a) { return qp(blo(a)); }
DI float q_hi(u32 a) { return qp(bhi(a)); }
DI float q_fma(float a, float b, float c) { return qp(__builtin_fmaf(a, b, c)); }
DI float q_fmas(float a, float b, float c) { return qp(__builtin_fmaf(a, b, c)); }
DI float q_mul(float a, float b) { return qp(a * b); }
DI float q_mulk(float a) { return qp(a * -1.4426950408889634f); }
DI float q_add1(float a) { return qp(a + 1.f); }
DI float q_exp(float a) { return qp(__builtin_amdgcn_exp2f(a)); }
DI float q_rcp(float a) { return qp(__builtin_amdgcn_rcpf(a)); }
DI u32 q_cvt(float a, float b) { u32 r = pk(a, b); PIN(r); return r; }
DI float q_mov(float a) { return qp(a); }
DI u32 opq(u32 v) { asm volatile("" : "+v"(v)); return v; }

DI float sel(u64 m, float a, float b) { return __builtin_amdgcn_inverse_ballot_w64(m) ? a : b; }
DI v2u attend(v2u kp, v2u vp, v2u kc, v2u vc, v2u q, u64 m0, u64 m1, u32 hm, bool noprev) {
  const f32x4 z = {0.f, 0.f, 0.f, 0.f};
  const f32x4 sp = mfma16(kp, q, z);
  const f32x4 sc = mfma16(kc, q, z);
  const float K2 = 0.25f * 1.4426950408889634f;
  const u64 par = 0xaaaaaaaaaaaaaaaaull;
  float p0 = sel(par, sp[1], sp[0]), p1 = sel(par, sp[3], sp[2]);
  if (noprev) { p0 = -INFINITY; p1 = -INFINITY; }
  const float t0 = sel(m0, sel(par, sc[1], sc[0]), p0), t1 = sel(m1, sel(par, sc[3], sc[2]), p1);
  const float m = x32_max(x16_max(fmaxf(t0, t1)));
  const float mk = -m * K2;
  const float e0 = __builtin_amdgcn_exp2f(__builtin_fmaf(t0, K2, mk)), e1 = __builtin_amdgcn_exp2f(__builtin_fmaf(t1, K2, mk));
  const u32 w0 = pk(e0, e0) & hm, w1 = pk(e1, e1) & hm;
  v4u pf;
  pf.x = __builtin_amdgcn_inverse_ballot_w64(m0) ? 0u : w0; pf.y = __builtin_amdgcn_inverse_ballot_w64(m1) ? 0u : w1;
  pf.z = __builtin_amdgcn_inverse_ballot_w64(m0) ? w0 : 0u; pf.w = __builtin_amdgcn_inverse_ballot_w64(m1) ? w1 : 0u;
  const f32x4 o = mfma32(cat(vp, vc), pf, z);
  float sum = e0 + e1;
  sum = sw16_add(sum, sum); sum = sw32_add(sum, sum);
  return pk4(o * __builtin_amdgcn_rcpf(sum));
}

template <int N> using IC = std::integral_constant<int, N>;
template <int I, int N, class F> DI void sfor(F&& f) { if constexpr (I < N) { f(IC<I>{}); sfor<I + 1, N>(f); } }

__global__ void __launch_bounds__(512, 2) fused_block(Params p) {
  extern __shared__ __attribute__((aligned(16))) char smem[];
  LAS char* L = (LAS char*)smem;
  const int tid = threadIdx.x, lane = tid & 63, w = __builtin_amdgcn_readfirstlane(tid >> 6);
  const int c16 = lane & 15, g4 = lane >> 4;
  const int b = blockIdx.x;
  int g, j;
  if (XMAP == 0) { g = (b & 7) * 4 + ((b >> 3) & 3); j = b >> 5; }
  else if (XMAP == 2) { const int x = b & 7, r = b >> 3; g = (x >> 1) * 8 + (r & 7); j = (x & 1) * 4 + (r >> 3); }
  else { const int x = b & 7, r = b >> 3; g = (x >> 2) * 16 + (r & 15); j = (x & 3) * 2 + (r >> 4); }
  const rsrc_t rX = __builtin_amdgcn_make_buffer_rsrc((void*)p.x, 0, S_ * D_ * 2, 0x00020000);
  const rsrc_t rQKV = __builtin_amdgcn_make_buffer_rsrc((void*)p.wqkv, 0, D_ * 3 * D_ * 2, 0x00020000);
  const rsrc_t rWO = __builtin_amdgcn_make_buffer_rsrc((void*)p.wo, 0, D_ * D_ * 2, 0x00020000);
  const rsrc_t rW1 = __builtin_amdgcn_make_buffer_rsrc((void*)p.w1, 0, D_ * F_ * 2, 0x00020000);
  const rsrc_t rW3 = __builtin_amdgcn_make_buffer_rsrc((void*)p.w3, 0, D_ * F_ * 2, 0x00020000);
  const rsrc_t rW2 = __builtin_amdgcn_make_buffer_rsrc((void*)p.w2, 0, F_ * D_ * 2, 0x00020000);
  const rsrc_t rC = __builtin_amdgcn_make_buffer_rsrc((void*)p.c, 0, D_ * 2, 0x00020000);
  const rsrc_t rMOD = __builtin_amdgcn_make_buffer_rsrc((void*)p.wmod, 0, D_ * 6 * D_ * 2, 0x00020000);
  const rsrc_t rWS = __builtin_amdgcn_make_buffer_rsrc((void*)p.ws, 0, WS_BYTES, 0x00020000);
  const rsrc_t rOUT = __builtin_amdgcn_make_buffer_rsrc((void*)p.out, 0, S_ * D_ * 2, 0x00020000);
  u64 tA_ = 0, tB_ = 0;
  STAMP(0);

  u32 ep;
  { const u32 vz = 0; asm volatile("buffer_load_dword %0, %1, %2, 0 offen sc1" : "=v"(ep) : "v"(vz), "s"(rWS)); }
  u32 vl16 = lane * 16; PIN(vl16);
  const int bm = b < 192 ? b : b - 192, mq = bm / 48, cl = bm - 48 * mq;
  v4u wm[4]; v2u cv; u32 ep0;
  if (w < 4) {
    const u32 vm = (tid >> 3) * 24576 + (tid & 7) * 16, s0 = mq * (128 * 6144) + cl * 128, s1 = s0 + 6144, s2 = s0 + 12288, s3 = s0 + 18432;
    LD16(wm[0], vm, rMOD, s0, 0); LD16(wm[1], vm, rMOD, s1, 0); LD16(wm[2], vm, rMOD, s2, 0); LD16(wm[3], vm, rMOD, s3, 0);
    const u32 vc = (tid >> 3) * 8, scq = mq * 256;
    LD8(cv, vc, rC, scq, 0);
    const u32 vz = 0; asm volatile("buffer_load_dword %0, %1, %2, 0 offen sc1" : "=v"(ep0) : "v"(vz), "s"(rWS));
  }
  if (tid < 16) *(LAS u32*)(L + L_CNT + tid * 4) = 0u;
  BAR();
  const int st_qd = lane >> 2, st_r = (st_qd & 3) + 4 * (st_qd >> 3);
  const int st_col = ((st_qd >> 2) & 1) * 64 + (((lane & 3) ^ ((w & 1) << 1)) << 4);
  const int rotp = g & 7;
  u32 vB1 = st_r * 3072 + st_col; PIN(vB1);
  const u32 mB1 = (w >> 2) * 12288 + (w & 3) * 1024, sB1 = (w >> 2) * 98304 + (w & 3) * 24576 + j * 128;
  auto stage_b1 = [&](auto PC, auto BC) {
    constexpr int pair = decltype(PC)::value, base = decltype(BC)::value;
    const u32 sp = ((pair + rotp) & 7) * 196608 + sB1;
    dma<base, 0>(mB1, vB1, rQKV, sp); dma<base + 4096, 1024>(mB1, vB1, rQKV, sp); dma<base + 8192, 2048>(mB1, vB1, rQKV, sp);
  };
  auto b1_piece = [&](auto PC, auto BC, auto TC) {
    constexpr int pair = decltype(PC)::value, base = decltype(BC)::value, ty = decltype(TC)::value;
    dma<base + ty * 4096, ty * 1024>(mB1, vB1, rQKV, ((pair + rotp) & 7) * 196608 + sB1);
  };
  const int hd = w & 3, blk2 = w >> 2, kq = w & 3, mh = w >> 2;
  const int cs0 = j < 4 ? 22 * j : 88 + 21 * (j - 4);
  const int cend = cs0 + (j < 4 ? 22 : 21);
  STAMP(1);
  const int T0 = 12 * (g >> 4) + 3 * (w & 3), rcb = min(cs0 + T0, 168);
  if (w == 4) touch((lane >> 1) * 2752 + (lane & 1) * 128, rW1, (g & 15) * (32 * 2752) + min(cs0 + 12 * (g >> 4), 160) * 16);
  if (w == 5) touch((lane >> 1) * 2752 + (lane & 1) * 128, rW3, (g & 15) * (32 * 2752) + min(cs0 + 12 * (g >> 4), 160) * 16);
  if (w == 6) touch((lane & 31) * 1024 + (lane >> 5) * (g < 11 ? 32 * 32768 : 0), rW2, g * 32768 + j * 128);
  if (w == 7) touch((lane & 31) * 1024, rWO, (g & 15) * 32768 + j * 128);
  if (w < 4) {
    asm volatile("s_waitcnt vmcnt(0)" : "+v"(cv), "+v"(wm[0]), "+v"(wm[1]), "+v"(wm[2]), "+v"(wm[3]), "+v"(ep0)::"memory");
    const u32 tg = __builtin_amdgcn_readfirstlane(ep0) + 1u;
    const f32x4 cf = {blo(cv.x), bhi(cv.x), blo(cv.y), bhi(cv.y)};
    float sc[4];
#pragma unroll
    for (int r = 0; r < 4; ++r) sc[r] = cf[r] * __builtin_amdgcn_rcpf(1.f + __builtin_amdgcn_exp2f(cf[r] * -1.4426950408889634f));
    f32x2 v[4];
#pragma unroll
    for (int i = 0; i < 4; ++i) v[i] = (unp(wm[0][i]) * sc[0] + unp(wm[1][i]) * sc[1]) + (unp(wm[2][i]) * sc[2] + unp(wm[3][i]) * sc[3]);
    const float a0 = sw32_add(v[0].x, v[2].x), a1 = sw32_add(v[0].y, v[2].y), a2 = sw32_add(v[1].x, v[3].x), a3 = sw32_add(v[1].y, v[3].y);
    float t0 = sw16_add(a0, a2), t1 = sw16_add(a1, a3);
    t0 += dppf<0x128>(t0); t1 += dppf<0x128>(t1);
    f32x2 o = {t0, t1};
    *(LAS f32x2*)(L + L_RED + w * 256 + (lane & 7) * 32 + g4 * 8) = o;
    LGW();
    u32 old = 0;
    if (lane == 0) old = __hip_atomic_fetch_add((LAS u32*)(L + L_CNT + 28), 1u, __ATOMIC_RELAXED, __HIP_MEMORY_SCOPE_WORKGROUP);
    if (__builtin_amdgcn_readfirstlane(old) == 3u) {
      asm volatile("" ::: "memory");
      float s = 0.f;
#pragma unroll
      for (int i = 0; i < 4; ++i) s += *(LAS float*)(L + L_RED + i * 256 + lane * 4);
      v2u gr; gr.x = asu(s); gr.y = tg;
      if (b < 192) __builtin_amdgcn_raw_buffer_store_b64(gr, rWS, lane * 8, WS_MODG + (mq * 3072 + cl * 64) * 8, 16);
      __builtin_amdgcn_raw_buffer_store_b32(tg, rWS, 0, WS_ALIVE + b * 4, 16);
    }
  }
  STAMP(2);
  v4u xr[10];
  {
    const int r0 = 64 * g - 16 + w;
    const u32 s0 = (u32)max(r0, 0) * 1024u, s1 = (u32)max(r0 + 8, 0) * 1024u;
    LD16(xr[0], vl16, rX, s0, 0); LD16(xr[1], vl16, rX, s1, 0);
#pragma unroll
    for (int i = 2; i < 10; ++i) { const u32 so = (u32)(r0 + 8 * i) * 1024u; LD16(xr[i], vl16, rX, so, 0); }
  }
  stage_b1(IC<0>{}, IC<L_XY>{});
  int afo[4];
#pragma unroll
  for (int i = 0; i < 4; ++i) { afo[i] = blk2 * 32768 + c16 * 1024 + (((4 * i + g4) ^ c16) << 4); PIN(afo[i]); }
  const u64 am0 = __builtin_amdgcn_ballot_w64((c16 & 14) >= 4 * g4), am1 = __builtin_amdgcn_ballot_w64((c16 & 14) >= 4 * g4 + 2);
  u32 ahm = (lane & 1) ? 0xffff0000u : 0xffffu; PIN(ahm);
  u32 vOst = c16 * 16 + g4 * 256 + (hd & 1) * 8; PIN(vOst);
  int tB1 = g4 * 1024 + 64 * (c16 >> 2) + 8 * (lane & 3) + 32 * ((hd & 1) ^ (g4 & 1)) + 256 * (hd >> 1); PIN(tB1);
  int hw0 = w * 1024 + ((((lane - 8 * rotp) & 63) ^ w) << 4); PIN(hw0);
#define vfl ((opq(vl16) & 112u) << 3)
  STAMP(3);
  v4u bw[48];
  auto read_b1 = [&](int pair, int base, int off) {
#pragma unroll
    for (int kk = 0; kk < 2; ++kk)
#pragma unroll
      for (int ty = 0; ty < 3; ++ty) {
        LAS char* a = L + base + (off + kk * 12288 + ty * 4096);
        bw[(2 * pair + kk) * 3 + ty] = cat(trd(a), trd(a + 512));
      }
  };
  f32x2 xn[10][4];
  v4u ga_[4]; v2u pv;
  const u32 sMG = WS_MODG + w * 1024;
  {
    float u[10];
    sfor<0, 10>([&](auto I) {
      constexpr int i = decltype(I)::value;
      if constexpr (i == 0) asm volatile("s_waitcnt vmcnt(11)" : "+v"(xr[0]), "+v"(xr[1]), "+v"(ep)::"memory");
      if constexpr (i == 2) asm volatile("s_waitcnt vmcnt(9)" : "+v"(xr[2]), "+v"(xr[3])::"memory");
      if constexpr (i == 4) asm volatile("s_waitcnt vmcnt(10)" : "+v"(xr[4]), "+v"(xr[5])::"memory");
      if constexpr (i == 6) asm volatile("s_waitcnt vmcnt(8)" : "+v"(xr[6]), "+v"(xr[7])::"memory");
      if constexpr (i == 8) asm volatile("s_waitcnt vmcnt(9)" : "+v"(xr[8]), "+v"(xr[9])::"memory");
      float s = 0.f, q = 0.f;
#pragma unroll
      for (int e = 0; e < 4; ++e) { s = dot2(xr[i][e], 0x3f803f80u, s); q = dot2(xr[i][e], xr[i][e], q); }
      u[i] = sw32_add(s, q);
      PIN(u[i]);
      if constexpr (i == 3) stage_b1(IC<1>{}, IC<L_XY + 24576>{});
      if constexpr (i == 7) stage_b1(IC<2>{}, IC<L_XY + 49152>{});
      if constexpr (i == 9) {
        const u32 s0 = sMG, s1 = sMG + 24576, s2 = sMG + 49152, s3 = sMG + 73728;
        ld16s<0>(ga_[0], vl16, rWS, s0); ld16s<0>(ga_[1], vl16, rWS, s1); ld16s<0>(ga_[2], vl16, rWS, s2); ld16s<0>(ga_[3], vl16, rWS, s3);
      }
    });
    float t[5];
#pragma unroll
    for (int i = 0; i < 5; ++i) t[i] = row16_sum(sw16_add(u[i], u[i + 5]));
    float v = t[0];
#pragma unroll
    for (int i = 1; i < 5; ++i) v = c16 == i ? t[i] : v;
    auto r = __builtin_amdgcn_permlane32_swap(asu(v), asu(v), false, false);
    const float mean = asf(r[0]) * (1.f / 512.f);
    const float var = fmaxf(asf(r[1]) * (1.f / 512.f) - mean * mean, 0.f);
    float rs = __builtin_amdgcn_rsqf(var + 1e-5f), nm = -mean * rs;
    PIN(rs); PIN(nm);
    STAMP(4);
    { const u32 vo = ((lane >> 3) & 3) * 24576 + (lane & 7) * 8, so = WS_MODG + (1024 + 8 * b) * 8; LD8S(pv, vo, rWS, so, 0); }
    if (w >= 4) {
      const int u = lane >> 2, row = (u & 3) | ((u >> 1) & 4) | ((u << 1) & 8);
      const u32 vo = row * 2752 + (lane & 3) * 16, so = (g & 15) * (32 * 2752) + rcb * 16, mb = L_Z + 49152 + (w & 3) * 4096;
      dma<0, 0>(mb, vo, rW1, so); dma<1024, 16 * 2752>(mb, vo, rW1, so); dma<2048, 0>(mb, vo, rW3, so); dma<3072, 16 * 2752>(mb, vo, rW3, so);
    } else {
      const int u = lane >> 1, row = (u & 3) | ((u >> 1) & 4) | ((u << 1) & 8) | (u & 16);
      const u32 vo = row * 1024 + (lane & 1) * 16, so = g * 32768 + j * 128 + w * 32, mb = L_Z + 65536 + w * 2048;
      dma<0, 0>(mb, vo, rW2, so); dma<1024, 0>(mb, vo, rW2, so + (g < 11 ? 32 * 32768 : 0));
      const int rwo = (u & 7) | ((u >> 1) & 8) | ((u << 1) & 16);
      dma<0, 0>(76800 + w * 1024, rwo * 1024 + (lane & 1) * 16, rWO, (g & 15) * 32768 + j * 128 + w * 32);
    }
    sfor<0, 10>([&](auto I) {
      constexpr int i = decltype(I)::value;
      if constexpr (i == 2) stage_b1(IC<3>{}, IC<L_Z>{});
      if constexpr (i == 6) stage_b1(IC<4>{}, IC<L_Z + 24576>{});
      const float a = rdl(rs, (i / 5) * 16 + i % 5), c = rdl(nm, (i / 5) * 16 + i % 5);
#pragma unroll
      for (int e = 0; e < 4; ++e) { xn[i][e] = unp(xr[i][e]) * a + c; PIN(xn[i][e]); }
    });
  }
  const u32 tag = __builtin_amdgcn_readfirstlane(ep) + 1u;
  u32 m1; asm("s_lshr_b32 %0, 1, %1" : "=s"(m1) : "s"(w) : "scc");
#define RAISE(fo) raise_flag(rWS, (fo), tag, vl16, m1)
#define SIGNAL(idx, fo) signal_flag(rWS, L_CNT + 4 * (idx), (fo), tag)
  STAMP(5);
  asm volatile("s_waitcnt vmcnt(9)" : "+v"(ga_[0]), "+v"(ga_[1]), "+v"(ga_[2]), "+v"(ga_[3]), "+v"(pv)::"memory");
  auto gsum = [&](v4u (&a)[4], f32x2& o) -> u32 {
    o.x = (asf(a[0].x) + asf(a[1].x)) + (asf(a[2].x) + asf(a[3].x)); o.y = (asf(a[0].z) + asf(a[1].z)) + (asf(a[2].z) + asf(a[3].z));
    return ((a[0].y ^ tag) | (a[0].w ^ tag) | (a[1].y ^ tag) | (a[1].w ^ tag)) | ((a[2].y ^ tag) | (a[2].w ^ tag) | (a[3].y ^ tag) | (a[3].w ^ tag));
  };
  auto gather = [&](u32 voff, u32 soff, f32x2& o) {
    for (u32 sp = 0; sp < SPIN_CAP; ++sp) {
      asm volatile("" ::: "memory");
      v4u a[4];
#pragma unroll
      for (int q = 0; q < 4; ++q) a[q] = __builtin_amdgcn_raw_buffer_load_b128(rWS, voff, soff + q * 24576, 16);
      if (!__builtin_amdgcn_ballot_w64(gsum(a, o) != 0u)) break;
    }
  };
  const float one = w >= 4 ? 1.f : 0.f;
  {
    f32x2 mA;
    if (__builtin_amdgcn_ballot_w64(gsum(ga_, mA) != 0u)) gather(vl16, sMG, mA);
    *(LAS f32x2*)(L + L_MODA + w * 512 + (vl16 >> 1)) = mA + one;
  }
  BAR();
  if (w == 7) {
    const u32 vo = ((lane >> 3) & 3) * 24576 + (lane & 7) * 8, so = WS_MODG + (1024 + 8 * b) * 8;
    for (u32 sp = 0; sp < SPIN_CAP && __builtin_amdgcn_ballot_w64(pv.y != tag); ++sp) {
      asm volatile("" ::: "memory");
      pv = __builtin_amdgcn_raw_buffer_load_b64(rWS, vo, so, 16);
    }
    float sv = asf(pv.x);
    sv += dppf<0x128>(sv); sv = sw16_add(sv, sv);
    pv.x = asu(sv);
    __builtin_amdgcn_raw_buffer_store_b64(pv, rWS, (lane & 7) * 8, WS_MODF + (1024 + 8 * b) * 8, 16);
  }
  STAMP(6);
  int vma = L_MODA + 2 * vl16; PIN(vma);
  const f32x4 ms0 = *(LAS f32x4*)(L + vma), ms1 = *(LAS f32x4*)(L + vma + 16);
  const f32x4 ma0 = *(LAS f32x4*)(L + vma + 2048), ma1 = *(LAS f32x4*)(L + vma + 2064);
  int tz0 = tB1 + L_Z; PIN(tz0);
  { int t2 = tB1 + 49152; PIN(t2); read_b1(0, tB1, 0); read_b1(1, tB1, 24576); read_b1(2, t2, 0); }
  {
    VMW(6);
    if (w >= 4) {
      LAS char* a0 = L + L_Z + 49152 + (w & 3) * 4096 + ((lane >> 1) & 1) * 2048 + 64 * ((c16 >> 2) + 4 * (g4 & 1) + 16 * (g4 >> 1)) + 8 * (lane & 1);
#pragma unroll
      for (int ct = 0; ct < 3; ++ct) {
        const int c = min(cs0 + T0 + ct - rcb, 3) * 16;
        const v4u f = cat(trd(a0 + c), trd(a0 + c + 512));
        const u32 sf = WS_B4T + ((j * 16 + (g & 15)) * 24 + T0 + ct) * 1024;
        asm volatile("buffer_store_dwordx4 %0, %1, %2, %3 offen sc1\n\ts_nop 1" ::"v"(f), "v"(vl16), "s"(rWS), "s"(sf) : "memory");
      }
    }
    else {
      LAS char* a0 = L + L_Z + 65536 + w * 2048 + 32 * ((c16 >> 2) + 4 * (g4 & 1) + 16 * (g4 >> 1)) + 8 * (lane & 3);
#pragma unroll
      for (int i = 0; i < 2; ++i) {
        const v4u f = cat(trd(a0 + i * 1024), trd(a0 + i * 1024 + 256));
        const u32 sf = WS_B5T + ((j * 43 + g + (i && g < 11 ? 32 : 0)) * 4 + w) * 1024;
        asm volatile("buffer_store_dwordx4 %0, %1, %2, %3 offen sc1\n\ts_nop 1" ::"v"(f), "v"(vl16), "s"(rWS), "s"(sf) : "memory");
      }
      LAS char* a1 = L + 76800 + w * 1024 + 32 * ((c16 >> 2) + 4 * (g4 & 1) + 16 * (g4 >> 1)) + 8 * (lane & 3);
      const v4u f = cat(trd(a1), trd(a1 + 256));
      const u32 sf = WS_B3T + ((j * 16 + (g & 15)) * 4 + w) * 1024;
      asm volatile("buffer_store_dwordx4 %0, %1, %2, %3 offen sc1\n\ts_nop 1" ::"v"(f), "v"(vl16), "s"(rWS), "s"(sf) : "memory");
    }
  }
  BAR();
  STAMP(7);
  {
    int hw1 = (hw0 ^ 128) + 8192; PIN(hw1);
    sfor<0, 10>([&](auto I) {
      constexpr int i = decltype(I)::value;
      v4u o;
      o.x = pk2(xn[i][0] * ma0.xy + ms0.xy); o.y = pk2(xn[i][1] * ma0.zw + ms0.zw);
      o.z = pk2(xn[i][2] * ma1.xy + ms1.xy); o.w = pk2(xn[i][3] * ma1.zw + ms1.zw);
      if constexpr (i == 8) { hw0 += 65536; hw1 += 65536; }
      *(LAS v4u*)(L + ((i & 1) ? hw1 : hw0) + ((i >> 1) & 3) * 16384) = o;
      SCHED();
    });
  }
  VMW(0);
  BAR();
  RAISE(WS_RFLAG + (j * 32 + g) * 4);
  u32 flr;
  STAMP(8);

  const f32x4 z4 = {0.f, 0.f, 0.f, 0.f};
#define AFRAG(off, ks) (*(LAS v4u*)(L + afo[(ks) & 3] + ((off) + ((ks) >> 2) * 256)))
  const int ch0 = cs0 + 3 * w;
  constexpr int ND = NODMA ? 0 : 1;
  v4u b4[48];
  const u32 sB4 = WS_B4T + (j * 16 * 24 + 3 * w) * 1024;
  auto load_b4 = [&](auto NC) {
    constexpr int n = decltype(NC)::value, ks = n / 3, ct = n % 3;
    const u32 so = sB4 + ((ks + 2 * rotp) & 15) * 24576;
    v4u& r = b4[n];
    if constexpr (ct == 0) LD16S(r, vl16, rWS, so, 0); else if constexpr (ct == 1) LD16S(r, vl16, rWS, so, 1024); else LD16S(r, vl16, rWS, so, 2048);
  };
  auto landed_b4 = [&](auto AC, auto BC) {
#pragma unroll
    for (int i = decltype(AC)::value; i < decltype(BC)::value; ++i) { v4u& r = b4[i]; PIN(r); }
  };
  v4u gb_, gg_;
  u32 vgg, fl0, vRow;
  int pw, pr;
  f32x4 aKh = z4, aVh = z4, aQ = z4, aK = z4, aV = z4, bQ = z4, bK = z4, bV = z4;
  {
    v4u ah = AFRAG(0, 0), a1 = AFRAG(16384, 0), a2 = AFRAG(32768, 0);
    int l3, c3, g3; f32x2 mB, mG;
    auto job = [&](auto NC) {
      constexpr int n = decltype(NC)::value;
      if constexpr (n == 3) { l3 = opq(vl16) >> 4; c3 = l3 & 15; g3 = l3 >> 4; PIN(c3); PIN(g3); vgg = (vl16 < 512 ? 8192 : 20480 - 512) + 512 * j + vl16; PIN(vgg); }
      if constexpr (n == 7) { vRow = (8 * w + (l3 >> 3)) * 1024 + (l3 & 7) * 16; PIN(vRow); }
      if constexpr (n == 8) { pr = (8 * w + (l3 >> 3)) * 256 + (((2 * (l3 & 7)) ^ ((l3 >> 3) & 7)) << 4); PIN(pr); }
      if constexpr (n == 10) { pw = kq * 16384 + (32 * mh + c3) * 256 + ((g3 ^ (c3 & 3)) << 4) + ((c3 & 4) << 4); PIN(pw); }
      if constexpr (n == 31) { mB.x = asf(gb_.x) + one; PIN(mB.x); mB.y = asf(gb_.z) + one; PIN(mB.y); *(LAS f32x2*)(L + L_MODA + w * 512 + (vl16 >> 1)) = mB; }
      if constexpr (n == 32) { mG.x = asf(gg_.x); mG.y = asf(gg_.z); *(LAS f32x2*)(L + L_GAB + (vl16 >> 1)) = mG; }
      if constexpr (n == 34) { fl0 = (gb_.y ^ tag) | (gb_.w ^ tag) | (gg_.y ^ tag) | (gg_.w ^ tag); PIN(fl0); }
      if constexpr (n == 7) { const u32 s0 = WS_MODF + 12288 + w * 1024; LD16S(gb_, vl16, rWS, s0, 0); }
      if constexpr (n == 9) { const u32 s0 = WS_MODF; LD16S(gg_, vgg, rWS, s0, 0); }
      if constexpr (n < 6 && !(n & 1)) b1_piece(IC<5>{}, IC<L_Z + 49152>{}, IC<n / 2>{});
      if constexpr (n == 6) arrive(L_CNT + 8);
      if constexpr (n == 10) await8(L_CNT + 8);
      if constexpr (n >= 10 && n < 22 && !(n & 1)) b1_piece(IC<6 + (n - 10) / 6>{}, IC<L_Z + ((n - 10) / 6) * 24576>{}, IC<((n - 10) / 2) % 3>{});
      if constexpr (n == 21) { const u32 vr = (vl16 >> 2) & 124, sr = WS_RFLAG + j * 128; LD4S(flr, vr, rWS, sr); }
      if constexpr (n == 26) {
        asm volatile("s_waitcnt vmcnt(0)" : "+v"(gb_), "+v"(gg_), "+v"(flr)::"memory");
        arrive(L_CNT + 12);
        const u32 vr = (vl16 >> 2) & 124, sr = WS_RFLAG + j * 128; u32 cnt = SPIN_CAP;
        asm volatile("1:\n\tv_cmp_ne_u32 vcc, %5, %0\n\ts_cbranch_vccz 2f\n\tbuffer_load_dword %0, %2, %3, %4 offen sc1\n\ts_waitcnt vmcnt(0)\n\t"
                     "s_sub_u32 %1, %1, 1\n\ts_cbranch_scc0 1b\n\t2:" : "+v"(flr), "+s"(cnt) : "v"(vr), "s"(rWS), "s"(sr), "s"(tag) : "vcc", "scc", "memory");
      }
      if constexpr (n == 28) { await8(L_CNT + 12); int tz2 = tz0 + 49152; PIN(tz2); read_b1(5, tz2, 0); }
      if constexpr (n == 32) read_b1(6, tz0, 0);
      if constexpr (n == 37) read_b1(7, tz0, 24576);
      if constexpr (n >= 27 && n < 47 && (n & 1)) load_b4(IC<(n - 27) / 2>{});
    };
    sfor<0, 16>([&](auto KS) {
      constexpr int ks = decltype(KS)::value;
      KSTAMP(1, ks);
      if constexpr (ks == 0) read_b1(3, tz0, 0);
      if constexpr (ks == 1) read_b1(4, tz0, 24576);
      SCHED();
      v4u nh = ah, n1 = a1, n2 = a2;
      aKh = mfma32(bw[ks * 3 + 1], ah, aKh);
      aVh = mfma32(ah, bw[ks * 3 + 2], aVh);
      if constexpr (ks < 15) nh = AFRAG(0, ks + 1);
      job(IC<3 * ks>{});
      SCHED();
      aQ = mfma32(bw[ks * 3 + 0], a1, aQ);
      aK = mfma32(bw[ks * 3 + 1], a1, aK);
      aV = mfma32(a1, bw[ks * 3 + 2], aV);
      if constexpr (ks < 15) n1 = AFRAG(16384, ks + 1);
      job(IC<3 * ks + 1>{});
      SCHED();
      bQ = mfma32(bw[ks * 3 + 0], a2, bQ);
      bK = mfma32(bw[ks * 3 + 1], a2, bK);
      bV = mfma32(a2, bw[ks * 3 + 2], bV);
      if constexpr (ks < 15) n2 = AFRAG(32768, ks + 1);
      job(IC<3 * ks + 2>{});
      ah = nh; a1 = n1; a2 = n2;
      SCHED();
    });
  }
  STAMP(9); KSTAMP(1, 16);
  v4u b3[16];
  auto load_b3 = [&](auto NC) { constexpr int n = decltype(NC)::value; ld16s<(n & 3) * 1024>(b3[n], vl16, rWS, WS_B3T + (j * 16 + 4 * kq + (n >> 2)) * 4096); };
  {
    const u32 sOst = WS_O + ((g * 4 + 2 * blk2) * 16 + 2 * j + (hd >> 1)) * 1024;
    const v2u k1p = pk4(aK), v1p = pk4(aV);
    sfor<0, 6>(load_b3);
    const v2u o0 = attend(pk4(aKh), pk4(aVh), k1p, v1p, pk4(aQ), am0, am1, ahm, g == 0 && blk2 == 0);
    const v2u o1 = attend(k1p, v1p, pk4(bK), pk4(bV), pk4(bQ), am0, am1, ahm, false);
    __builtin_amdgcn_raw_buffer_store_b64(o0, rWS, vOst, sOst, 16);
    __builtin_amdgcn_raw_buffer_store_b64(o1, rWS, vOst, sOst + 16384, 16);
  }
  if (__builtin_amdgcn_ballot_w64(fl0 != 0u)) {
    for (u32 sp = 0; sp < SPIN_CAP; ++sp) {
      asm volatile("" ::: "memory");
      gb_ = __builtin_amdgcn_raw_buffer_load_b128(rWS, vl16, WS_MODF + 12288 + w * 1024, 16);
      gg_ = __builtin_amdgcn_raw_buffer_load_b128(rWS, vgg, WS_MODF, 16);
      if (!__builtin_amdgcn_ballot_w64(((gb_.y ^ tag) | (gb_.w ^ tag) | (gg_.y ^ tag) | (gg_.w ^ tag)) != 0u)) break;
    }
    const f32x2 mB = {asf(gb_.x) + one, asf(gb_.z) + one}, mG = {asf(gg_.x), asf(gg_.z)};
    *(LAS f32x2*)(L + L_MODA + w * 512 + (vl16 >> 1)) = mB;
    *(LAS f32x2*)(L + L_GAB + (vl16 >> 1)) = mG;
  }
  STAMP(10);
  VMW(0);
  landed_b4(IC<0>{}, IC<10>{});
  BAR();
  RAISE(flag_off(0, g, 0) + j * 128);
  STAMP(11);

  v4u xv;
  { const u32 sx = g * 65536 + j * 128; LD16(xv, vRow, rX, sx, 0); }
  sfor<6, 12>(load_b3);
  const u64 care2 = 0x0101010101010101ull * (3u << (2 * kq));
  poll_flags(rWS, vfl, flag_off(0, g, 0), care2, tag);
  PIN(xv);
  sfor<12, 16>(load_b3);
  v4u af[8];
  {
    const u32 so = WS_O + ((g * 4 + 2 * mh) * 16 + 4 * kq) * 1024, so1 = so + 16384;
    LD16S(af[0], vl16, rWS, so, 0); LD16S(af[1], vl16, rWS, so, 1024); LD16S(af[2], vl16, rWS, so, 2048); LD16S(af[3], vl16, rWS, so, 3072);
    LD16S(af[4], vl16, rWS, so1, 0); LD16S(af[5], vl16, rWS, so1, 1024); LD16S(af[6], vl16, rWS, so1, 2048); LD16S(af[7], vl16, rWS, so1, 3072);
  }
  STAMP(12);
  sfor<0, 2>([&](auto M) {
    constexpr int m = decltype(M)::value;
    asm volatile("s_waitcnt vmcnt(%c4)" : "+v"(af[4 * m]), "+v"(af[4 * m + 1]), "+v"(af[4 * m + 2]), "+v"(af[4 * m + 3]) : "i"(m == 0 ? 4 : 2) : "memory");
    if constexpr (m == 0) {
#pragma unroll
      for (int i = 0; i < 16; ++i) PIN(b3[i]);
    }
    f32x4 acc[4] = {z4, z4, z4, z4};
    sfor<0, 4>([&](auto SC) {
      constexpr int sx = decltype(SC)::value;
#pragma unroll
      for (int n = 0; n < 4; ++n) acc[n] = mfma32(b3[sx * 4 + n], af[4 * m + sx], acc[n]);
      if constexpr (sx & 1) load_b4(IC<10 + 2 * m + (sx >> 1)>{});
      SCHED();
    });
#pragma unroll
    for (int n = 0; n < 4; ++n) *(LAS f32x4*)(L + ((n & 1) ? (pw ^ 64) : pw) + (m * 4096 + (n >> 1) * 128)) = acc[n];
  });
  BAR();
  STAMP(13);
  sfor<14, 18>(load_b4);
  {
    f32x4 y0 = *(LAS f32x4*)(L + pr), y1 = *(LAS f32x4*)(L + (pr ^ 16));
#pragma unroll
    for (int q = 1; q < 4; ++q) { y0 += *(LAS f32x4*)(L + pr + q * 16384); y1 += *(LAS f32x4*)(L + (pr ^ 16) + q * 16384); }
    const int ga = L_GAB + ((vl16 & 112) << 1);
    const f32x4 g0 = *(LAS f32x4*)(L + ga), g1 = *(LAS f32x4*)(L + ga + 16);
    const f32x4 x0 = f32x4{blo(xv.x), bhi(xv.x), blo(xv.y), bhi(xv.y)} + g0 * y0, x1 = f32x4{blo(xv.z), bhi(xv.z), blo(xv.w), bhi(xv.w)} + g1 * y1;
    { const v4u o = cat(pk4(x0), pk4(x1)); const u32 so = WS_X1 + g * 65536 + j * 128;
      asm volatile("buffer_store_dwordx4 %0, %1, %2, %3 offen sc1\n\ts_nop 1" ::"v"(o), "v"(vRow), "s"(rWS), "s"(so) : "memory"); }
    const f32x4 sv = x0 + x1;
    float mean = (sv.x + sv.y) + (sv.z + sv.w);
    mean += dppf<0xB1>(mean); mean += dppf<0x4E>(mean); mean += dppf<0x141>(mean);
    mean *= 1.f / 64.f;
    const f32x4 d0 = x0 - mean, d1 = x1 - mean, dd = d0 * d0 + d1 * d1;
    float m2 = (dd.x + dd.y) + (dd.z + dd.w);
    m2 += dppf<0xB1>(m2); m2 += dppf<0x4E>(m2); m2 += dppf<0x141>(m2);
    v2u st; st.x = asu(mean); st.y = asu(m2);
    __builtin_amdgcn_raw_buffer_store_b64(st, rWS, (vRow >> 10) << 6, WS_STAT + g * 4096 + j * 8, 16);
  }
  STAMP(14);
  VMW(0);
  landed_b4(IC<10>{}, IC<18>{});
  BAR();
  RAISE(flag_off(1, g, 0) + j * 128);
  sfor<18, 24>(load_b4);
  STAMP(15);

  v4u x1v[4]; v2u stv;
  auto ln2_load = [&](int h) {
    const u32 so = WS_X1 + g * 65536 + (32 * (w >> 2) + 16 * h + 4 * (w & 3)) * 1024;
    LD16S(x1v[0], vl16, rWS, so, 0); LD16S(x1v[1], vl16, rWS, so, 1024); LD16S(x1v[2], vl16, rWS, so, 2048); LD16S(x1v[3], vl16, rWS, so, 3072);
    const u32 vs = (vl16 >> 1) & 255, ss = WS_STAT + g * 4096 + (32 * (w >> 2) + 16 * h + 4 * (w & 3)) * 64;
    LD8S(stv, vs, rWS, ss, 0);
  };
  float l2r, l2n;
  auto ln2_stats = [&]() {
    const float mj = asf(stv.x);
    float mean = mj;
    mean += dppf<0xB1>(mean); mean += dppf<0x4E>(mean); mean += dppf<0x141>(mean);
    mean *= 0.125f;
    const float dm = mj - mean;
    float m2 = asf(stv.y) + 64.f * dm * dm;
    m2 += dppf<0xB1>(m2); m2 += dppf<0x4E>(m2); m2 += dppf<0x141>(m2);
    l2r = __builtin_amdgcn_rsqf(m2 * (1.f / 512.f) + 1e-5f);
    l2n = -mean * l2r;
  };
  u32 vH;
  u32 hmask[3];
#pragma unroll
  for (int ct = 0; ct < 3; ++ct)
    asm("s_cmp_lt_u32 %1, %2\n\ts_cselect_b32 %0, -1, 0" : "=s"(hmask[ct]) : "s"((u32)(ch0 + ct)), "s"((u32)cend) : "scc");
  auto hh_store = [&](v2u o, int ct, int h) {
    const u32 so = WS_HH + ((g * 4 + h) * 172 + ch0 + ct) * 256;
    asm volatile("s_mov_b32 exec_lo, %4\n\ts_mov_b32 exec_hi, %4\n\tbuffer_store_dwordx2 %0, %1, %2, %3 offen sc1\n\ts_mov_b64 exec, -1"
                 ::"v"(o), "v"(vH), "s"(rWS), "s"(so), "s"(hmask[ct]) : "memory");
  };
  auto swiglu_tile = [&](f32x4 (&acc)[3][2], int ct, int h) {
    f32x4 G, U, R;
#pragma unroll
    for (int e = 0; e < 4; ++e) {
      auto r = __builtin_amdgcn_permlane32_swap(asu(acc[ct][0][e]), asu(acc[ct][1][e]), false, false);
      G[e] = asf(r[0]); U[e] = asf(r[1]);
    }
    const f32x4 T = G * -1.4426950408889634f;
#pragma unroll
    for (int e = 0; e < 4; ++e) R[e] = __builtin_amdgcn_rcpf(1.f + __builtin_amdgcn_exp2f(T[e]));
    hh_store(pk4((G * U) * R), ct, h);
  };
#pragma unroll
  for (int i = 0; i < 4; ++i) { afo[i] -= blk2 * 32768; PIN(afo[i]); }
  int vm2 = L_MODA + 2 * vl16; PIN(vm2);
  int h2w = w * 4096 + (((vl16 - 128 * rotp) & 1023) ^ ((4 * w & 15) << 4)); PIN(h2w);
  poll_flags(rWS, vfl, flag_off(1, g, 0), ~0ull, tag);
  landed_b4(IC<18>{}, IC<24>{});
  ln2_load(0);
  sfor<24, 28>(load_b4);
  {
    const f32x4 s0 = *(LAS f32x4*)(L + vm2), s1 = *(LAS f32x4*)(L + vm2 + 16), a0 = *(LAS f32x4*)(L + vm2 + 2048), a1 = *(LAS f32x4*)(L + vm2 + 2064);
    asm volatile("s_waitcnt vmcnt(%c5)" : "+v"(x1v[0]), "+v"(x1v[1]), "+v"(x1v[2]), "+v"(x1v[3]), "+v"(stv) : "i"(4) : "memory");
    ln2_stats();
#pragma unroll
    for (int i = 0; i < 4; ++i) {
      const float rs = rdl(l2r, 8 * i), nn = rdl(l2n, 8 * i);
      v4u o;
      o.x = pk2((unp(x1v[i].x) * rs + nn) * a0.xy + s0.xy); o.y = pk2((unp(x1v[i].y) * rs + nn) * a0.zw + s0.zw);
      o.z = pk2((unp(x1v[i].z) * rs + nn) * a1.xy + s1.xy); o.w = pk2((unp(x1v[i].w) * rs + nn) * a1.zw + s1.zw);
      *(LAS v4u*)(L + (h2w ^ (i << 4)) + i * 1024) = o;
    }
  }
  ln2_load(1);
  BAR();
  STAMP(16);
  f32x4 accA[3][2] = {{z4, z4}, {z4, z4}, {z4, z4}};
  {
    f32x4 cs, ca; float rsv[4], nnv, t[4]; v2u ov;
    auto ln_op = [&](auto NC) {
      constexpr int n = decltype(NC)::value, u = n / 16, k = n % 16, hf = u & 1, i = u >> 1;
      if constexpr (u < 8) {
        if constexpr (k == 0) {
          cs = *(LAS f32x4*)(L + vm2 + hf * 16); ca = *(LAS f32x4*)(L + vm2 + 2048 + hf * 16);
          if constexpr (hf == 0) nnv = q_mov(rdl(l2n, 8 * i));
        }
        else if constexpr (k == 1) t[0] = q_shl(x1v[i][2 * hf]);
        else if constexpr (k == 2) t[1] = q_hi(x1v[i][2 * hf]);
        else if constexpr (k == 3) t[2] = q_shl(x1v[i][2 * hf + 1]);
        else if constexpr (k == 4) t[3] = q_hi(x1v[i][2 * hf + 1]);
        else if constexpr (k < 9) t[k - 5] = q_fmas(t[k - 5], rsv[i], nnv);
        else if constexpr (k < 13) t[k - 9] = q_fma(t[k - 9], ca[k - 9], cs[k - 9]);
        else if constexpr (k == 13) ov.x = q_cvt(t[0], t[1]);
        else if constexpr (k == 14) ov.y = q_cvt(t[2], t[3]);
        else {
          *(LAS v2u*)(L + (h2w ^ (i << 4)) + (32768 + i * 1024 + hf * 8)) = ov;
        }
      }
    };
    v4u a0 = AFRAG(0, 0), a1 = AFRAG(16384, 0);
    sfor<0, 16>([&](auto KS) {
      constexpr int ks = decltype(KS)::value;
      KSTAMP(3, ks);
      v4u n0 = a0, n1 = a1;
      if constexpr (ks < 15) { n0 = AFRAG(0, ks + 1); n1 = AFRAG(16384, ks + 1); }
      if constexpr (ks == 3) {
        asm volatile("s_waitcnt vmcnt(6)" : "+v"(x1v[0]), "+v"(x1v[1]), "+v"(x1v[2]), "+v"(x1v[3]), "+v"(stv)::"memory");
        landed_b4(IC<24>{}, IC<28>{});
      }
      if constexpr (ks == 9) { VMW(4); landed_b4(IC<28>{}, IC<39>{}); }
      if constexpr (ks == 13) { VMW(2); landed_b4(IC<39>{}, IC<46>{}); }
      if constexpr (ks == 15) { VMW(0); landed_b4(IC<46>{}, IC<48>{}); }
      SCHED();
      if constexpr (ks == 3) { ln2_stats(); PIN(l2r); PIN(l2n);
#pragma unroll
        for (int i = 0; i < 4; ++i) rsv[i] = rdl(l2r, 8 * i); }
      sfor<0, 6>([&](auto MC) {
        constexpr int m = decltype(MC)::value, ct = m >> 1;
        if constexpr (m & 1) accA[ct][1] = mfma32(b4[ks * 3 + ct], a1, accA[ct][1]); else accA[ct][0] = mfma32(b4[ks * 3 + ct], a0, accA[ct][0]);
        if constexpr (ks < 6 && m % 3 == 0) load_b4(IC<28 + ks * 2 + m / 3>{});
        if constexpr (ks >= 6 && ks < 9 && m == 0) load_b4(IC<40 + (ks - 6)>{});
        if constexpr (ks >= 9 && ks < 12 && (m == 0 || (m == 3 && ks < 11))) load_b4(IC<43 + (ks - 9) * 2 + (m == 3)>{});
        if constexpr (ks >= 4) {
          constexpr int o = (ks - 4) * 15 + (m >> 1) * 5 + (m & 1) * 2;
          ln_op(IC<o>{}); ln_op(IC<o + 1>{});
          if constexpr (m & 1) ln_op(IC<o + 2>{});
          SCHED();
        }
      });
      a0 = n0; a1 = n1;
      SCHED();
    });
  }
  vH = (vl16 & 255) + ((vl16 >> 5) & 8) + (vl16 >> 9) * (2 * 172 * 256); PIN(vH);
  BAR();
  STAMP(17); KSTAMP(3, 16);
  f32x4 accB[3][2] = {{z4, z4}, {z4, z4}, {z4, z4}};
  u32 vRow5; int pr5, pw5, gb5;
  const int kb5 = 5 * w + min(w, 3);
  u64 care5 = 0x0101010101010101ull * ((0x80c06030180c0603ull >> (8 * w)) & 255u);
  asm volatile("" : "+s"(care5));
  v4u b5[24], hA[12], hB[12]; v2u xqA, xqB;
  const u32 sB5 = WS_B5T + (j * 43 + kb5) * 4096, d5 = (w + 5) >> 3;
  auto load_b5 = [&](auto NC) {
    constexpr int n = decltype(NC)::value, sx = n / 4, t = n % 4;
    const u32 so = sB5 + (sx == 5 ? (5 - d5) * 4096 : sx * 4096);
    ld16s<t * 1024>(b5[n], vl16, rWS, so);
  };
  auto load_h = [&](auto NC, v4u (&hf)[12], int h) {
    constexpr int n = decltype(NC)::value, sx = n / 2, m = n % 2;
    const u32 so = WS_HH + ((g * 4 + 2 * m + h) * 172 + 4 * kb5) * 256 + (sx == 5 ? (5 - d5) * 1024 : sx * 1024);
    ld16s<0>(hf[n], vl16, rWS, so);
  };
  auto poll5 = [&](int h) {
    const u32 fo = flag_off(2, g, h), vf = vfl; u32 t, cnt = SPIN_CAP;
    asm volatile("1:\n\tbuffer_load_dword %0, %2, %3, %4 offen sc1\n\ts_waitcnt vmcnt(0)\n\tv_cmp_ne_u32 vcc, %5, %0\n\ts_and_b64 vcc, vcc, %6\n\ts_cbranch_scc0 2f\n\t"
                 "s_sub_u32 %1, %1, 1\n\ts_cbranch_scc0 1b\n\t2:" : "=&v"(t), "+s"(cnt) : "v"(vf), "s"(rWS), "s"(fo), "s"(tag), "s"(care5) : "vcc", "scc", "memory");
  };
  {
    float G[4], U[4], T[4]; v2u ov;
    auto sw_op = [&](auto NC) {
      constexpr int n = decltype(NC)::value, ct = n / 22, k = n % 22;
      if constexpr (ct < 3) {
        if constexpr (k < 4) { auto r = __builtin_amdgcn_permlane32_swap(asu(accA[ct][0][k]), asu(accA[ct][1][k]), false, false); G[k] = asf(r[0]); U[k] = asf(r[1]); PIN(G[k]); PIN(U[k]); }
        else if constexpr (k < 6) { T[2 * (k - 4)] = q_mulk(G[2 * (k - 4)]); T[2 * (k - 4) + 1] = q_mulk(G[2 * (k - 4) + 1]); }
        else if constexpr (k < 10) T[k - 6] = q_exp(T[k - 6]);
        else if constexpr (k < 12) { T[2 * (k - 10)] = q_add1(T[2 * (k - 10)]); T[2 * (k - 10) + 1] = q_add1(T[2 * (k - 10) + 1]); }
        else if constexpr (k < 16) T[k - 12] = q_rcp(T[k - 12]);
        else if constexpr (k < 18) { G[2 * (k - 16)] = q_mul(G[2 * (k - 16)], U[2 * (k - 16)]); G[2 * (k - 16) + 1] = q_mul(G[2 * (k - 16) + 1], U[2 * (k - 16) + 1]); }
        else if constexpr (k < 20) { G[2 * (k - 18)] = q_mul(G[2 * (k - 18)], T[2 * (k - 18)]); G[2 * (k - 18) + 1] = q_mul(G[2 * (k - 18) + 1], T[2 * (k - 18) + 1]); }
        else if constexpr (k == 20) { ov.x = q_cvt(G[0], G[1]); ov.y = q_cvt(G[2], G[3]); }
        else hh_store(ov, ct, 0);
      }
    };
    v4u a0 = AFRAG(32768, 0), a1 = AFRAG(49152, 0);
    int l5, c5, g5;
    sfor<0, 16>([&](auto KS) {
      constexpr int ks = decltype(KS)::value;
      KSTAMP(4, ks);
      v4u n0 = a0, n1 = a1;
      if constexpr (ks > 0 && ks < 15) { n0 = AFRAG(32768, ks + 1); n1 = AFRAG(49152, ks + 1); }
      if constexpr (ks == 7) { VMW(0); SIGNAL(1, flag_off(2, g, 0) + j * 128); }
      if constexpr (ks == 14) poll5(0);
      SCHED();
      sfor<0, 6>([&](auto MC) {
        constexpr int m = decltype(MC)::value, ct = m >> 1, n = ks * 6 + m;
        if constexpr (m & 1) accB[ct][1] = mfma32(b4[ks * 3 + ct], a1, accB[ct][1]); else accB[ct][0] = mfma32(b4[ks * 3 + ct], a0, accB[ct][0]);
        if constexpr (n == 3) n0 = AFRAG(32768, 1);
        if constexpr (n == 5) n1 = AFRAG(49152, 1);
        if constexpr (n < 33) { sw_op(IC<2 * n>{}); sw_op(IC<2 * n + 1>{}); }
        if constexpr (n >= 12 && n < 32 && n % 4 == 0) load_b5(IC<(n - 12) / 4>{});
        if constexpr (n >= 42 && n < 75 && n % 3 == 0) load_b5(IC<5 + (n - 42) / 3>{});
        if constexpr (n >= 84 && n % 2 == 0) load_h(IC<(n - 84) / 2>{}, hA, 0);
        if constexpr (n == 38) { l5 = opq(vl16) >> 4; c5 = l5 & 15; g5 = l5 >> 4; PIN(c5); PIN(g5); }
        if constexpr (n == 40) { vRow5 = (32 * (w >> 2) + 4 * (w & 3) + g5) * 1024 + c5 * 8; PIN(vRow5); }
        if constexpr (n == 42) { pr5 = (4 * w + g5) * 256 + ((c5 ^ ((4 * w + g5) & 7)) << 4); PIN(pr5); }
        if constexpr (n == 44) { pw5 = w * 8192 + c5 * 256 + ((c5 & 4) << 4); PIN(pw5); }
        if constexpr (n == 46) { pw5 += (g5 ^ (c5 & 3)) << 4; PIN(pw5); gb5 = L_GAB + 256 + c5 * 16; PIN(gb5); }
        SCHED();
      });
      a0 = n0; a1 = n1;
      SCHED();
    });
  }
  STAMP(18); KSTAMP(4, 16);
  PIN(accB[0][0]); PIN(accB[0][1]); PIN(accB[1][0]); PIN(accB[1][1]); PIN(accB[2][0]); PIN(accB[2][1]);
  sfor<6, 9>([&](auto I) { load_h(I, hA, 0); });
  swiglu_tile(accB, 0, 1);
  sfor<9, 12>([&](auto I) { load_h(I, hA, 0); });
  swiglu_tile(accB, 1, 1);
  swiglu_tile(accB, 2, 1);
  VMW(0);
  BAR();
  RAISE(flag_off(2, g, 1) + j * 128);
  STAMP(19);
#pragma unroll
  for (int i = 0; i < 12; ++i) PIN(hA[i]);
#pragma unroll
  for (int i = 0; i < 16; ++i) PIN(b5[i]);
  sfor<16, 20>(load_b5);
  auto p5_half = [&](auto HC, v4u (&hf)[12]) {
    constexpr int h = decltype(HC)::value;
    f32x4 acc[2][4] = {{z4, z4, z4, z4}, {z4, z4, z4, z4}};
    sfor<0, 6>([&](auto SC) {
      constexpr int sx = decltype(SC)::value;
      if constexpr (h == 0 && sx == 1) { sfor<20, 24>(load_b5);
        const u32 sq = WS_X1 + g * 65536 + j * 128, sq1 = sq + 16384, vr5 = vRow5; const rsrc_t rr = rWS; LD8S(xqA, vr5, rr, sq, 0); LD8S(xqB, vr5, rr, sq1, 0); }
      if constexpr (h == 0 && sx == 4) {
        poll5(1);
#pragma unroll
        for (int i = 16; i < 24; ++i) PIN(b5[i]);
        PIN(xqA); PIN(xqB);
      }
      if constexpr (h == 1 && sx == 0) asm volatile("s_waitcnt vmcnt(9)" : "+v"(hf[0]), "+v"(hf[1]), "+v"(hf[2]), "+v"(hf[3])::"memory");
      if constexpr (h == 1 && sx == 2) asm volatile("s_waitcnt vmcnt(5)" : "+v"(hf[4]), "+v"(hf[5]), "+v"(hf[6]), "+v"(hf[7])::"memory");
      if constexpr (h == 1 && sx == 4) asm volatile("s_waitcnt vmcnt(1)" : "+v"(hf[8]), "+v"(hf[9]), "+v"(hf[10]), "+v"(hf[11])::"memory");
      if constexpr (sx == 5) { if (w >= 3) { hf[10] = v4u{0u, 0u, 0u, 0u}; hf[11] = v4u{0u, 0u, 0u, 0u}; } }
#pragma unroll
      for (int m = 0; m < 2; ++m) {
#pragma unroll
        for (int n = 0; n < 4; ++n) acc[m][n] = mfma32(b5[sx * 4 + n], hf[2 * sx + m], acc[m][n]);
        if constexpr (h == 0 && sx >= 4) { if (m == 0) { load_h(IC<4 * (sx - 4)>{}, hB, 1); load_h(IC<4 * (sx - 4) + 1>{}, hB, 1); } else { load_h(IC<4 * (sx - 4) + 2>{}, hB, 1); load_h(IC<4 * (sx - 4) + 3>{}, hB, 1); } }
        SCHED();
      }
    });
    const int pwh = h ? opq(pw5 + 65536) : pw5;
#pragma unroll
    for (int m = 0; m < 2; ++m)
#pragma unroll
      for (int n = 0; n < 4; ++n) *(LAS f32x4*)(L + ((n & 1) ? (pwh ^ 64) : pwh) + (m * 4096 + (n >> 1) * 128)) = acc[m][n];
    if constexpr (h == 0) sfor<8, 12>([&](auto I) { load_h(I, hB, 1); });
    BAR();
    const int prh = h ? opq(pr5 + 65536) : pr5;
    f32x4 y = *(LAS f32x4*)(L + prh);
#pragma unroll
    for (int q = 1; q < 8; ++q) y += *(LAS f32x4*)(L + prh + q * 8192);
    const f32x4 gbv = *(LAS f32x4*)(L + gb5);
    const v2u xx = h ? xqB : xqA;
    const f32x4 xb = {blo(xx.x), bhi(xx.x), blo(xx.y), bhi(xx.y)};
    __builtin_amdgcn_raw_buffer_store_b64(pk4(xb + gbv * y), rOUT, vRow5, g * 65536 + h * 16384 + j * 128, 0);
  };
  STAMP(20);
  p5_half(IC<0>{}, hA);
  STAMP(21);
  p5_half(IC<1>{}, hB);
  STAMP(22);
  if (b == 40 && w == 0) { u64 t0 = __builtin_amdgcn_s_memrealtime(); const u64 d = 4 * (tB_ - tA_); while (__builtin_amdgcn_s_memrealtime() - t0 < d && d < 20000) {} }
  if (b == 0 && w == 0) {
    for (u32 sp = 0; sp < SPIN_CAP; ++sp) {
      asm volatile("" ::: "memory");
      const v4u v = __builtin_amdgcn_raw_buffer_load_b128(rWS, vl16, WS_ALIVE, 16);
      if (__all(v.x == tag && v.y == tag && v.z == tag && v.w == tag)) break;
    }
    __builtin_amdgcn_raw_buffer_store_b32(tag, rWS, 0, WS_EPOCH, 16);
  }
}
}

extern "C" void kernel_launch(void* const* d_in, const int* in_sizes, int n_in, void* d_out, int out_size, void* d_ws, size_t ws_size,
                              hipStream_t stream) {
  Params p;
  p.x = (const u16*)d_in[0]; p.wqkv = (const u16*)d_in[1]; p.wo = (const u16*)d_in[2];
  p.w1 = (const u16*)d_in[4]; p.w3 = (const u16*)d_in[5]; p.w2 = (const u16*)d_in[6];
  p.c = (const u16*)d_in[7]; p.wmod = (const u16*)d_in[8];
  p.out = (u16*)d_out; p.ws = (char*)d_ws;
  (void)hipFuncSetAttribute((const void*)fused_block, hipFuncAttributeMaxDynamicSharedMemorySize, L_TOTAL);
  fused_block<<<256, 512, L_TOTAL, stream>>>(p);
}
```

```cpp
#include <hip/hip_runtime.h>
#include <stdint.h>

#define KDBG 0
#define NODMA 0
#define CODEWARM 0
#define L2WARM 1
#define EARLYT 1
#define XMAP 2

#define DI __device__ __forceinline__
#define GAS __attribute__((address_space(1)))
#define LAS __attribute__((address_space(3)))
typedef unsigned u32;
typedef unsigned short u16;
typedef unsigned long long u64;
typedef u32 v4u __attribute__((ext_vector_type(4)));
typedef u32 v2u __attribute__((ext_vector_type(2)));
typedef float f32x4 __attribute__((ext_vector_type(4)));
typedef float f32x2 __attribute__((ext_vector_type(2)));
typedef __bf16 bf16x8 __attribute__((ext_vector_type(8)));
typedef __bf16 bf16x2 __attribute__((ext_vector_type(2)));
typedef short s16x4 __attribute__((ext_vector_type(4)));
typedef __amdgpu_buffer_rsrc_t rsrc_t;
typedef GAS u32 gu32;
typedef GAS u64 gu64;
#define RLX_AGENT __ATOMIC_RELAXED, __HIP_MEMORY_SCOPE_AGENT

namespace {
constexpr int S_ = 2048, D_ = 512, F_ = 1376;
constexpr u32 WS_EPOCH = 0;
constexpr u32 WS_FLAG = 32768;
constexpr u32 WS_STAT = 262144;
constexpr u32 WS_MODG = 524288;
constexpr u32 WS_MODF = 655360;
constexpr u32 WS_O = 1u << 20;
constexpr u32 WS_X1 = 3u << 20;
constexpr u32 WS_HH = 5u << 20;
constexpr u32 WS_RFLAG = 8192;
constexpr u32 WS_B4T = 11u << 20;
constexpr u32 WS_B5T = 14u << 20;
constexpr u32 WS_B3T = (15u << 20) + (512u << 10);
constexpr u32 WS_DBG = 16u << 20;
constexpr u32 WS_BYTES = 32u << 20;
constexpr int L_XY = 0;
constexpr int L_Z = 81920;
constexpr int L_MISC = 158720;
constexpr int L_TOTAL = 163840;
constexpr int L_RED = 73728;
constexpr int L_MODA = L_MISC;
constexpr int L_CNT = L_MISC + 4096;
constexpr int L_GAB = L_MISC + 4096 + 64;
constexpr int L_JUNK = L_MISC + 4096 + 64 + 512;
constexpr int L_STAMP = 156672;
constexpr u32 SPIN_CAP = 1u << 18;
constexpr u32 CODE_BYTES = 26112;

DI f32x4 mfma32(v4u a, v4u b, f32x4 c) {
  return __builtin_amdgcn_mfma_f32_16x16x32_bf16(__builtin_bit_cast(bf16x8, a), __builtin_bit_cast(bf16x8, b), c, 0, 0, 0);
}
DI f32x4 mfma16(v2u a, v2u b, f32x4 c) {
  return __builtin_amdgcn_mfma_f32_16x16x16bf16_1k(__builtin_bit_cast(s16x4, a), __builtin_bit_cast(s16x4, b), c, 0, 0, 0);
}
DI u32 pk(float lo, float hi) { f32x2 v = {lo, hi}; return __builtin_bit_cast(u32, __builtin_convertvector(v, bf16x2)); }
DI u32 pk2(f32x2 v) { return __builtin_bit_cast(u32, __builtin_convertvector(v, bf16x2)); }
DI v2u pk4(f32x4 v) { v2u r; r.x = pk(v.x, v.y); r.y = pk(v.z, v.w); return r; }
DI float asf(u32 u) { return __builtin_bit_cast(float, u); }
DI u32 asu(float f) { return __builtin_bit_cast(u32, f); }
DI float blo(u32 u) { return asf(u << 16); }
DI float bhi(u32 u) { return asf(u & 0xffff0000u); }
DI f32x2 unp(u32 u) { f32x2 r = {blo(u), bhi(u)}; return r; }
DI float dot2(u32 a, u32 b, float c) { return __builtin_amdgcn_fdot2_f32_bf16(__builtin_bit_cast(bf16x2, a), __builtin_bit_cast(bf16x2, b), c, false); }
DI v2u trd(LAS char* p) { return __builtin_bit_cast(v2u, __builtin_amdgcn_ds_read_tr16_b64_v4i16((LAS s16x4*)p)); }
DI v4u cat(v2u a, v2u b) { v4u r; r.x = a.x; r.y = a.y; r.z = b.x; r.w = b.y; return r; }
template <int MI, int SI> DI void dma(u32 mb, u32 voff, rsrc_t srd, u32 sb) {
  u32 t;
  asm volatile("s_add_u32 m0, %1, %c2\n\ts_add_u32 %0, %3, %c4\n\tbuffer_load_dwordx4 %5, %6, %0 offen lds"
               : "=&s"(t) : "s"(mb), "i"(MI), "s"(sb), "i"(SI), "v"(voff), "s"(srd) : "scc");
}
DI void touch(u32 voff, rsrc_t srd, u32 sb) {
  asm volatile("s_mov_b32 m0, %c0\n\ts_nop 0\n\tbuffer_load_dword %1, %2, %3 offen lds" ::"i"(L_JUNK), "v"(voff), "s"(srd), "s"(sb));
}
#define BAR() asm volatile("s_waitcnt lgkmcnt(0)\n\ts_barrier" ::: "memory")
#define VMW(n) asm volatile("s_waitcnt vmcnt(" #n ")" ::: "memory")
#define LGW() asm volatile("s_waitcnt lgkmcnt(0)" ::: "memory")
#define SCHED() __builtin_amdgcn_sched_barrier(0)
#define LD16(dst, voff, srd, soff, IMM) asm volatile("buffer_load_dwordx4 %0, %1, %2, %3 offen offset:" #IMM : "=v"(dst) : "v"(voff), "s"(srd), "s"(soff))
#define LD16S(dst, voff, srd, soff, IMM) asm volatile("buffer_load_dwordx4 %0, %1, %2, %3 offen offset:" #IMM " sc1" : "=v"(dst) : "v"(voff), "s"(srd), "s"(soff))
#define LD8(dst, voff, srd, soff, IMM) asm volatile("buffer_load_dwordx2 %0, %1, %2, %3 offen offset:" #IMM : "=v"(dst) : "v"(voff), "s"(srd), "s"(soff))
#define LD8S(dst, voff, srd, soff, IMM) asm volatile("buffer_load_dwordx2 %0, %1, %2, %3 offen offset:" #IMM " sc1" : "=v"(dst) : "v"(voff), "s"(srd), "s"(soff))
#define LD4(dst, voff, srd, soff) asm volatile("buffer_load_dword %0, %1, %2, %3 offen" : "=v"(dst) : "v"(voff), "s"(srd), "s"(soff))
#define LD4S(dst, voff, srd, soff) asm volatile("buffer_load_dword %0, %1, %2, %3 offen sc1" : "=v"(dst) : "v"(voff), "s"(srd), "s"(soff))

template <int IMM> DI void ld16s(v4u& r, u32 v, rsrc_t rs, u32 so) { asm volatile("buffer_load_dwordx4 %0, %1, %2, %3 offen offset:%c4 sc1" : "=v"(r) : "v"(v), "s"(rs), "s"(so), "i"(IMM)); }
template <int CTRL> DI float dppf(float v) { return asf(__builtin_amdgcn_update_dpp(0, asu(v), CTRL, 0xf, 0xf, true)); }
DI float row16_sum(float v) { v += dppf<0x128>(v); v += dppf<0x124>(v); v += dppf<0x122>(v); v += dppf<0x121>(v); return v; }
DI float sw32_add(float a, float b) { auto r = __builtin_amdgcn_permlane32_swap(asu(a), asu(b), false, false); return asf(r[0]) + asf(r[1]); }
DI float sw16_add(float a, float b) { auto r = __builtin_amdgcn_permlane16_swap(asu(a), asu(b), false, false); return asf(r[0]) + asf(r[1]); }
DI float x16_max(float v) { auto r = __builtin_amdgcn_permlane16_swap(asu(v), asu(v), false, false); return fmaxf(asf(r[0]), asf(r[1])); }
DI float x32_max(float v) { auto r = __builtin_amdgcn_permlane32_swap(asu(v), asu(v), false, false); return fmaxf(asf(r[0]), asf(r[1])); }
DI int fresh_lane() { int l; asm volatile("v_mbcnt_lo_u32_b32 %0, -1, 0\n\tv_mbcnt_hi_u32_b32 %0, -1, %0" : "=v"(l)); return l; }
DI float rdl(float v, int l) { return asf(__builtin_amdgcn_readlane(asu(v), l)); }

struct Params {
  const u16 *x, *wqkv, *wo, *w1, *w3, *w2, *c, *wmod;
  u16* out;
  char* ws;
};

#define LOOPSEL 0
#define STAMP(i) do {} while (0)
#define KSTAMP(lp, i) do {} while (0)
#define STAMP_DUMP() do {} while (0)

DI u32 flag_off(int phase, int g, int h) { return WS_FLAG + ((phase * 32 + g) * 2 + h) * 1024; }
DI void poll_flags(rsrc_t rWS, u32 vfl, u32 foff, u64 care, u32 tag) {
  for (u32 sp = 0; sp < SPIN_CAP; ++sp) {
    asm volatile("" ::: "memory");
    const u32 v = __builtin_amdgcn_raw_buffer_load_b32(rWS, vfl, foff, 16);
    if (!(__builtin_amdgcn_ballot_w64(v != tag) & care)) break;
  }
  VMW(0);
}
DI void raise_flag(rsrc_t rWS, u32 foff, u32 vtag, u32 vzero, u32 m1) {
  asm volatile("s_mov_b32 exec_hi, 0\n\ts_mov_b32 exec_lo, %4\n\tbuffer_store_dword %0, %1, %2, %3 offen sc1\n\ts_mov_b64 exec, -1" ::"v"(vtag), "v"(vzero), "s"(rWS), "s"(foff), "s"(m1) : "memory");
}
DI void signal_flag(rsrc_t rWS, u32 cnt_addr, u32 foff, u32 tag) {
  u32 t, a = cnt_addr, one = 1u, vt = tag, vz = 0u;
  asm volatile("s_mov_b64 exec, 1\n\tds_add_rtn_u32 %0, %1, %2\n\ts_waitcnt lgkmcnt(0)\n\tv_cmp_eq_u32 vcc, 7, %0\n\ts_and_b64 exec, exec, vcc\n\t"
               "buffer_store_dword %3, %4, %5, %6 offen sc1\n\ts_mov_b64 exec, -1"
               : "=&v"(t) : "v"(a), "v"(one), "v"(vt), "v"(vz), "s"(rWS), "s"(foff) : "vcc", "scc", "memory");
}
DI void arrive(u32 cnt_addr) {
  u32 a = cnt_addr, one = 1u;
  asm volatile("s_waitcnt lgkmcnt(0)\n\ts_mov_b64 exec, 1\n\tds_add_u32 %0, %1\n\ts_mov_b64 exec, -1" ::"v"(a), "v"(one) : "memory");
}
DI void await8(u32 cnt_addr) {
  u32 a = cnt_addr, t, sv;
  asm volatile("1:\n\tds_read_b32 %0, %2\n\ts_waitcnt lgkmcnt(0)\n\tv_readfirstlane_b32 %1, %0\n\ts_nop 0\n\ts_cmp_lt_u32 %1, 8\n\ts_cbranch_scc1 1b"
               : "=&v"(t), "=&s"(sv) : "v"(a) : "scc", "memory");
}
#define PIN(x) asm volatile("" : "+v"(x))
DI float qp(float r) { PIN(r); return r; }
DI float q_shl(u32 a) { return qp(blo(a)); }
DI float q_hi(u32 a) { return qp(bhi(a)); }
DI float q_fma(float a, float b, float c) { return qp(__builtin_fmaf(a, b, c)); }
DI float q_fmas(float a, float b, float c) { return qp(__builtin_fmaf(a, b, c)); }
DI float q_mul(float a, float b) { return qp(a * b); }
DI float q_mulk(float a) { return qp(a * -1.4426950408889634f); }
DI float q_add1(float a) { return qp(a + 1.f); }
DI float q_exp(float a) { return qp(__builtin_amdgcn_exp2f(a)); }
DI float q_rcp(float a) { return qp(__builtin_amdgcn_rcpf(a)); }
DI u32 q_cvt(float a, float b) { u32 r = pk(a, b); PIN(r); return r; }
DI float q_mov(float a) { return qp(a); }
DI u32 opq(u32 v) { asm volatile("" : "+v"(v)); return v; }

DI float sel(u64 m, float a, float b) { return __builtin_amdgcn_inverse_ballot_w64(m) ? a : b; }
DI v2u attend(v2u kp, v2u vp, v2u kc, v2u vc, v2u q, u64 m0, u64 m1, u32 hm, bool noprev) {
  const f32x4 z = {0.f, 0.f, 0.f, 0.f};
  const f32x4 sp = mfma16(kp, q, z);
  const f32x4 sc = mfma16(kc, q, z);
  const float K2 = 0.25f * 1.4426950408889634f;
  const u64 par = 0xaaaaaaaaaaaaaaaaull;
  float p0 = sel(par, sp[1], sp[0]), p1 = sel(par, sp[3], sp[2]);
  if (noprev) { p0 = -INFINITY; p1 = -INFINITY; }
  const float t0 = sel(m0, sel(par, sc[1], sc[0]), p0), t1 = sel(m1, sel(par, sc[3], sc[2]), p1);
  const float m = x32_max(x16_max(fmaxf(t0, t1)));
  const float mk = -m * K2;
  const float e0 = __builtin_amdgcn_exp2f(__builtin_fmaf(t0, K2, mk)), e1 = __builtin_amdgcn_exp2f(__builtin_fmaf(t1, K2, mk));
  const u32 w0 = pk(e0, e0) & hm, w1 = pk(e1, e1) & hm;
  v4u pf;
  pf.x = __builtin_amdgcn_inverse_ballot_w64(m0) ? 0u : w0; pf.y = __builtin_amdgcn_inverse_ballot_w64(m1) ? 0u : w1;
  pf.z = __builtin_amdgcn_inverse_ballot_w64(m0) ? w0 : 0u; pf.w = __builtin_amdgcn_inverse_ballot_w64(m1) ? w1 : 0u;
  const f32x4 o = mfma32(cat(vp, vc), pf, z);
  float sum = e0 + e1;
  sum = sw16_add(sum, sum); sum = sw32_add(sum, sum);
  return pk4(o * __builtin_amdgcn_rcpf(sum));
}

template <int N> using IC = std::integral_constant<int, N>;
template <int I, int N, class F> DI void sfor(F&& f) { if constexpr (I < N) { f(IC<I>{}); sfor<I + 1, N>(f); } }

__global__ void __launch_bounds__(512, 2) fused_block(Params p) {
  extern __shared__ __attribute__((aligned(16))) char smem[];
  LAS char* L = (LAS char*)smem;
  const int tid = threadIdx.x, lane = tid & 63, w = __builtin_amdgcn_readfirstlane(tid >> 6);
  const int c16 = lane & 15, g4 = lane >> 4;
  const int b = blockIdx.x;
  int g, j;
  if (XMAP == 0) { g = (b & 7) * 4 + ((b >> 3) & 3); j = b >> 5; }
  else if (XMAP == 2) { const int x = b & 7, r = b >> 3; g = (x >> 1) * 8 + (r & 7); j = (x & 1) * 4 + (r >> 3); }
  else { const int x = b & 7, r = b >> 3; g = (x >> 2) * 16 + (r & 15); j = (x & 3) * 2 + (r >> 4); }
  const rsrc_t rX = __builtin_amdgcn_make_buffer_rsrc((void*)p.x, 0, S_ * D_ * 2, 0x00020000);
  const rsrc_t rQKV = __builtin_amdgcn_make_buffer_rsrc((void*)p.wqkv, 0, D_ * 3 * D_ * 2, 0x00020000);
  const rsrc_t rWO = __builtin_amdgcn_make_buffer_rsrc((void*)p.wo, 0, D_ * D_ * 2, 0x00020000);
  const rsrc_t rW1 = __builtin_amdgcn_make_buffer_rsrc((void*)p.w1, 0, D_ * F_ * 2, 0x00020000);
  const rsrc_t rW3 = __builtin_amdgcn_make_buffer_rsrc((void*)p.w3, 0, D_ * F_ * 2, 0x00020000);
  const rsrc_t rW2 = __builtin_amdgcn_make_buffer_rsrc((void*)p.w2, 0, F_ * D_ * 2, 0x00020000);
  const rsrc_t rC = __builtin_amdgcn_make_buffer_rsrc((void*)p.c, 0, D_ * 2, 0x00020000);
  const rsrc_t rMOD = __builtin_amdgcn_make_buffer_rsrc((void*)p.wmod, 0, D_ * 6 * D_ * 2, 0x00020000);
  const rsrc_t rWS = __builtin_amdgcn_make_buffer_rsrc((void*)p.ws, 0, WS_BYTES, 0x00020000);
  const rsrc_t rOUT = __builtin_amdgcn_make_buffer_rsrc((void*)p.out, 0, S_ * D_ * 2, 0x00020000);
  STAMP(0);

  u32 ep;
  { const u32 vz = 0; asm volatile("buffer_load_dword %0, %1, %2, 0 offen sc1" : "=v"(ep) : "v"(vz), "s"(rWS)); }
  u32 vl16 = lane * 16; PIN(vl16);
  const int bm = b < 192 ? b : b - 192, mq = bm / 48, cl = bm - 48 * mq;
  v4u wm[4]; v2u cv; u32 ep0;
  if (w < 4) {
    const u32 vm = (tid >> 3) * 24576 + (tid & 7) * 16, s0 = mq * (128 * 6144) + cl * 128, s1 = s0 + 6144, s2 = s0 + 12288, s3 = s0 + 18432;
    LD16(wm[0], vm, rMOD, s0, 0); LD16(wm[1], vm, rMOD, s1, 0); LD16(wm[2], vm, rMOD, s2, 0); LD16(wm[3], vm, rMOD, s3, 0);
    const u32 vc = (tid >> 3) * 8, scq = mq * 256;
    LD8(cv, vc, rC, scq, 0);
    const u32 vz = 0; asm volatile("buffer_load_dword %0, %1, %2, 0 offen sc1" : "=v"(ep0) : "v"(vz), "s"(rWS));
  }
  if (tid < 16) *(LAS u32*)(L + L_CNT + tid * 4) = 0u;
  BAR();
  const int st_qd = lane >> 2, st_r = (st_qd & 3) + 4 * (st_qd >> 3);
  const int st_col = ((st_qd >> 2) & 1) * 64 + (((lane & 3) ^ ((w & 1) << 1)) << 4);
  const int rotp = g & 7;
  u32 vB1 = st_r * 3072 + st_col; PIN(vB1);
  const u32 mB1 = (w >> 2) * 12288 + (w & 3) * 1024, sB1 = (w >> 2) * 98304 + (w & 3) * 24576 + j * 128;
  auto stage_b1 = [&](auto PC, auto BC) {
    constexpr int pair = decltype(PC)::value, base = decltype(BC)::value;
    const u32 sp = ((pair + rotp) & 7) * 196608 + sB1;
    dma<base, 0>(mB1, vB1, rQKV, sp); dma<base + 4096, 1024>(mB1, vB1, rQKV, sp); dma<base + 8192, 2048>(mB1, vB1, rQKV, sp);
  };
  auto b1_piece = [&](auto PC, auto BC, auto TC) {
    constexpr int pair = decltype(PC)::value, base = decltype(BC)::value, ty = decltype(TC)::value;
    dma<base + ty * 4096, ty * 1024>(mB1, vB1, rQKV, ((pair + rotp) & 7) * 196608 + sB1);
  };
  const int hd = w & 3, blk2 = w >> 2, kq = w & 3, mh = w >> 2;
  const int cs0 = j < 4 ? 22 * j : 88 + 21 * (j - 4);
  const int cend = cs0 + (j < 4 ? 22 : 21);
  STAMP(1);
  const int T0 = 12 * (g >> 4) + 3 * (w & 3), rcb = min(cs0 + T0, 168);
  if (w == 4) touch((lane >> 1) * 2752 + (lane & 1) * 128, rW1, (g & 15) * (32 * 2752) + min(cs0 + 12 * (g >> 4), 160) * 16);
  if (w == 5) touch((lane >> 1) * 2752 + (lane & 1) * 128, rW3, (g & 15) * (32 * 2752) + min(cs0 + 12 * (g >> 4), 160) * 16);
  if (w == 6) touch((lane & 31) * 1024 + (lane >> 5) * (g < 11 ? 32 * 32768 : 0), rW2, g * 32768 + j * 128);
  if (w == 7) touch((lane & 31) * 1024, rWO, (g & 15) * 32768 + j * 128);
  if (w < 4) {
    asm volatile("s_waitcnt vmcnt(0)" : "+v"(cv), "+v"(wm[0]), "+v"(wm[1]), "+v"(wm[2]), "+v"(wm[3]), "+v"(ep0)::"memory");
    const u32 tg = __builtin_amdgcn_readfirstlane(ep0) + 1u;
    const f32x4 cf = {blo(cv.x), bhi(cv.x), blo(cv.y), bhi(cv.y)};
    float sc[4];
#pragma unroll
    for (int r = 0; r < 4; ++r) sc[r] = cf[r] * __builtin_amdgcn_rcpf(1.f + __builtin_amdgcn_exp2f(cf[r] * -1.4426950408889634f));
    f32x2 v[4];
#pragma unroll
    for (int i = 0; i < 4; ++i) v[i] = (unp(wm[0][i]) * sc[0] + unp(wm[1][i]) * sc[1]) + (unp(wm[2][i]) * sc[2] + unp(wm[3][i]) * sc[3]);
    const float a0 = sw32_add(v[0].x, v[2].x), a1 = sw32_add(v[0].y, v[2].y), a2 = sw32_add(v[1].x, v[3].x), a3 = sw32_add(v[1].y, v[3].y);
    float t0 = sw16_add(a0, a2), t1 = sw16_add(a1, a3);
    t0 += dppf<0x128>(t0); t1 += dppf<0x128>(t1);
    f32x2 o = {t0, t1};
    *(LAS f32x2*)(L + L_RED + w * 256 + (lane & 7) * 32 + g4 * 8) = o;
    LGW();
    u32 old = 0;
    if (lane == 0) old = __hip_atomic_fetch_add((LAS u32*)(L + L_CNT + 28), 1u, __ATOMIC_RELAXED, __HIP_MEMORY_SCOPE_WORKGROUP);
    if (__builtin_amdgcn_readfirstlane(old) == 3u) {
      asm volatile("" ::: "memory");
      float s = 0.f;
#pragma unroll
      for (int i = 0; i < 4; ++i) s += *(LAS float*)(L + L_RED + i * 256 + lane * 4);
      v2u gr; gr.x = asu(s); gr.y = tg;
      if (b < 192) __builtin_amdgcn_raw_buffer_store_b64(gr, rWS, lane * 8, WS_MODG + (mq * 3072 + cl * 64) * 8, 16);
    }
  }
  STAMP(2);
  v4u xr[10];
  {
    const int r0 = 64 * g - 16 + w;
    const u32 s0 = (u32)max(r0, 0) * 1024u, s1 = (u32)max(r0 + 8, 0) * 1024u;
    LD16(xr[0], vl16, rX, s0, 0); LD16(xr[1], vl16, rX, s1, 0);
#pragma unroll
    for (int i = 2; i < 10; ++i) { const u32 so = (u32)(r0 + 8 * i) * 1024u; LD16(xr[i], vl16, rX, so, 0); }
  }
  stage_b1(IC<0>{}, IC<L_XY>{});
  int afo[4];
#pragma unroll
  for (int i = 0; i < 4; ++i) { afo[i] = blk2 * 32768 + c16 * 1024 + (((4 * i + g4) ^ c16) << 4); PIN(afo[i]); }
  const u64 am0 = __builtin_amdgcn_ballot_w64((c16 & 14) >= 4 * g4), am1 = __builtin_amdgcn_ballot_w64((c16 & 14) >= 4 * g4 + 2);
  u32 ahm = (lane & 1) ? 0xffff0000u : 0xffffu; PIN(ahm);
  u32 vOst = c16 * 16 + g4 * 256 + (hd & 1) * 8; PIN(vOst);
  int tB1 = g4 * 1024 + 64 * (c16 >> 2) + 8 * (lane & 3) + 32 * ((hd & 1) ^ (g4 & 1)) + 256 * (hd >> 1); PIN(tB1);
  int hw0 = w * 1024 + ((((lane - 8 * rotp) & 63) ^ w) << 4); PIN(hw0);
#define vfl ((opq(vl16) & 112u) << 3)
  STAMP(3);
  v4u bw[48];
  auto read_b1 = [&](int pair, int base, int off) {
#pragma unroll
    for (int kk = 0; kk < 2; ++kk)
#pragma unroll
      for (int ty = 0; ty < 3; ++ty) {
        LAS char* a = L + base + (off + kk * 12288 + ty * 4096);
        bw[(2 * pair + kk) * 3 + ty] = cat(trd(a), trd(a + 512));
      }
  };
  f32x2 xn[10][4];
  v4u ga_[4]; v2u pv;
  const u32 sMG = WS_MODG + w * 1024;
  {
    float u[10];
    sfor<0, 10>([&](auto I) {
      constexpr int i = decltype(I)::value;
      if constexpr (i == 0) asm volatile("s_waitcnt vmcnt(11)" : "+v"(xr[0]), "+v"(xr[1]), "+v"(ep)::"memory");
      if constexpr (i == 2) asm volatile("s_waitcnt vmcnt(9)" : "+v"(xr[2]), "+v"(xr[3])::"memory");
      if constexpr (i == 4) asm volatile("s_waitcnt vmcnt(10)" : "+v"(xr[4]), "+v"(xr[5])::"memory");
      if constexpr (i == 6) asm volatile("s_waitcnt vmcnt(8)" : "+v"(xr[6]), "+v"(xr[7])::"memory");
      if constexpr (i == 8) asm volatile("s_waitcnt vmcnt(9)" : "+v"(xr[8]), "+v"(xr[9])::"memory");
      float s = 0.f, q = 0.f;
#pragma unroll
      for (int e = 0; e < 4; ++e) { s = dot2(xr[i][e], 0x3f803f80u, s); q = dot2(xr[i][e], xr[i][e], q); }
      u[i] = sw32_add(s, q);
      PIN(u[i]);
      if constexpr (i == 3) stage_b1(IC<1>{}, IC<L_XY + 24576>{});
      if constexpr (i == 7) stage_b1(IC<2>{}, IC<L_XY + 49152>{});
      if constexpr (i == 9) {
        const u32 s0 = sMG, s1 = sMG + 24576, s2 = sMG + 49152, s3 = sMG + 73728;
        ld16s<0>(ga_[0], vl16, rWS, s0); ld16s<0>(ga_[1], vl16, rWS, s1); ld16s<0>(ga_[2], vl16, rWS, s2); ld16s<0>(ga_[3], vl16, rWS, s3);
      }
    });
    float t[5];
#pragma unroll
    for (int i = 0; i < 5; ++i) t[i] = row16_sum(sw16_add(u[i], u[i + 5]));
    float v = t[0];
#pragma unroll
    for (int i = 1; i < 5; ++i) v = c16 == i ? t[i] : v;
    auto r = __builtin_amdgcn_permlane32_swap(asu(v), asu(v), false, false);
    const float mean = asf(r[0]) * (1.f / 512.f);
    const float var = fmaxf(asf(r[1]) * (1.f / 512.f) - mean * mean, 0.f);
    float rs = __builtin_amdgcn_rsqf(var + 1e-5f), nm = -mean * rs;
    PIN(rs); PIN(nm);
    STAMP(4);
    { const u32 vo = ((lane >> 3) & 3) * 24576 + (lane & 7) * 8, so = WS_MODG + (1024 + 8 * b) * 8; LD8S(pv, vo, rWS, so, 0); }
    if (w >= 4) {
      const int u = lane >> 2, row = (u & 3) | ((u >> 1) & 4) | ((u << 1) & 8);
      const u32 vo = row * 2752 + (lane & 3) * 16, so = (g & 15) * (32 * 2752) + rcb * 16, mb = L_Z + 49152 + (w & 3) * 4096;
      dma<0, 0>(mb, vo, rW1, so); dma<1024, 16 * 2752>(mb, vo, rW1, so); dma<2048, 0>(mb, vo, rW3, so); dma<3072, 16 * 2752>(mb, vo, rW3, so);
    } else {
      const int u = lane >> 1, row = (u & 3) | ((u >> 1) & 4) | ((u << 1) & 8) | (u & 16);
      const u32 vo = row * 1024 + (lane & 1) * 16, so = g * 32768 + j * 128 + w * 32, mb = L_Z + 65536 + w * 2048;
      dma<0, 0>(mb, vo, rW2, so); dma<1024, 0>(mb, vo, rW2, so + (g < 11 ? 32 * 32768 : 0));
      const int rwo = (u & 7) | ((u >> 1) & 8) | ((u << 1) & 16);
      dma<0, 0>(76800 + w * 1024, rwo * 1024 + (lane & 1) * 16, rWO, (g & 15) * 32768 + j * 128 + w * 32);
    }
    sfor<0, 10>([&](auto I) {
      constexpr int i = decltype(I)::value;
      if constexpr (i == 2) stage_b1(IC<3>{}, IC<L_Z>{});
      if constexpr (i == 6) stage_b1(IC<4>{}, IC<L_Z + 24576>{});
      const float a = rdl(rs, (i / 5) * 16 + i % 5), c = rdl(nm, (i / 5) * 16 + i % 5);
#pragma unroll
      for (int e = 0; e < 4; ++e) { xn[i][e] = unp(xr[i][e]) * a + c; PIN(xn[i][e]); }
    });
  }
  const u32 tag = __builtin_amdgcn_readfirstlane(ep) + 1u;
  u32 m1; asm("s_lshr_b32 %0, 1, %1" : "=s"(m1) : "s"(w) : "scc");
#define RAISE(fo) raise_flag(rWS, (fo), tag, vl16, m1)
#define SIGNAL(idx, fo) signal_flag(rWS, L_CNT + 4 * (idx), (fo), tag)
  STAMP(5);
  asm volatile("s_waitcnt vmcnt(9)" : "+v"(ga_[0]), "+v"(ga_[1]), "+v"(ga_[2]), "+v"(ga_[3]), "+v"(pv)::"memory");
  auto gsum = [&](v4u (&a)[4], f32x2& o) -> u32 {
    o.x = (asf(a[0].x) + asf(a[1].x)) + (asf(a[2].x) + asf(a[3].x)); o.y = (asf(a[0].z) + asf(a[1].z)) + (asf(a[2].z) + asf(a[3].z));
    return ((a[0].y ^ tag) | (a[0].w ^ tag) | (a[1].y ^ tag) | (a[1].w ^ tag)) | ((a[2].y ^ tag) | (a[2].w ^ tag) | (a[3].y ^ tag) | (a[3].w ^ tag));
  };
  auto gather = [&](u32 voff, u32 soff, f32x2& o) {
    for (u32 sp = 0; sp < SPIN_CAP; ++sp) {
      asm volatile("" ::: "memory");
      v4u a[4];
#pragma unroll
      for (int q = 0; q < 4; ++q) a[q] = __builtin_amdgcn_raw_buffer_load_b128(rWS, voff, soff + q * 24576, 16);
      if (!__builtin_amdgcn_ballot_w64(gsum(a, o) != 0u)) break;
    }
  };
  const float one = w >= 4 ? 1.f : 0.f;
  {
    f32x2 mA;
    if (__builtin_amdgcn_ballot_w64(gsum(ga_, mA) != 0u)) gather(vl16, sMG, mA);
    *(LAS f32x2*)(L + L_MODA + w * 512 + (vl16 >> 1)) = mA + one;
  }
  BAR();
  if (w == 7) {
    const u32 vo = ((lane >> 3) & 3) * 24576 + (lane & 7) * 8, so = WS_MODG + (1024 + 8 * b) * 8;
    for (u32 sp = 0; sp < SPIN_CAP && __builtin_amdgcn_ballot_w64(pv.y != tag); ++sp) {
      asm volatile("" ::: "memory");
      pv = __builtin_amdgcn_raw_buffer_load_b64(rWS, vo, so, 16);
    }
    float sv = asf(pv.x);
    sv += dppf<0x128>(sv); sv = sw16_add(sv, sv);
    pv.x = asu(sv);
    __builtin_amdgcn_raw_buffer_store_b64(pv, rWS, (lane & 7) * 8, WS_MODF + (1024 + 8 * b) * 8, 16);
  }
  STAMP(6);
  int vma = L_MODA + 2 * vl16; PIN(vma);
  const f32x4 ms0 = *(LAS f32x4*)(L + vma), ms1 = *(LAS f32x4*)(L + vma + 16);
  const f32x4 ma0 = *(LAS f32x4*)(L + vma + 2048), ma1 = *(LAS f32x4*)(L + vma + 2064);
  int tz0 = tB1 + L_Z; PIN(tz0);
  { int t2 = tB1 + 49152; PIN(t2); read_b1(0, tB1, 0); read_b1(1, tB1, 24576); read_b1(2, t2, 0); }
  {
    VMW(6);
    if (w >= 4) {
      LAS char* a0 = L + L_Z + 49152 + (w & 3) * 4096 + ((lane >> 1) & 1) * 2048 + 64 * ((c16 >> 2) + 4 * (g4 & 1) + 16 * (g4 >> 1)) + 8 * (lane & 1);
#pragma unroll
      for (int ct = 0; ct < 3; ++ct) {
        const int c = min(cs0 + T0 + ct - rcb, 3) * 16;
        const v4u f = cat(trd(a0 + c), trd(a0 + c + 512));
        const u32 sf = WS_B4T + ((j * 16 + (g & 15)) * 24 + T0 + ct) * 1024;
        asm volatile("buffer_store_dwordx4 %0, %1, %2, %3 offen sc1\n\ts_nop 1" ::"v"(f), "v"(vl16), "s"(rWS), "s"(sf) : "memory");
      }
    }
    else {
      LAS char* a0 = L + L_Z + 65536 + w * 2048 + 32 * ((c16 >> 2) + 4 * (g4 & 1) + 16 * (g4 >> 1)) + 8 * (lane & 3);
#pragma unroll
      for (int i = 0; i < 2; ++i) {
        const v4u f = cat(trd(a0 + i * 1024), trd(a0 + i * 1024 + 256));
        const u32 sf = WS_B5T + ((j * 43 + g + (i && g < 11 ? 32 : 0)) * 4 + w) * 1024;
        asm volatile("buffer_store_dwordx4 %0, %1, %2, %3 offen sc1\n\ts_nop 1" ::"v"(f), "v"(vl16), "s"(rWS), "s"(sf) : "memory");
      }
      LAS char* a1 = L + 76800 + w * 1024 + 32 * ((c16 >> 2) + 4 * (g4 & 1) + 16 * (g4 >> 1)) + 8 * (lane & 3);
      const v4u f = cat(trd(a1), trd(a1 + 256));
      const u32 sf = WS_B3T + ((j * 16 + (g & 15)) * 4 + w) * 1024;
      asm volatile("buffer_store_dwordx4 %0, %1, %2, %3 offen sc1\n\ts_nop 1" ::"v"(f), "v"(vl16), "s"(rWS), "s"(sf) : "memory");
    }
  }
  BAR();
  STAMP(7);
  {
    int hw1 = (hw0 ^ 128) + 8192; PIN(hw1);
    sfor<0, 10>([&](auto I) {
      constexpr int i = decltype(I)::value;
      v4u o;
      o.x = pk2(xn[i][0] * ma0.xy + ms0.xy); o.y = pk2(xn[i][1] * ma0.zw + ms0.zw);
      o.z = pk2(xn[i][2] * ma1.xy + ms1.xy); o.w = pk2(xn[i][3] * ma1.zw + ms1.zw);
      if constexpr (i == 8) { hw0 += 65536; hw1 += 65536; }
      *(LAS v4u*)(L + ((i & 1) ? hw1 : hw0) + ((i >> 1) & 3) * 16384) = o;
      SCHED();
    });
  }
  VMW(0);
  BAR();
  RAISE(WS_RFLAG + (j * 32 + g) * 4);
  u32 flr;
  STAMP(8);

  const f32x4 z4 = {0.f, 0.f, 0.f, 0.f};
#define AFRAG(off, ks) (*(LAS v4u*)(L + afo[(ks) & 3] + ((off) + ((ks) >> 2) * 256)))
  const int ch0 = cs0 + 3 * w;
  constexpr int ND = NODMA ? 0 : 1;
  v4u b4[48];
  const u32 sB4 = WS_B4T + (j * 16 * 24 + 3 * w) * 1024;
  auto load_b4 = [&](auto NC) {
    constexpr int n = decltype(NC)::value, ks = n / 3, ct = n % 3;
    const u32 so = sB4 + ((ks + 2 * rotp) & 15) * 24576;
    v4u& r = b4[n];
    if constexpr (ct == 0) LD16S(r, vl16, rWS, so, 0); else if constexpr (ct == 1) LD16S(r, vl16, rWS, so, 1024); else LD16S(r, vl16, rWS, so, 2048);
  };
  auto landed_b4 = [&](auto AC, auto BC) {
#pragma unroll
    for (int i = decltype(AC)::value; i < decltype(BC)::value; ++i) { v4u& r = b4[i]; PIN(r); }
  };
  v4u gb_, gg_;
  u32 vgg, fl0, vRow;
  int pw, pr;
  f32x4 aKh = z4, aVh = z4, aQ = z4, aK = z4, aV = z4, bQ = z4, bK = z4, bV = z4;
  {
    v4u ah = AFRAG(0, 0), a1 = AFRAG(16384, 0), a2 = AFRAG(32768, 0);
    int l3, c3, g3; f32x2 mB, mG;
    auto job = [&](auto NC) {
      constexpr int n = decltype(NC)::value;
      if constexpr (n == 3) { l3 = opq(vl16) >> 4; c3 = l3 & 15; g3 = l3 >> 4; PIN(c3); PIN(g3); vgg = (vl16 < 512 ? 8192 : 20480 - 512) + 512 * j + vl16; PIN(vgg); }
      if constexpr (n == 7) { vRow = (8 * w + (l3 >> 3)) * 1024 + (l3 & 7) * 16; PIN(vRow); }
      if constexpr (n == 8) { pr = (8 * w + (l3 >> 3)) * 256 + (((2 * (l3 & 7)) ^ ((l3 >> 3) & 7)) << 4); PIN(pr); }
      if constexpr (n == 10) { pw = kq * 16384 + (32 * mh + c3) * 256 + ((g3 ^ (c3 & 3)) << 4) + ((c3 & 4) << 4); PIN(pw); }
      if constexpr (n == 31) { mB.x = asf(gb_.x) + one; PIN(mB.x); mB.y = asf(gb_.z) + one; PIN(mB.y); *(LAS f32x2*)(L + L_MODA + w * 512 + (vl16 >> 1)) = mB; }
      if constexpr (n == 32) { mG.x = asf(gg_.x); mG.y = asf(gg_.z); *(LAS f32x2*)(L + L_GAB + (vl16 >> 1)) = mG; }
      if constexpr (n == 34) { fl0 = (gb_.y ^ tag) | (gb_.w ^ tag) | (gg_.y ^ tag) | (gg_.w ^ tag); PIN(fl0); }
      if constexpr (n == 7) { const u32 s0 = WS_MODF + 12288 + w * 1024; LD16S(gb_, vl16, rWS, s0, 0); }
      if constexpr (n == 9) { const u32 s0 = WS_MODF; LD16S(gg_, vgg, rWS, s0, 0); }
      if constexpr (n < 6 && !(n & 1)) b1_piece(IC<5>{}, IC<L_Z + 49152>{}, IC<n / 2>{});
      if constexpr (n == 6) arrive(L_CNT + 8);
      if constexpr (n == 10) await8(L_CNT + 8);
      if constexpr (n >= 10 && n < 22 && !(n & 1)) b1_piece(IC<6 + (n - 10) / 6>{}, IC<L_Z + ((n - 10) / 6) * 24576>{}, IC<((n - 10) / 2) % 3>{});
      if constexpr (n == 21) { const u32 vr = (vl16 >> 2) & 124, sr = WS_RFLAG + j * 128; LD4S(flr, vr, rWS, sr); }
      if constexpr (n == 26) {
        asm volatile("s_waitcnt vmcnt(0)" : "+v"(gb_), "+v"(gg_), "+v"(flr)::"memory");
        arrive(L_CNT + 12);
        const u32 vr = (vl16 >> 2) & 124, sr = WS_RFLAG + j * 128; u32 cnt = SPIN_CAP;
        asm volatile("1:\n\tv_cmp_ne_u32 vcc, %5, %0\n\ts_cbranch_vccz 2f\n\tbuffer_load_dword %0, %2, %3, %4 offen sc1\n\ts_waitcnt vmcnt(0)\n\t"
                     "s_sub_u32 %1, %1, 1\n\ts_cbranch_scc0 1b\n\t2:" : "+v"(flr), "+s"(cnt) : "v"(vr), "s"(rWS), "s"(sr), "s"(tag) : "vcc", "scc", "memory");
      }
      if constexpr (n == 28) { await8(L_CNT + 12); int tz2 = tz0 + 49152; PIN(tz2); read_b1(5, tz2, 0); }
      if constexpr (n == 32) read_b1(6, tz0, 0);
      if constexpr (n == 37) read_b1(7, tz0, 24576);
      if constexpr (n >= 27 && n < 47 && (n & 1)) load_b4(IC<(n - 27) / 2>{});
    };
    sfor<0, 16>([&](auto KS) {
      constexpr int ks = decltype(KS)::value;
      KSTAMP(1, ks);
      if constexpr (ks == 0) read_b1(3, tz0, 0);
      if constexpr (ks == 1) read_b1(4, tz0, 24576);
      SCHED();
      v4u nh = ah, n1 = a1, n2 = a2;
      aKh = mfma32(bw[ks * 3 + 1], ah, aKh);
      aVh = mfma32(ah, bw[ks * 3 + 2], aVh);
      if constexpr (ks < 15) nh = AFRAG(0, ks + 1);
      job(IC<3 * ks>{});
      SCHED();
      aQ = mfma32(bw[ks * 3 + 0], a1, aQ);
      aK = mfma32(bw[ks * 3 + 1], a1, aK);
      aV = mfma32(a1, bw[ks * 3 + 2], aV);
      if constexpr (ks < 15) n1 = AFRAG(16384, ks + 1);
      job(IC<3 * ks + 1>{});
      SCHED();
      bQ = mfma32(bw[ks * 3 + 0], a2, bQ);
      bK = mfma32(bw[ks * 3 + 1], a2, bK);
      bV = mfma32(a2, bw[ks * 3 + 2], bV);
      if constexpr (ks < 15) n2 = AFRAG(32768, ks + 1);
      job(IC<3 * ks + 2>{});
      ah = nh; a1 = n1; a2 = n2;
      SCHED();
    });
  }
  STAMP(9); KSTAMP(1, 16);
  v4u b3[16];
  auto load_b3 = [&](auto NC) { constexpr int n = decltype(NC)::value; ld16s<(n & 3) * 1024>(b3[n], vl16, rWS, WS_B3T + (j * 16 + 4 * kq + (n >> 2)) * 4096); };
  {
    const u32 sOst = WS_O + ((g * 4 + 2 * blk2) * 16 + 2 * j + (hd >> 1)) * 1024;
    const v2u k1p = pk4(aK), v1p = pk4(aV);
    sfor<0, 6>(load_b3);
    const v2u o0 = attend(pk4(aKh), pk4(aVh), k1p, v1p, pk4(aQ), am0, am1, ahm, g == 0 && blk2 == 0);
    const v2u o1 = attend(k1p, v1p, pk4(bK), pk4(bV), pk4(bQ), am0, am1, ahm, false);
    __builtin_amdgcn_raw_buffer_store_b64(o0, rWS, vOst, sOst, 16);
    __builtin_amdgcn_raw_buffer_store_b64(o1, rWS, vOst, sOst + 16384, 16);
  }
  if (__builtin_amdgcn_ballot_w64(fl0 != 0u)) {
    for (u32 sp = 0; sp < SPIN_CAP; ++sp) {
      asm volatile("" ::: "memory");
      gb_ = __builtin_amdgcn_raw_buffer_load_b128(rWS, vl16, WS_MODF + 12288 + w * 1024, 16);
      gg_ = __builtin_amdgcn_raw_buffer_load_b128(rWS, vgg, WS_MODF, 16);
      if (!__builtin_amdgcn_ballot_w64(((gb_.y ^ tag) | (gb_.w ^ tag) | (gg_.y ^ tag) | (gg_.w ^ tag)) != 0u)) break;
    }
    const f32x2 mB = {asf(gb_.x) + one, asf(gb_.z) + one}, mG = {asf(gg_.x), asf(gg_.z)};
    *(LAS f32x2*)(L + L_MODA + w * 512 + (vl16 >> 1)) = mB;
    *(LAS f32x2*)(L + L_GAB + (vl16 >> 1)) = mG;
  }
  STAMP(10);
  VMW(0);
  landed_b4(IC<0>{}, IC<10>{});
  BAR();
  RAISE(flag_off(0, g, 0) + j * 128);
  STAMP(11);
  if (b == 0 && w == 7) {
    for (u32 sp = 0; sp < SPIN_CAP; ++sp) {
      asm volatile("" ::: "memory");
      const v4u v = __builtin_amdgcn_raw_buffer_load_b128(rWS, vl16, WS_RFLAG, 16);
      if (__all(v.x == tag && v.y == tag && v.z == tag && v.w == tag)) break;
    }
    __builtin_amdgcn_raw_buffer_store_b32(tag, rWS, 0, WS_EPOCH, 16);
  }

  v4u xv;
  { const u32 sx = g * 65536 + j * 128; LD16(xv, vRow, rX, sx, 0); }
  sfor<6, 12>(load_b3);
  const u64 care2 = 0x0101010101010101ull * (3u << (2 * kq));
  poll_flags(rWS, vfl, flag_off(0, g, 0), care2, tag);
  PIN(xv);
  sfor<12, 16>(load_b3);
  v4u af[8];
  {
    const u32 so = WS_O + ((g * 4 + 2 * mh) * 16 + 4 * kq) * 1024, so1 = so + 16384;
    LD16S(af[0], vl16, rWS, so, 0); LD16S(af[1], vl16, rWS, so, 1024); LD16S(af[2], vl16, rWS, so, 2048); LD16S(af[3], vl16, rWS, so, 3072);
    LD16S(af[4], vl16, rWS, so1, 0); LD16S(af[5], vl16, rWS, so1, 1024); LD16S(af[6], vl16, rWS, so1, 2048); LD16S(af[7], vl16, rWS, so1, 3072);
  }
  STAMP(12);
  sfor<0, 2>([&](auto M) {
    constexpr int m = decltype(M)::value;
    asm volatile("s_waitcnt vmcnt(%c4)" : "+v"(af[4 * m]), "+v"(af[4 * m + 1]), "+v"(af[4 * m + 2]), "+v"(af[4 * m + 3]) : "i"(m == 0 ? 4 : 2) : "memory");
    if constexpr (m == 0) {
#pragma unroll
      for (int i = 0; i < 16; ++i) PIN(b3[i]);
    }
    f32x4 acc[4] = {z4, z4, z4, z4};
    sfor<0, 4>([&](auto SC) {
      constexpr int sx = decltype(SC)::value;
#pragma unroll
      for (int n = 0; n < 4; ++n) acc[n] = mfma32(b3[sx * 4 + n], af[4 * m + sx], acc[n]);
      if constexpr (sx & 1) load_b4(IC<10 + 2 * m + (sx >> 1)>{});
      SCHED();
    });
#pragma unroll
    for (int n = 0; n < 4; ++n) *(LAS f32x4*)(L + ((n & 1) ? (pw ^ 64) : pw) + (m * 4096 + (n >> 1) * 128)) = acc[n];
  });
  BAR();
  STAMP(13);
  sfor<14, 18>(load_b4);
  {
    f32x4 y0 = *(LAS f32x4*)(L + pr), y1 = *(LAS f32x4*)(L + (pr ^ 16));
#pragma unroll
    for (int q = 1; q < 4; ++q) { y0 += *(LAS f32x4*)(L + pr + q * 16384); y1 += *(LAS f32x4*)(L + (pr ^ 16) + q * 16384); }
    const int ga = L_GAB + ((vl16 & 112) << 1);
    const f32x4 g0 = *(LAS f32x4*)(L + ga), g1 = *(LAS f32x4*)(L + ga + 16);
    const f32x4 x0 = f32x4{blo(xv.x), bhi(xv.x), blo(xv.y), bhi(xv.y)} + g0 * y0, x1 = f32x4{blo(xv.z), bhi(xv.z), blo(xv.w), bhi(xv.w)} + g1 * y1;
    { const v4u o = cat(pk4(x0), pk4(x1)); const u32 so = WS_X1 + g * 65536 + j * 128;
      asm volatile("buffer_store_dwordx4 %0, %1, %2, %3 offen sc1\n\ts_nop 1" ::"v"(o), "v"(vRow), "s"(rWS), "s"(so) : "memory"); }
    const f32x4 sv = x0 + x1;
    float mean = (sv.x + sv.y) + (sv.z + sv.w);
    mean += dppf<0xB1>(mean); mean += dppf<0x4E>(mean); mean += dppf<0x141>(mean);
    mean *= 1.f / 64.f;
    const f32x4 d0 = x0 - mean, d1 = x1 - mean, dd = d0 * d0 + d1 * d1;
    float m2 = (dd.x + dd.y) + (dd.z + dd.w);
    m2 += dppf<0xB1>(m2); m2 += dppf<0x4E>(m2); m2 += dppf<0x141>(m2);
    v2u st; st.x = asu(mean); st.y = asu(m2);
    __builtin_amdgcn_raw_buffer_store_b64(st, rWS, (vRow >> 10) << 6, WS_STAT + g * 4096 + j * 8, 16);
  }
  STAMP(14);
  VMW(0);
  landed_b4(IC<10>{}, IC<18>{});
  BAR();
  RAISE(flag_off(1, g, 0) + j * 128);
  sfor<18, 24>(load_b4);
  STAMP(15);

  v4u x1v[4]; v2u stv;
  auto ln2_load = [&](int h) {
    const u32 so = WS_X1 + g * 65536 + (32 * (w >> 2) + 16 * h + 4 * (w & 3)) * 1024;
    LD16S(x1v[0], vl16, rWS, so, 0); LD16S(x1v[1], vl16, rWS, so, 1024); LD16S(x1v[2], vl16, rWS, so, 2048); LD16S(x1v[3], vl16, rWS, so, 3072);
    const u32 vs = (vl16 >> 1) & 255, ss = WS_STAT + g * 4096 + (32 * (w >> 2) + 16 * h + 4 * (w & 3)) * 64;
    LD8S(stv, vs, rWS, ss, 0);
  };
  float l2r, l2n;
  auto ln2_stats = [&]() {
    const float mj = asf(stv.x);
    float mean = mj;
    mean += dppf<0xB1>(mean); mean += dppf<0x4E>(mean); mean += dppf<0x141>(mean);
    mean *= 0.125f;
    const float dm = mj - mean;
    float m2 = asf(stv.y) + 64.f * dm * dm;
    m2 += dppf<0xB1>(m2); m2 += dppf<0x4E>(m2); m2 += dppf<0x141>(m2);
    l2r = __builtin_amdgcn_rsqf(m2 * (1.f / 512.f) + 1e-5f);
    l2n = -mean * l2r;
  };
  u32 vH;
  u32 hmask[3];
#pragma unroll
  for (int ct = 0; ct < 3; ++ct)
    asm("s_cmp_lt_u32 %1, %2\n\ts_cselect_b32 %0, -1, 0" : "=s"(hmask[ct]) : "s"((u32)(ch0 + ct)), "s"((u32)cend) : "scc");
  auto hh_store = [&](v2u o, int ct, int h) {
    const u32 so = WS_HH + ((g * 4 + h) * 172 + ch0 + ct) * 256;
    asm volatile("s_mov_b32 exec_lo, %4\n\ts_mov_b32 exec_hi, %4\n\tbuffer_store_dwordx2 %0, %1, %2, %3 offen sc1\n\ts_mov_b64 exec, -1"
                 ::"v"(o), "v"(vH), "s"(rWS), "s"(so), "s"(hmask[ct]) : "memory");
  };
  auto swiglu_tile = [&](f32x4 (&acc)[3][2], int ct, int h) {
    f32x4 G, U, R;
#pragma unroll
    for (int e = 0; e < 4; ++e) {
      auto r = __builtin_amdgcn_permlane32_swap(asu(acc[ct][0][e]), asu(acc[ct][1][e]), false, false);
      G[e] = asf(r[0]); U[e] = asf(r[1]);
    }
    const f32x4 T = G * -1.4426950408889634f;
#pragma unroll
    for (int e = 0; e < 4; ++e) R[e] = __builtin_amdgcn_rcpf(1.f + __builtin_amdgcn_exp2f(T[e]));
    hh_store(pk4((G * U) * R), ct, h);
  };
#pragma unroll
  for (int i = 0; i < 4; ++i) { afo[i] -= blk2 * 32768; PIN(afo[i]); }
  int vm2 = L_MODA + 2 * vl16; PIN(vm2);
  int h2w = w * 4096 + (((vl16 - 128 * rotp) & 1023) ^ ((4 * w & 15) << 4)); PIN(h2w);
  poll_flags(rWS, vfl, flag_off(1, g, 0), ~0ull, tag);
  landed_b4(IC<18>{}, IC<24>{});
  ln2_load(0);
  sfor<24, 28>(load_b4);
  {
    const f32x4 s0 = *(LAS f32x4*)(L + vm2), s1 = *(LAS f32x4*)(L + vm2 + 16), a0 = *(LAS f32x4*)(L + vm2 + 2048), a1 = *(LAS f32x4*)(L + vm2 + 2064);
    asm volatile("s_waitcnt vmcnt(%c5)" : "+v"(x1v[0]), "+v"(x1v[1]), "+v"(x1v[2]), "+v"(x1v[3]), "+v"(stv) : "i"(4) : "memory");
    ln2_stats();
#pragma unroll
    for (int i = 0; i < 4; ++i) {
      const float rs = rdl(l2r, 8 * i), nn = rdl(l2n, 8 * i);
      v4u o;
      o.x = pk2((unp(x1v[i].x) * rs + nn) * a0.xy + s0.xy); o.y = pk2((unp(x1v[i].y) * rs + nn) * a0.zw + s0.zw);
      o.z = pk2((unp(x1v[i].z) * rs + nn) * a1.xy + s1.xy); o.w = pk2((unp(x1v[i].w) * rs + nn) * a1.zw + s1.zw);
      *(LAS v4u*)(L + (h2w ^ (i << 4)) + i * 1024) = o;
    }
  }
  ln2_load(1);
  BAR();
  STAMP(16);
  f32x4 accA[3][2] = {{z4, z4}, {z4, z4}, {z4, z4}};
  {
    f32x4 cs, ca; float rsv[4], nnv, t[4]; v2u ov;
    auto ln_op = [&](auto NC) {
      constexpr int n = decltype(NC)::value, u = n / 16, k = n % 16, hf = u & 1, i = u >> 1;
      if constexpr (u < 8) {
        if constexpr (k == 0) {
          cs = *(LAS f32x4*)(L + vm2 + hf * 16); ca = *(LAS f32x4*)(L + vm2 + 2048 + hf * 16);
          if constexpr (hf == 0) nnv = q_mov(rdl(l2n, 8 * i));
        }
        else if constexpr (k == 1) t[0] = q_shl(x1v[i][2 * hf]);
        else if constexpr (k == 2) t[1] = q_hi(x1v[i][2 * hf]);
        else if constexpr (k == 3) t[2] = q_shl(x1v[i][2 * hf + 1]);
        else if constexpr (k == 4) t[3] = q_hi(x1v[i][2 * hf + 1]);
        else if constexpr (k < 9) t[k - 5] = q_fmas(t[k - 5], rsv[i], nnv);
        else if constexpr (k < 13) t[k - 9] = q_fma(t[k - 9], ca[k - 9], cs[k - 9]);
        else if constexpr (k == 13) ov.x = q_cvt(t[0], t[1]);
        else if constexpr (k == 14) ov.y = q_cvt(t[2], t[3]);
        else {
          *(LAS v2u*)(L + (h2w ^ (i << 4)) + (32768 + i * 1024 + hf * 8)) = ov;
        }
      }
    };
    v4u a0 = AFRAG(0, 0), a1 = AFRAG(16384, 0);
    sfor<0, 16>([&](auto KS) {
      constexpr int ks = decltype(KS)::value;
      KSTAMP(3, ks);
      v4u n0 = a0, n1 = a1;
      if constexpr (ks < 15) { n0 = AFRAG(0, ks + 1); n1 = AFRAG(16384, ks + 1); }
      if constexpr (ks == 3) {
        asm volatile("s_waitcnt vmcnt(6)" : "+v"(x1v[0]), "+v"(x1v[1]), "+v"(x1v[2]), "+v"(x1v[3]), "+v"(stv)::"memory");
        landed_b4(IC<24>{}, IC<28>{});
      }
      if constexpr (ks == 9) { VMW(4); landed_b4(IC<28>{}, IC<39>{}); }
      if constexpr (ks == 13) { VMW(2); landed_b4(IC<39>{}, IC<46>{}); }
      if constexpr (ks == 15) { VMW(0); landed_b4(IC<46>{}, IC<48>{}); }
      SCHED();
      if constexpr (ks == 3) { ln2_stats(); PIN(l2r); PIN(l2n);
#pragma unroll
        for (int i = 0; i < 4; ++i) rsv[i] = rdl(l2r, 8 * i); }
      sfor<0, 6>([&](auto MC) {
        constexpr int m = decltype(MC)::value, ct = m >> 1;
        if constexpr (m & 1) accA[ct][1] = mfma32(b4[ks * 3 + ct], a1, accA[ct][1]); else accA[ct][0] = mfma32(b4[ks * 3 + ct], a0, accA[ct][0]);
        if constexpr (ks < 6 && m % 3 == 0) load_b4(IC<28 + ks * 2 + m / 3>{});
        if constexpr (ks >= 6 && ks < 9 && m == 0) load_b4(IC<40 + (ks - 6)>{});
        if constexpr (ks >= 9 && ks < 12 && (m == 0 || (m == 3 && ks < 11))) load_b4(IC<43 + (ks - 9) * 2 + (m == 3)>{});
        if constexpr (ks >= 4) {
          constexpr int o = (ks - 4) * 15 + (m >> 1) * 5 + (m & 1) * 2;
          ln_op(IC<o>{}); ln_op(IC<o + 1>{});
          if constexpr (m & 1) ln_op(IC<o + 2>{});
          SCHED();
        }
      });
      a0 = n0; a1 = n1;
      SCHED();
    });
  }
  vH = (vl16 & 255) + ((vl16 >> 5) & 8) + (vl16 >> 9) * (2 * 172 * 256); PIN(vH);
  BAR();
  STAMP(17); KSTAMP(3, 16);
  f32x4 accB[3][2] = {{z4, z4}, {z4, z4}, {z4, z4}};
  u32 vRow5; int pr5, pw5, gb5;
  const int kb5 = 5 * w + min(w, 3);
  u64 care5 = 0x0101010101010101ull * ((0x80c06030180c0603ull >> (8 * w)) & 255u);
  asm volatile("" : "+s"(care5));
  v4u b5[24], hA[12], hB[12]; v2u xqA, xqB;
  const u32 sB5 = WS_B5T + (j * 43 + kb5) * 4096, d5 = (w + 5) >> 3;
  auto load_b5 = [&](auto NC) {
    constexpr int n = decltype(NC)::value, sx = n / 4, t = n % 4;
    const u32 so = sB5 + (sx == 5 ? (5 - d5) * 4096 : sx * 4096);
    ld16s<t * 1024>(b5[n], vl16, rWS, so);
  };
  auto load_h = [&](auto NC, v4u (&hf)[12], int h) {
    constexpr int n = decltype(NC)::value, sx = n / 2, m = n % 2;
    const u32 so = WS_HH + ((g * 4 + 2 * m + h) * 172 + 4 * kb5) * 256 + (sx == 5 ? (5 - d5) * 1024 : sx * 1024);
    ld16s<0>(hf[n], vl16, rWS, so);
  };
  auto poll5 = [&](int h) {
    const u32 fo = flag_off(2, g, h), vf = vfl; u32 t, cnt = SPIN_CAP;
    asm volatile("1:\n\tbuffer_load_dword %0, %2, %3, %4 offen sc1\n\ts_waitcnt vmcnt(0)\n\tv_cmp_ne_u32 vcc, %5, %0\n\ts_and_b64 vcc, vcc, %6\n\ts_cbranch_scc0 2f\n\t"
                 "s_sub_u32 %1, %1, 1\n\ts_cbranch_scc0 1b\n\t2:" : "=&v"(t), "+s"(cnt) : "v"(vf), "s"(rWS), "s"(fo), "s"(tag), "s"(care5) : "vcc", "scc", "memory");
  };
  {
    float G[4], U[4], T[4]; v2u ov;
    auto sw_op = [&](auto NC) {
      constexpr int n = decltype(NC)::value, ct = n / 22, k = n % 22;
      if constexpr (ct < 3) {
        if constexpr (k < 4) { auto r = __builtin_amdgcn_permlane32_swap(asu(accA[ct][0][k]), asu(accA[ct][1][k]), false, false); G[k] = asf(r[0]); U[k] = asf(r[1]); PIN(G[k]); PIN(U[k]); }
        else if constexpr (k < 6) { T[2 * (k - 4)] = q_mulk(G[2 * (k - 4)]); T[2 * (k - 4) + 1] = q_mulk(G[2 * (k - 4) + 1]); }
        else if constexpr (k < 10) T[k - 6] = q_exp(T[k - 6]);
        else if constexpr (k < 12) { T[2 * (k - 10)] = q_add1(T[2 * (k - 10)]); T[2 * (k - 10) + 1] = q_add1(T[2 * (k - 10) + 1]); }
        else if constexpr (k < 16) T[k - 12] = q_rcp(T[k - 12]);
        else if constexpr (k < 18) { G[2 * (k - 16)] = q_mul(G[2 * (k - 16)], U[2 * (k - 16)]); G[2 * (k - 16) + 1] = q_mul(G[2 * (k - 16) + 1], U[2 * (k - 16) + 1]); }
        else if constexpr (k < 20) { G[2 * (k - 18)] = q_mul(G[2 * (k - 18)], T[2 * (k - 18)]); G[2 * (k - 18) + 1] = q_mul(G[2 * (k - 18) + 1], T[2 * (k - 18) + 1]); }
        else if constexpr (k == 20) { ov.x = q_cvt(G[0], G[1]); ov.y = q_cvt(G[2], G[3]); }
        else hh_store(ov, ct, 0);
      }
    };
    v4u a0 = AFRAG(32768, 0), a1 = AFRAG(49152, 0);
    int l5, c5, g5;
    sfor<0, 16>([&](auto KS) {
      constexpr int ks = decltype(KS)::value;
      KSTAMP(4, ks);
      v4u n0 = a0, n1 = a1;
      if constexpr (ks > 0 && ks < 15) { n0 = AFRAG(32768, ks + 1); n1 = AFRAG(49152, ks + 1); }
      if constexpr (ks == 7) { VMW(0); SIGNAL(1, flag_off(2, g, 0) + j * 128); }
      if constexpr (ks == 14) poll5(0);
      SCHED();
      sfor<0, 6>([&](auto MC) {
        constexpr int m = decltype(MC)::value, ct = m >> 1, n = ks * 6 + m;
        if constexpr (m & 1) accB[ct][1] = mfma32(b4[ks * 3 + ct], a1, accB[ct][1]); else accB[ct][0] = mfma32(b4[ks * 3 + ct], a0, accB[ct][0]);
        if constexpr (n == 3) n0 = AFRAG(32768, 1);
        if constexpr (n == 5) n1 = AFRAG(49152, 1);
        if constexpr (n < 33) { sw_op(IC<2 * n>{}); sw_op(IC<2 * n + 1>{}); }
        if constexpr (n >= 12 && n < 32 && n % 4 == 0) load_b5(IC<(n - 12) / 4>{});
        if constexpr (n >= 42 && n < 75 && n % 3 == 0) load_b5(IC<5 + (n - 42) / 3>{});
        if constexpr (n >= 84 && n % 2 == 0) load_h(IC<(n - 84) / 2>{}, hA, 0);
        if constexpr (n == 38) { l5 = opq(vl16) >> 4; c5 = l5 & 15; g5 = l5 >> 4; PIN(c5); PIN(g5); }
        if constexpr (n == 40) { vRow5 = (32 * (w >> 2) + 4 * (w & 3) + g5) * 1024 + c5 * 8; PIN(vRow5); }
        if constexpr (n == 42) { pr5 = (4 * w + g5) * 256 + ((c5 ^ ((4 * w + g5) & 7)) << 4); PIN(pr5); }
        if constexpr (n == 44) { pw5 = w * 8192 + c5 * 256 + ((c5 & 4) << 4); PIN(pw5); }
        if constexpr (n == 46) { pw5 += (g5 ^ (c5 & 3)) << 4; PIN(pw5); gb5 = L_GAB + 256 + c5 * 16; PIN(gb5); }
        SCHED();
      });
      a0 = n0; a1 = n1;
      SCHED();
    });
  }
  STAMP(18); KSTAMP(4, 16);
  PIN(accB[0][0]); PIN(accB[0][1]); PIN(accB[1][0]); PIN(accB[1][1]); PIN(accB[2][0]); PIN(accB[2][1]);
  sfor<6, 9>([&](auto I) { load_h(I, hA, 0); });
  swiglu_tile(accB, 0, 1);
  sfor<9, 12>([&](auto I) { load_h(I, hA, 0); });
  swiglu_tile(accB, 1, 1);
  swiglu_tile(accB, 2, 1);
  VMW(0);
  BAR();
  RAISE(flag_off(2, g, 1) + j * 128);
  STAMP(19);
#pragma unroll
  for (int i = 0; i < 12; ++i) PIN(hA[i]);
#pragma unroll
  for (int i = 0; i < 16; ++i) PIN(b5[i]);
  sfor<16, 20>(load_b5);
  auto p5_half = [&](auto HC, v4u (&hf)[12]) {
    constexpr int h = decltype(HC)::value;
    f32x4 acc[2][4] = {{z4, z4, z4, z4}, {z4, z4, z4, z4}};
    sfor<0, 6>([&](auto SC) {
      constexpr int sx = decltype(SC)::value;
      if constexpr (h == 0 && sx == 1) { sfor<20, 24>(load_b5);
        const u32 sq = WS_X1 + g * 65536 + j * 128, sq1 = sq + 16384, vr5 = vRow5; const rsrc_t rr = rWS; LD8S(xqA, vr5, rr, sq, 0); LD8S(xqB, vr5, rr, sq1, 0); }
      if constexpr (h == 0 && sx == 4) {
        poll5(1);
#pragma unroll
        for (int i = 16; i < 24; ++i) PIN(b5[i]);
        PIN(xqA); PIN(xqB);
      }
      if constexpr (h == 1 && sx == 0) asm volatile("s_waitcnt vmcnt(9)" : "+v"(hf[0]), "+v"(hf[1]), "+v"(hf[2]), "+v"(hf[3])::"memory");
      if constexpr (h == 1 && sx == 2) asm volatile("s_waitcnt vmcnt(5)" : "+v"(hf[4]), "+v"(hf[5]), "+v"(hf[6]), "+v"(hf[7])::"memory");
      if constexpr (h == 1 && sx == 4) asm volatile("s_waitcnt vmcnt(1)" : "+v"(hf[8]), "+v"(hf[9]), "+v"(hf[10]), "+v"(hf[11])::"memory");
      if constexpr (sx == 5) { if (w >= 3) { hf[10] = v4u{0u, 0u, 0u, 0u}; hf[11] = v4u{0u, 0u, 0u, 0u}; } }
#pragma unroll
      for (int m = 0; m < 2; ++m) {
#pragma unroll
        for (int n = 0; n < 4; ++n) acc[m][n] = mfma32(b5[sx * 4 + n], hf[2 * sx + m], acc[m][n]);
        if constexpr (h == 0 && sx >= 4) { if (m == 0) { load_h(IC<4 * (sx - 4)>{}, hB, 1); load_h(IC<4 * (sx - 4) + 1>{}, hB, 1); } else { load_h(IC<4 * (sx - 4) + 2>{}, hB, 1); load_h(IC<4 * (sx - 4) + 3>{}, hB, 1); } }
        SCHED();
      }
    });
    const int pwh = h ? opq(pw5 + 65536) : pw5;
#pragma unroll
    for (int m = 0; m < 2; ++m)
#pragma unroll
      for (int n = 0; n < 4; ++n) *(LAS f32x4*)(L + ((n & 1) ? (pwh ^ 64) : pwh) + (m * 4096 + (n >> 1) * 128)) = acc[m][n];
    if constexpr (h == 0) sfor<8, 12>([&](auto I) { load_h(I, hB, 1); });
    BAR();
    const int prh = h ? opq(pr5 + 65536) : pr5;
    f32x4 y = *(LAS f32x4*)(L + prh);
#pragma unroll
    for (int q = 1; q < 8; ++q) y += *(LAS f32x4*)(L + prh + q * 8192);
    const f32x4 gbv = *(LAS f32x4*)(L + gb5);
    const v2u xx = h ? xqB : xqA;
    const f32x4 xb = {blo(xx.x), bhi(xx.x), blo(xx.y), bhi(xx.y)};
    __builtin_amdgcn_raw_buffer_store_b64(pk4(xb + gbv * y), rOUT, vRow5, g * 65536 + h * 16384 + j * 128, 2);
  };
  STAMP(20);
  p5_half(IC<0>{}, hA);
  STAMP(21);
  p5_half(IC<1>{}, hB);
  STAMP(22);
  STAMP_DUMP();
}
}

extern "C" void kernel_launch(void* const* d_in, const int* in_sizes, int n_in, void* d_out, int out_size, void* d_ws, size_t ws_size,
                              hipStream_t stream) {
  Params p;
  p.x = (const u16*)d_in[0]; p.wqkv = (const u16*)d_in[1]; p.wo = (const u16*)d_in[2];
  p.w1 = (const u16*)d_in[4]; p.w3 = (const u16*)d_in[5]; p.w2 = (const u16*)d_in[6];
  p.c = (const u16*)d_in[7]; p.wmod = (const u16*)d_in[8];
  p.out = (u16*)d_out; p.ws = (char*)d_ws;
  (void)hipFuncSetAttribute((const void*)fused_block, hipFuncAttributeMaxDynamicSharedMemorySize, L_TOTAL);
  fused_block<<<256, 512, L_TOTAL, stream>>>(p);
}
```

```cpp
#include <hip/hip_runtime.h>
#include <stdint.h>

#define KDBG 0
#define NODMA 0
#define CODEWARM 0
#define L2WARM 1
#define EARLYT 0
#define XMAP 2

#define DI __device__ __forceinline__
#define GAS __attribute__((address_space(1)))
#define LAS __attribute__((address_space(3)))
typedef unsigned u32;
typedef unsigned short u16;
typedef unsigned long long u64;
typedef u32 v4u __attribute__((ext_vector_type(4)));
typedef u32 v2u __attribute__((ext_vector_type(2)));
typedef float f32x4 __attribute__((ext_vector_type(4)));
typedef float f32x2 __attribute__((ext_vector_type(2)));
typedef __bf16 bf16x8 __attribute__((ext_vector_type(8)));
typedef __bf16 bf16x2 __attribute__((ext_vector_type(2)));
typedef short s16x4 __attribute__((ext_vector_type(4)));
typedef __amdgpu_buffer_rsrc_t rsrc_t;
typedef GAS u32 gu32;
typedef GAS u64 gu64;
#define RLX_AGENT __ATOMIC_RELAXED, __HIP_MEMORY_SCOPE_AGENT

namespace {
constexpr int S_ = 2048, D_ = 512, F_ = 1376;
constexpr u32 WS_EPOCH = 0;
constexpr u32 WS_FLAG = 32768;
constexpr u32 WS_STAT = 262144;
constexpr u32 WS_MODG = 524288;
constexpr u32 WS_MODF = 655360;
constexpr u32 WS_O = 1u << 20;
constexpr u32 WS_X1 = 3u << 20;
constexpr u32 WS_HH = 5u << 20;
constexpr u32 WS_RFLAG = 8192;
constexpr u32 WS_B4T = 11u << 20;
constexpr u32 WS_B5T = 14u << 20;
constexpr u32 WS_B3T = (15u << 20) + (512u << 10);
constexpr u32 WS_DBG = 16u << 20;
constexpr u32 WS_BYTES = 32u << 20;
constexpr int L_XY = 0;
constexpr int L_Z = 81920;
constexpr int L_MISC = 158720;
constexpr int L_TOTAL = 163840;
constexpr int L_RED = 73728;
constexpr int L_MODA = L_MISC;
constexpr int L_CNT = L_MISC + 4096;
constexpr int L_GAB = L_MISC + 4096 + 64;
constexpr int L_JUNK = L_MISC + 4096 + 64 + 512;
constexpr int L_STAMP = 156672;
constexpr u32 SPIN_CAP = 1u << 18;
constexpr u32 CODE_BYTES = 26112;

DI f32x4 mfma32(v4u a, v4u b, f32x4 c) {
  return __builtin_amdgcn_mfma_f32_16x16x32_bf16(__builtin_bit_cast(bf16x8, a), __builtin_bit_cast(bf16x8, b), c, 0, 0, 0);
}
DI f32x4 mfma16(v2u a, v2u b, f32x4 c) {
  return __builtin_amdgcn_mfma_f32_16x16x16bf16_1k(__builtin_bit_cast(s16x4, a), __builtin_bit_cast(s16x4, b), c, 0, 0, 0);
}
DI u32 pk(float lo, float hi) { f32x2 v = {lo, hi}; return __builtin_bit_cast(u32, __builtin_convertvector(v, bf16x2)); }
DI u32 pk2(f32x2 v) { return __builtin_bit_cast(u32, __builtin_convertvector(v, bf16x2)); }
DI v2u pk4(f32x4 v) { v2u r; r.x = pk(v.x, v.y); r.y = pk(v.z, v.w); return r; }
DI float asf(u32 u) { return __builtin_bit_cast(float, u); }
DI u32 asu(float f) { return __builtin_bit_cast(u32, f); }
DI float blo(u32 u) { return asf(u << 16); }
DI float bhi(u32 u) { return asf(u & 0xffff0000u); }
DI f32x2 unp(u32 u) { f32x2 r = {blo(u), bhi(u)}; return r; }
DI float dot2(u32 a, u32 b, float c) { return __builtin_amdgcn_fdot2_f32_bf16(__builtin_bit_cast(bf16x2, a), __builtin_bit_cast(bf16x2, b), c, false); }
DI v2u trd(LAS char* p) { return __builtin_bit_cast(v2u, __builtin_amdgcn_ds_read_tr16_b64_v4i16((LAS s16x4*)p)); }
DI v4u cat(v2u a, v2u b) { v4u r; r.x = a.x; r.y = a.y; r.z = b.x; r.w = b.y; return r; }
template <int MI, int SI> DI void dma(u32 mb, u32 voff, rsrc_t srd, u32 sb) {
  u32 t;
  asm volatile("s_add_u32 m0, %1, %c2\n\ts_add_u32 %0, %3, %c4\n\tbuffer_load_dwordx4 %5, %6, %0 offen lds"
               : "=&s"(t) : "s"(mb), "i"(MI), "s"(sb), "i"(SI), "v"(voff), "s"(srd) : "scc");
}
DI void touch(u32 voff, rsrc_t srd, u32 sb) {
  asm volatile("s_mov_b32 m0, %c0\n\ts_nop 0\n\tbuffer_load_dword %1, %2, %3 offen lds" ::"i"(L_JUNK), "v"(voff), "s"(srd), "s"(sb));
}
#define BAR() asm volatile("s_waitcnt lgkmcnt(0)\n\ts_barrier" ::: "memory")
#define VMW(n) asm volatile("s_waitcnt vmcnt(" #n ")" ::: "memory")
#define LGW() asm volatile("s_waitcnt lgkmcnt(0)" ::: "memory")
#define SCHED() __builtin_amdgcn_sched_barrier(0)
#define LD16(dst, voff, srd, soff, IMM) asm volatile("buffer_load_dwordx4 %0, %1, %2, %3 offen offset:" #IMM : "=v"(dst) : "v"(voff), "s"(srd), "s"(soff))
#define LD16S(dst, voff, srd, soff, IMM) asm volatile("buffer_load_dwordx4 %0, %1, %2, %3 offen offset:" #IMM " sc1" : "=v"(dst) : "v"(voff), "s"(srd), "s"(soff))
#define LD8(dst, voff, srd, soff, IMM) asm volatile("buffer_load_dwordx2 %0, %1, %2, %3 offen offset:" #IMM : "=v"(dst) : "v"(voff), "s"(srd), "s"(soff))
#define LD8S(dst, voff, srd, soff, IMM) asm volatile("buffer_load_dwordx2 %0, %1, %2, %3 offen offset:" #IMM " sc1" : "=v"(dst) : "v"(voff), "s"(srd), "s"(soff))
#define LD4(dst, voff, srd, soff) asm volatile("buffer_load_dword %0, %1, %2, %3 offen" : "=v"(dst) : "v"(voff), "s"(srd), "s"(soff))
#define LD4S(dst, voff, srd, soff) asm volatile("buffer_load_dword %0, %1, %2, %3 offen sc1" : "=v"(dst) : "v"(voff), "s"(srd), "s"(soff))

template <int IMM> DI void ld16s(v4u& r, u32 v, rsrc_t rs, u32 so) { asm volatile("buffer_load_dwordx4 %0, %1, %2, %3 offen offset:%c4 sc1" : "=v"(r) : "v"(v), "s"(rs), "s"(so), "i"(IMM)); }
template <int CTRL> DI float dppf(float v) { return asf(__builtin_amdgcn_update_dpp(0, asu(v), CTRL, 0xf, 0xf, true)); }
DI float row16_sum(float v) { v += dppf<0x128>(v); v += dppf<0x124>(v); v += dppf<0x122>(v); v += dppf<0x121>(v); return v; }
DI float sw32_add(float a, float b) { auto r = __builtin_amdgcn_permlane32_swap(asu(a), asu(b), false, false); return asf(r[0]) + asf(r[1]); }
DI float sw16_add(float a, float b) { auto r = __builtin_amdgcn_permlane16_swap(asu(a), asu(b), false, false); return asf(r[0]) + asf(r[1]); }
DI float x16_max(float v) { auto r = __builtin_amdgcn_permlane16_swap(asu(v), asu(v), false, false); return fmaxf(asf(r[0]), asf(r[1])); }
DI float x32_max(float v) { auto r = __builtin_amdgcn_permlane32_swap(asu(v), asu(v), false, false); return fmaxf(asf(r[0]), asf(r[1])); }
DI int fresh_lane() { int l; asm volatile("v_mbcnt_lo_u32_b32 %0, -1, 0\n\tv_mbcnt_hi_u32_b32 %0, -1, %0" : "=v"(l)); return l; }
DI float rdl(float v, int l) { return asf(__builtin_amdgcn_readlane(asu(v), l)); }

struct Params {
  const u16 *x, *wqkv, *wo, *w1, *w3, *w2, *c, *wmod;
  u16* out;
  char* ws;
};

#define LOOPSEL 0
#define STAMP(i) do {} while (0)
#define KSTAMP(lp, i) do {} while (0)
#define STAMP_DUMP() do {} while (0)

DI u32 flag_off(int phase, int g, int h) { return WS_FLAG + ((phase * 32 + g) * 2 + h) * 1024; }
DI void poll_flags(rsrc_t rWS, u32 vfl, u32 foff, u64 care, u32 tag) {
  for (u32 sp = 0; sp < SPIN_CAP; ++sp) {
    asm volatile("" ::: "memory");
    const u32 v = __builtin_amdgcn_raw_buffer_load_b32(rWS, vfl, foff, 16);
    if (!(__builtin_amdgcn_ballot_w64(v != tag) & care)) break;
  }
  VMW(0);
}
DI void raise_flag(rsrc_t rWS, u32 foff, u32 vtag, u32 vzero, u32 m1) {
  asm volatile("s_mov_b32 exec_hi, 0\n\ts_mov_b32 exec_lo, %4\n\tbuffer_store_dword %0, %1, %2, %3 offen sc1\n\ts_mov_b64 exec, -1" ::"v"(vtag), "v"(vzero), "s"(rWS), "s"(foff), "s"(m1) : "memory");
}
DI void signal_flag(rsrc_t rWS, u32 cnt_addr, u32 foff, u32 tag) {
  u32 t, a = cnt_addr, one = 1u, vt = tag, vz = 0u;
  asm volatile("s_mov_b64 exec, 1\n\tds_add_rtn_u32 %0, %1, %2\n\ts_waitcnt lgkmcnt(0)\n\tv_cmp_eq_u32 vcc, 7, %0\n\ts_and_b64 exec, exec, vcc\n\t"
               "buffer_store_dword %3, %4, %5, %6 offen sc1\n\ts_mov_b64 exec, -1"
               : "=&v"(t) : "v"(a), "v"(one), "v"(vt), "v"(vz), "s"(rWS), "s"(foff) : "vcc", "scc", "memory");
}
DI void arrive(u32 cnt_addr) {
  u32 a = cnt_addr, one = 1u;
  asm volatile("s_waitcnt lgkmcnt(0)\n\ts_mov_b64 exec, 1\n\tds_add_u32 %0, %1\n\ts_mov_b64 exec, -1" ::"v"(a), "v"(one) : "memory");
}
DI void await8(u32 cnt_addr) {
  u32 a = cnt_addr, t, sv;
  asm volatile("1:\n\tds_read_b32 %0, %2\n\ts_waitcnt lgkmcnt(0)\n\tv_readfirstlane_b32 %1, %0\n\ts_nop 0\n\ts_cmp_lt_u32 %1, 8\n\ts_cbranch_scc1 1b"
               : "=&v"(t), "=&s"(sv) : "v"(a) : "scc", "memory");
}
DI void raise_when8(rsrc_t rWS, u32 cnt_addr, u32 foff, u32 tag, u32 w7) {
  u32 a = cnt_addr, t, sv, vt = tag, vz = 0u;
  asm volatile("s_cmp_eq_u32 %7, 0\n\ts_cbranch_scc1 2f\n\t1:\n\tds_read_b32 %0, %2\n\ts_waitcnt lgkmcnt(0)\n\tv_readfirstlane_b32 %1, %0\n\ts_nop 0\n\ts_cmp_lt_u32 %1, 8\n\ts_cbranch_scc1 1b\n\t"
               "s_mov_b64 exec, 1\n\tbuffer_store_dword %3, %4, %5, %6 offen sc1\n\ts_mov_b64 exec, -1\n\t2:"
               : "=&v"(t), "=&s"(sv) : "v"(a), "v"(vt), "v"(vz), "s"(rWS), "s"(foff), "s"(w7) : "scc", "memory");
}
#define PIN(x) asm volatile("" : "+v"(x))
DI float qp(float r) { PIN(r); return r; }
DI float q_shl(u32 a) { return qp(blo(a)); }
DI float q_hi(u32 a) { return qp(bhi(a)); }
DI float q_fma(float a, float b, float c) { return qp(__builtin_fmaf(a, b, c)); }
DI float q_fmas(float a, float b, float c) { return qp(__builtin_fmaf(a, b, c)); }
DI float q_mul(float a, float b) { return qp(a * b); }
DI float q_mulk(float a) { return qp(a * -1.4426950408889634f); }
DI float q_add1(float a) { return qp(a + 1.f); }
DI float q_exp(float a) { return qp(__builtin_amdgcn_exp2f(a)); }
DI float q_rcp(float a) { return qp(__builtin_amdgcn_rcpf(a)); }
DI u32 q_cvt(float a, float b) { u32 r = pk(a, b); PIN(r); return r; }
DI float q_mov(float a) { return qp(a); }
DI u32 opq(u32 v) { asm volatile("" : "+v"(v)); return v; }

DI float sel(u64 m, float a, float b) { return __builtin_amdgcn_inverse_ballot_w64(m) ? a : b; }
DI v2u attend(v2u kp, v2u vp, v2u kc, v2u vc, v2u q, u64 m0, u64 m1, u32 hm, bool noprev) {
  const f32x4 z = {0.f, 0.f, 0.f, 0.f};
  const f32x4 sp = mfma16(kp, q, z);
  const f32x4 sc = mfma16(kc, q, z);
  const float K2 = 0.25f * 1.4426950408889634f;
  const u64 par = 0xaaaaaaaaaaaaaaaaull;
  float p0 = sel(par, sp[1], sp[0]), p1 = sel(par, sp[3], sp[2]);
  if (noprev) { p0 = -INFINITY; p1 = -INFINITY; }
  const float t0 = sel(m0, sel(par, sc[1], sc[0]), p0), t1 = sel(m1, sel(par, sc[3], sc[2]), p1);
  const float m = x32_max(x16_max(fmaxf(t0, t1)));
  const float mk = -m * K2;
  const float e0 = __builtin_amdgcn_exp2f(__builtin_fmaf(t0, K2, mk)), e1 = __builtin_amdgcn_exp2f(__builtin_fmaf(t1, K2, mk));
  const u32 w0 = pk(e0, e0) & hm, w1 = pk(e1, e1) & hm;
  v4u pf;
  pf.x = __builtin_amdgcn_inverse_ballot_w64(m0) ? 0u : w0; pf.y = __builtin_amdgcn_inverse_ballot_w64(m1) ? 0u : w1;
  pf.z = __builtin_amdgcn_inverse_ballot_w64(m0) ? w0 : 0u; pf.w = __builtin_amdgcn_inverse_ballot_w64(m1) ? w1 : 0u;
  const f32x4 o = mfma32(cat(vp, vc), pf, z);
  float sum = e0 + e1;
  sum = sw16_add(sum, sum); sum = sw32_add(sum, sum);
  return pk4(o * __builtin_amdgcn_rcpf(sum));
}

template <int N> using IC = std::integral_constant<int, N>;
template <int I, int N, class F> DI void sfor(F&& f) { if constexpr (I < N) { f(IC<I>{}); sfor<I + 1, N>(f); } }

__global__ void __launch_bounds__(512, 2) fused_block(Params p) {
  extern __shared__ __attribute__((aligned(16))) char smem[];
  LAS char* L = (LAS char*)smem;
  const int tid = threadIdx.x, lane = tid & 63, w = __builtin_amdgcn_readfirstlane(tid >> 6);
  const int c16 = lane & 15, g4 = lane >> 4;
  const int b = blockIdx.x;
  int g, j;
  if (XMAP == 0) { g = (b & 7) * 4 + ((b >> 3) & 3); j = b >> 5; }
  else if (XMAP == 2) { const int x = b & 7, r = b >> 3; g = (x >> 1) * 8 + (r & 7); j = (x & 1) * 4 + (r >> 3); }
  else { const int x = b & 7, r = b >> 3; g = (x >> 2) * 16 + (r & 15); j = (x & 3) * 2 + (r >> 4); }
  const rsrc_t rX = __builtin_amdgcn_make_buffer_rsrc((void*)p.x, 0, S_ * D_ * 2, 0x00020000);
  const rsrc_t rQKV = __builtin_amdgcn_make_buffer_rsrc((void*)p.wqkv, 0, D_ * 3 * D_ * 2, 0x00020000);
  const rsrc_t rWO = __builtin_amdgcn_make_buffer_rsrc((void*)p.wo, 0, D_ * D_ * 2, 0x00020000);
  const rsrc_t rW1 = __builtin_amdgcn_make_buffer_rsrc((void*)p.w1, 0, D_ * F_ * 2, 0x00020000);
  const rsrc_t rW3 = __builtin_amdgcn_make_buffer_rsrc((void*)p.w3, 0, D_ * F_ * 2, 0x00020000);
  const rsrc_t rW2 = __builtin_amdgcn_make_buffer_rsrc((void*)p.w2, 0, F_ * D_ * 2, 0x00020000);
  const rsrc_t rC = __builtin_amdgcn_make_buffer_rsrc((void*)p.c, 0, D_ * 2, 0x00020000);
  const rsrc_t rMOD = __builtin_amdgcn_make_buffer_rsrc((void*)p.wmod, 0, D_ * 6 * D_ * 2, 0x00020000);
  const rsrc_t rWS = __builtin_amdgcn_make_buffer_rsrc((void*)p.ws, 0, WS_BYTES, 0x00020000);
  const rsrc_t rOUT = __builtin_amdgcn_make_buffer_rsrc((void*)p.out, 0, S_ * D_ * 2, 0x00020000);
  STAMP(0);

  u32 ep;
  { const u32 vz = 0; asm volatile("buffer_load_dword %0, %1, %2, 0 offen sc1" : "=v"(ep) : "v"(vz), "s"(rWS)); }
  u32 vl16 = lane * 16; PIN(vl16);
  const int bm = b < 192 ? b : b - 192, mq = bm / 48, cl = bm - 48 * mq;
  v4u wm[4]; v2u cv; u32 ep0;
  if (w < 4) {
    const u32 vm = (tid >> 3) * 24576 + (tid & 7) * 16, s0 = mq * (128 * 6144) + cl * 128, s1 = s0 + 6144, s2 = s0 + 12288, s3 = s0 + 18432;
    LD16(wm[0], vm, rMOD, s0, 0); LD16(wm[1], vm, rMOD, s1, 0); LD16(wm[2], vm, rMOD, s2, 0); LD16(wm[3], vm, rMOD, s3, 0);
    const u32 vc = (tid >> 3) * 8, scq = mq * 256;
    LD8(cv, vc, rC, scq, 0);
    const u32 vz = 0; asm volatile("buffer_load_dword %0, %1, %2, 0 offen sc1" : "=v"(ep0) : "v"(vz), "s"(rWS));
  }
  if (tid < 16) *(LAS u32*)(L + L_CNT + tid * 4) = 0u;
  BAR();
  const int st_qd = lane >> 2, st_r = (st_qd & 3) + 4 * (st_qd >> 3);
  const int st_col = ((st_qd >> 2) & 1) * 64 + (((lane & 3) ^ ((w & 1) << 1)) << 4);
  const int rotp = g & 7;
  u32 vB1 = st_r * 3072 + st_col; PIN(vB1);
  const u32 mB1 = (w >> 2) * 12288 + (w & 3) * 1024, sB1 = (w >> 2) * 98304 + (w & 3) * 24576 + j * 128;
  auto stage_b1 = [&](auto PC, auto BC) {
    constexpr int pair = decltype(PC)::value, base = decltype(BC)::value;
    const u32 sp = ((pair + rotp) & 7) * 196608 + sB1;
    dma<base, 0>(mB1, vB1, rQKV, sp); dma<base + 4096, 1024>(mB1, vB1, rQKV, sp); dma<base + 8192, 2048>(mB1, vB1, rQKV, sp);
  };
  auto b1_piece = [&](auto PC, auto BC, auto TC) {
    constexpr int pair = decltype(PC)::value, base = decltype(BC)::value, ty = decltype(TC)::value;
    dma<base + ty * 4096, ty * 1024>(mB1, vB1, rQKV, ((pair + rotp) & 7) * 196608 + sB1);
  };
  const int hd = w & 3, blk2 = w >> 2, kq = w & 3, mh = w >> 2;
  const int cs0 = j < 4 ? 22 * j : 88 + 21 * (j - 4);
  const int cend = cs0 + (j < 4 ? 22 : 21);
  STAMP(1);
  const int T0 = 12 * (g >> 4) + 3 * (w & 3), rcb = min(cs0 + T0, 168);
  if (w < 4) {
    asm volatile("s_waitcnt vmcnt(0)" : "+v"(cv), "+v"(wm[0]), "+v"(wm[1]), "+v"(wm[2]), "+v"(wm[3]), "+v"(ep0)::"memory");
    const u32 tg = __builtin_amdgcn_readfirstlane(ep0) + 1u;
    const f32x4 cf = {blo(cv.x), bhi(cv.x), blo(cv.y), bhi(cv.y)};
    float sc[4];
#pragma unroll
    for (int r = 0; r < 4; ++r) sc[r] = cf[r] * __builtin_amdgcn_rcpf(1.f + __builtin_amdgcn_exp2f(cf[r] * -1.4426950408889634f));
    f32x2 v[4];
#pragma unroll
    for (int i = 0; i < 4; ++i) v[i] = (unp(wm[0][i]) * sc[0] + unp(wm[1][i]) * sc[1]) + (unp(wm[2][i]) * sc[2] + unp(wm[3][i]) * sc[3]);
    const float a0 = sw32_add(v[0].x, v[2].x), a1 = sw32_add(v[0].y, v[2].y), a2 = sw32_add(v[1].x, v[3].x), a3 = sw32_add(v[1].y, v[3].y);
    float t0 = sw16_add(a0, a2), t1 = sw16_add(a1, a3);
    t0 += dppf<0x128>(t0); t1 += dppf<0x128>(t1);
    f32x2 o = {t0, t1};
    *(LAS f32x2*)(L + L_RED + w * 256 + (lane & 7) * 32 + g4 * 8) = o;
    LGW();
    u32 old = 0;
    if (lane == 0) old = __hip_atomic_fetch_add((LAS u32*)(L + L_CNT + 28), 1u, __ATOMIC_RELAXED, __HIP_MEMORY_SCOPE_WORKGROUP);
    if (__builtin_amdgcn_readfirstlane(old) == 3u) {
      asm volatile("" ::: "memory");
      float s = 0.f;
#pragma unroll
      for (int i = 0; i < 4; ++i) s += *(LAS float*)(L + L_RED + i * 256 + lane * 4);
      v2u gr; gr.x = asu(s); gr.y = tg;
      if (b < 192) __builtin_amdgcn_raw_buffer_store_b64(gr, rWS, lane * 8, WS_MODG + (mq * 3072 + cl * 64) * 8, 16);
    }
  }
  STAMP(2);
  v4u xr[10];
  {
    const int r0 = 64 * g - 16 + w;
    const u32 s0 = (u32)max(r0, 0) * 1024u, s1 = (u32)max(r0 + 8, 0) * 1024u;
    LD16(xr[0], vl16, rX, s0, 0); LD16(xr[1], vl16, rX, s1, 0);
#pragma unroll
    for (int i = 2; i < 10; ++i) { const u32 so = (u32)(r0 + 8 * i) * 1024u; LD16(xr[i], vl16, rX, so, 0); }
  }
  stage_b1(IC<0>{}, IC<L_XY>{});
  int afo[4];
#pragma unroll
  for (int i = 0; i < 4; ++i) { afo[i] = blk2 * 32768 + c16 * 1024 + (((4 * i + g4) ^ c16) << 4); PIN(afo[i]); }
  const u64 am0 = __builtin_amdgcn_ballot_w64((c16 & 14) >= 4 * g4), am1 = __builtin_amdgcn_ballot_w64((c16 & 14) >= 4 * g4 + 2);
  u32 ahm = (lane & 1) ? 0xffff0000u : 0xffffu; PIN(ahm);
  u32 vOst = c16 * 16 + g4 * 256 + (hd & 1) * 8; PIN(vOst);
  int tB1 = g4 * 1024 + 64 * (c16 >> 2) + 8 * (lane & 3) + 32 * ((hd & 1) ^ (g4 & 1)) + 256 * (hd >> 1); PIN(tB1);
  int hw0 = w * 1024 + ((((lane - 8 * rotp) & 63) ^ w) << 4); PIN(hw0);
#define vfl ((opq(vl16) & 112u) << 3)
  STAMP(3);
  v4u bw[48];
  auto read_b1 = [&](int pair, int base, int off) {
#pragma unroll
    for (int kk = 0; kk < 2; ++kk)
#pragma unroll
      for (int ty = 0; ty < 3; ++ty) {
        LAS char* a = L + base + (off + kk * 12288 + ty * 4096);
        bw[(2 * pair + kk) * 3 + ty] = cat(trd(a), trd(a + 512));
      }
  };
  f32x2 xn[10][4];
  v4u ga_[4]; v2u pv;
  const u32 sMG = WS_MODG + w * 1024;
  {
    float u[10];
    sfor<0, 10>([&](auto I) {
      constexpr int i = decltype(I)::value;
      if constexpr (i == 0) asm volatile("s_waitcnt vmcnt(11)" : "+v"(xr[0]), "+v"(xr[1]), "+v"(ep)::"memory");
      if constexpr (i == 2) asm volatile("s_waitcnt vmcnt(9)" : "+v"(xr[2]), "+v"(xr[3])::"memory");
      if constexpr (i == 4) asm volatile("s_waitcnt vmcnt(10)" : "+v"(xr[4]), "+v"(xr[5])::"memory");
      if constexpr (i == 6) asm volatile("s_waitcnt vmcnt(8)" : "+v"(xr[6]), "+v"(xr[7])::"memory");
      if constexpr (i == 8) asm volatile("s_waitcnt vmcnt(9)" : "+v"(xr[8]), "+v"(xr[9])::"memory");
      float s = 0.f, q = 0.f;
#pragma unroll
      for (int e = 0; e < 4; ++e) { s = dot2(xr[i][e], 0x3f803f80u, s); q = dot2(xr[i][e], xr[i][e], q); }
      u[i] = sw32_add(s, q);
      PIN(u[i]);
      if constexpr (i == 3) stage_b1(IC<1>{}, IC<L_XY + 24576>{});
      if constexpr (i == 7) stage_b1(IC<2>{}, IC<L_XY + 49152>{});
      if constexpr (i == 9) {
        const u32 s0 = sMG, s1 = sMG + 24576, s2 = sMG + 49152, s3 = sMG + 73728;
        ld16s<0>(ga_[0], vl16, rWS, s0); ld16s<0>(ga_[1], vl16, rWS, s1); ld16s<0>(ga_[2], vl16, rWS, s2); ld16s<0>(ga_[3], vl16, rWS, s3);
      }
    });
    float t[5];
#pragma unroll
    for (int i = 0; i < 5; ++i) t[i] = row16_sum(sw16_add(u[i], u[i + 5]));
    float v = t[0];
#pragma unroll
    for (int i = 1; i < 5; ++i) v = c16 == i ? t[i] : v;
    auto r = __builtin_amdgcn_permlane32_swap(asu(v), asu(v), false, false);
    const float mean = asf(r[0]) * (1.f / 512.f);
    const float var = fmaxf(asf(r[1]) * (1.f / 512.f) - mean * mean, 0.f);
    float rs = __builtin_amdgcn_rsqf(var + 1e-5f), nm = -mean * rs;
    PIN(rs); PIN(nm);
    STAMP(4);
    { const u32 vo = ((lane >> 3) & 3) * 24576 + (lane & 7) * 8, so = WS_MODG + (1024 + 8 * b) * 8; LD8S(pv, vo, rWS, so, 0); }
    if (w >= 4) {
      const int u = lane >> 2, row = (u & 3) | ((u >> 1) & 4) | ((u << 1) & 8);
      const u32 vo = row * 2752 + (lane & 3) * 16, so = (g & 15) * (32 * 2752) + rcb * 16, mb = L_Z + 49152 + (w & 3) * 4096;
      dma<0, 0>(mb, vo, rW1, so); dma<1024, 16 * 2752>(mb, vo, rW1, so); dma<2048, 0>(mb, vo, rW3, so); dma<3072, 16 * 2752>(mb, vo, rW3, so);
    } else {
      const int u = lane >> 1, row = (u & 3) | ((u >> 1) & 4) | ((u << 1) & 8) | (u & 16);
      const u32 vo = row * 1024 + (lane & 1) * 16, so = g * 32768 + j * 128 + w * 32, mb = L_Z + 65536 + w * 2048;
      dma<0, 0>(mb, vo, rW2, so); dma<1024, 0>(mb, vo, rW2, so + (g < 11 ? 32 * 32768 : 0));
      const int rwo = (u & 7) | ((u >> 1) & 8) | ((u << 1) & 16);
      dma<0, 0>(76800 + w * 1024, rwo * 1024 + (lane & 1) * 16, rWO, (g & 15) * 32768 + j * 128 + w * 32);
    }
    sfor<0, 10>([&](auto I) {
      constexpr int i = decltype(I)::value;
      if constexpr (i == 2) stage_b1(IC<3>{}, IC<L_Z>{});
      if constexpr (i == 6) stage_b1(IC<4>{}, IC<L_Z + 24576>{});
      const float a = rdl(rs, (i / 5) * 16 + i % 5), c = rdl(nm, (i / 5) * 16 + i % 5);
#pragma unroll
      for (int e = 0; e < 4; ++e) { xn[i][e] = unp(xr[i][e]) * a + c; PIN(xn[i][e]); }
    });
  }
  const u32 tag = __builtin_amdgcn_readfirstlane(ep) + 1u;
  u32 m1; asm("s_lshr_b32 %0, 1, %1" : "=s"(m1) : "s"(w) : "scc");
#define RAISE(fo) raise_flag(rWS, (fo), tag, vl16, m1)
#define SIGNAL(idx, fo) signal_flag(rWS, L_CNT + 4 * (idx), (fo), tag)
  STAMP(5);
  asm volatile("s_waitcnt vmcnt(9)" : "+v"(ga_[0]), "+v"(ga_[1]), "+v"(ga_[2]), "+v"(ga_[3]), "+v"(pv)::"memory");
  auto gsum = [&](v4u (&a)[4], f32x2& o) -> u32 {
    o.x = (asf(a[0].x) + asf(a[1].x)) + (asf(a[2].x) + asf(a[3].x)); o.y = (asf(a[0].z) + asf(a[1].z)) + (asf(a[2].z) + asf(a[3].z));
    return ((a[0].y ^ tag) | (a[0].w ^ tag) | (a[1].y ^ tag) | (a[1].w ^ tag)) | ((a[2].y ^ tag) | (a[2].w ^ tag) | (a[3].y ^ tag) | (a[3].w ^ tag));
  };
  auto gather = [&](u32 voff, u32 soff, f32x2& o) {
    for (u32 sp = 0; sp < SPIN_CAP; ++sp) {
      asm volatile("" ::: "memory");
      v4u a[4];
#pragma unroll
      for (int q = 0; q < 4; ++q) a[q] = __builtin_amdgcn_raw_buffer_load_b128(rWS, voff, soff + q * 24576, 16);
      if (!__builtin_amdgcn_ballot_w64(gsum(a, o) != 0u)) break;
    }
  };
  const float one = w >= 4 ? 1.f : 0.f;
  {
    f32x2 mA;
    if (__builtin_amdgcn_ballot_w64(gsum(ga_, mA) != 0u)) gather(vl16, sMG, mA);
    *(LAS f32x2*)(L + L_MODA + w * 512 + (vl16 >> 1)) = mA + one;
  }
  BAR();
  if (w == 7) {
    const u32 vo = ((lane >> 3) & 3) * 24576 + (lane & 7) * 8, so = WS_MODG + (1024 + 8 * b) * 8;
    for (u32 sp = 0; sp < SPIN_CAP && __builtin_amdgcn_ballot_w64(pv.y != tag); ++sp) {
      asm volatile("" ::: "memory");
      pv = __builtin_amdgcn_raw_buffer_load_b64(rWS, vo, so, 16);
    }
    float sv = asf(pv.x);
    sv += dppf<0x128>(sv); sv = sw16_add(sv, sv);
    pv.x = asu(sv);
    __builtin_amdgcn_raw_buffer_store_b64(pv, rWS, (lane & 7) * 8, WS_MODF + (1024 + 8 * b) * 8, 16);
  }
  STAMP(6);
  int vma = L_MODA + 2 * vl16; PIN(vma);
  const f32x4 ms0 = *(LAS f32x4*)(L + vma), ms1 = *(LAS f32x4*)(L + vma + 16);
  const f32x4 ma0 = *(LAS f32x4*)(L + vma + 2048), ma1 = *(LAS f32x4*)(L + vma + 2064);
  int tz0 = tB1 + L_Z; PIN(tz0);
  { int t2 = tB1 + 49152; PIN(t2); read_b1(0, tB1, 0); read_b1(1, tB1, 24576); read_b1(2, t2, 0); }
  {
    VMW(6);
    if (w >= 4) {
      LAS char* a0 = L + L_Z + 49152 + (w & 3) * 4096 + ((lane >> 1) & 1) * 2048 + 64 * ((c16 >> 2) + 4 * (g4 & 1) + 16 * (g4 >> 1)) + 8 * (lane & 1);
#pragma unroll
      for (int ct = 0; ct < 3; ++ct) {
        const int c = min(cs0 + T0 + ct - rcb, 3) * 16;
        const v4u f = cat(trd(a0 + c), trd(a0 + c + 512));
        const u32 sf = WS_B4T + ((j * 16 + (g & 15)) * 24 + T0 + ct) * 1024;
        asm volatile("buffer_store_dwordx4 %0, %1, %2, %3 offen sc1\n\ts_nop 1" ::"v"(f), "v"(vl16), "s"(rWS), "s"(sf) : "memory");
      }
    }
    else {
      LAS char* a0 = L + L_Z + 65536 + w * 2048 + 32 * ((c16 >> 2) + 4 * (g4 & 1) + 16 * (g4 >> 1)) + 8 * (lane & 3);
#pragma unroll
      for (int i = 0; i < 2; ++i) {
        const v4u f = cat(trd(a0 + i * 1024), trd(a0 + i * 1024 + 256));
        const u32 sf = WS_B5T + ((j * 43 + g + (i && g < 11 ? 32 : 0)) * 4 + w) * 1024;
        asm volatile("buffer_store_dwordx4 %0, %1, %2, %3 offen sc1\n\ts_nop 1" ::"v"(f), "v"(vl16), "s"(rWS), "s"(sf) : "memory");
      }
      LAS char* a1 = L + 76800 + w * 1024 + 32 * ((c16 >> 2) + 4 * (g4 & 1) + 16 * (g4 >> 1)) + 8 * (lane & 3);
      const v4u f = cat(trd(a1), trd(a1 + 256));
      const u32 sf = WS_B3T + ((j * 16 + (g & 15)) * 4 + w) * 1024;
      asm volatile("buffer_store_dwordx4 %0, %1, %2, %3 offen sc1\n\ts_nop 1" ::"v"(f), "v"(vl16), "s"(rWS), "s"(sf) : "memory");
    }
  }
  BAR();
  STAMP(7);
  {
    int hw1 = (hw0 ^ 128) + 8192; PIN(hw1);
    sfor<0, 10>([&](auto I) {
      constexpr int i = decltype(I)::value;
      v4u o;
      o.x = pk2(xn[i][0] * ma0.xy + ms0.xy); o.y = pk2(xn[i][1] * ma0.zw + ms0.zw);
      o.z = pk2(xn[i][2] * ma1.xy + ms1.xy); o.w = pk2(xn[i][3] * ma1.zw + ms1.zw);
      if constexpr (i == 8) { hw0 += 65536; hw1 += 65536; }
      *(LAS v4u*)(L + ((i & 1) ? hw1 : hw0) + ((i >> 1) & 3) * 16384) = o;
      SCHED();
    });
  }
  VMW(0);
  BAR();
  RAISE(WS_RFLAG + (j * 32 + g) * 4);
  u32 flr;
  STAMP(8);

  const f32x4 z4 = {0.f, 0.f, 0.f, 0.f};
#define AFRAG(off, ks) (*(LAS v4u*)(L + afo[(ks) & 3] + ((off) + ((ks) >> 2) * 256)))
  const int ch0 = cs0 + 3 * w;
  constexpr int ND = NODMA ? 0 : 1;
  v4u b4[48];
  const u32 sB4 = WS_B4T + (j * 16 * 24 + 3 * w) * 1024;
  auto load_b4 = [&](auto NC) {
    constexpr int n = decltype(NC)::value, ks = n / 3, ct = n % 3;
    const u32 so = sB4 + ((ks + 2 * rotp) & 15) * 24576;
    v4u& r = b4[n];
    if constexpr (ct == 0) LD16S(r, vl16, rWS, so, 0); else if constexpr (ct == 1) LD16S(r, vl16, rWS, so, 1024); else LD16S(r, vl16, rWS, so, 2048);
  };
  auto landed_b4 = [&](auto AC, auto BC) {
#pragma unroll
    for (int i = decltype(AC)::value; i < decltype(BC)::value; ++i) { v4u& r = b4[i]; PIN(r); }
  };
  v4u b3[16];
  auto load_b3 = [&](auto NC) { constexpr int n = decltype(NC)::value; ld16s<(n & 3) * 1024>(b3[n], vl16, rWS, WS_B3T + (j * 16 + 4 * kq + (n >> 2)) * 4096); };
  v4u gb_, gg_;
  u32 vgg, fl0, vRow;
  int pw, pr;
  f32x4 aKh = z4, aVh = z4, aQ = z4, aK = z4, aV = z4, bQ = z4, bK = z4, bV = z4;
  {
    v4u ah = AFRAG(0, 0), a1 = AFRAG(16384, 0), a2 = AFRAG(32768, 0);
    int l3, c3, g3; f32x2 mB, mG;
    auto job = [&](auto NC) {
      constexpr int n = decltype(NC)::value;
      if constexpr (n == 3) { l3 = opq(vl16) >> 4; c3 = l3 & 15; g3 = l3 >> 4; PIN(c3); PIN(g3); vgg = (vl16 < 512 ? 8192 : 20480 - 512) + 512 * j + vl16; PIN(vgg); }
      if constexpr (n == 7) { vRow = (8 * w + (l3 >> 3)) * 1024 + (l3 & 7) * 16; PIN(vRow); }
      if constexpr (n == 8) { pr = (8 * w + (l3 >> 3)) * 256 + (((2 * (l3 & 7)) ^ ((l3 >> 3) & 7)) << 4); PIN(pr); }
      if constexpr (n == 10) { pw = kq * 16384 + (32 * mh + c3) * 256 + ((g3 ^ (c3 & 3)) << 4) + ((c3 & 4) << 4); PIN(pw); }
      if constexpr (n == 31) { mB.x = asf(gb_.x) + one; PIN(mB.x); mB.y = asf(gb_.z) + one; PIN(mB.y); *(LAS f32x2*)(L + L_MODA + w * 512 + (vl16 >> 1)) = mB; }
      if constexpr (n == 32) { mG.x = asf(gg_.x); mG.y = asf(gg_.z); *(LAS f32x2*)(L + L_GAB + (vl16 >> 1)) = mG; }
      if constexpr (n == 34) { fl0 = (gb_.y ^ tag) | (gb_.w ^ tag) | (gg_.y ^ tag) | (gg_.w ^ tag); PIN(fl0); }
      if constexpr (n == 7) { const u32 s0 = WS_MODF + 12288 + w * 1024; LD16S(gb_, vl16, rWS, s0, 0); }
      if constexpr (n == 9) { const u32 s0 = WS_MODF; LD16S(gg_, vgg, rWS, s0, 0); }
      if constexpr (n < 6 && !(n & 1)) b1_piece(IC<5>{}, IC<L_Z + 49152>{}, IC<n / 2>{});
      if constexpr (n == 6) arrive(L_CNT + 8);
      if constexpr (n == 10) await8(L_CNT + 8);
      if constexpr (n >= 10 && n < 22 && !(n & 1)) b1_piece(IC<6 + (n - 10) / 6>{}, IC<L_Z + ((n - 10) / 6) * 24576>{}, IC<((n - 10) / 2) % 3>{});
      if constexpr (n == 21) { const u32 vr = (vl16 >> 2) & 124, sr = WS_RFLAG + j * 128; LD4S(flr, vr, rWS, sr); }
      if constexpr (n == 26) {
        asm volatile("s_waitcnt vmcnt(0)" : "+v"(gb_), "+v"(gg_), "+v"(flr)::"memory");
        arrive(L_CNT + 12);
        const u32 vr = (vl16 >> 2) & 124, sr = WS_RFLAG + j * 128; u32 cnt = SPIN_CAP;
        asm volatile("1:\n\tv_cmp_ne_u32 vcc, %5, %0\n\ts_cbranch_vccz 2f\n\tbuffer_load_dword %0, %2, %3, %4 offen sc1\n\ts_waitcnt vmcnt(0)\n\t"
                     "s_sub_u32 %1, %1, 1\n\ts_cbranch_scc0 1b\n\t2:" : "+v"(flr), "+s"(cnt) : "v"(vr), "s"(rWS), "s"(sr), "s"(tag) : "vcc", "scc", "memory");
      }
      if constexpr (n == 28) { await8(L_CNT + 12); int tz2 = tz0 + 49152; PIN(tz2); read_b1(5, tz2, 0); }
      if constexpr (n == 32) read_b1(6, tz0, 0);
      if constexpr (n == 37) read_b1(7, tz0, 24576);
      if constexpr (n >= 27 && n < 47 && (n & 1)) load_b3(IC<(n - 27) / 2>{});
    };
    sfor<0, 16>([&](auto KS) {
      constexpr int ks = decltype(KS)::value;
      KSTAMP(1, ks);
      if constexpr (ks == 0) read_b1(3, tz0, 0);
      if constexpr (ks == 1) read_b1(4, tz0, 24576);
      SCHED();
      v4u nh = ah, n1 = a1, n2 = a2;
      aKh = mfma32(bw[ks * 3 + 1], ah, aKh);
      aVh = mfma32(ah, bw[ks * 3 + 2], aVh);
      if constexpr (ks < 15) nh = AFRAG(0, ks + 1);
      job(IC<3 * ks>{});
      SCHED();
      aQ = mfma32(bw[ks * 3 + 0], a1, aQ);
      aK = mfma32(bw[ks * 3 + 1], a1, aK);
      aV = mfma32(a1, bw[ks * 3 + 2], aV);
      if constexpr (ks < 15) n1 = AFRAG(16384, ks + 1);
      job(IC<3 * ks + 1>{});
      SCHED();
      bQ = mfma32(bw[ks * 3 + 0], a2, bQ);
      bK = mfma32(bw[ks * 3 + 1], a2, bK);
      bV = mfma32(a2, bw[ks * 3 + 2], bV);
      if constexpr (ks < 15) n2 = AFRAG(32768, ks + 1);
      job(IC<3 * ks + 2>{});
      ah = nh; a1 = n1; a2 = n2;
      SCHED();
    });
  }
  STAMP(9); KSTAMP(1, 16);
  {
    const u32 sOst = WS_O + ((g * 4 + 2 * blk2) * 16 + 2 * j + (hd >> 1)) * 1024;
    const v2u k1p = pk4(aK), v1p = pk4(aV);
    const v2u o0 = attend(pk4(aKh), pk4(aVh), k1p, v1p, pk4(aQ), am0, am1, ahm, g == 0 && blk2 == 0);
    const v2u o1 = attend(k1p, v1p, pk4(bK), pk4(bV), pk4(bQ), am0, am1, ahm, false);
    __builtin_amdgcn_raw_buffer_store_b64(o0, rWS, vOst, sOst, 16);
    __builtin_amdgcn_raw_buffer_store_b64(o1, rWS, vOst, sOst + 16384, 16);
  }
  if (__builtin_amdgcn_ballot_w64(fl0 != 0u)) {
    for (u32 sp = 0; sp < SPIN_CAP; ++sp) {
      asm volatile("" ::: "memory");
      gb_ = __builtin_amdgcn_raw_buffer_load_b128(rWS, vl16, WS_MODF + 12288 + w * 1024, 16);
      gg_ = __builtin_amdgcn_raw_buffer_load_b128(rWS, vgg, WS_MODF, 16);
      if (!__builtin_amdgcn_ballot_w64(((gb_.y ^ tag) | (gb_.w ^ tag) | (gg_.y ^ tag) | (gg_.w ^ tag)) != 0u)) break;
    }
    const f32x2 mB = {asf(gb_.x) + one, asf(gb_.z) + one}, mG = {asf(gg_.x), asf(gg_.z)};
    *(LAS f32x2*)(L + L_MODA + w * 512 + (vl16 >> 1)) = mB;
    *(LAS f32x2*)(L + L_GAB + (vl16 >> 1)) = mG;
  }
  STAMP(10);
  VMW(0);
  BAR();
  RAISE(flag_off(0, g, 0) + j * 128);
  STAMP(11);
  if (b == 0 && w == 7) {
    for (u32 sp = 0; sp < SPIN_CAP; ++sp) {
      asm volatile("" ::: "memory");
      const v4u v = __builtin_amdgcn_raw_buffer_load_b128(rWS, vl16, WS_RFLAG, 16);
      if (__all(v.x == tag && v.y == tag && v.z == tag && v.w == tag)) break;
    }
    __builtin_amdgcn_raw_buffer_store_b32(tag, rWS, 0, WS_EPOCH, 16);
  }

  v4u xv;
  { const u32 sx = g * 65536 + j * 128; LD16(xv, vRow, rX, sx, 0); }
  sfor<10, 16>(load_b3);
  const u64 care2 = 0x0101010101010101ull * (3u << (2 * kq));
  poll_flags(rWS, vfl, flag_off(0, g, 0), care2, tag);
  PIN(xv);
  v4u af[8];
  {
    const u32 so = WS_O + ((g * 4 + 2 * mh) * 16 + 4 * kq) * 1024, so1 = so + 16384;
    LD16S(af[0], vl16, rWS, so, 0); LD16S(af[1], vl16, rWS, so, 1024); LD16S(af[2], vl16, rWS, so, 2048); LD16S(af[3], vl16, rWS, so, 3072);
    LD16S(af[4], vl16, rWS, so1, 0); LD16S(af[5], vl16, rWS, so1, 1024); LD16S(af[6], vl16, rWS, so1, 2048); LD16S(af[7], vl16, rWS, so1, 3072);
  }
  STAMP(12);
  sfor<0, 2>([&](auto M) {
    constexpr int m = decltype(M)::value;
    asm volatile("s_waitcnt vmcnt(%c4)" : "+v"(af[4 * m]), "+v"(af[4 * m + 1]), "+v"(af[4 * m + 2]), "+v"(af[4 * m + 3]) : "i"(m == 0 ? 4 : 2) : "memory");
    if constexpr (m == 0) {
#pragma unroll
      for (int i = 0; i < 16; ++i) PIN(b3[i]);
    }
    f32x4 acc[4] = {z4, z4, z4, z4};
    sfor<0, 4>([&](auto SC) {
      constexpr int sx = decltype(SC)::value;
#pragma unroll
      for (int n = 0; n < 4; ++n) acc[n] = mfma32(b3[sx * 4 + n], af[4 * m + sx], acc[n]);
      if constexpr (sx & 1) load_b4(IC<2 * m + (sx >> 1)>{});
      SCHED();
    });
#pragma unroll
    for (int n = 0; n < 4; ++n) *(LAS f32x4*)(L + ((n & 1) ? (pw ^ 64) : pw) + (m * 4096 + (n >> 1) * 128)) = acc[n];
  });
  BAR();
  STAMP(13);
  v4u x1p;
  {
    f32x4 y0 = *(LAS f32x4*)(L + pr), y1 = *(LAS f32x4*)(L + (pr ^ 16));
#pragma unroll
    for (int q = 1; q < 4; ++q) { y0 += *(LAS f32x4*)(L + pr + q * 16384); y1 += *(LAS f32x4*)(L + (pr ^ 16) + q * 16384); }
    const int ga = L_GAB + ((vl16 & 112) << 1);
    const f32x4 g0 = *(LAS f32x4*)(L + ga), g1 = *(LAS f32x4*)(L + ga + 16);
    const f32x4 x0 = f32x4{blo(xv.x), bhi(xv.x), blo(xv.y), bhi(xv.y)} + g0 * y0, x1 = f32x4{blo(xv.z), bhi(xv.z), blo(xv.w), bhi(xv.w)} + g1 * y1;
    { const v4u o = cat(pk4(x0), pk4(x1)); x1p = o; const u32 so = WS_X1 + g * 65536 + j * 128;
      asm volatile("buffer_store_dwordx4 %0, %1, %2, %3 offen sc1\n\ts_nop 1" ::"v"(o), "v"(vRow), "s"(rWS), "s"(so) : "memory"); }
    const f32x4 sv = x0 + x1;
    float mean = (sv.x + sv.y) + (sv.z + sv.w);
    mean += dppf<0xB1>(mean); mean += dppf<0x4E>(mean); mean += dppf<0x141>(mean);
    mean *= 1.f / 64.f;
    const f32x4 d0 = x0 - mean, d1 = x1 - mean, dd = d0 * d0 + d1 * d1;
    float m2 = (dd.x + dd.y) + (dd.z + dd.w);
    m2 += dppf<0xB1>(m2); m2 += dppf<0x4E>(m2); m2 += dppf<0x141>(m2);
    v2u st; st.x = asu(mean); st.y = asu(m2);
    __builtin_amdgcn_raw_buffer_store_b64(st, rWS, (vRow >> 10) << 6, WS_STAT + g * 4096 + j * 8, 16);
  }
  STAMP(14);
  VMW(0);
  BAR();
  RAISE(flag_off(1, g, 0) + j * 128);
  sfor<4, 9>(load_b4);
  STAMP(15);

  u32 hmask[3];
#pragma unroll
  for (int ct = 0; ct < 3; ++ct)
    asm("s_cmp_lt_u32 %1, %2\n\ts_cselect_b32 %0, -1, 0" : "=s"(hmask[ct]) : "s"((u32)(ch0 + ct)), "s"((u32)cend) : "scc");
#pragma unroll
  for (int i = 0; i < 4; ++i) { afo[i] -= blk2 * 32768; PIN(afo[i]); }
  int vm2 = L_MODA + 2 * vl16; PIN(vm2);
  int h2w = w * 8192 + (((vl16 - 128 * rotp) & 1023) ^ ((8 * w & 15) << 4)); PIN(h2w);
  const f32x4 ls0 = *(LAS f32x4*)(L + vm2), ls1 = *(LAS f32x4*)(L + vm2 + 16), la0 = *(LAS f32x4*)(L + vm2 + 2048), la1 = *(LAS f32x4*)(L + vm2 + 2064);
  poll_flags(rWS, vfl, flag_off(1, g, 0), ~0ull, tag);
  landed_b4(IC<0>{}, IC<9>{});
  {
    v4u x1v[8]; v2u stv;
    const u32 so = WS_X1 + g * 65536 + w * 8192, so2 = so + 4096;
    { const u32 vs = vl16 >> 1, ss = WS_STAT + g * 4096 + w * 512; LD8S(stv, vs, rWS, ss, 0); }
    LD16S(x1v[0], vl16, rWS, so, 0); LD16S(x1v[1], vl16, rWS, so, 1024); LD16S(x1v[2], vl16, rWS, so, 2048); LD16S(x1v[3], vl16, rWS, so, 3072);
    LD16S(x1v[4], vl16, rWS, so2, 0); LD16S(x1v[5], vl16, rWS, so2, 1024); LD16S(x1v[6], vl16, rWS, so2, 2048); LD16S(x1v[7], vl16, rWS, so2, 3072);
    sfor<9, 12>(load_b4);
    asm volatile("s_waitcnt vmcnt(11)" : "+v"(stv)::"memory");
    const float mj = asf(stv.x);
    float mean = mj;
    mean += dppf<0xB1>(mean); mean += dppf<0x4E>(mean); mean += dppf<0x141>(mean);
    mean *= 0.125f;
    const float dm = mj - mean;
    float m2 = asf(stv.y) + 64.f * dm * dm;
    m2 += dppf<0xB1>(m2); m2 += dppf<0x4E>(m2); m2 += dppf<0x141>(m2);
    const float l2r = __builtin_amdgcn_rsqf(m2 * (1.f / 512.f) + 1e-5f), l2n = -mean * l2r;
    sfor<0, 8>([&](auto I) {
      constexpr int i = decltype(I)::value;
      v4u& xx = x1v[i];
      asm volatile("s_waitcnt vmcnt(%c1)" : "+v"(xx) : "i"(10 - i) : "memory");
      const float rs = rdl(l2r, 8 * i), nn = rdl(l2n, 8 * i);
      v4u o;
      o.x = pk2((unp(xx.x) * rs + nn) * la0.xy + ls0.xy); o.y = pk2((unp(xx.y) * rs + nn) * la0.zw + ls0.zw);
      o.z = pk2((unp(xx.z) * rs + nn) * la1.xy + ls1.xy); o.w = pk2((unp(xx.w) * rs + nn) * la1.zw + ls1.zw);
      *(LAS v4u*)(L + (h2w ^ (i << 4)) + i * 1024) = o;
    });
  }
  BAR();
  STAMP(16);
  const int kb5 = 5 * w + min(w, 3);
  u64 care5 = 0x0101010101010101ull * ((0x80c06030180c0603ull >> (8 * w)) & 255u);
  asm volatile("" : "+s"(care5));
  v4u b5[24];
  const u32 sB5 = WS_B5T + (j * 43 + kb5) * 4096, d5 = (w + 5) >> 3;
  auto load_b5 = [&](auto NC) {
    constexpr int n = decltype(NC)::value, sx = n / 4, t = n % 4;
    ld16s<t * 1024>(b5[n], vl16, rWS, sB5 + (sx == 5 ? (5 - d5) * 4096 : sx * 4096));
  };
  f32x4 acc4[3][4];
#pragma unroll
  for (int ct = 0; ct < 3; ++ct)
#pragma unroll
    for (int m = 0; m < 4; ++m) acc4[ct][m] = z4;
  u32 vH; int pw5;
  {
    v4u a[4] = {AFRAG(0, 0), AFRAG(16384, 0), AFRAG(32768, 0), AFRAG(49152, 0)};
    sfor<0, 16>([&](auto KS) {
      constexpr int ks = decltype(KS)::value;
      KSTAMP(3, ks);
      if constexpr (ks >= 3 && ks < 13) { VMW(9); landed_b4(IC<3 * ks>{}, IC<3 * ks + 3>{}); }
      if constexpr (ks >= 13) { VMW(9); landed_b4(IC<3 * ks>{}, IC<3 * ks + 3>{}); }
      v4u nx[4] = {a[0], a[1], a[2], a[3]};
      sfor<0, 4>([&](auto MC) {
        constexpr int m = decltype(MC)::value;
        if constexpr (ks < 15) nx[m] = AFRAG(16384 * m, ks + 1);
        if constexpr (m < 3) { if constexpr (ks < 12) load_b4(IC<12 + 3 * ks + m>{}); else load_b5(IC<3 * (ks - 12) + m>{}); }
        SCHED();
#pragma unroll
        for (int ct = 0; ct < 3; ++ct) acc4[ct][m] = mfma32(b4[ks * 3 + ct], a[m], acc4[ct][m]);
        if constexpr (ks == 8 && m == 0) { vH = (vl16 & 255) + ((vl16 >> 5) & 8) + (vl16 >> 9) * (172 * 256); PIN(vH); }
        if constexpr (ks == 9 && m == 0) { const int l5 = opq(vl16) >> 4, c5 = l5 & 15, g5 = l5 >> 4;
          pw5 = w * 16384 + c5 * 256 + ((c5 & 4) << 4) + ((g5 ^ (c5 & 3)) << 4); PIN(pw5); }
        SCHED();
      });
#pragma unroll
      for (int m = 0; m < 4; ++m) a[m] = nx[m];
    });
  }
#pragma unroll
  for (int ct = 0; ct < 3; ++ct)
#pragma unroll
    for (int m = 0; m < 4; ++m) PIN(acc4[ct][m]);
  STAMP(17); KSTAMP(3, 16);
#pragma unroll
  for (int ct = 0; ct < 3; ++ct)
#pragma unroll
    for (int mp = 0; mp < 2; ++mp) {
      f32x4 G, U, R;
#pragma unroll
      for (int e = 0; e < 4; ++e) {
        auto r = __builtin_amdgcn_permlane32_swap(asu(acc4[ct][2 * mp][e]), asu(acc4[ct][2 * mp + 1][e]), false, false);
        G[e] = asf(r[0]); U[e] = asf(r[1]);
      }
      const f32x4 T = G * -1.4426950408889634f;
#pragma unroll
      for (int e = 0; e < 4; ++e) R[e] = __builtin_amdgcn_rcpf(1.f + __builtin_amdgcn_exp2f(T[e]));
      const v2u o = pk4((G * U) * R);
      const u32 so = WS_HH + ((g * 4 + 2 * mp) * 172 + ch0 + ct) * 256;
      asm volatile("s_mov_b32 exec_lo, %4\n\ts_mov_b32 exec_hi, %4\n\tbuffer_store_dwordx2 %0, %1, %2, %3 offen sc1\n\ts_mov_b64 exec, -1"
                   ::"v"(o), "v"(vH), "s"(rWS), "s"(so), "s"(hmask[ct]) : "memory");
    }
  STAMP(18);
  sfor<12, 16>(load_b5);
  VMW(4);
  BAR();
  RAISE(flag_off(2, g, 0) + j * 128);
  STAMP(19);
  sfor<16, 22>(load_b5);
  poll_flags(rWS, vfl, flag_off(2, g, 0), care5, tag);
#pragma unroll
  for (int i = 0; i < 22; ++i) PIN(b5[i]);
  v4u hf[16];
  auto load_h = [&](auto NC) {
    constexpr int n = decltype(NC)::value, sx = n / 4, m = n % 4;
    ld16s<0>(hf[n & 15], vl16, rWS, WS_HH + ((g * 4 + m) * 172 + 4 * kb5) * 256 + (sx == 5 ? (5 - d5) * 1024 : sx * 1024));
  };
  sfor<0, 16>(load_h);
  sfor<22, 24>(load_b5);
  STAMP(20);
  {
    f32x4 acc[4][4];
#pragma unroll
    for (int m = 0; m < 4; ++m)
#pragma unroll
      for (int n = 0; n < 4; ++n) acc[m][n] = z4;
    sfor<0, 6>([&](auto SC) {
      constexpr int sx = decltype(SC)::value, r = (sx & 3) * 4;
      constexpr int nw = sx == 0 ? 14 : sx == 1 ? 14 : sx == 2 ? 14 : sx == 3 ? 10 : sx == 4 ? 4 : 0;
      asm volatile("s_waitcnt vmcnt(%c4)" : "+v"(hf[r]), "+v"(hf[r + 1]), "+v"(hf[r + 2]), "+v"(hf[r + 3]) : "i"(nw) : "memory");
      if constexpr (sx == 4) { PIN(b5[22]); PIN(b5[23]); }
      if constexpr (sx == 5) { if (w >= 3) { hf[r] = v4u{0u, 0u, 0u, 0u}; hf[r + 1] = v4u{0u, 0u, 0u, 0u}; hf[r + 2] = v4u{0u, 0u, 0u, 0u}; hf[r + 3] = v4u{0u, 0u, 0u, 0u}; } }
      sfor<0, 4>([&](auto MC) {
        constexpr int m = decltype(MC)::value;
#pragma unroll
        for (int n = 0; n < 4; ++n) acc[m][n] = mfma32(b5[sx * 4 + n], hf[r + m], acc[m][n]);
        SCHED();
        if constexpr (sx < 2) load_h(IC<16 + 4 * sx + m>{});
        SCHED();
      });
    });
#pragma unroll
    for (int m = 0; m < 4; ++m)
#pragma unroll
      for (int n = 0; n < 4; ++n) *(LAS f32x4*)(L + ((n & 1) ? (pw5 ^ 64) : pw5) + (m * 4096 + (n >> 1) * 128)) = acc[m][n];
  }
  BAR();
  STAMP(21);
  {
    const int l6 = opq(vl16) >> 4;
    const int p0 = opq((8 * w + (l6 >> 3)) * 256 + (((2 * (l6 & 7)) ^ ((l6 >> 3) & 7)) << 4)), p1 = opq(p0 + 65536);
    f32x4 y0 = *(LAS f32x4*)(L + p0), y1 = *(LAS f32x4*)(L + (p0 ^ 16));
#pragma unroll
    for (int q = 1; q < 8; ++q) { const int pb = q < 4 ? p0 : p1; y0 += *(LAS f32x4*)(L + pb + (q & 3) * 16384); y1 += *(LAS f32x4*)(L + (pb ^ 16) + (q & 3) * 16384); }
    const int gb = L_GAB + 256 + (l6 & 7) * 32;
    const f32x4 g0 = *(LAS f32x4*)(L + gb), g1 = *(LAS f32x4*)(L + gb + 16);
    const f32x4 o0 = f32x4{blo(x1p.x), bhi(x1p.x), blo(x1p.y), bhi(x1p.y)} + g0 * y0, o1 = f32x4{blo(x1p.z), bhi(x1p.z), blo(x1p.w), bhi(x1p.w)} + g1 * y1;
    const v4u o = cat(pk4(o0), pk4(o1));
    const u32 vo = (8 * w + (l6 >> 3)) * 1024 + (l6 & 7) * 16, so = g * 65536 + j * 128;
    asm volatile("buffer_store_dwordx4 %0, %1, %2, %3 offen sc1\n\ts_nop 1" ::"v"(o), "v"(vo), "s"(rOUT), "s"(so) : "memory");
  }
  STAMP(22);
  STAMP_DUMP();
}
}

extern "C" void kernel_launch(void* const* d_in, const int* in_sizes, int n_in, void* d_out, int out_size, void* d_ws, size_t ws_size,
                              hipStream_t stream) {
  Params p;
  p.x = (const u16*)d_in[0]; p.wqkv = (const u16*)d_in[1]; p.wo = (const u16*)d_in[2];
  p.w1 = (const u16*)d_in[4]; p.w3 = (const u16*)d_in[5]; p.w2 = (const u16*)d_in[6];
  p.c = (const u16*)d_in[7]; p.wmod = (const u16*)d_in[8];
  p.out = (u16*)d_out; p.ws = (char*)d_ws;
  (void)hipFuncSetAttribute((const void*)fused_block, hipFuncAttributeMaxDynamicSharedMemorySize, L_TOTAL);
  fused_block<<<256, 512, L_TOTAL, stream>>>(p);
}
```

```cpp
#include <hip/hip_runtime.h>
#include <stdint.h>

#define KDBG 0
#define NODMA 0
#define CODEWARM 0
#define L2WARM 1
#define EARLYT 0
#define XMAP 2

#define DI __device__ __forceinline__
#define GAS __attribute__((address_space(1)))
#define LAS __attribute__((address_space(3)))
typedef unsigned u32;
typedef unsigned short u16;
typedef unsigned long long u64;
typedef u32 v4u __attribute__((ext_vector_type(4)));
typedef u32 v2u __attribute__((ext_vector_type(2)));
typedef float f32x4 __attribute__((ext_vector_type(4)));
typedef float f32x2 __attribute__((ext_vector_type(2)));
typedef __bf16 bf16x8 __attribute__((ext_vector_type(8)));
typedef __bf16 bf16x2 __attribute__((ext_vector_type(2)));
typedef short s16x4 __attribute__((ext_vector_type(4)));
typedef __amdgpu_buffer_rsrc_t rsrc_t;
typedef GAS u32 gu32;
typedef GAS u64 gu64;
#define RLX_AGENT __ATOMIC_RELAXED, __HIP_MEMORY_SCOPE_AGENT

namespace {
constexpr int S_ = 2048, D_ = 512, F_ = 1376;
constexpr u32 WS_EPOCH = 0;
constexpr u32 WS_FLAG = 32768;
constexpr u32 WS_STAT = 262144;
constexpr u32 WS_MODG = 524288;
constexpr u32 WS_MODF = 655360;
constexpr u32 WS_O = 1u << 20;
constexpr u32 WS_X1 = 3u << 20;
constexpr u32 WS_HH = 5u << 20;
constexpr u32 WS_RFLAG = 8192;
constexpr u32 WS_B4T = 11u << 20;
constexpr u32 WS_B5T = 14u << 20;
constexpr u32 WS_B3T = (15u << 20) + (512u << 10);
constexpr u32 WS_DBG = 16u << 20;
constexpr u32 WS_BYTES = 32u << 20;
constexpr int L_XY = 0;
constexpr int L_Z = 81920;
constexpr int L_MISC = 158720;
constexpr int L_TOTAL = 163840;
constexpr int L_RED = 73728;
constexpr int L_MODA = L_MISC;
constexpr int L_CNT = L_MISC + 4096;
constexpr int L_GAB = L_MISC + 4096 + 64;
constexpr int L_JUNK = L_MISC + 4096 + 64 + 512;
constexpr int L_STAMP = 156672;
constexpr u32 SPIN_CAP = 1u << 18;
constexpr u32 CODE_BYTES = 26112;

DI f32x4 mfma32(v4u a, v4u b, f32x4 c) {
  return __builtin_amdgcn_mfma_f32_16x16x32_bf16(__builtin_bit_cast(bf16x8, a), __builtin_bit_cast(bf16x8, b), c, 0, 0, 0);
}
DI f32x4 mfma16(v2u a, v2u b, f32x4 c) {
  return __builtin_amdgcn_mfma_f32_16x16x16bf16_1k(__builtin_bit_cast(s16x4, a), __builtin_bit_cast(s16x4, b), c, 0, 0, 0);
}
DI u32 pk(float lo, float hi) { f32x2 v = {lo, hi}; return __builtin_bit_cast(u32, __builtin_convertvector(v, bf16x2)); }
DI u32 pk2(f32x2 v) { return __builtin_bit_cast(u32, __builtin_convertvector(v, bf16x2)); }
DI v2u pk4(f32x4 v) { v2u r; r.x = pk(v.x, v.y); r.y = pk(v.z, v.w); return r; }
DI float asf(u32 u) { return __builtin_bit_cast(float, u); }
DI u32 asu(float f) { return __builtin_bit_cast(u32, f); }
DI float blo(u32 u) { return asf(u << 16); }
DI float bhi(u32 u) { return asf(u & 0xffff0000u); }
DI f32x2 unp(u32 u) { f32x2 r = {blo(u), bhi(u)}; return r; }
DI float dot2(u32 a, u32 b, float c) { return __builtin_amdgcn_fdot2_f32_bf16(__builtin_bit_cast(bf16x2, a), __builtin_bit_cast(bf16x2, b), c, false); }
DI v2u trd(LAS char* p) { return __builtin_bit_cast(v2u, __builtin_amdgcn_ds_read_tr16_b64_v4i16((LAS s16x4*)p)); }
DI v4u cat(v2u a, v2u b) { v4u r; r.x = a.x; r.y = a.y; r.z = b.x; r.w = b.y; return r; }
template <int MI, int SI> DI void dma(u32 mb, u32 voff, rsrc_t srd, u32 sb) {
  u32 t;
  asm volatile("s_add_u32 m0, %1, %c2\n\ts_add_u32 %0, %3, %c4\n\tbuffer_load_dwordx4 %5, %6, %0 offen lds"
               : "=&s"(t) : "s"(mb), "i"(MI), "s"(sb), "i"(SI), "v"(voff), "s"(srd) : "scc");
}
DI void touch(u32 voff, rsrc_t srd, u32 sb) {
  asm volatile("s_mov_b32 m0, %c0\n\ts_nop 0\n\tbuffer_load_dword %1, %2, %3 offen lds" ::"i"(L_JUNK), "v"(voff), "s"(srd), "s"(sb));
}
#define BAR() asm volatile("s_waitcnt lgkmcnt(0)\n\ts_barrier" ::: "memory")
#define VMW(n) asm volatile("s_waitcnt vmcnt(" #n ")" ::: "memory")
#define LGW() asm volatile("s_waitcnt lgkmcnt(0)" ::: "memory")
#define SCHED() __builtin_amdgcn_sched_barrier(0)
#define LD16(dst, voff, srd, soff, IMM) asm volatile("buffer_load_dwordx4 %0, %1, %2, %3 offen offset:" #IMM : "=v"(dst) : "v"(voff), "s"(srd), "s"(soff))
#define LD16S(dst, voff, srd, soff, IMM) asm volatile("buffer_load_dwordx4 %0, %1, %2, %3 offen offset:" #IMM " sc1" : "=v"(dst) : "v"(voff), "s"(srd), "s"(soff))
#define LD8(dst, voff, srd, soff, IMM) asm volatile("buffer_load_dwordx2 %0, %1, %2, %3 offen offset:" #IMM : "=v"(dst) : "v"(voff), "s"(srd), "s"(soff))
#define LD8S(dst, voff, srd, soff, IMM) asm volatile("buffer_load_dwordx2 %0, %1, %2, %3 offen offset:" #IMM " sc1" : "=v"(dst) : "v"(voff), "s"(srd), "s"(soff))
#define LD4(dst, voff, srd, soff) asm volatile("buffer_load_dword %0, %1, %2, %3 offen" : "=v"(dst) : "v"(voff), "s"(srd), "s"(soff))
#define LD4S(dst, voff, srd, soff) asm volatile("buffer_load_dword %0, %1, %2, %3 offen sc1" : "=v"(dst) : "v"(voff), "s"(srd), "s"(soff))

template <int IMM> DI void ld16s(v4u& r, u32 v, rsrc_t rs, u32 so) { asm volatile("buffer_load_dwordx4 %0, %1, %2, %3 offen offset:%c4 sc1" : "=v"(r) : "v"(v), "s"(rs), "s"(so), "i"(IMM)); }
template <int CTRL> DI float dppf(float v) { return asf(__builtin_amdgcn_update_dpp(0, asu(v), CTRL, 0xf, 0xf, true)); }
DI float row16_sum(float v) { v += dppf<0x128>(v); v += dppf<0x124>(v); v += dppf<0x122>(v); v += dppf<0x121>(v); return v; }
DI float sw32_add(float a, float b) { auto r = __builtin_amdgcn_permlane32_swap(asu(a), asu(b), false, false); return asf(r[0]) + asf(r[1]); }
DI float sw16_add(float a, float b) { auto r = __builtin_amdgcn_permlane16_swap(asu(a), asu(b), false, false); return asf(r[0]) + asf(r[1]); }
DI float x16_max(float v) { auto r = __builtin_amdgcn_permlane16_swap(asu(v), asu(v), false, false); return fmaxf(asf(r[0]), asf(r[1])); }
DI float x32_max(float v) { auto r = __builtin_amdgcn_permlane32_swap(asu(v), asu(v), false, false); return fmaxf(asf(r[0]), asf(r[1])); }
DI int fresh_lane() { int l; asm volatile("v_mbcnt_lo_u32_b32 %0, -1, 0\n\tv_mbcnt_hi_u32_b32 %0, -1, %0" : "=v"(l)); return l; }
DI float rdl(float v, int l) { return asf(__builtin_amdgcn_readlane(asu(v), l)); }

struct Params {
  const u16 *x, *wqkv, *wo, *w1, *w3, *w2, *c, *wmod;
  u16* out;
  char* ws;
};

#define LOOPSEL 0
#define STAMP(i) do {} while (0)
#define KSTAMP(lp, i) do {} while (0)
#define STAMP_DUMP() do {} while (0)

DI u32 flag_off(int phase, int g, int h) { return WS_FLAG + ((phase * 32 + g) * 2 + h) * 1024; }
DI void poll_flags(rsrc_t rWS, u32 vfl, u32 foff, u64 care, u32 tag) {
  for (u32 sp = 0; sp < SPIN_CAP; ++sp) {
    asm volatile("" ::: "memory");
    const u32 v = __builtin_amdgcn_raw_buffer_load_b32(rWS, vfl, foff, 16);
    if (!(__builtin_amdgcn_ballot_w64(v != tag) & care)) break;
  }
  VMW(0);
}
DI void raise_flag(rsrc_t rWS, u32 foff, u32 vtag, u32 vzero, u32 m1) {
  asm volatile("s_mov_b32 exec_hi, 0\n\ts_mov_b32 exec_lo, %4\n\tbuffer_store_dword %0, %1, %2, %3 offen sc1\n\ts_mov_b64 exec, -1" ::"v"(vtag), "v"(vzero), "s"(rWS), "s"(foff), "s"(m1) : "memory");
}
DI void signal_flag(rsrc_t rWS, u32 cnt_addr, u32 foff, u32 tag) {
  u32 t, a = cnt_addr, one = 1u, vt = tag, vz = 0u;
  asm volatile("s_mov_b64 exec, 1\n\tds_add_rtn_u32 %0, %1, %2\n\ts_waitcnt lgkmcnt(0)\n\tv_cmp_eq_u32 vcc, 7, %0\n\ts_and_b64 exec, exec, vcc\n\t"
               "buffer_store_dword %3, %4, %5, %6 offen sc1\n\ts_mov_b64 exec, -1"
               : "=&v"(t) : "v"(a), "v"(one), "v"(vt), "v"(vz), "s"(rWS), "s"(foff) : "vcc", "scc", "memory");
}
DI void arrive(u32 cnt_addr) {
  u32 a = cnt_addr, one = 1u;
  asm volatile("s_waitcnt lgkmcnt(0)\n\ts_mov_b64 exec, 1\n\tds_add_u32 %0, %1\n\ts_mov_b64 exec, -1" ::"v"(a), "v"(one) : "memory");
}
DI void await8(u32 cnt_addr) {
  u32 a = cnt_addr, t, sv;
  asm volatile("1:\n\tds_read_b32 %0, %2\n\ts_waitcnt lgkmcnt(0)\n\tv_readfirstlane_b32 %1, %0\n\ts_nop 0\n\ts_cmp_lt_u32 %1, 8\n\ts_cbranch_scc1 1b"
               : "=&v"(t), "=&s"(sv) : "v"(a) : "scc", "memory");
}
DI void raise_when8(rsrc_t rWS, u32 cnt_addr, u32 foff, u32 tag, u32 w7) {
  u32 a = cnt_addr, t, sv, vt = tag, vz = 0u;
  asm volatile("s_cmp_eq_u32 %7, 0\n\ts_cbranch_scc1 2f\n\t1:\n\tds_read_b32 %0, %2\n\ts_waitcnt lgkmcnt(0)\n\tv_readfirstlane_b32 %1, %0\n\ts_nop 0\n\ts_cmp_lt_u32 %1, 8\n\ts_cbranch_scc1 1b\n\t"
               "s_mov_b64 exec, 1\n\tbuffer_store_dword %3, %4, %5, %6 offen sc1\n\ts_mov_b64 exec, -1\n\t2:"
               : "=&v"(t), "=&s"(sv) : "v"(a), "v"(vt), "v"(vz), "s"(rWS), "s"(foff), "s"(w7) : "scc", "memory");
}
#define PIN(x) asm volatile("" : "+v"(x))
DI float qp(float r) { PIN(r); return r; }
DI float q_shl(u32 a) { return qp(blo(a)); }
DI float q_hi(u32 a) { return qp(bhi(a)); }
DI float q_fma(float a, float b, float c) { return qp(__builtin_fmaf(a, b, c)); }
DI float q_fmas(float a, float b, float c) { return qp(__builtin_fmaf(a, b, c)); }
DI float q_mul(float a, float b) { return qp(a * b); }
DI float q_mulk(float a) { return qp(a * -1.4426950408889634f); }
DI float q_add1(float a) { return qp(a + 1.f); }
DI float q_exp(float a) { return qp(__builtin_amdgcn_exp2f(a)); }
DI float q_rcp(float a) { return qp(__builtin_amdgcn_rcpf(a)); }
DI u32 q_cvt(float a, float b) { u32 r = pk(a, b); PIN(r); return r; }
DI float q_mov(float a) { return qp(a); }
DI u32 opq(u32 v) { asm volatile("" : "+v"(v)); return v; }

DI float sel(u64 m, float a, float b) { return __builtin_amdgcn_inverse_ballot_w64(m) ? a : b; }
DI v2u attend(v2u kp, v2u vp, v2u kc, v2u vc, v2u q, u64 m0, u64 m1, u32 hm, bool noprev) {
  const f32x4 z = {0.f, 0.f, 0.f, 0.f};
  const f32x4 sp = mfma16(kp, q, z);
  const f32x4 sc = mfma16(kc, q, z);
  const float K2 = 0.25f * 1.4426950408889634f;
  const u64 par = 0xaaaaaaaaaaaaaaaaull;
  float p0 = sel(par, sp[1], sp[0]), p1 = sel(par, sp[3], sp[2]);
  if (noprev) { p0 = -INFINITY; p1 = -INFINITY; }
  const float t0 = sel(m0, sel(par, sc[1], sc[0]), p0), t1 = sel(m1, sel(par, sc[3], sc[2]), p1);
  const float m = x32_max(x16_max(fmaxf(t0, t1)));
  const float mk = -m * K2;
  const float e0 = __builtin_amdgcn_exp2f(__builtin_fmaf(t0, K2, mk)), e1 = __builtin_amdgcn_exp2f(__builtin_fmaf(t1, K2, mk));
  const u32 w0 = pk(e0, e0) & hm, w1 = pk(e1, e1) & hm;
  v4u pf;
  pf.x = __builtin_amdgcn_inverse_ballot_w64(m0) ? 0u : w0; pf.y = __builtin_amdgcn_inverse_ballot_w64(m1) ? 0u : w1;
  pf.z = __builtin_amdgcn_inverse_ballot_w64(m0) ? w0 : 0u; pf.w = __builtin_amdgcn_inverse_ballot_w64(m1) ? w1 : 0u;
  const f32x4 o = mfma32(cat(vp, vc), pf, z);
  float sum = e0 + e1;
  sum = sw16_add(sum, sum); sum = sw32_add(sum, sum);
  const float inv = __builtin_amdgcn_rcpf(sum);
  return pk4(f32x4{qp(o.x * inv), qp(o.y * inv), qp(o.z * inv), qp(o.w * inv)});
}

template <int N> using IC = std::integral_constant<int, N>;
template <int I, int N, class F> DI void sfor(F&& f) { if constexpr (I < N) { f(IC<I>{}); sfor<I + 1, N>(f); } }

__global__ void __launch_bounds__(512, 2) fused_block(Params p) {
  extern __shared__ __attribute__((aligned(16))) char smem[];
  LAS char* L = (LAS char*)smem;
  const int tid = threadIdx.x, lane = tid & 63, w = __builtin_amdgcn_readfirstlane(tid >> 6);
  const int c16 = lane & 15, g4 = lane >> 4;
  const int b = blockIdx.x;
  int g, j;
  if (XMAP == 0) { g = (b & 7) * 4 + ((b >> 3) & 3); j = b >> 5; }
  else if (XMAP == 2) { const int x = b & 7, r = b >> 3; g = (x >> 1) * 8 + (r & 7); j = (x & 1) * 4 + (r >> 3); }
  else { const int x = b & 7, r = b >> 3; g = (x >> 2) * 16 + (r & 15); j = (x & 3) * 2 + (r >> 4); }
  const rsrc_t rX = __builtin_amdgcn_make_buffer_rsrc((void*)p.x, 0, S_ * D_ * 2, 0x00020000);
  const rsrc_t rQKV = __builtin_amdgcn_make_buffer_rsrc((void*)p.wqkv, 0, D_ * 3 * D_ * 2, 0x00020000);
  const rsrc_t rWO = __builtin_amdgcn_make_buffer_rsrc((void*)p.wo, 0, D_ * D_ * 2, 0x00020000);
  const rsrc_t rW1 = __builtin_amdgcn_make_buffer_rsrc((void*)p.w1, 0, D_ * F_ * 2, 0x00020000);
  const rsrc_t rW3 = __builtin_amdgcn_make_buffer_rsrc((void*)p.w3, 0, D_ * F_ * 2, 0x00020000);
  const rsrc_t rW2 = __builtin_amdgcn_make_buffer_rsrc((void*)p.w2, 0, F_ * D_ * 2, 0x00020000);
  const rsrc_t rC = __builtin_amdgcn_make_buffer_rsrc((void*)p.c, 0, D_ * 2, 0x00020000);
  const rsrc_t rMOD = __builtin_amdgcn_make_buffer_rsrc((void*)p.wmod, 0, D_ * 6 * D_ * 2, 0x00020000);
  const rsrc_t rWS = __builtin_amdgcn_make_buffer_rsrc((void*)p.ws, 0, WS_BYTES, 0x00020000);
  const rsrc_t rOUT = __builtin_amdgcn_make_buffer_rsrc((void*)p.out, 0, S_ * D_ * 2, 0x00020000);
  STAMP(0);

  u32 ep;
  { const u32 vz = 0; asm volatile("buffer_load_dword %0, %1, %2, 0 offen sc1" : "=v"(ep) : "v"(vz), "s"(rWS)); }
  u32 vl16 = lane * 16; PIN(vl16);
  const int bm = b < 192 ? b : b - 192, mq = bm / 48, cl = bm - 48 * mq;
  v4u wm[4]; v2u cv; u32 ep0;
  if (w < 4) {
    const u32 vm = (tid >> 3) * 24576 + (tid & 7) * 16, s0 = mq * (128 * 6144) + cl * 128, s1 = s0 + 6144, s2 = s0 + 12288, s3 = s0 + 18432;
    LD16(wm[0], vm, rMOD, s0, 0); LD16(wm[1], vm, rMOD, s1, 0); LD16(wm[2], vm, rMOD, s2, 0); LD16(wm[3], vm, rMOD, s3, 0);
    const u32 vc = (tid >> 3) * 8, scq = mq * 256;
    LD8(cv, vc, rC, scq, 0);
    const u32 vz = 0; asm volatile("buffer_load_dword %0, %1, %2, 0 offen sc1" : "=v"(ep0) : "v"(vz), "s"(rWS));
  }
  if (tid < 16) *(LAS u32*)(L + L_CNT + tid * 4) = 0u;
  BAR();
  const int st_qd = lane >> 2, st_r = (st_qd & 3) + 4 * (st_qd >> 3);
  const int st_col = ((st_qd >> 2) & 1) * 64 + (((lane & 3) ^ ((w & 1) << 1)) << 4);
  const int rotp = g & 7;
  u32 vB1 = st_r * 3072 + st_col; PIN(vB1);
  const u32 mB1 = (w >> 2) * 12288 + (w & 3) * 1024, sB1 = (w >> 2) * 98304 + (w & 3) * 24576 + j * 128;
  auto stage_b1 = [&](auto PC, auto BC) {
    constexpr int pair = decltype(PC)::value, base = decltype(BC)::value;
    const u32 sp = ((pair + rotp) & 7) * 196608 + sB1;
    dma<base, 0>(mB1, vB1, rQKV, sp); dma<base + 4096, 1024>(mB1, vB1, rQKV, sp); dma<base + 8192, 2048>(mB1, vB1, rQKV, sp);
  };
  auto b1_piece = [&](auto PC, auto BC, auto TC) {
    constexpr int pair = decltype(PC)::value, base = decltype(BC)::value, ty = decltype(TC)::value;
    dma<base + ty * 4096, ty * 1024>(mB1, vB1, rQKV, ((pair + rotp) & 7) * 196608 + sB1);
  };
  const int hd = w & 3, blk2 = w >> 2, kq = w & 3, mh = w >> 2;
  const int cs0 = j < 4 ? 22 * j : 88 + 21 * (j - 4);
  const int cend = cs0 + (j < 4 ? 22 : 21);
  STAMP(1);
  const int T0 = 12 * (g >> 4) + 3 * (w & 3), rcb = min(cs0 + T0, 168);
  if (w < 4) {
    asm volatile("s_waitcnt vmcnt(0)" : "+v"(cv), "+v"(wm[0]), "+v"(wm[1]), "+v"(wm[2]), "+v"(wm[3]), "+v"(ep0)::"memory");
    const u32 tg = __builtin_amdgcn_readfirstlane(ep0) + 1u;
    const f32x4 cf = {blo(cv.x), bhi(cv.x), blo(cv.y), bhi(cv.y)};
    float sc[4];
#pragma unroll
    for (int r = 0; r < 4; ++r) sc[r] = cf[r] * __builtin_amdgcn_rcpf(1.f + __builtin_amdgcn_exp2f(cf[r] * -1.4426950408889634f));
    f32x2 v[4];
#pragma unroll
    for (int i = 0; i < 4; ++i) v[i] = (unp(wm[0][i]) * sc[0] + unp(wm[1][i]) * sc[1]) + (unp(wm[2][i]) * sc[2] + unp(wm[3][i]) * sc[3]);
    const float a0 = sw32_add(v[0].x, v[2].x), a1 = sw32_add(v[0].y, v[2].y), a2 = sw32_add(v[1].x, v[3].x), a3 = sw32_add(v[1].y, v[3].y);
    float t0 = sw16_add(a0, a2), t1 = sw16_add(a1, a3);
    t0 += dppf<0x128>(t0); t1 += dppf<0x128>(t1);
    f32x2 o = {t0, t1};
    *(LAS f32x2*)(L + L_RED + w * 256 + (lane & 7) * 32 + g4 * 8) = o;
    LGW();
    u32 old = 0;
    if (lane == 0) old = __hip_atomic_fetch_add((LAS u32*)(L + L_CNT + 28), 1u, __ATOMIC_RELAXED, __HIP_MEMORY_SCOPE_WORKGROUP);
    if (__builtin_amdgcn_readfirstlane(old) == 3u) {
      asm volatile("" ::: "memory");
      float s = 0.f;
#pragma unroll
      for (int i = 0; i < 4; ++i) s += *(LAS float*)(L + L_RED + i * 256 + lane * 4);
      v2u gr; gr.x = asu(s); gr.y = tg;
      if (b < 192) __builtin_amdgcn_raw_buffer_store_b64(gr, rWS, lane * 8, WS_MODG + (mq * 3072 + cl * 64) * 8, 16);
    }
  }
  STAMP(2);
  v4u xr[10];
  {
    const int r0 = 64 * g - 16 + w;
    const u32 s0 = (u32)max(r0, 0) * 1024u, s1 = (u32)max(r0 + 8, 0) * 1024u;
    LD16(xr[0], vl16, rX, s0, 0); LD16(xr[1], vl16, rX, s1, 0);
#pragma unroll
    for (int i = 2; i < 10; ++i) { const u32 so = (u32)(r0 + 8 * i) * 1024u; LD16(xr[i], vl16, rX, so, 0); }
  }
  stage_b1(IC<0>{}, IC<L_XY>{});
  int afo[4];
#pragma unroll
  for (int i = 0; i < 4; ++i) { afo[i] = blk2 * 32768 + c16 * 1024 + (((4 * i + g4) ^ c16) << 4); PIN(afo[i]); }
  const u64 am0 = __builtin_amdgcn_ballot_w64((c16 & 14) >= 4 * g4), am1 = __builtin_amdgcn_ballot_w64((c16 & 14) >= 4 * g4 + 2);
  u32 ahm = (lane & 1) ? 0xffff0000u : 0xffffu; PIN(ahm);
  u32 vOst = c16 * 16 + g4 * 256 + (hd & 1) * 8; PIN(vOst);
  int tB1 = g4 * 1024 + 64 * (c16 >> 2) + 8 * (lane & 3) + 32 * ((hd & 1) ^ (g4 & 1)) + 256 * (hd >> 1); PIN(tB1);
  int hw0 = w * 1024 + ((((lane - 8 * rotp) & 63) ^ w) << 4); PIN(hw0);
#define vfl ((opq(vl16) & 112u) << 3)
  STAMP(3);
  v4u bw[48];
  auto read_b1 = [&](int pair, int base, int off) {
#pragma unroll
    for (int kk = 0; kk < 2; ++kk)
#pragma unroll
      for (int ty = 0; ty < 3; ++ty) {
        LAS char* a = L + base + (off + kk * 12288 + ty * 4096);
        bw[(2 * pair + kk) * 3 + ty] = cat(trd(a), trd(a + 512));
      }
  };
  f32x2 xn[10][4];
  v4u ga_[4]; v2u pv;
  const u32 sMG = WS_MODG + w * 1024;
  {
    float u[10];
    sfor<0, 10>([&](auto I) {
      constexpr int i = decltype(I)::value;
      if constexpr (i == 0) asm volatile("s_waitcnt vmcnt(11)" : "+v"(xr[0]), "+v"(xr[1]), "+v"(ep)::"memory");
      if constexpr (i == 2) asm volatile("s_waitcnt vmcnt(9)" : "+v"(xr[2]), "+v"(xr[3])::"memory");
      if constexpr (i == 4) asm volatile("s_waitcnt vmcnt(10)" : "+v"(xr[4]), "+v"(xr[5])::"memory");
      if constexpr (i == 6) asm volatile("s_waitcnt vmcnt(8)" : "+v"(xr[6]), "+v"(xr[7])::"memory");
      if constexpr (i == 8) asm volatile("s_waitcnt vmcnt(9)" : "+v"(xr[8]), "+v"(xr[9])::"memory");
      float s = 0.f, q = 0.f;
#pragma unroll
      for (int e = 0; e < 4; ++e) { s = dot2(xr[i][e], 0x3f803f80u, s); q = dot2(xr[i][e], xr[i][e], q); }
      u[i] = sw32_add(s, q);
      PIN(u[i]);
      if constexpr (i == 3) stage_b1(IC<1>{}, IC<L_XY + 24576>{});
      if constexpr (i == 7) stage_b1(IC<2>{}, IC<L_XY + 49152>{});
      if constexpr (i == 9) {
        const u32 s0 = sMG, s1 = sMG + 24576, s2 = sMG + 49152, s3 = sMG + 73728;
        ld16s<0>(ga_[0], vl16, rWS, s0); ld16s<0>(ga_[1], vl16, rWS, s1); ld16s<0>(ga_[2], vl16, rWS, s2); ld16s<0>(ga_[3], vl16, rWS, s3);
      }
    });
    float t[5];
#pragma unroll
    for (int i = 0; i < 5; ++i) t[i] = row16_sum(sw16_add(u[i], u[i + 5]));
    float v = t[0];
#pragma unroll
    for (int i = 1; i < 5; ++i) v = c16 == i ? t[i] : v;
    auto r = __builtin_amdgcn_permlane32_swap(asu(v), asu(v), false, false);
    const float mean = asf(r[0]) * (1.f / 512.f);
    const float var = fmaxf(asf(r[1]) * (1.f / 512.f) - mean * mean, 0.f);
    float rs = __builtin_amdgcn_rsqf(var + 1e-5f), nm = -mean * rs;
    PIN(rs); PIN(nm);
    STAMP(4);
    { const u32 vo = ((lane >> 3) & 3) * 24576 + (lane & 7) * 8, so = WS_MODG + (1024 + 8 * b) * 8; LD8S(pv, vo, rWS, so, 0); }
    if (w >= 4) {
      const int u = lane >> 2, row = (u & 3) | ((u >> 1) & 4) | ((u << 1) & 8);
      const u32 vo = row * 2752 + (lane & 3) * 16, so = (g & 15) * (32 * 2752) + rcb * 16, mb = L_Z + 49152 + (w & 3) * 4096;
      dma<0, 0>(mb, vo, rW1, so); dma<1024, 16 * 2752>(mb, vo, rW1, so); dma<2048, 0>(mb, vo, rW3, so); dma<3072, 16 * 2752>(mb, vo, rW3, so);
    } else {
      const int u = lane >> 1, row = (u & 3) | ((u >> 1) & 4) | ((u << 1) & 8) | (u & 16);
      const u32 vo = row * 1024 + (lane & 1) * 16, so = g * 32768 + j * 128 + w * 32, mb = L_Z + 65536 + w * 2048;
      dma<0, 0>(mb, vo, rW2, so); dma<1024, 0>(mb, vo, rW2, so + (g < 11 ? 32 * 32768 : 0));
      const int rwo = (u & 7) | ((u >> 1) & 8) | ((u << 1) & 16);
      dma<0, 0>(76800 + w * 1024, rwo * 1024 + (lane & 1) * 16, rWO, (g & 15) * 32768 + j * 128 + w * 32);
    }
    sfor<0, 10>([&](auto I) {
      constexpr int i = decltype(I)::value;
      if constexpr (i == 2) stage_b1(IC<3>{}, IC<L_Z>{});
      if constexpr (i == 6) stage_b1(IC<4>{}, IC<L_Z + 24576>{});
      const float a = rdl(rs, (i / 5) * 16 + i % 5), c = rdl(nm, (i / 5) * 16 + i % 5);
#pragma unroll
      for (int e = 0; e < 4; ++e) { xn[i][e] = unp(xr[i][e]) * a + c; PIN(xn[i][e]); }
    });
  }
  const u32 tag = __builtin_amdgcn_readfirstlane(ep) + 1u;
  u32 m1; asm("s_lshr_b32 %0, 1, %1" : "=s"(m1) : "s"(w) : "scc");
#define RAISE(fo) raise_flag(rWS, (fo), tag, vl16, m1)
#define SIGNAL(idx, fo) signal_flag(rWS, L_CNT + 4 * (idx), (fo), tag)
  STAMP(5);
  asm volatile("s_waitcnt vmcnt(9)" : "+v"(ga_[0]), "+v"(ga_[1]), "+v"(ga_[2]), "+v"(ga_[3]), "+v"(pv)::"memory");
  auto gsum = [&](v4u (&a)[4], f32x2& o) -> u32 {
    o.x = (asf(a[0].x) + asf(a[1].x)) + (asf(a[2].x) + asf(a[3].x)); o.y = (asf(a[0].z) + asf(a[1].z)) + (asf(a[2].z) + asf(a[3].z));
    return ((a[0].y ^ tag) | (a[0].w ^ tag) | (a[1].y ^ tag) | (a[1].w ^ tag)) | ((a[2].y ^ tag) | (a[2].w ^ tag) | (a[3].y ^ tag) | (a[3].w ^ tag));
  };
  auto gather = [&](u32 voff, u32 soff, f32x2& o) {
    for (u32 sp = 0; sp < SPIN_CAP; ++sp) {
      asm volatile("" ::: "memory");
      v4u a[4];
#pragma unroll
      for (int q = 0; q < 4; ++q) a[q] = __builtin_amdgcn_raw_buffer_load_b128(rWS, voff, soff + q * 24576, 16);
      if (!__builtin_amdgcn_ballot_w64(gsum(a, o) != 0u)) break;
    }
  };
  const float one = w >= 4 ? 1.f : 0.f;
  {
    f32x2 mA;
    if (__builtin_amdgcn_ballot_w64(gsum(ga_, mA) != 0u)) gather(vl16, sMG, mA);
    *(LAS f32x2*)(L + L_MODA + w * 512 + (vl16 >> 1)) = mA + one;
  }
  BAR();
  if (w == 7) {
    const u32 vo = ((lane >> 3) & 3) * 24576 + (lane & 7) * 8, so = WS_MODG + (1024 + 8 * b) * 8;
    for (u32 sp = 0; sp < SPIN_CAP && __builtin_amdgcn_ballot_w64(pv.y != tag); ++sp) {
      asm volatile("" ::: "memory");
      pv = __builtin_amdgcn_raw_buffer_load_b64(rWS, vo, so, 16);
    }
    float sv = asf(pv.x);
    sv += dppf<0x128>(sv); sv = sw16_add(sv, sv);
    pv.x = asu(sv);
    __builtin_amdgcn_raw_buffer_store_b64(pv, rWS, (lane & 7) * 8, WS_MODF + (1024 + 8 * b) * 8, 16);
  }
  STAMP(6);
  int vma = L_MODA + 2 * vl16; PIN(vma);
  const f32x4 ms0 = *(LAS f32x4*)(L + vma), ms1 = *(LAS f32x4*)(L + vma + 16);
  const f32x4 ma0 = *(LAS f32x4*)(L + vma + 2048), ma1 = *(LAS f32x4*)(L + vma + 2064);
  int tz0 = tB1 + L_Z; PIN(tz0);
  { int t2 = tB1 + 49152; PIN(t2); read_b1(0, tB1, 0); read_b1(1, tB1, 24576); read_b1(2, t2, 0); }
  {
    VMW(0);
    if (w >= 4) {
      LAS char* a0 = L + L_Z + 49152 + (w & 3) * 4096 + ((lane >> 1) & 1) * 2048 + 64 * ((c16 >> 2) + 4 * (g4 & 1) + 16 * (g4 >> 1)) + 8 * (lane & 1);
#pragma unroll
      for (int ct = 0; ct < 3; ++ct) {
        const int c = min(cs0 + T0 + ct - rcb, 3) * 16;
        const v4u f = cat(trd(a0 + c), trd(a0 + c + 512));
        const u32 sf = WS_B4T + ((j * 16 + (g & 15)) * 24 + T0 + ct) * 1024;
        asm volatile("buffer_store_dwordx4 %0, %1, %2, %3 offen sc1\n\ts_nop 1" ::"v"(f), "v"(vl16), "s"(rWS), "s"(sf) : "memory");
      }
    }
    else {
      LAS char* a0 = L + L_Z + 65536 + w * 2048 + 32 * ((c16 >> 2) + 4 * (g4 & 1) + 16 * (g4 >> 1)) + 8 * (lane & 3);
#pragma unroll
      for (int i = 0; i < 2; ++i) {
        const v4u f = cat(trd(a0 + i * 1024), trd(a0 + i * 1024 + 256));
        const u32 sf = WS_B5T + ((j * 43 + g + (i && g < 11 ? 32 : 0)) * 4 + w) * 1024;
        asm volatile("buffer_store_dwordx4 %0, %1, %2, %3 offen sc1\n\ts_nop 1" ::"v"(f), "v"(vl16), "s"(rWS), "s"(sf) : "memory");
      }
      LAS char* a1 = L + 76800 + w * 1024 + 32 * ((c16 >> 2) + 4 * (g4 & 1) + 16 * (g4 >> 1)) + 8 * (lane & 3);
      const v4u f = cat(trd(a1), trd(a1 + 256));
      const u32 sf = WS_B3T + ((j * 16 + (g & 15)) * 4 + w) * 1024;
      asm volatile("buffer_store_dwordx4 %0, %1, %2, %3 offen sc1\n\ts_nop 1" ::"v"(f), "v"(vl16), "s"(rWS), "s"(sf) : "memory");
    }
  }
  BAR();
  STAMP(7);
  {
    int hw1 = (hw0 ^ 128) + 8192; PIN(hw1);
    sfor<0, 10>([&](auto I) {
      constexpr int i = decltype(I)::value;
      v4u o;
      o.x = pk2(xn[i][0] * ma0.xy + ms0.xy); o.y = pk2(xn[i][1] * ma0.zw + ms0.zw);
      o.z = pk2(xn[i][2] * ma1.xy + ms1.xy); o.w = pk2(xn[i][3] * ma1.zw + ms1.zw);
      if constexpr (i == 8) { hw0 += 65536; hw1 += 65536; }
      *(LAS v4u*)(L + ((i & 1) ? hw1 : hw0) + ((i >> 1) & 3) * 16384) = o;
      if constexpr (i < 6 && !(i & 1)) b1_piece(IC<5>{}, IC<L_Z + 49152>{}, IC<i / 2>{});
      if constexpr (i == 4) read_b1(3, tz0, 0);
      if constexpr (i == 8) read_b1(4, tz0, 24576);
      SCHED();
    });
  }
  VMW(0);
  BAR();
  RAISE(WS_RFLAG + (j * 32 + g) * 4);
  u32 flr;
  STAMP(8);
  __builtin_amdgcn_s_setprio(2);

  const f32x4 z4 = {0.f, 0.f, 0.f, 0.f};
#define AFRAG(off, ks) (*(LAS v4u*)(L + afo[(ks) & 3] + ((off) + ((ks) >> 2) * 256)))
  const int ch0 = cs0 + 3 * w;
  constexpr int ND = NODMA ? 0 : 1;
  v4u b4[48];
  const u32 sB4 = WS_B4T + (j * 16 * 24 + 3 * w) * 1024;
  auto load_b4 = [&](auto NC) {
    constexpr int n = decltype(NC)::value, ks = n / 3, ct = n % 3;
    const u32 so = sB4 + ((ks + 2 * rotp) & 15) * 24576;
    v4u& r = b4[n];
    if constexpr (ct == 0) LD16S(r, vl16, rWS, so, 0); else if constexpr (ct == 1) LD16S(r, vl16, rWS, so, 1024); else LD16S(r, vl16, rWS, so, 2048);
  };
  auto landed_b4 = [&](auto AC, auto BC) {
#pragma unroll
    for (int i = decltype(AC)::value; i < decltype(BC)::value; ++i) { v4u& r = b4[i]; PIN(r); }
  };
  v4u b3[16];
  auto load_b3 = [&](auto NC) { constexpr int n = decltype(NC)::value; ld16s<(n & 3) * 1024>(b3[n], vl16, rWS, WS_B3T + (j * 16 + 4 * kq + (n >> 2)) * 4096); };
  v4u gb_, gg_;
  u32 vgg, fl0, vRow;
  int pw, pr;
  f32x4 aKh = z4, aVh = z4, aQ = z4, aK = z4, aV = z4, bQ = z4, bK = z4, bV = z4;
  {
    v4u ah = AFRAG(0, 0), a1 = AFRAG(16384, 0), a2 = AFRAG(32768, 0);
    int l3, c3, g3; f32x2 mB, mG;
    auto job = [&](auto NC) {
      constexpr int n = decltype(NC)::value;
      if constexpr (n == 3) { l3 = opq(vl16) >> 4; c3 = l3 & 15; g3 = l3 >> 4; PIN(c3); PIN(g3); vgg = (vl16 < 512 ? 8192 : 20480 - 512) + 512 * j + vl16; PIN(vgg); }
      if constexpr (n == 7) { vRow = (8 * w + (l3 >> 3)) * 1024 + (l3 & 7) * 16; PIN(vRow); }
      if constexpr (n == 8) { pr = (8 * w + (l3 >> 3)) * 256 + (((2 * (l3 & 7)) ^ ((l3 >> 3) & 7)) << 4); PIN(pr); }
      if constexpr (n == 10) { pw = kq * 16384 + (32 * mh + c3) * 256 + ((g3 ^ (c3 & 3)) << 4) + ((c3 & 4) << 4); PIN(pw); }
      if constexpr (n == 31) { mB.x = asf(gb_.x) + one; PIN(mB.x); mB.y = asf(gb_.z) + one; PIN(mB.y); *(LAS f32x2*)(L + L_MODA + w * 512 + (vl16 >> 1)) = mB; }
      if constexpr (n == 32) { mG.x = asf(gg_.x); mG.y = asf(gg_.z); *(LAS f32x2*)(L + L_GAB + (vl16 >> 1)) = mG; }
      if constexpr (n == 34) { fl0 = (gb_.y ^ tag) | (gb_.w ^ tag) | (gg_.y ^ tag) | (gg_.w ^ tag); PIN(fl0); }
      if constexpr (n == 13) { const u32 s0 = WS_MODF + 12288 + w * 1024; LD16S(gb_, vl16, rWS, s0, 0); }
      if constexpr (n == 15) { const u32 s0 = WS_MODF; LD16S(gg_, vgg, rWS, s0, 0); }
      if constexpr (n < 12 && !(n & 1)) b1_piece(IC<6 + n / 6>{}, IC<L_Z + (n / 6) * 24576>{}, IC<(n / 2) % 3>{});
      if constexpr (n == 16) { const u32 vr = (vl16 >> 2) & 124, sr = WS_RFLAG + j * 128; LD4S(flr, vr, rWS, sr); }
      if constexpr (n == 22) {
        asm volatile("s_waitcnt vmcnt(0)" : "+v"(gb_), "+v"(gg_), "+v"(flr)::"memory");
        arrive(L_CNT + 12);
        const u32 vr = (vl16 >> 2) & 124, sr = WS_RFLAG + j * 128; u32 cnt = SPIN_CAP;
        asm volatile("1:\n\tv_cmp_ne_u32 vcc, %5, %0\n\ts_cbranch_vccz 2f\n\tbuffer_load_dword %0, %2, %3, %4 offen sc1\n\ts_waitcnt vmcnt(0)\n\t"
                     "s_sub_u32 %1, %1, 1\n\ts_cbranch_scc0 1b\n\t2:" : "+v"(flr), "+s"(cnt) : "v"(vr), "s"(rWS), "s"(sr), "s"(tag) : "vcc", "scc", "memory");
      }
      if constexpr (n == 18) { int tz2 = tz0 + 49152; PIN(tz2); read_b1(5, tz2, 0); }
      if constexpr (n == 33) { await8(L_CNT + 12); read_b1(6, tz0, 0); }
      if constexpr (n == 38) read_b1(7, tz0, 24576);
      if constexpr (n >= 23 && n < 47 && (n & 1)) load_b3(IC<(n - 23) / 2>{});
    };
    sfor<0, 16>([&](auto KS) {
      constexpr int ks = decltype(KS)::value;
      KSTAMP(1, ks);
      SCHED();
      v4u nh = ah, n1 = a1, n2 = a2;
      aKh = mfma32(bw[ks * 3 + 1], ah, aKh);
      aVh = mfma32(ah, bw[ks * 3 + 2], aVh);
      if constexpr (ks < 15) nh = AFRAG(0, ks + 1);
      job(IC<3 * ks>{});
      SCHED();
      aQ = mfma32(bw[ks * 3 + 0], a1, aQ);
      aK = mfma32(bw[ks * 3 + 1], a1, aK);
      aV = mfma32(a1, bw[ks * 3 + 2], aV);
      if constexpr (ks < 15) n1 = AFRAG(16384, ks + 1);
      job(IC<3 * ks + 1>{});
      SCHED();
      bQ = mfma32(bw[ks * 3 + 0], a2, bQ);
      bK = mfma32(bw[ks * 3 + 1], a2, bK);
      bV = mfma32(a2, bw[ks * 3 + 2], bV);
      if constexpr (ks < 15) n2 = AFRAG(32768, ks + 1);
      job(IC<3 * ks + 2>{});
      ah = nh; a1 = n1; a2 = n2;
      SCHED();
    });
  }
  __builtin_amdgcn_s_setprio(0);
  STAMP(9); KSTAMP(1, 16);
  {
    const u32 sOst = WS_O + ((g * 4 + 2 * blk2) * 16 + 2 * j + (hd >> 1)) * 1024;
    const v2u k1p = pk4(aK), v1p = pk4(aV);
    sfor<12, 14>(load_b3);
    v2u o0 = attend(pk4(aKh), pk4(aVh), k1p, v1p, pk4(aQ), am0, am1, ahm, g == 0 && blk2 == 0);
    PIN(o0);
    sfor<14, 16>(load_b3);
    const v2u o1 = attend(k1p, v1p, pk4(bK), pk4(bV), pk4(bQ), am0, am1, ahm, false);
    __builtin_amdgcn_raw_buffer_store_b64(o0, rWS, vOst, sOst, 16);
    __builtin_amdgcn_raw_buffer_store_b64(o1, rWS, vOst, sOst + 16384, 16);
  }
  if (__builtin_amdgcn_ballot_w64(fl0 != 0u)) {
    for (u32 sp = 0; sp < SPIN_CAP; ++sp) {
      asm volatile("" ::: "memory");
      gb_ = __builtin_amdgcn_raw_buffer_load_b128(rWS, vl16, WS_MODF + 12288 + w * 1024, 16);
      gg_ = __builtin_amdgcn_raw_buffer_load_b128(rWS, vgg, WS_MODF, 16);
      if (!__builtin_amdgcn_ballot_w64(((gb_.y ^ tag) | (gb_.w ^ tag) | (gg_.y ^ tag) | (gg_.w ^ tag)) != 0u)) break;
    }
    const f32x2 mB = {asf(gb_.x) + one, asf(gb_.z) + one}, mG = {asf(gg_.x), asf(gg_.z)};
    *(LAS f32x2*)(L + L_MODA + w * 512 + (vl16 >> 1)) = mB;
    *(LAS f32x2*)(L + L_GAB + (vl16 >> 1)) = mG;
  }
  STAMP(10);
  VMW(0);
  BAR();
  RAISE(flag_off(0, g, 0) + j * 128);
  STAMP(11);
  if (b == 0 && w == 7) {
    for (u32 sp = 0; sp < SPIN_CAP; ++sp) {
      asm volatile("" ::: "memory");
      const v4u v = __builtin_amdgcn_raw_buffer_load_b128(rWS, vl16, WS_RFLAG, 16);
      if (__all(v.x == tag && v.y == tag && v.z == tag && v.w == tag)) break;
    }
    __builtin_amdgcn_raw_buffer_store_b32(tag, rWS, 0, WS_EPOCH, 16);
  }

  v4u xv;
  { const u32 sx = g * 65536 + j * 128; LD16(xv, vRow, rX, sx, 0); }
  sfor<0, 5>(load_b4);
  const u64 care2 = 0x0101010101010101ull * (3u << (2 * kq));
  poll_flags(rWS, vfl, flag_off(0, g, 0), care2, tag);
  PIN(xv);
  v4u af[8];
  {
    const u32 so = WS_O + ((g * 4 + 2 * mh) * 16 + 4 * kq) * 1024, so1 = so + 16384;
    LD16S(af[0], vl16, rWS, so, 0); LD16S(af[1], vl16, rWS, so, 1024); LD16S(af[2], vl16, rWS, so, 2048); LD16S(af[3], vl16, rWS, so, 3072);
    LD16S(af[4], vl16, rWS, so1, 0); LD16S(af[5], vl16, rWS, so1, 1024); LD16S(af[6], vl16, rWS, so1, 2048); LD16S(af[7], vl16, rWS, so1, 3072);
  }
  STAMP(12);
  sfor<0, 2>([&](auto M) {
    constexpr int m = decltype(M)::value;
    asm volatile("s_waitcnt vmcnt(%c4)" : "+v"(af[4 * m]), "+v"(af[4 * m + 1]), "+v"(af[4 * m + 2]), "+v"(af[4 * m + 3]) : "i"(m == 0 ? 4 : 2) : "memory");
    if constexpr (m == 0) {
#pragma unroll
      for (int i = 0; i < 16; ++i) PIN(b3[i]);
    }
    f32x4 acc[4] = {z4, z4, z4, z4};
    sfor<0, 4>([&](auto SC) {
      constexpr int sx = decltype(SC)::value;
#pragma unroll
      for (int n = 0; n < 4; ++n) acc[n] = mfma32(b3[sx * 4 + n], af[4 * m + sx], acc[n]);
      if constexpr (sx & 1) load_b4(IC<5 + 2 * m + (sx >> 1)>{});
      SCHED();
    });
#pragma unroll
    for (int n = 0; n < 4; ++n) *(LAS f32x4*)(L + ((n & 1) ? (pw ^ 64) : pw) + (m * 4096 + (n >> 1) * 128)) = acc[n];
  });
  BAR();
  STAMP(13);
  sfor<9, 12>(load_b4);
  v4u x1p;
  {
    f32x4 y0 = *(LAS f32x4*)(L + pr), y1 = *(LAS f32x4*)(L + (pr ^ 16));
#pragma unroll
    for (int q = 1; q < 4; ++q) { y0 += *(LAS f32x4*)(L + pr + q * 16384); y1 += *(LAS f32x4*)(L + (pr ^ 16) + q * 16384); }
    const int ga = L_GAB + ((vl16 & 112) << 1);
    const f32x4 g0 = *(LAS f32x4*)(L + ga), g1 = *(LAS f32x4*)(L + ga + 16);
    const f32x4 x0 = f32x4{blo(xv.x), bhi(xv.x), blo(xv.y), bhi(xv.y)} + g0 * y0, x1 = f32x4{blo(xv.z), bhi(xv.z), blo(xv.w), bhi(xv.w)} + g1 * y1;
    { const v4u o = cat(pk4(x0), pk4(x1)); x1p = o; const u32 so = WS_X1 + g * 65536 + j * 128;
      asm volatile("buffer_store_dwordx4 %0, %1, %2, %3 offen sc1\n\ts_nop 1" ::"v"(o), "v"(vRow), "s"(rWS), "s"(so) : "memory"); }
    const f32x4 sv = x0 + x1;
    float mean = (sv.x + sv.y) + (sv.z + sv.w);
    mean += dppf<0xB1>(mean); mean += dppf<0x4E>(mean); mean += dppf<0x141>(mean);
    mean *= 1.f / 64.f;
    const f32x4 d0 = x0 - mean, d1 = x1 - mean, dd = d0 * d0 + d1 * d1;
    float m2 = (dd.x + dd.y) + (dd.z + dd.w);
    m2 += dppf<0xB1>(m2); m2 += dppf<0x4E>(m2); m2 += dppf<0x141>(m2);
    v2u st; st.x = asu(mean); st.y = asu(m2);
    __builtin_amdgcn_raw_buffer_store_b64(st, rWS, (vRow >> 10) << 6, WS_STAT + g * 4096 + j * 8, 16);
  }
  STAMP(14);
  VMW(0);
  BAR();
  RAISE(flag_off(1, g, 0) + j * 128);
  sfor<12, 17>(load_b4);
  STAMP(15);

  u32 hmask[3];
#pragma unroll
  for (int ct = 0; ct < 3; ++ct)
    asm("s_cmp_lt_u32 %1, %2\n\ts_cselect_b32 %0, -1, 0" : "=s"(hmask[ct]) : "s"((u32)(ch0 + ct)), "s"((u32)cend) : "scc");
#pragma unroll
  for (int i = 0; i < 4; ++i) { afo[i] -= blk2 * 32768; PIN(afo[i]); }
  int vm2 = L_MODA + 2 * vl16; PIN(vm2);
  int h2w = w * 8192 + (((vl16 - 128 * rotp) & 1023) ^ ((8 * w & 15) << 4)); PIN(h2w);
  const f32x4 ls0 = *(LAS f32x4*)(L + vm2), ls1 = *(LAS f32x4*)(L + vm2 + 16), la0 = *(LAS f32x4*)(L + vm2 + 2048), la1 = *(LAS f32x4*)(L + vm2 + 2064);
  poll_flags(rWS, vfl, flag_off(1, g, 0), ~0ull, tag);
  landed_b4(IC<0>{}, IC<17>{});
  {
    v4u x1v[8]; v2u stv;
    const u32 so = WS_X1 + g * 65536 + w * 8192, so2 = so + 4096;
    { const u32 vs = vl16 >> 1, ss = WS_STAT + g * 4096 + w * 512; LD8S(stv, vs, rWS, ss, 0); }
    LD16S(x1v[0], vl16, rWS, so, 0); LD16S(x1v[1], vl16, rWS, so, 1024); LD16S(x1v[2], vl16, rWS, so, 2048); LD16S(x1v[3], vl16, rWS, so, 3072);
    LD16S(x1v[4], vl16, rWS, so2, 0); LD16S(x1v[5], vl16, rWS, so2, 1024); LD16S(x1v[6], vl16, rWS, so2, 2048); LD16S(x1v[7], vl16, rWS, so2, 3072);
    asm volatile("s_waitcnt vmcnt(8)" : "+v"(stv)::"memory");
    const float mj = asf(stv.x);
    float mean = mj;
    mean += dppf<0xB1>(mean); mean += dppf<0x4E>(mean); mean += dppf<0x141>(mean);
    mean *= 0.125f;
    const float dm = mj - mean;
    float m2 = asf(stv.y) + 64.f * dm * dm;
    m2 += dppf<0xB1>(m2); m2 += dppf<0x4E>(m2); m2 += dppf<0x141>(m2);
    const float l2r = __builtin_amdgcn_rsqf(m2 * (1.f / 512.f) + 1e-5f), l2n = -mean * l2r;
    sfor<0, 8>([&](auto I) {
      constexpr int i = decltype(I)::value;
      v4u& xx = x1v[i];
      asm volatile("s_waitcnt vmcnt(%c1)" : "+v"(xx) : "i"(i == 0 ? 7 : 6) : "memory");
      if constexpr (i >= 1) load_b4(IC<16 + i>{});
      const float rs = rdl(l2r, 8 * i), nn = rdl(l2n, 8 * i);
      v4u o;
      o.x = pk2((unp(xx.x) * rs + nn) * la0.xy + ls0.xy); o.y = pk2((unp(xx.y) * rs + nn) * la0.zw + ls0.zw);
      o.z = pk2((unp(xx.z) * rs + nn) * la1.xy + ls1.xy); o.w = pk2((unp(xx.w) * rs + nn) * la1.zw + ls1.zw);
      *(LAS v4u*)(L + (h2w ^ (i << 4)) + i * 1024) = o;
    });
  }
  BAR();
  STAMP(16);
  __builtin_amdgcn_s_setprio(2);
  const int kb5 = 5 * w + min(w, 3);
  u64 care5 = 0x0101010101010101ull * ((0x80c06030180c0603ull >> (8 * w)) & 255u);
  asm volatile("" : "+s"(care5));
  v4u b5[24];
  const u32 sB5 = WS_B5T + (j * 43 + kb5) * 4096, d5 = (w + 5) >> 3;
  auto load_b5 = [&](auto NC) {
    constexpr int n = decltype(NC)::value, sx = n / 4, t = n % 4;
    ld16s<t * 1024>(b5[n], vl16, rWS, sB5 + (sx == 5 ? (5 - d5) * 4096 : sx * 4096));
  };
  f32x4 acc4[3][4];
#pragma unroll
  for (int ct = 0; ct < 3; ++ct)
#pragma unroll
    for (int m = 0; m < 4; ++m) acc4[ct][m] = z4;
  u32 vH; int pw5;
  {
    v4u a[4] = {AFRAG(0, 0), AFRAG(16384, 0), AFRAG(32768, 0), AFRAG(49152, 0)};
    sfor<0, 16>([&](auto KS) {
      constexpr int ks = decltype(KS)::value;
      KSTAMP(3, ks);
      if constexpr (ks == 5) { VMW(10); landed_b4(IC<17>{}, IC<24>{}); }
      if constexpr (ks >= 8) { asm volatile("s_waitcnt vmcnt(%c0)" ::"i"(21 - ks) : "memory"); landed_b4(IC<3 * ks>{}, IC<3 * ks + 3>{}); }
      v4u nx[4] = {a[0], a[1], a[2], a[3]};
      sfor<0, 4>([&](auto MC) {
        constexpr int m = decltype(MC)::value;
        if constexpr (ks < 15) nx[m] = AFRAG(16384 * m, ks + 1);
        if constexpr (!(m & 1)) { if constexpr (ks < 12) load_b4(IC<24 + 2 * ks + m / 2>{}); else load_b5(IC<2 * (ks - 12) + m / 2>{}); }
        SCHED();
#pragma unroll
        for (int ct = 0; ct < 3; ++ct) acc4[ct][m] = mfma32(b4[ks * 3 + ct], a[m], acc4[ct][m]);
        if constexpr (ks == 8 && m == 0) { vH = (vl16 & 255) + ((vl16 >> 5) & 8) + (vl16 >> 9) * (172 * 256); PIN(vH); }
        if constexpr (ks == 9 && m == 0) { const int l5 = opq(vl16) >> 4, c5 = l5 & 15, g5 = l5 >> 4;
          pw5 = w * 16384 + c5 * 256 + ((c5 & 4) << 4) + ((g5 ^ (c5 & 3)) << 4); PIN(pw5); }
        SCHED();
      });
#pragma unroll
      for (int m = 0; m < 4; ++m) a[m] = nx[m];
    });
  }
#pragma unroll
  for (int ct = 0; ct < 3; ++ct)
#pragma unroll
    for (int m = 0; m < 4; ++m) PIN(acc4[ct][m]);
  __builtin_amdgcn_s_setprio(0);
  STAMP(17); KSTAMP(3, 16);
  sfor<0, 6>([&](auto UC) {
      constexpr int ct = decltype(UC)::value >> 1, mp = decltype(UC)::value & 1;
      load_b5(IC<8 + 2 * decltype(UC)::value>{}); load_b5(IC<9 + 2 * decltype(UC)::value>{});
      const u32 vHH = vH, hm = hmask[ct]; const rsrc_t rr = rWS;
      f32x4 G, U, R;
#pragma unroll
      for (int e = 0; e < 4; ++e) {
        auto r = __builtin_amdgcn_permlane32_swap(asu(acc4[ct][2 * mp][e]), asu(acc4[ct][2 * mp + 1][e]), false, false);
        G[e] = asf(r[0]); U[e] = asf(r[1]);
      }
#pragma unroll
      for (int e = 0; e < 4; ++e) R[e] = qp(qp(G[e] * U[e]) * __builtin_amdgcn_rcpf(1.f + __builtin_amdgcn_exp2f(qp(G[e] * -1.4426950408889634f))));
      const v2u o = pk4(R);
      const u32 so = WS_HH + ((g * 4 + 2 * mp) * 172 + ch0 + ct) * 256;
      asm volatile("s_mov_b32 exec_lo, %4\n\ts_mov_b32 exec_hi, %4\n\tbuffer_store_dwordx2 %0, %1, %2, %3 offen sc1\n\ts_mov_b64 exec, -1"
                   ::"v"(o), "v"(vHH), "s"(rr), "s"(so), "s"(hm) : "memory");
  });
  STAMP(18);
  VMW(0);
  BAR();
  RAISE(flag_off(2, g, 0) + j * 128);
  STAMP(19);
  sfor<20, 24>(load_b5);
  poll_flags(rWS, vfl, flag_off(2, g, 0), care5, tag);
#pragma unroll
  for (int i = 0; i < 24; ++i) PIN(b5[i]);
  v4u hf[16];
  auto load_h = [&](auto NC) {
    constexpr int n = decltype(NC)::value, sx = n / 4, m = n % 4;
    ld16s<0>(hf[n & 15], vl16, rWS, WS_HH + ((g * 4 + m) * 172 + 4 * kb5) * 256 + (sx == 5 ? (5 - d5) * 1024 : sx * 1024));
  };
  sfor<0, 16>(load_h);
  STAMP(20);
  {
    f32x4 acc[4][4];
#pragma unroll
    for (int m = 0; m < 4; ++m)
#pragma unroll
      for (int n = 0; n < 4; ++n) acc[m][n] = z4;
    sfor<0, 6>([&](auto SC) {
      constexpr int sx = decltype(SC)::value, r = (sx & 3) * 4;
      constexpr int nw = sx < 3 ? 12 : sx == 3 ? 8 : sx == 4 ? 4 : 0;
      asm volatile("s_waitcnt vmcnt(%c4)" : "+v"(hf[r]), "+v"(hf[r + 1]), "+v"(hf[r + 2]), "+v"(hf[r + 3]) : "i"(nw) : "memory");
      if constexpr (sx == 5) { if (w >= 3) { hf[r] = v4u{0u, 0u, 0u, 0u}; hf[r + 1] = v4u{0u, 0u, 0u, 0u}; hf[r + 2] = v4u{0u, 0u, 0u, 0u}; hf[r + 3] = v4u{0u, 0u, 0u, 0u}; } }
      sfor<0, 4>([&](auto MC) {
        constexpr int m = decltype(MC)::value;
#pragma unroll
        for (int n = 0; n < 4; ++n) acc[m][n] = mfma32(b5[sx * 4 + n], hf[r + m], acc[m][n]);
        SCHED();
        if constexpr (sx < 2) load_h(IC<16 + 4 * sx + m>{});
        SCHED();
      });
    });
#pragma unroll
    for (int m = 0; m < 4; ++m)
#pragma unroll
      for (int n = 0; n < 4; ++n) *(LAS f32x4*)(L + ((n & 1) ? (pw5 ^ 64) : pw5) + (m * 4096 + (n >> 1) * 128)) = acc[m][n];
  }
  BAR();
  STAMP(21);
  {
    const int l6 = opq(vl16) >> 4;
    const int p0 = opq((8 * w + (l6 >> 3)) * 256 + (((2 * (l6 & 7)) ^ ((l6 >> 3) & 7)) << 4)), p1 = opq(p0 + 65536);
    f32x4 y0 = *(LAS f32x4*)(L + p0), y1 = *(LAS f32x4*)(L + (p0 ^ 16));
#pragma unroll
    for (int q = 1; q < 8; ++q) { const int pb = q < 4 ? p0 : p1; y0 += *(LAS f32x4*)(L + pb + (q & 3) * 16384); y1 += *(LAS f32x4*)(L + (pb ^ 16) + (q & 3) * 16384); }
    const int gb = L_GAB + 256 + (l6 & 7) * 32;
    const f32x4 g0 = *(LAS f32x4*)(L + gb), g1 = *(LAS f32x4*)(L + gb + 16);
    const f32x4 o0 = f32x4{blo(x1p.x), bhi(x1p.x), blo(x1p.y), bhi(x1p.y)} + g0 * y0, o1 = f32x4{blo(x1p.z), bhi(x1p.z), blo(x1p.w), bhi(x1p.w)} + g1 * y1;
    const v4u o = cat(pk4(o0), pk4(o1));
    const u32 vo = (8 * w + (l6 >> 3)) * 1024 + (l6 & 7) * 16, so = g * 65536 + j * 128;
    asm volatile("buffer_store_dwordx4 %0, %1, %2, %3 offen sc1\n\ts_nop 1" ::"v"(o), "v"(vo), "s"(rOUT), "s"(so) : "memory");
  }
  STAMP(22);
  STAMP_DUMP();
}
}

extern "C" void kernel_launch(void* const* d_in, const int* in_sizes, int n_in, void* d_out, int out_size, void* d_ws, size_t ws_size,
                              hipStream_t stream) {
  Params p;
  p.x = (const u16*)d_in[0]; p.wqkv = (const u16*)d_in[1]; p.wo = (const u16*)d_in[2];
  p.w1 = (const u16*)d_in[4]; p.w3 = (const u16*)d_in[5]; p.w2 = (const u16*)d_in[6];
  p.c = (const u16*)d_in[7]; p.wmod = (const u16*)d_in[8];
  p.out = (u16*)d_out; p.ws = (char*)d_ws;
  (void)hipFuncSetAttribute((const void*)fused_block, hipFuncAttributeMaxDynamicSharedMemorySize, L_TOTAL);
  fused_block<<<256, 512, L_TOTAL, stream>>>(p);
}
```

```cpp
#include <hip/hip_runtime.h>
#include <stdint.h>

#define KDBG 0
#define NODMA 0
#define CODEWARM 0
#define L2WARM 1
#define EARLYT 0
#define XMAP 2

#define DI __device__ __forceinline__
#define GAS __attribute__((address_space(1)))
#define LAS __attribute__((address_space(3)))
typedef unsigned u32;
typedef unsigned short u16;
typedef unsigned long long u64;
typedef u32 v4u __attribute__((ext_vector_type(4)));
typedef u32 v2u __attribute__((ext_vector_type(2)));
typedef float f32x4 __attribute__((ext_vector_type(4)));
typedef float f32x2 __attribute__((ext_vector_type(2)));
typedef __bf16 bf16x8 __attribute__((ext_vector_type(8)));
typedef __bf16 bf16x2 __attribute__((ext_vector_type(2)));
typedef short s16x4 __attribute__((ext_vector_type(4)));
typedef __amdgpu_buffer_rsrc_t rsrc_t;
typedef GAS u32 gu32;
typedef GAS u64 gu64;
#define RLX_AGENT __ATOMIC_RELAXED, __HIP_MEMORY_SCOPE_AGENT

namespace {
constexpr int S_ = 2048, D_ = 512, F_ = 1376;
constexpr u32 WS_EPOCH = 0;
constexpr u32 WS_FLAG = 32768;
constexpr u32 WS_STAT = 262144;
constexpr u32 WS_MODG = 524288;
constexpr u32 WS_MODF = 655360;
constexpr u32 WS_O = 1u << 20;
constexpr u32 WS_X1 = 3u << 20;
constexpr u32 WS_HH = 5u << 20;
constexpr u32 WS_RFLAG = 8192;
constexpr u32 WS_B4T = 11u << 20;
constexpr u32 WS_B5T = 14u << 20;
constexpr u32 WS_B3T = (15u << 20) + (512u << 10);
constexpr u32 WS_DBG = 16u << 20;
constexpr u32 WS_BYTES = 32u << 20;
constexpr int L_XY = 0;
constexpr int L_Z = 81920;
constexpr int L_MISC = 158720;
constexpr int L_TOTAL = 163840;
constexpr int L_RED = 73728;
constexpr int L_MODA = L_MISC;
constexpr int L_CNT = L_MISC + 4096;
constexpr int L_GAB = L_MISC + 4096 + 64;
constexpr int L_JUNK = L_MISC + 4096 + 64 + 512;
constexpr int L_STAMP = 156672;
constexpr u32 SPIN_CAP = 1u << 18;
constexpr u32 CODE_BYTES = 26112;

DI f32x4 mfma32(v4u a, v4u b, f32x4 c) {
  return __builtin_amdgcn_mfma_f32_16x16x32_bf16(__builtin_bit_cast(bf16x8, a), __builtin_bit_cast(bf16x8, b), c, 0, 0, 0);
}
DI f32x4 mfma16(v2u a, v2u b, f32x4 c) {
  return __builtin_amdgcn_mfma_f32_16x16x16bf16_1k(__builtin_bit_cast(s16x4, a), __builtin_bit_cast(s16x4, b), c, 0, 0, 0);
}
DI u32 pk(float lo, float hi) { f32x2 v = {lo, hi}; return __builtin_bit_cast(u32, __builtin_convertvector(v, bf16x2)); }
DI u32 pk2(f32x2 v) { return __builtin_bit_cast(u32, __builtin_convertvector(v, bf16x2)); }
DI v2u pk4(f32x4 v) { v2u r; r.x = pk(v.x, v.y); r.y = pk(v.z, v.w); return r; }
DI float asf(u32 u) { return __builtin_bit_cast(float, u); }
DI u32 asu(float f) { return __builtin_bit_cast(u32, f); }
DI float blo(u32 u) { return asf(u << 16); }
DI float bhi(u32 u) { return asf(u & 0xffff0000u); }
DI f32x2 unp(u32 u) { f32x2 r = {blo(u), bhi(u)}; return r; }
DI float dot2(u32 a, u32 b, float c) { return __builtin_amdgcn_fdot2_f32_bf16(__builtin_bit_cast(bf16x2, a), __builtin_bit_cast(bf16x2, b), c, false); }
DI v2u trd(LAS char* p) { return __builtin_bit_cast(v2u, __builtin_amdgcn_ds_read_tr16_b64_v4i16((LAS s16x4*)p)); }
DI v4u cat(v2u a, v2u b) { v4u r; r.x = a.x; r.y = a.y; r.z = b.x; r.w = b.y; return r; }
template <int MI, int SI> DI void dma(u32 mb, u32 voff, rsrc_t srd, u32 sb) {
  u32 t;
  asm volatile("s_add_u32 m0, %1, %c2\n\ts_add_u32 %0, %3, %c4\n\tbuffer_load_dwordx4 %5, %6, %0 offen lds"
               : "=&s"(t) : "s"(mb), "i"(MI), "s"(sb), "i"(SI), "v"(voff), "s"(srd) : "scc");
}
DI void touch(u32 voff, rsrc_t srd, u32 sb) {
  asm volatile("s_mov_b32 m0, %c0\n\ts_nop 0\n\tbuffer_load_dword %1, %2, %3 offen lds" ::"i"(L_JUNK), "v"(voff), "s"(srd), "s"(sb));
}
#define BAR() asm volatile("s_waitcnt lgkmcnt(0)\n\ts_barrier" ::: "memory")
#define VMW(n) asm volatile("s_waitcnt vmcnt(" #n ")" ::: "memory")
#define LGW() asm volatile("s_waitcnt lgkmcnt(0)" ::: "memory")
#define SCHED() __builtin_amdgcn_sched_barrier(0)
#define LD16(dst, voff, srd, soff, IMM) asm volatile("buffer_load_dwordx4 %0, %1, %2, %3 offen offset:" #IMM : "=v"(dst) : "v"(voff), "s"(srd), "s"(soff))
#define LD16S(dst, voff, srd, soff, IMM) asm volatile("buffer_load_dwordx4 %0, %1, %2, %3 offen offset:" #IMM " sc1" : "=v"(dst) : "v"(voff), "s"(srd), "s"(soff))
#define LD8(dst, voff, srd, soff, IMM) asm volatile("buffer_load_dwordx2 %0, %1, %2, %3 offen offset:" #IMM : "=v"(dst) : "v"(voff), "s"(srd), "s"(soff))
#define LD8S(dst, voff, srd, soff, IMM) asm volatile("buffer_load_dwordx2 %0, %1, %2, %3 offen offset:" #IMM " sc1" : "=v"(dst) : "v"(voff), "s"(srd), "s"(soff))
#define LD4(dst, voff, srd, soff) asm volatile("buffer_load_dword %0, %1, %2, %3 offen" : "=v"(dst) : "v"(voff), "s"(srd), "s"(soff))
#define LD4S(dst, voff, srd, soff) asm volatile("buffer_load_dword %0, %1, %2, %3 offen sc1" : "=v"(dst) : "v"(voff), "s"(srd), "s"(soff))

template <int IMM> DI void ld16s(v4u& r, u32 v, rsrc_t rs, u32 so) { asm volatile("buffer_load_dwordx4 %0, %1, %2, %3 offen offset:%c4 sc1" : "=v"(r) : "v"(v), "s"(rs), "s"(so), "i"(IMM)); }
template <int CTRL> DI float dppf(float v) { return asf(__builtin_amdgcn_update_dpp(0, asu(v), CTRL, 0xf, 0xf, true)); }
DI float row16_sum(float v) { v += dppf<0x128>(v); v += dppf<0x124>(v); v += dppf<0x122>(v); v += dppf<0x121>(v); return v; }
DI float sw32_add(float a, float b) { auto r = __builtin_amdgcn_permlane32_swap(asu(a), asu(b), false, false); return asf(r[0]) + asf(r[1]); }
DI float sw16_add(float a, float b) { auto r = __builtin_amdgcn_permlane16_swap(asu(a), asu(b), false, false); return asf(r[0]) + asf(r[1]); }
DI float x16_max(float v) { auto r = __builtin_amdgcn_permlane16_swap(asu(v), asu(v), false, false); return fmaxf(asf(r[0]), asf(r[1])); }
DI float x32_max(float v) { auto r = __builtin_amdgcn_permlane32_swap(asu(v), asu(v), false, false); return fmaxf(asf(r[0]), asf(r[1])); }
DI int fresh_lane() { int l; asm volatile("v_mbcnt_lo_u32_b32 %0, -1, 0\n\tv_mbcnt_hi_u32_b32 %0, -1, %0" : "=v"(l)); return l; }
DI float rdl(float v, int l) { return asf(__builtin_amdgcn_readlane(asu(v), l)); }

struct Params {
  const u16 *x, *wqkv, *wo, *w1, *w3, *w2, *c, *wmod;
  u16* out;
  char* ws;
};

#define LOOPSEL 0
#define STAMP(i) do {} while (0)
#define KSTAMP(lp, i) do {} while (0)
#define STAMP_DUMP() do {} while (0)

DI u32 flag_off(int phase, int g, int h) { return WS_FLAG + ((phase * 32 + g) * 2 + h) * 1024; }
DI void poll_flags(rsrc_t rWS, u32 vfl, u32 foff, u64 care, u32 tag) {
  for (u32 sp = 0; sp < SPIN_CAP; ++sp) {
    asm volatile("" ::: "memory");
    const u32 v = __builtin_amdgcn_raw_buffer_load_b32(rWS, vfl, foff, 16);
    if (!(__builtin_amdgcn_ballot_w64(v != tag) & care)) break;
  }
  VMW(0);
}
DI void raise_flag(rsrc_t rWS, u32 foff, u32 vtag, u32 vzero, u32 m1) {
  asm volatile("s_mov_b32 exec_hi, 0\n\ts_mov_b32 exec_lo, %4\n\tbuffer_store_dword %0, %1, %2, %3 offen sc1\n\ts_mov_b64 exec, -1" ::"v"(vtag), "v"(vzero), "s"(rWS), "s"(foff), "s"(m1) : "memory");
}
DI void signal_flag(rsrc_t rWS, u32 cnt_addr, u32 foff, u32 tag) {
  u32 t, a = cnt_addr, one = 1u, vt = tag, vz = 0u;
  asm volatile("s_mov_b64 exec, 1\n\tds_add_rtn_u32 %0, %1, %2\n\ts_waitcnt lgkmcnt(0)\n\tv_cmp_eq_u32 vcc, 7, %0\n\ts_and_b64 exec, exec, vcc\n\t"
               "buffer_store_dword %3, %4, %5, %6 offen sc1\n\ts_mov_b64 exec, -1"
               : "=&v"(t) : "v"(a), "v"(one), "v"(vt), "v"(vz), "s"(rWS), "s"(foff) : "vcc", "scc", "memory");
}
DI void arrive(u32 cnt_addr) {
  u32 a = cnt_addr, one = 1u;
  asm volatile("s_waitcnt lgkmcnt(0)\n\ts_mov_b64 exec, 1\n\tds_add_u32 %0, %1\n\ts_mov_b64 exec, -1" ::"v"(a), "v"(one) : "memory");
}
DI void await8(u32 cnt_addr) {
  u32 a = cnt_addr, t, sv;
  asm volatile("1:\n\tds_read_b32 %0, %2\n\ts_waitcnt lgkmcnt(0)\n\tv_readfirstlane_b32 %1, %0\n\ts_nop 0\n\ts_cmp_lt_u32 %1, 8\n\ts_cbranch_scc1 1b"
               : "=&v"(t), "=&s"(sv) : "v"(a) : "scc", "memory");
}
DI void raise_when8(rsrc_t rWS, u32 cnt_addr, u32 foff, u32 tag, u32 w7) {
  u32 a = cnt_addr, t, sv, vt = tag, vz = 0u;
  asm volatile("s_cmp_eq_u32 %7, 0\n\ts_cbranch_scc1 2f\n\t1:\n\tds_read_b32 %0, %2\n\ts_waitcnt lgkmcnt(0)\n\tv_readfirstlane_b32 %1, %0\n\ts_nop 0\n\ts_cmp_lt_u32 %1, 8\n\ts_cbranch_scc1 1b\n\t"
               "s_mov_b64 exec, 1\n\tbuffer_store_dword %3, %4, %5, %6 offen sc1\n\ts_mov_b64 exec, -1\n\t2:"
               : "=&v"(t), "=&s"(sv) : "v"(a), "v"(vt), "v"(vz), "s"(rWS), "s"(foff), "s"(w7) : "scc", "memory");
}
#define PIN(x) asm volatile("" : "+v"(x))
DI float qp(float r) { PIN(r); return r; }
DI float q_shl(u32 a) { return qp(blo(a)); }
DI float q_hi(u32 a) { return qp(bhi(a)); }
DI float q_fma(float a, float b, float c) { return qp(__builtin_fmaf(a, b, c)); }
DI float q_fmas(float a, float b, float c) { return qp(__builtin_fmaf(a, b, c)); }
DI float q_mul(float a, float b) { return qp(a * b); }
DI float q_mulk(float a) { return qp(a * -1.4426950408889634f); }
DI float q_add1(float a) { return qp(a + 1.f); }
DI float q_exp(float a) { return qp(__builtin_amdgcn_exp2f(a)); }
DI float q_rcp(float a) { return qp(__builtin_amdgcn_rcpf(a)); }
DI u32 q_cvt(float a, float b) { u32 r = pk(a, b); PIN(r); return r; }
DI float q_mov(float a) { return qp(a); }
DI u32 opq(u32 v) { asm volatile("" : "+v"(v)); return v; }

DI float sel(u64 m, float a, float b) { return __builtin_amdgcn_inverse_ballot_w64(m) ? a : b; }
DI v2u attend(v2u kp, v2u vp, v2u kc, v2u vc, v2u q, u64 m0, u64 m1, u32 hm, bool noprev) {
  const f32x4 z = {0.f, 0.f, 0.f, 0.f};
  const f32x4 sp = mfma16(kp, q, z);
  const f32x4 sc = mfma16(kc, q, z);
  const float K2 = 0.25f * 1.4426950408889634f;
  const u64 par = 0xaaaaaaaaaaaaaaaaull;
  float p0 = sel(par, sp[1], sp[0]), p1 = sel(par, sp[3], sp[2]);
  if (noprev) { p0 = -INFINITY; p1 = -INFINITY; }
  const float t0 = sel(m0, sel(par, sc[1], sc[0]), p0), t1 = sel(m1, sel(par, sc[3], sc[2]), p1);
  const float m = x32_max(x16_max(fmaxf(t0, t1)));
  const float mk = -m * K2;
  const float e0 = __builtin_amdgcn_exp2f(__builtin_fmaf(t0, K2, mk)), e1 = __builtin_amdgcn_exp2f(__builtin_fmaf(t1, K2, mk));
  const u32 w0 = pk(e0, e0) & hm, w1 = pk(e1, e1) & hm;
  v4u pf;
  pf.x = __builtin_amdgcn_inverse_ballot_w64(m0) ? 0u : w0; pf.y = __builtin_amdgcn_inverse_ballot_w64(m1) ? 0u : w1;
  pf.z = __builtin_amdgcn_inverse_ballot_w64(m0) ? w0 : 0u; pf.w = __builtin_amdgcn_inverse_ballot_w64(m1) ? w1 : 0u;
  const f32x4 o = mfma32(cat(vp, vc), pf, z);
  float sum = e0 + e1;
  sum = sw16_add(sum, sum); sum = sw32_add(sum, sum);
  const float inv = __builtin_amdgcn_rcpf(sum);
  return pk4(f32x4{qp(o.x * inv), qp(o.y * inv), qp(o.z * inv), qp(o.w * inv)});
}

template <int N> using IC = std::integral_constant<int, N>;
template <int I, int N, class F> DI void sfor(F&& f) { if constexpr (I < N) { f(IC<I>{}); sfor<I + 1, N>(f); } }

__global__ void __launch_bounds__(512, 2) fused_block(Params p) {
  extern __shared__ __attribute__((aligned(16))) char smem[];
  LAS char* L = (LAS char*)smem;
  const int tid = threadIdx.x, lane = tid & 63, w = __builtin_amdgcn_readfirstlane(tid >> 6);
  const int c16 = lane & 15, g4 = lane >> 4;
  const int b = blockIdx.x;
  int g, j;
  if (XMAP == 0) { g = (b & 7) * 4 + ((b >> 3) & 3); j = b >> 5; }
  else if (XMAP == 2) { const int x = b & 7, r = b >> 3; g = (x >> 1) * 8 + (r & 7); j = (x & 1) * 4 + (r >> 3); }
  else { const int x = b & 7, r = b >> 3; g = (x >> 2) * 16 + (r & 15); j = (x & 3) * 2 + (r >> 4); }
  const rsrc_t rX = __builtin_amdgcn_make_buffer_rsrc((void*)p.x, 0, S_ * D_ * 2, 0x00020000);
  const rsrc_t rQKV = __builtin_amdgcn_make_buffer_rsrc((void*)p.wqkv, 0, D_ * 3 * D_ * 2, 0x00020000);
  const rsrc_t rWO = __builtin_amdgcn_make_buffer_rsrc((void*)p.wo, 0, D_ * D_ * 2, 0x00020000);
  const rsrc_t rW1 = __builtin_amdgcn_make_buffer_rsrc((void*)p.w1, 0, D_ * F_ * 2, 0x00020000);
  const rsrc_t rW3 = __builtin_amdgcn_make_buffer_rsrc((void*)p.w3, 0, D_ * F_ * 2, 0x00020000);
  const rsrc_t rW2 = __builtin_amdgcn_make_buffer_rsrc((void*)p.w2, 0, F_ * D_ * 2, 0x00020000);
  const rsrc_t rC = __builtin_amdgcn_make_buffer_rsrc((void*)p.c, 0, D_ * 2, 0x00020000);
  const rsrc_t rMOD = __builtin_amdgcn_make_buffer_rsrc((void*)p.wmod, 0, D_ * 6 * D_ * 2, 0x00020000);
  const rsrc_t rWS = __builtin_amdgcn_make_buffer_rsrc((void*)p.ws, 0, WS_BYTES, 0x00020000);
  const rsrc_t rOUT = __builtin_amdgcn_make_buffer_rsrc((void*)p.out, 0, S_ * D_ * 2, 0x00020000);
  STAMP(0);

  u32 ep;
  { const u32 vz = 0; asm volatile("buffer_load_dword %0, %1, %2, 0 offen sc1" : "=v"(ep) : "v"(vz), "s"(rWS)); }
  u32 vl16 = lane * 16; PIN(vl16);
  const int bm = b < 192 ? b : b - 192, mq = bm / 48, cl = bm - 48 * mq;
  v4u wm[4]; v2u cv; u32 ep0;
  if (w < 4) {
    const u32 vm = (tid >> 3) * 24576 + (tid & 7) * 16, s0 = mq * (128 * 6144) + cl * 128, s1 = s0 + 6144, s2 = s0 + 12288, s3 = s0 + 18432;
    LD16(wm[0], vm, rMOD, s0, 0); LD16(wm[1], vm, rMOD, s1, 0); LD16(wm[2], vm, rMOD, s2, 0); LD16(wm[3], vm, rMOD, s3, 0);
    const u32 vc = (tid >> 3) * 8, scq = mq * 256;
    LD8(cv, vc, rC, scq, 0);
    const u32 vz = 0; asm volatile("buffer_load_dword %0, %1, %2, 0 offen sc1" : "=v"(ep0) : "v"(vz), "s"(rWS));
  }
  if (tid < 16) *(LAS u32*)(L + L_CNT + tid * 4) = 0u;
  BAR();
  const int st_qd = lane >> 2, st_r = (st_qd & 3) + 4 * (st_qd >> 3);
  const int st_col = ((st_qd >> 2) & 1) * 64 + (((lane & 3) ^ ((w & 1) << 1)) << 4);
  const int rotp = g & 7;
  u32 vB1 = st_r * 3072 + st_col; PIN(vB1);
  const u32 mB1 = (w >> 2) * 12288 + (w & 3) * 1024, sB1 = (w >> 2) * 98304 + (w & 3) * 24576 + j * 128;
  auto stage_b1 = [&](auto PC, auto BC) {
    constexpr int pair = decltype(PC)::value, base = decltype(BC)::value;
    const u32 sp = ((pair + rotp) & 7) * 196608 + sB1;
    dma<base, 0>(mB1, vB1, rQKV, sp); dma<base + 4096, 1024>(mB1, vB1, rQKV, sp); dma<base + 8192, 2048>(mB1, vB1, rQKV, sp);
  };
  auto b1_piece = [&](auto PC, auto BC, auto TC) {
    constexpr int pair = decltype(PC)::value, base = decltype(BC)::value, ty = decltype(TC)::value;
    dma<base + ty * 4096, ty * 1024>(mB1, vB1, rQKV, ((pair + rotp) & 7) * 196608 + sB1);
  };
  const int hd = w & 3, blk2 = w >> 2, kq = w & 3, mh = w >> 2;
  const int cs0 = j < 4 ? 22 * j : 88 + 21 * (j - 4);
  const int cend = cs0 + (j < 4 ? 22 : 21);
  STAMP(1);
  const int T0 = 12 * (g >> 4) + 3 * (w & 3), rcb = min(cs0 + T0, 168);
  if (w < 4) {
    asm volatile("s_waitcnt vmcnt(0)" : "+v"(cv), "+v"(wm[0]), "+v"(wm[1]), "+v"(wm[2]), "+v"(wm[3]), "+v"(ep0)::"memory");
    const u32 tg = __builtin_amdgcn_readfirstlane(ep0) + 1u;
    const f32x4 cf = {blo(cv.x), bhi(cv.x), blo(cv.y), bhi(cv.y)};
    float sc[4];
#pragma unroll
    for (int r = 0; r < 4; ++r) sc[r] = cf[r] * __builtin_amdgcn_rcpf(1.f + __builtin_amdgcn_exp2f(cf[r] * -1.4426950408889634f));
    f32x2 v[4];
#pragma unroll
    for (int i = 0; i < 4; ++i) v[i] = (unp(wm[0][i]) * sc[0] + unp(wm[1][i]) * sc[1]) + (unp(wm[2][i]) * sc[2] + unp(wm[3][i]) * sc[3]);
    const float a0 = sw32_add(v[0].x, v[2].x), a1 = sw32_add(v[0].y, v[2].y), a2 = sw32_add(v[1].x, v[3].x), a3 = sw32_add(v[1].y, v[3].y);
    float t0 = sw16_add(a0, a2), t1 = sw16_add(a1, a3);
    t0 += dppf<0x128>(t0); t1 += dppf<0x128>(t1);
    f32x2 o = {t0, t1};
    *(LAS f32x2*)(L + L_RED + w * 256 + (lane & 7) * 32 + g4 * 8) = o;
    LGW();
    u32 old = 0;
    if (lane == 0) old = __hip_atomic_fetch_add((LAS u32*)(L + L_CNT + 28), 1u, __ATOMIC_RELAXED, __HIP_MEMORY_SCOPE_WORKGROUP);
    if (__builtin_amdgcn_readfirstlane(old) == 3u) {
      asm volatile("" ::: "memory");
      float s = 0.f;
#pragma unroll
      for (int i = 0; i < 4; ++i) s += *(LAS float*)(L + L_RED + i * 256 + lane * 4);
      v2u gr; gr.x = asu(s); gr.y = tg;
      if (b < 192) __builtin_amdgcn_raw_buffer_store_b64(gr, rWS, lane * 8, WS_MODG + (mq * 3072 + cl * 64) * 8, 16);
    }
  }
  STAMP(2);
  v4u xr[10];
  {
    const int r0 = 64 * g - 16 + w;
    const u32 s0 = (u32)max(r0, 0) * 1024u, s1 = (u32)max(r0 + 8, 0) * 1024u;
    LD16(xr[0], vl16, rX, s0, 0); LD16(xr[1], vl16, rX, s1, 0);
#pragma unroll
    for (int i = 2; i < 10; ++i) { const u32 so = (u32)(r0 + 8 * i) * 1024u; LD16(xr[i], vl16, rX, so, 0); }
  }
  stage_b1(IC<0>{}, IC<L_XY>{});
  int afo[4];
#pragma unroll
  for (int i = 0; i < 4; ++i) { afo[i] = blk2 * 32768 + c16 * 1024 + (((4 * i + g4) ^ c16) << 4); PIN(afo[i]); }
  const u64 am0 = __builtin_amdgcn_ballot_w64((c16 & 14) >= 4 * g4), am1 = __builtin_amdgcn_ballot_w64((c16 & 14) >= 4 * g4 + 2);
  u32 ahm = (lane & 1) ? 0xffff0000u : 0xffffu; PIN(ahm);
  u32 vOst = c16 * 16 + g4 * 256 + (hd & 1) * 8; PIN(vOst);
  int tB1 = g4 * 1024 + 64 * (c16 >> 2) + 8 * (lane & 3) + 32 * ((hd & 1) ^ (g4 & 1)) + 256 * (hd >> 1); PIN(tB1);
  int hw0 = w * 1024 + ((((lane - 8 * rotp) & 63) ^ w) << 4); PIN(hw0);
#define vfl ((opq(vl16) & 112u) << 3)
  STAMP(3);
  v4u bw[48];
  auto read_b1 = [&](int pair, int base, int off) {
#pragma unroll
    for (int kk = 0; kk < 2; ++kk)
#pragma unroll
      for (int ty = 0; ty < 3; ++ty) {
        LAS char* a = L + base + (off + kk * 12288 + ty * 4096);
        bw[(2 * pair + kk) * 3 + ty] = cat(trd(a), trd(a + 512));
      }
  };
  f32x2 xn[10][4];
  v4u ga_[4]; v2u pv;
  const u32 sMG = WS_MODG + w * 1024;
  {
    float u[10];
    sfor<0, 10>([&](auto I) {
      constexpr int i = decltype(I)::value;
      if constexpr (i == 0) asm volatile("s_waitcnt vmcnt(11)" : "+v"(xr[0]), "+v"(xr[1]), "+v"(ep)::"memory");
      if constexpr (i == 2) asm volatile("s_waitcnt vmcnt(9)" : "+v"(xr[2]), "+v"(xr[3])::"memory");
      if constexpr (i == 4) asm volatile("s_waitcnt vmcnt(10)" : "+v"(xr[4]), "+v"(xr[5])::"memory");
      if constexpr (i == 6) asm volatile("s_waitcnt vmcnt(8)" : "+v"(xr[6]), "+v"(xr[7])::"memory");
      if constexpr (i == 8) asm volatile("s_waitcnt vmcnt(9)" : "+v"(xr[8]), "+v"(xr[9])::"memory");
      float s = 0.f, q = 0.f;
#pragma unroll
      for (int e = 0; e < 4; ++e) { s = dot2(xr[i][e], 0x3f803f80u, s); q = dot2(xr[i][e], xr[i][e], q); }
      u[i] = sw32_add(s, q);
      PIN(u[i]);
      if constexpr (i == 3) stage_b1(IC<1>{}, IC<L_XY + 24576>{});
      if constexpr (i == 7) stage_b1(IC<2>{}, IC<L_XY + 49152>{});
      if constexpr (i == 9) {
        const u32 s0 = sMG, s1 = sMG + 24576, s2 = sMG + 49152, s3 = sMG + 73728;
        ld16s<0>(ga_[0], vl16, rWS, s0); ld16s<0>(ga_[1], vl16, rWS, s1); ld16s<0>(ga_[2], vl16, rWS, s2); ld16s<0>(ga_[3], vl16, rWS, s3);
      }
    });
    float t[5];
#pragma unroll
    for (int i = 0; i < 5; ++i) t[i] = row16_sum(sw16_add(u[i], u[i + 5]));
    float v = t[0];
#pragma unroll
    for (int i = 1; i < 5; ++i) v = c16 == i ? t[i] : v;
    auto r = __builtin_amdgcn_permlane32_swap(asu(v), asu(v), false, false);
    const float mean = asf(r[0]) * (1.f / 512.f);
    const float var = fmaxf(asf(r[1]) * (1.f / 512.f) - mean * mean, 0.f);
    float rs = __builtin_amdgcn_rsqf(var + 1e-5f), nm = -mean * rs;
    PIN(rs); PIN(nm);
    STAMP(4);
    { const u32 vo = ((lane >> 3) & 3) * 24576 + (lane & 7) * 8, so = WS_MODG + (1024 + 8 * b) * 8; LD8S(pv, vo, rWS, so, 0); }
    if (w >= 4) {
      const int u = lane >> 2, row = (u & 3) | ((u >> 1) & 4) | ((u << 1) & 8);
      const u32 vo = row * 2752 + (lane & 3) * 16, so = (g & 15) * (32 * 2752) + rcb * 16, mb = L_Z + 49152 + (w & 3) * 4096;
      dma<0, 0>(mb, vo, rW1, so); dma<1024, 16 * 2752>(mb, vo, rW1, so); dma<2048, 0>(mb, vo, rW3, so); dma<3072, 16 * 2752>(mb, vo, rW3, so);
    } else {
      const int u = lane >> 1, row = (u & 3) | ((u >> 1) & 4) | ((u << 1) & 8) | (u & 16);
      const u32 vo = row * 1024 + (lane & 1) * 16, so = g * 32768 + j * 128 + w * 32, mb = L_Z + 65536 + w * 2048;
      dma<0, 0>(mb, vo, rW2, so); dma<1024, 0>(mb, vo, rW2, so + (g < 11 ? 32 * 32768 : 0));
      const int rwo = (u & 7) | ((u >> 1) & 8) | ((u << 1) & 16);
      dma<0, 0>(76800 + w * 1024, rwo * 1024 + (lane & 1) * 16, rWO, (g & 15) * 32768 + j * 128 + w * 32);
    }
    sfor<0, 10>([&](auto I) {
      constexpr int i = decltype(I)::value;
      if constexpr (i == 2) stage_b1(IC<3>{}, IC<L_Z>{});
      if constexpr (i == 6) stage_b1(IC<4>{}, IC<L_Z + 24576>{});
      const float a = rdl(rs, (i / 5) * 16 + i % 5), c = rdl(nm, (i / 5) * 16 + i % 5);
#pragma unroll
      for (int e = 0; e < 4; ++e) { xn[i][e] = unp(xr[i][e]) * a + c; PIN(xn[i][e]); }
    });
  }
  const u32 tag = __builtin_amdgcn_readfirstlane(ep) + 1u;
  u32 m1; asm("s_lshr_b32 %0, 1, %1" : "=s"(m1) : "s"(w) : "scc");
#define RAISE(fo) raise_flag(rWS, (fo), tag, vl16, m1)
#define SIGNAL(idx, fo) signal_flag(rWS, L_CNT + 4 * (idx), (fo), tag)
  STAMP(5);
  asm volatile("s_waitcnt vmcnt(9)" : "+v"(ga_[0]), "+v"(ga_[1]), "+v"(ga_[2]), "+v"(ga_[3]), "+v"(pv)::"memory");
  auto gsum = [&](v4u (&a)[4], f32x2& o) -> u32 {
    o.x = (asf(a[0].x) + asf(a[1].x)) + (asf(a[2].x) + asf(a[3].x)); o.y = (asf(a[0].z) + asf(a[1].z)) + (asf(a[2].z) + asf(a[3].z));
    return ((a[0].y ^ tag) | (a[0].w ^ tag) | (a[1].y ^ tag) | (a[1].w ^ tag)) | ((a[2].y ^ tag) | (a[2].w ^ tag) | (a[3].y ^ tag) | (a[3].w ^ tag));
  };
  auto gather = [&](u32 voff, u32 soff, f32x2& o) {
    for (u32 sp = 0; sp < SPIN_CAP; ++sp) {
      asm volatile("" ::: "memory");
      v4u a[4];
#pragma unroll
      for (int q = 0; q < 4; ++q) a[q] = __builtin_amdgcn_raw_buffer_load_b128(rWS, voff, soff + q * 24576, 16);
      if (!__builtin_amdgcn_ballot_w64(gsum(a, o) != 0u)) break;
    }
  };
  const float one = w >= 4 ? 1.f : 0.f;
  {
    f32x2 mA;
    if (__builtin_amdgcn_ballot_w64(gsum(ga_, mA) != 0u)) gather(vl16, sMG, mA);
    *(LAS f32x2*)(L + L_MODA + w * 512 + (vl16 >> 1)) = mA + one;
  }
  BAR();
  if (w == 7) {
    const u32 vo = ((lane >> 3) & 3) * 24576 + (lane & 7) * 8, so = WS_MODG + (1024 + 8 * b) * 8;
    for (u32 sp = 0; sp < SPIN_CAP && __builtin_amdgcn_ballot_w64(pv.y != tag); ++sp) {
      asm volatile("" ::: "memory");
      pv = __builtin_amdgcn_raw_buffer_load_b64(rWS, vo, so, 16);
    }
    float sv = asf(pv.x);
    sv += dppf<0x128>(sv); sv = sw16_add(sv, sv);
    pv.x = asu(sv);
    __builtin_amdgcn_raw_buffer_store_b64(pv, rWS, (lane & 7) * 8, WS_MODF + (1024 + 8 * b) * 8, 16);
  }
  STAMP(6);
  int vma = L_MODA + 2 * vl16; PIN(vma);
  const f32x4 ms0 = *(LAS f32x4*)(L + vma), ms1 = *(LAS f32x4*)(L + vma + 16);
  const f32x4 ma0 = *(LAS f32x4*)(L + vma + 2048), ma1 = *(LAS f32x4*)(L + vma + 2064);
  int tz0 = tB1 + L_Z; PIN(tz0);
  { int t2 = tB1 + 49152; PIN(t2); read_b1(0, tB1, 0); read_b1(1, tB1, 24576); read_b1(2, t2, 0); }
  {
    VMW(6);
    if (w >= 4) {
      LAS char* a0 = L + L_Z + 49152 + (w & 3) * 4096 + ((lane >> 1) & 1) * 2048 + 64 * ((c16 >> 2) + 4 * (g4 & 1) + 16 * (g4 >> 1)) + 8 * (lane & 1);
#pragma unroll
      for (int ct = 0; ct < 3; ++ct) {
        const int c = min(cs0 + T0 + ct - rcb, 3) * 16;
        const v4u f = cat(trd(a0 + c), trd(a0 + c + 512));
        const u32 sf = WS_B4T + ((j * 16 + (g & 15)) * 24 + T0 + ct) * 1024;
        asm volatile("buffer_store_dwordx4 %0, %1, %2, %3 offen sc1\n\ts_nop 1" ::"v"(f), "v"(vl16), "s"(rWS), "s"(sf) : "memory");
      }
    }
    else {
      LAS char* a0 = L + L_Z + 65536 + w * 2048 + 32 * ((c16 >> 2) + 4 * (g4 & 1) + 16 * (g4 >> 1)) + 8 * (lane & 3);
#pragma unroll
      for (int i = 0; i < 2; ++i) {
        const v4u f = cat(trd(a0 + i * 1024), trd(a0 + i * 1024 + 256));
        const u32 sf = WS_B5T + ((j * 43 + g + (i && g < 11 ? 32 : 0)) * 4 + w) * 1024;
        asm volatile("buffer_store_dwordx4 %0, %1, %2, %3 offen sc1\n\ts_nop 1" ::"v"(f), "v"(vl16), "s"(rWS), "s"(sf) : "memory");
      }
      LAS char* a1 = L + 76800 + w * 1024 + 32 * ((c16 >> 2) + 4 * (g4 & 1) + 16 * (g4 >> 1)) + 8 * (lane & 3);
      const v4u f = cat(trd(a1), trd(a1 + 256));
      const u32 sf = WS_B3T + ((j * 16 + (g & 15)) * 4 + w) * 1024;
      asm volatile("buffer_store_dwordx4 %0, %1, %2, %3 offen sc1\n\ts_nop 1" ::"v"(f), "v"(vl16), "s"(rWS), "s"(sf) : "memory");
    }
  }
  BAR();
  STAMP(7);
  {
    int hw1 = (hw0 ^ 128) + 8192; PIN(hw1);
    sfor<0, 10>([&](auto I) {
      constexpr int i = decltype(I)::value;
      v4u o;
      o.x = pk2(xn[i][0] * ma0.xy + ms0.xy); o.y = pk2(xn[i][1] * ma0.zw + ms0.zw);
      o.z = pk2(xn[i][2] * ma1.xy + ms1.xy); o.w = pk2(xn[i][3] * ma1.zw + ms1.zw);
      if constexpr (i == 8) { hw0 += 65536; hw1 += 65536; }
      *(LAS v4u*)(L + ((i & 1) ? hw1 : hw0) + ((i >> 1) & 3) * 16384) = o;
      SCHED();
    });
  }
  VMW(0);
  BAR();
  RAISE(WS_RFLAG + (j * 32 + g) * 4);
  u32 flr;
  STAMP(8);
  __builtin_amdgcn_s_setprio(2);

  const f32x4 z4 = {0.f, 0.f, 0.f, 0.f};
#define AFRAG(off, ks) (*(LAS v4u*)(L + afo[(ks) & 3] + ((off) + ((ks) >> 2) * 256)))
  const int ch0 = cs0 + 3 * w;
  constexpr int ND = NODMA ? 0 : 1;
  v4u b4[48];
  const u32 sB4 = WS_B4T + (j * 16 * 24 + 3 * w) * 1024;
  auto load_b4 = [&](auto NC) {
    constexpr int n = decltype(NC)::value, ks = n / 3, ct = n % 3;
    const u32 so = sB4 + ((ks + 2 * rotp) & 15) * 24576;
    v4u& r = b4[n];
    if constexpr (ct == 0) LD16S(r, vl16, rWS, so, 0); else if constexpr (ct == 1) LD16S(r, vl16, rWS, so, 1024); else LD16S(r, vl16, rWS, so, 2048);
  };
  auto landed_b4 = [&](auto AC, auto BC) {
#pragma unroll
    for (int i = decltype(AC)::value; i < decltype(BC)::value; ++i) { v4u& r = b4[i]; PIN(r); }
  };
  v4u b3[16];
  auto load_b3 = [&](auto NC) { constexpr int n = decltype(NC)::value; ld16s<(n & 3) * 1024>(b3[n], vl16, rWS, WS_B3T + (j * 16 + 4 * kq + (n >> 2)) * 4096); };
  v4u gb_, gg_;
  u32 vgg, fl0, vRow;
  int pw, pr;
  f32x4 aKh = z4, aVh = z4, aQ = z4, aK = z4, aV = z4, bQ = z4, bK = z4, bV = z4;
  {
    v4u ah = AFRAG(0, 0), a1 = AFRAG(16384, 0), a2 = AFRAG(32768, 0);
    int l3, c3, g3; f32x2 mB, mG;
    auto job = [&](auto NC) {
      constexpr int n = decltype(NC)::value;
      if constexpr (n == 3) { l3 = opq(vl16) >> 4; c3 = l3 & 15; g3 = l3 >> 4; PIN(c3); PIN(g3); vgg = (vl16 < 512 ? 8192 : 20480 - 512) + 512 * j + vl16; PIN(vgg); }
      if constexpr (n == 7) { vRow = (8 * w + (l3 >> 3)) * 1024 + (l3 & 7) * 16; PIN(vRow); }
      if constexpr (n == 8) { pr = (8 * w + (l3 >> 3)) * 256 + (((2 * (l3 & 7)) ^ ((l3 >> 3) & 7)) << 4); PIN(pr); }
      if constexpr (n == 10) { pw = kq * 16384 + (32 * mh + c3) * 256 + ((g3 ^ (c3 & 3)) << 4) + ((c3 & 4) << 4); PIN(pw); }
      if constexpr (n == 31) { mB.x = asf(gb_.x) + one; PIN(mB.x); mB.y = asf(gb_.z) + one; PIN(mB.y); *(LAS f32x2*)(L + L_MODA + w * 512 + (vl16 >> 1)) = mB; }
      if constexpr (n == 32) { mG.x = asf(gg_.x); mG.y = asf(gg_.z); *(LAS f32x2*)(L + L_GAB + (vl16 >> 1)) = mG; }
      if constexpr (n == 34) { fl0 = (gb_.y ^ tag) | (gb_.w ^ tag) | (gg_.y ^ tag) | (gg_.w ^ tag); PIN(fl0); }
      if constexpr (n == 7) { const u32 s0 = WS_MODF + 12288 + w * 1024; LD16S(gb_, vl16, rWS, s0, 0); }
      if constexpr (n == 9) { const u32 s0 = WS_MODF; LD16S(gg_, vgg, rWS, s0, 0); }
      if constexpr (n < 6 && !(n & 1)) b1_piece(IC<5>{}, IC<L_Z + 49152>{}, IC<n / 2>{});
      if constexpr (n == 6) arrive(L_CNT + 8);
      if constexpr (n == 10) await8(L_CNT + 8);
      if constexpr (n >= 10 && n < 22 && !(n & 1)) b1_piece(IC<6 + (n - 10) / 6>{}, IC<L_Z + ((n - 10) / 6) * 24576>{}, IC<((n - 10) / 2) % 3>{});
      if constexpr (n == 21) { const u32 vr = (vl16 >> 2) & 124, sr = WS_RFLAG + j * 128; LD4S(flr, vr, rWS, sr); }
      if constexpr (n == 26) {
        asm volatile("s_waitcnt vmcnt(0)" : "+v"(gb_), "+v"(gg_), "+v"(flr)::"memory");
        arrive(L_CNT + 12);
        const u32 vr = (vl16 >> 2) & 124, sr = WS_RFLAG + j * 128; u32 cnt = SPIN_CAP;
        asm volatile("1:\n\tv_cmp_ne_u32 vcc, %5, %0\n\ts_cbranch_vccz 2f\n\tbuffer_load_dword %0, %2, %3, %4 offen sc1\n\ts_waitcnt vmcnt(0)\n\t"
                     "s_sub_u32 %1, %1, 1\n\ts_cbranch_scc0 1b\n\t2:" : "+v"(flr), "+s"(cnt) : "v"(vr), "s"(rWS), "s"(sr), "s"(tag) : "vcc", "scc", "memory");
      }
      if constexpr (n == 28) { await8(L_CNT + 12); int tz2 = tz0 + 49152; PIN(tz2); read_b1(5, tz2, 0); }
      if constexpr (n == 32) read_b1(6, tz0, 0);
      if constexpr (n == 37) read_b1(7, tz0, 24576);
      if constexpr (n >= 27 && n < 47 && (n & 1)) load_b3(IC<(n - 27) / 2>{});
    };
    sfor<0, 16>([&](auto KS) {
      constexpr int ks = decltype(KS)::value;
      KSTAMP(1, ks);
      if constexpr (ks == 0) read_b1(3, tz0, 0);
      if constexpr (ks == 1) read_b1(4, tz0, 24576);
      SCHED();
      v4u nh = ah, n1 = a1, n2 = a2;
      aKh = mfma32(bw[ks * 3 + 1], ah, aKh);
      aVh = mfma32(ah, bw[ks * 3 + 2], aVh);
      if constexpr (ks < 15) nh = AFRAG(0, ks + 1);
      job(IC<3 * ks>{});
      SCHED();
      aQ = mfma32(bw[ks * 3 + 0], a1, aQ);
      aK = mfma32(bw[ks * 3 + 1], a1, aK);
      aV = mfma32(a1, bw[ks * 3 + 2], aV);
      if constexpr (ks < 15) n1 = AFRAG(16384, ks + 1);
      job(IC<3 * ks + 1>{});
      SCHED();
      bQ = mfma32(bw[ks * 3 + 0], a2, bQ);
      bK = mfma32(bw[ks * 3 + 1], a2, bK);
      bV = mfma32(a2, bw[ks * 3 + 2], bV);
      if constexpr (ks < 15) n2 = AFRAG(32768, ks + 1);
      job(IC<3 * ks + 2>{});
      ah = nh; a1 = n1; a2 = n2;
      SCHED();
    });
  }
  __builtin_amdgcn_s_setprio(0);
  STAMP(9); KSTAMP(1, 16);
  {
    const u32 sOst = WS_O + ((g * 4 + 2 * blk2) * 16 + 2 * j + (hd >> 1)) * 1024;
    const v2u k1p = pk4(aK), v1p = pk4(aV);
    const v2u o0 = attend(pk4(aKh), pk4(aVh), k1p, v1p, pk4(aQ), am0, am1, ahm, g == 0 && blk2 == 0);
    const v2u o1 = attend(k1p, v1p, pk4(bK), pk4(bV), pk4(bQ), am0, am1, ahm, false);
    __builtin_amdgcn_raw_buffer_store_b64(o0, rWS, vOst, sOst, 16);
    __builtin_amdgcn_raw_buffer_store_b64(o1, rWS, vOst, sOst + 16384, 16);
  }
  if (__builtin_amdgcn_ballot_w64(fl0 != 0u)) {
    for (u32 sp = 0; sp < SPIN_CAP; ++sp) {
      asm volatile("" ::: "memory");
      gb_ = __builtin_amdgcn_raw_buffer_load_b128(rWS, vl16, WS_MODF + 12288 + w * 1024, 16);
      gg_ = __builtin_amdgcn_raw_buffer_load_b128(rWS, vgg, WS_MODF, 16);
      if (!__builtin_amdgcn_ballot_w64(((gb_.y ^ tag) | (gb_.w ^ tag) | (gg_.y ^ tag) | (gg_.w ^ tag)) != 0u)) break;
    }
    const f32x2 mB = {asf(gb_.x) + one, asf(gb_.z) + one}, mG = {asf(gg_.x), asf(gg_.z)};
    *(LAS f32x2*)(L + L_MODA + w * 512 + (vl16 >> 1)) = mB;
    *(LAS f32x2*)(L + L_GAB + (vl16 >> 1)) = mG;
  }
  STAMP(10);
  VMW(0);
  BAR();
  RAISE(flag_off(0, g, 0) + j * 128);
  STAMP(11);
  if (b == 0 && w == 7) {
    for (u32 sp = 0; sp < SPIN_CAP; ++sp) {
      asm volatile("" ::: "memory");
      const v4u v = __builtin_amdgcn_raw_buffer_load_b128(rWS, vl16, WS_RFLAG, 16);
      if (__all(v.x == tag && v.y == tag && v.z == tag && v.w == tag)) break;
    }
    __builtin_amdgcn_raw_buffer_store_b32(tag, rWS, 0, WS_EPOCH, 16);
  }

  v4u xv;
  { const u32 sx = g * 65536 + j * 128; LD16(xv, vRow, rX, sx, 0); }
  sfor<10, 16>(load_b3);
  const u64 care2 = 0x0101010101010101ull * (3u << (2 * kq));
  poll_flags(rWS, vfl, flag_off(0, g, 0), care2, tag);
  PIN(xv);
  v4u af[8];
  {
    const u32 so = WS_O + ((g * 4 + 2 * mh) * 16 + 4 * kq) * 1024, so1 = so + 16384;
    LD16S(af[0], vl16, rWS, so, 0); LD16S(af[1], vl16, rWS, so, 1024); LD16S(af[2], vl16, rWS, so, 2048); LD16S(af[3], vl16, rWS, so, 3072);
    LD16S(af[4], vl16, rWS, so1, 0); LD16S(af[5], vl16, rWS, so1, 1024); LD16S(af[6], vl16, rWS, so1, 2048); LD16S(af[7], vl16, rWS, so1, 3072);
  }
  STAMP(12);
  sfor<0, 2>([&](auto M) {
    constexpr int m = decltype(M)::value;
    asm volatile("s_waitcnt vmcnt(%c4)" : "+v"(af[4 * m]), "+v"(af[4 * m + 1]), "+v"(af[4 * m + 2]), "+v"(af[4 * m + 3]) : "i"(m == 0 ? 4 : 2) : "memory");
    if constexpr (m == 0) {
#pragma unroll
      for (int i = 0; i < 16; ++i) PIN(b3[i]);
    }
    f32x4 acc[4] = {z4, z4, z4, z4};
    sfor<0, 4>([&](auto SC) {
      constexpr int sx = decltype(SC)::value;
#pragma unroll
      for (int n = 0; n < 4; ++n) acc[n] = mfma32(b3[sx * 4 + n], af[4 * m + sx], acc[n]);
      if constexpr (sx & 1) load_b4(IC<2 * m + (sx >> 1)>{});
      SCHED();
    });
#pragma unroll
    for (int n = 0; n < 4; ++n) *(LAS f32x4*)(L + ((n & 1) ? (pw ^ 64) : pw) + (m * 4096 + (n >> 1) * 128)) = acc[n];
  });
  BAR();
  STAMP(13);
  v4u x1p;
  {
    f32x4 y0 = *(LAS f32x4*)(L + pr), y1 = *(LAS f32x4*)(L + (pr ^ 16));
#pragma unroll
    for (int q = 1; q < 4; ++q) { y0 += *(LAS f32x4*)(L + pr + q * 16384); y1 += *(LAS f32x4*)(L + (pr ^ 16) + q * 16384); }
    const int ga = L_GAB + ((vl16 & 112) << 1);
    const f32x4 g0 = *(LAS f32x4*)(L + ga), g1 = *(LAS f32x4*)(L + ga + 16);
    const f32x4 x0 = f32x4{blo(xv.x), bhi(xv.x), blo(xv.y), bhi(xv.y)} + g0 * y0, x1 = f32x4{blo(xv.z), bhi(xv.z), blo(xv.w), bhi(xv.w)} + g1 * y1;
    { const v4u o = cat(pk4(x0), pk4(x1)); x1p = o; const u32 so = WS_X1 + g * 65536 + j * 128;
      asm volatile("buffer_store_dwordx4 %0, %1, %2, %3 offen sc1\n\ts_nop 1" ::"v"(o), "v"(vRow), "s"(rWS), "s"(so) : "memory"); }
    const f32x4 sv = x0 + x1;
    float mean = (sv.x + sv.y) + (sv.z + sv.w);
    mean += dppf<0xB1>(mean); mean += dppf<0x4E>(mean); mean += dppf<0x141>(mean);
    mean *= 1.f / 64.f;
    const f32x4 d0 = x0 - mean, d1 = x1 - mean, dd = d0 * d0 + d1 * d1;
    float m2 = (dd.x + dd.y) + (dd.z + dd.w);
    m2 += dppf<0xB1>(m2); m2 += dppf<0x4E>(m2); m2 += dppf<0x141>(m2);
    v2u st; st.x = asu(mean); st.y = asu(m2);
    __builtin_amdgcn_raw_buffer_store_b64(st, rWS, (vRow >> 10) << 6, WS_STAT + g * 4096 + j * 8, 16);
  }
  STAMP(14);
  VMW(0);
  BAR();
  RAISE(flag_off(1, g, 0) + j * 128);
  sfor<4, 9>(load_b4);
  STAMP(15);

  u32 hmask[3];
#pragma unroll
  for (int ct = 0; ct < 3; ++ct)
    asm("s_cmp_lt_u32 %1, %2\n\ts_cselect_b32 %0, -1, 0" : "=s"(hmask[ct]) : "s"((u32)(ch0 + ct)), "s"((u32)cend) : "scc");
#pragma unroll
  for (int i = 0; i < 4; ++i) { afo[i] -= blk2 * 32768; PIN(afo[i]); }
  int vm2 = L_MODA + 2 * vl16; PIN(vm2);
  int h2w = w * 8192 + (((vl16 - 128 * rotp) & 1023) ^ ((8 * w & 15) << 4)); PIN(h2w);
  const f32x4 ls0 = *(LAS f32x4*)(L + vm2), ls1 = *(LAS f32x4*)(L + vm2 + 16), la0 = *(LAS f32x4*)(L + vm2 + 2048), la1 = *(LAS f32x4*)(L + vm2 + 2064);
  poll_flags(rWS, vfl, flag_off(1, g, 0), ~0ull, tag);
  landed_b4(IC<0>{}, IC<9>{});
  {
    v4u x1v[8]; v2u stv;
    const u32 so = WS_X1 + g * 65536 + w * 8192, so2 = so + 4096;
    { const u32 vs = vl16 >> 1, ss = WS_STAT + g * 4096 + w * 512; LD8S(stv, vs, rWS, ss, 0); }
    LD16S(x1v[0], vl16, rWS, so, 0); LD16S(x1v[1], vl16, rWS, so, 1024); LD16S(x1v[2], vl16, rWS, so, 2048); LD16S(x1v[3], vl16, rWS, so, 3072);
    LD16S(x1v[4], vl16, rWS, so2, 0); LD16S(x1v[5], vl16, rWS, so2, 1024); LD16S(x1v[6], vl16, rWS, so2, 2048); LD16S(x1v[7], vl16, rWS, so2, 3072);
    sfor<9, 12>(load_b4);
    asm volatile("s_waitcnt vmcnt(11)" : "+v"(stv)::"memory");
    const float mj = asf(stv.x);
    float mean = mj;
    mean += dppf<0xB1>(mean); mean += dppf<0x4E>(mean); mean += dppf<0x141>(mean);
    mean *= 0.125f;
    const float dm = mj - mean;
    float m2 = asf(stv.y) + 64.f * dm * dm;
    m2 += dppf<0xB1>(m2); m2 += dppf<0x4E>(m2); m2 += dppf<0x141>(m2);
    const float l2r = __builtin_amdgcn_rsqf(m2 * (1.f / 512.f) + 1e-5f), l2n = -mean * l2r;
    sfor<0, 8>([&](auto I) {
      constexpr int i = decltype(I)::value;
      v4u& xx = x1v[i];
      asm volatile("s_waitcnt vmcnt(%c1)" : "+v"(xx) : "i"(10 - i) : "memory");
      const float rs = rdl(l2r, 8 * i), nn = rdl(l2n, 8 * i);
      v4u o;
      o.x = pk2((unp(xx.x) * rs + nn) * la0.xy + ls0.xy); o.y = pk2((unp(xx.y) * rs + nn) * la0.zw + ls0.zw);
      o.z = pk2((unp(xx.z) * rs + nn) * la1.xy + ls1.xy); o.w = pk2((unp(xx.w) * rs + nn) * la1.zw + ls1.zw);
      *(LAS v4u*)(L + (h2w ^ (i << 4)) + i * 1024) = o;
    });
  }
  BAR();
  STAMP(16);
  __builtin_amdgcn_s_setprio(2);
  const int kb5 = 5 * w + min(w, 3);
  u64 care5 = 0x0101010101010101ull * ((0x80c06030180c0603ull >> (8 * w)) & 255u);
  asm volatile("" : "+s"(care5));
  v4u b5[24];
  const u32 sB5 = WS_B5T + (j * 43 + kb5) * 4096, d5 = (w + 5) >> 3;
  auto load_b5 = [&](auto NC) {
    constexpr int n = decltype(NC)::value, sx = n / 4, t = n % 4;
    ld16s<t * 1024>(b5[n], vl16, rWS, sB5 + (sx == 5 ? (5 - d5) * 4096 : sx * 4096));
  };
  f32x4 acc4[3][4];
#pragma unroll
  for (int ct = 0; ct < 3; ++ct)
#pragma unroll
    for (int m = 0; m < 4; ++m) acc4[ct][m] = z4;
  u32 vH; int pw5;
  {
    v4u a[4] = {AFRAG(0, 0), AFRAG(16384, 0), AFRAG(32768, 0), AFRAG(49152, 0)};
    sfor<0, 16>([&](auto KS) {
      constexpr int ks = decltype(KS)::value;
      KSTAMP(3, ks);
      if constexpr (ks >= 3 && ks < 13) { VMW(9); landed_b4(IC<3 * ks>{}, IC<3 * ks + 3>{}); }
      if constexpr (ks >= 13) { VMW(9); landed_b4(IC<3 * ks>{}, IC<3 * ks + 3>{}); }
      v4u nx[4] = {a[0], a[1], a[2], a[3]};
      sfor<0, 4>([&](auto MC) {
        constexpr int m = decltype(MC)::value;
        if constexpr (ks < 15) nx[m] = AFRAG(16384 * m, ks + 1);
        if constexpr (m < 3) { if constexpr (ks < 12) load_b4(IC<12 + 3 * ks + m>{}); else load_b5(IC<3 * (ks - 12) + m>{}); }
        SCHED();
#pragma unroll
        for (int ct = 0; ct < 3; ++ct) acc4[ct][m] = mfma32(b4[ks * 3 + ct], a[m], acc4[ct][m]);
        if constexpr (ks == 8 && m == 0) { vH = (vl16 & 255) + ((vl16 >> 5) & 8) + (vl16 >> 9) * (172 * 256); PIN(vH); }
        if constexpr (ks == 9 && m == 0) { const int l5 = opq(vl16) >> 4, c5 = l5 & 15, g5 = l5 >> 4;
          pw5 = w * 16384 + c5 * 256 + ((c5 & 4) << 4) + ((g5 ^ (c5 & 3)) << 4); PIN(pw5); }
        SCHED();
      });
#pragma unroll
      for (int m = 0; m < 4; ++m) a[m] = nx[m];
    });
  }
#pragma unroll
  for (int ct = 0; ct < 3; ++ct)
#pragma unroll
    for (int m = 0; m < 4; ++m) PIN(acc4[ct][m]);
  __builtin_amdgcn_s_setprio(0);
  STAMP(17); KSTAMP(3, 16);
#pragma unroll
  for (int ct = 0; ct < 3; ++ct)
#pragma unroll
    for (int mp = 0; mp < 2; ++mp) {
      f32x4 G, U, R;
#pragma unroll
      for (int e = 0; e < 4; ++e) {
        auto r = __builtin_amdgcn_permlane32_swap(asu(acc4[ct][2 * mp][e]), asu(acc4[ct][2 * mp + 1][e]), false, false);
        G[e] = asf(r[0]); U[e] = asf(r[1]);
      }
#pragma unroll
      for (int e = 0; e < 4; ++e) R[e] = qp(qp(G[e] * U[e]) * __builtin_amdgcn_rcpf(1.f + __builtin_amdgcn_exp2f(qp(G[e] * -1.4426950408889634f))));
      const v2u o = pk4(R);
      const u32 so = WS_HH + ((g * 4 + 2 * mp) * 172 + ch0 + ct) * 256;
      asm volatile("s_mov_b32 exec_lo, %4\n\ts_mov_b32 exec_hi, %4\n\tbuffer_store_dwordx2 %0, %1, %2, %3 offen sc1\n\ts_mov_b64 exec, -1"
                   ::"v"(o), "v"(vH), "s"(rWS), "s"(so), "s"(hmask[ct]) : "memory");
    }
  STAMP(18);
  sfor<12, 16>(load_b5);
  VMW(4);
  BAR();
  RAISE(flag_off(2, g, 0) + j * 128);
  STAMP(19);
  sfor<16, 22>(load_b5);
  poll_flags(rWS, vfl, flag_off(2, g, 0), care5, tag);
#pragma unroll
  for (int i = 0; i < 22; ++i) PIN(b5[i]);
  v4u hf[16];
  auto load_h = [&](auto NC) {
    constexpr int n = decltype(NC)::value, sx = n / 4, m = n % 4;
    ld16s<0>(hf[n & 15], vl16, rWS, WS_HH + ((g * 4 + m) * 172 + 4 * kb5) * 256 + (sx == 5 ? (5 - d5) * 1024 : sx * 1024));
  };
  sfor<0, 16>(load_h);
  sfor<22, 24>(load_b5);
  STAMP(20);
  {
    f32x4 acc[4][4];
#pragma unroll
    for (int m = 0; m < 4; ++m)
#pragma unroll
      for (int n = 0; n < 4; ++n) acc[m][n] = z4;
    sfor<0, 6>([&](auto SC) {
      constexpr int sx = decltype(SC)::value, r = (sx & 3) * 4;
      constexpr int nw = sx == 0 ? 14 : sx == 1 ? 14 : sx == 2 ? 14 : sx == 3 ? 10 : sx == 4 ? 4 : 0;
      asm volatile("s_waitcnt vmcnt(%c4)" : "+v"(hf[r]), "+v"(hf[r + 1]), "+v"(hf[r + 2]), "+v"(hf[r + 3]) : "i"(nw) : "memory");
      if constexpr (sx == 4) { PIN(b5[22]); PIN(b5[23]); }
      if constexpr (sx == 5) { if (w >= 3) { hf[r] = v4u{0u, 0u, 0u, 0u}; hf[r + 1] = v4u{0u, 0u, 0u, 0u}; hf[r + 2] = v4u{0u, 0u, 0u, 0u}; hf[r + 3] = v4u{0u, 0u, 0u, 0u}; } }
      sfor<0, 4>([&](auto MC) {
        constexpr int m = decltype(MC)::value;
#pragma unroll
        for (int n = 0; n < 4; ++n) acc[m][n] = mfma32(b5[sx * 4 + n], hf[r + m], acc[m][n]);
        SCHED();
        if constexpr (sx < 2) load_h(IC<16 + 4 * sx + m>{});
        SCHED();
      });
    });
#pragma unroll
    for (int m = 0; m < 4; ++m)
#pragma unroll
      for (int n = 0; n < 4; ++n) *(LAS f32x4*)(L + ((n & 1) ? (pw5 ^ 64) : pw5) + (m * 4096 + (n >> 1) * 128)) = acc[m][n];
  }
  BAR();
  STAMP(21);
  {
    const int l6 = opq(vl16) >> 4;
    const int p0 = opq((8 * w + (l6 >> 3)) * 256 + (((2 * (l6 & 7)) ^ ((l6 >> 3) & 7)) << 4)), p1 = opq(p0 + 65536);
    f32x4 y0 = *(LAS f32x4*)(L + p0), y1 = *(LAS f32x4*)(L + (p0 ^ 16));
#pragma unroll
    for (int q = 1; q < 8; ++q) { const int pb = q < 4 ? p0 : p1; y0 += *(LAS f32x4*)(L + pb + (q & 3) * 16384); y1 += *(LAS f32x4*)(L + (pb ^ 16) + (q & 3) * 16384); }
    const int gb = L_GAB + 256 + (l6 & 7) * 32;
    const f32x4 g0 = *(LAS f32x4*)(L + gb), g1 = *(LAS f32x4*)(L + gb + 16);
    const f32x4 o0 = f32x4{blo(x1p.x), bhi(x1p.x), blo(x1p.y), bhi(x1p.y)} + g0 * y0, o1 = f32x4{blo(x1p.z), bhi(x1p.z), blo(x1p.w), bhi(x1p.w)} + g1 * y1;
    const v4u o = cat(pk4(o0), pk4(o1));
    const u32 vo = (8 * w + (l6 >> 3)) * 1024 + (l6 & 7) * 16, so = g * 65536 + j * 128;
    asm volatile("buffer_store_dwordx4 %0, %1, %2, %3 offen sc1\n\ts_nop 1" ::"v"(o), "v"(vo), "s"(rOUT), "s"(so) : "memory");
  }
  STAMP(22);
  STAMP_DUMP();
}
}

extern "C" void kernel_launch(void* const* d_in, const int* in_sizes, int n_in, void* d_out, int out_size, void* d_ws, size_t ws_size,
                              hipStream_t stream) {
  Params p;
  p.x = (const u16*)d_in[0]; p.wqkv = (const u16*)d_in[1]; p.wo = (const u16*)d_in[2];
  p.w1 = (const u16*)d_in[4]; p.w3 = (const u16*)d_in[5]; p.w2 = (const u16*)d_in[6];
  p.c = (const u16*)d_in[7]; p.wmod = (const u16*)d_in[8];
  p.out = (u16*)d_out; p.ws = (char*)d_ws;
  (void)hipFuncSetAttribute((const void*)fused_block, hipFuncAttributeMaxDynamicSharedMemorySize, L_TOTAL);
  fused_block<<<256, 512, L_TOTAL, stream>>>(p);
}
```

```cpp
#define MULT 0
#define BSEL 40
#include <hip/hip_runtime.h>
#include <stdint.h>

#define KDBG 0
#define NODMA 0
#define CODEWARM 0
#define L2WARM 1
#define EARLYT 0
#define XMAP 2

#define DI __device__ __forceinline__
#define GAS __attribute__((address_space(1)))
#define LAS __attribute__((address_space(3)))
typedef unsigned u32;
typedef unsigned short u16;
typedef unsigned long long u64;
typedef u32 v4u __attribute__((ext_vector_type(4)));
typedef u32 v2u __attribute__((ext_vector_type(2)));
typedef float f32x4 __attribute__((ext_vector_type(4)));
typedef float f32x2 __attribute__((ext_vector_type(2)));
typedef __bf16 bf16x8 __attribute__((ext_vector_type(8)));
typedef __bf16 bf16x2 __attribute__((ext_vector_type(2)));
typedef short s16x4 __attribute__((ext_vector_type(4)));
typedef __amdgpu_buffer_rsrc_t rsrc_t;
typedef GAS u32 gu32;
typedef GAS u64 gu64;
#define RLX_AGENT __ATOMIC_RELAXED, __HIP_MEMORY_SCOPE_AGENT

namespace {
constexpr int S_ = 2048, D_ = 512, F_ = 1376;
constexpr u32 WS_EPOCH = 0;
constexpr u32 WS_FLAG = 32768;
constexpr u32 WS_STAT = 262144;
constexpr u32 WS_MODG = 524288;
constexpr u32 WS_MODF = 655360;
constexpr u32 WS_O = 1u << 20;
constexpr u32 WS_X1 = 3u << 20;
constexpr u32 WS_HH = 5u << 20;
constexpr u32 WS_RFLAG = 8192;
constexpr u32 WS_B4T = 11u << 20;
constexpr u32 WS_B5T = 14u << 20;
constexpr u32 WS_B3T = (15u << 20) + (512u << 10);
constexpr u32 WS_DBG = 16u << 20;
constexpr u32 WS_BYTES = 32u << 20;
constexpr int L_XY = 0;
constexpr int L_Z = 81920;
constexpr int L_MISC = 158720;
constexpr int L_TOTAL = 163840;
constexpr int L_RED = 73728;
constexpr int L_MODA = L_MISC;
constexpr int L_CNT = L_MISC + 4096;
constexpr int L_GAB = L_MISC + 4096 + 64;
constexpr int L_JUNK = L_MISC + 4096 + 64 + 512;
constexpr int L_STAMP = 156672;
constexpr u32 SPIN_CAP = 1u << 18;
constexpr u32 CODE_BYTES = 26112;

DI f32x4 mfma32(v4u a, v4u b, f32x4 c) {
  return __builtin_amdgcn_mfma_f32_16x16x32_bf16(__builtin_bit_cast(bf16x8, a), __builtin_bit_cast(bf16x8, b), c, 0, 0, 0);
}
DI f32x4 mfma16(v2u a, v2u b, f32x4 c) {
  return __builtin_amdgcn_mfma_f32_16x16x16bf16_1k(__builtin_bit_cast(s16x4, a), __builtin_bit_cast(s16x4, b), c, 0, 0, 0);
}
DI u32 pk(float lo, float hi) { f32x2 v = {lo, hi}; return __builtin_bit_cast(u32, __builtin_convertvector(v, bf16x2)); }
DI u32 pk2(f32x2 v) { return __builtin_bit_cast(u32, __builtin_convertvector(v, bf16x2)); }
DI v2u pk4(f32x4 v) { v2u r; r.x = pk(v.x, v.y); r.y = pk(v.z, v.w); return r; }
DI float asf(u32 u) { return __builtin_bit_cast(float, u); }
DI u32 asu(float f) { return __builtin_bit_cast(u32, f); }
DI float blo(u32 u) { return asf(u << 16); }
DI float bhi(u32 u) { return asf(u & 0xffff0000u); }
DI f32x2 unp(u32 u) { f32x2 r = {blo(u), bhi(u)}; return r; }
DI float dot2(u32 a, u32 b, float c) { return __builtin_amdgcn_fdot2_f32_bf16(__builtin_bit_cast(bf16x2, a), __builtin_bit_cast(bf16x2, b), c, false); }
DI v2u trd(LAS char* p) { return __builtin_bit_cast(v2u, __builtin_amdgcn_ds_read_tr16_b64_v4i16((LAS s16x4*)p)); }
DI v4u cat(v2u a, v2u b) { v4u r; r.x = a.x; r.y = a.y; r.z = b.x; r.w = b.y; return r; }
template <int MI, int SI> DI void dma(u32 mb, u32 voff, rsrc_t srd, u32 sb) {
  u32 t;
  asm volatile("s_add_u32 m0, %1, %c2\n\ts_add_u32 %0, %3, %c4\n\tbuffer_load_dwordx4 %5, %6, %0 offen lds"
               : "=&s"(t) : "s"(mb), "i"(MI), "s"(sb), "i"(SI), "v"(voff), "s"(srd) : "scc");
}
DI void touch(u32 voff, rsrc_t srd, u32 sb) {
  asm volatile("s_mov_b32 m0, %c0\n\ts_nop 0\n\tbuffer_load_dword %1, %2, %3 offen lds" ::"i"(L_JUNK), "v"(voff), "s"(srd), "s"(sb));
}
#define BAR() asm volatile("s_waitcnt lgkmcnt(0)\n\ts_barrier" ::: "memory")
#define VMW(n) asm volatile("s_waitcnt vmcnt(" #n ")" ::: "memory")
#define LGW() asm volatile("s_waitcnt lgkmcnt(0)" ::: "memory")
#define SCHED() __builtin_amdgcn_sched_barrier(0)
#define LD16(dst, voff, srd, soff, IMM) asm volatile("buffer_load_dwordx4 %0, %1, %2, %3 offen offset:" #IMM : "=v"(dst) : "v"(voff), "s"(srd), "s"(soff))
#define LD16S(dst, voff, srd, soff, IMM) asm volatile("buffer_load_dwordx4 %0, %1, %2, %3 offen offset:" #IMM " sc1" : "=v"(dst) : "v"(voff), "s"(srd), "s"(soff))
#define LD8(dst, voff, srd, soff, IMM) asm volatile("buffer_load_dwordx2 %0, %1, %2, %3 offen offset:" #IMM : "=v"(dst) : "v"(voff), "s"(srd), "s"(soff))
#define LD8S(dst, voff, srd, soff, IMM) asm volatile("buffer_load_dwordx2 %0, %1, %2, %3 offen offset:" #IMM " sc1" : "=v"(dst) : "v"(voff), "s"(srd), "s"(soff))
#define LD4(dst, voff, srd, soff) asm volatile("buffer_load_dword %0, %1, %2, %3 offen" : "=v"(dst) : "v"(voff), "s"(srd), "s"(soff))
#define LD4S(dst, voff, srd, soff) asm volatile("buffer_load_dword %0, %1, %2, %3 offen sc1" : "=v"(dst) : "v"(voff), "s"(srd), "s"(soff))

template <int IMM> DI void ld16s(v4u& r, u32 v, rsrc_t rs, u32 so) { asm volatile("buffer_load_dwordx4 %0, %1, %2, %3 offen offset:%c4 sc1" : "=v"(r) : "v"(v), "s"(rs), "s"(so), "i"(IMM)); }
template <int CTRL> DI float dppf(float v) { return asf(__builtin_amdgcn_update_dpp(0, asu(v), CTRL, 0xf, 0xf, true)); }
DI float row16_sum(float v) { v += dppf<0x128>(v); v += dppf<0x124>(v); v += dppf<0x122>(v); v += dppf<0x121>(v); return v; }
DI float sw32_add(float a, float b) { auto r = __builtin_amdgcn_permlane32_swap(asu(a), asu(b), false, false); return asf(r[0]) + asf(r[1]); }
DI float sw16_add(float a, float b) { auto r = __builtin_amdgcn_permlane16_swap(asu(a), asu(b), false, false); return asf(r[0]) + asf(r[1]); }
DI float x16_max(float v) { auto r = __builtin_amdgcn_permlane16_swap(asu(v), asu(v), false, false); return fmaxf(asf(r[0]), asf(r[1])); }
DI float x32_max(float v) { auto r = __builtin_amdgcn_permlane32_swap(asu(v), asu(v), false, false); return fmaxf(asf(r[0]), asf(r[1])); }
DI int fresh_lane() { int l; asm volatile("v_mbcnt_lo_u32_b32 %0, -1, 0\n\tv_mbcnt_hi_u32_b32 %0, -1, %0" : "=v"(l)); return l; }
DI float rdl(float v, int l) { return asf(__builtin_amdgcn_readlane(asu(v), l)); }

struct Params {
  const u16 *x, *wqkv, *wo, *w1, *w3, *w2, *c, *wmod;
  u16* out;
  char* ws;
};

#define LOOPSEL 0
#define STAMP(i) do { if constexpr ((i) == 0) asm volatile("s_memrealtime %0\n\ts_waitcnt lgkmcnt(0)" : "=s"(tA_)::"memory"); } while (0)
#define KSTAMP(lp, i) do {} while (0)
#define STAMP_DUMP() do {} while (0)

DI u32 flag_off(int phase, int g, int h) { return WS_FLAG + ((phase * 32 + g) * 2 + h) * 1024; }
DI void poll_flags(rsrc_t rWS, u32 vfl, u32 foff, u64 care, u32 tag) {
  for (u32 sp = 0; sp < SPIN_CAP; ++sp) {
    asm volatile("" ::: "memory");
    const u32 v = __builtin_amdgcn_raw_buffer_load_b32(rWS, vfl, foff, 16);
    if (!(__builtin_amdgcn_ballot_w64(v != tag) & care)) break;
  }
  VMW(0);
}
DI void raise_flag(rsrc_t rWS, u32 foff, u32 vtag, u32 vzero, u32 m1) {
  asm volatile("s_mov_b32 exec_hi, 0\n\ts_mov_b32 exec_lo, %4\n\tbuffer_store_dword %0, %1, %2, %3 offen sc1\n\ts_mov_b64 exec, -1" ::"v"(vtag), "v"(vzero), "s"(rWS), "s"(foff), "s"(m1) : "memory");
}
DI void signal_flag(rsrc_t rWS, u32 cnt_addr, u32 foff, u32 tag) {
  u32 t, a = cnt_addr, one = 1u, vt = tag, vz = 0u;
  asm volatile("s_mov_b64 exec, 1\n\tds_add_rtn_u32 %0, %1, %2\n\ts_waitcnt lgkmcnt(0)\n\tv_cmp_eq_u32 vcc, 7, %0\n\ts_and_b64 exec, exec, vcc\n\t"
               "buffer_store_dword %3, %4, %5, %6 offen sc1\n\ts_mov_b64 exec, -1"
               : "=&v"(t) : "v"(a), "v"(one), "v"(vt), "v"(vz), "s"(rWS), "s"(foff) : "vcc", "scc", "memory");
}
DI void arrive(u32 cnt_addr) {
  u32 a = cnt_addr, one = 1u;
  asm volatile("s_waitcnt lgkmcnt(0)\n\ts_mov_b64 exec, 1\n\tds_add_u32 %0, %1\n\ts_mov_b64 exec, -1" ::"v"(a), "v"(one) : "memory");
}
DI void await8(u32 cnt_addr) {
  u32 a = cnt_addr, t, sv;
  asm volatile("1:\n\tds_read_b32 %0, %2\n\ts_waitcnt lgkmcnt(0)\n\tv_readfirstlane_b32 %1, %0\n\ts_nop 0\n\ts_cmp_lt_u32 %1, 8\n\ts_cbranch_scc1 1b"
               : "=&v"(t), "=&s"(sv) : "v"(a) : "scc", "memory");
}
DI void raise_when8(rsrc_t rWS, u32 cnt_addr, u32 foff, u32 tag, u32 w7) {
  u32 a = cnt_addr, t, sv, vt = tag, vz = 0u;
  asm volatile("s_cmp_eq_u32 %7, 0\n\ts_cbranch_scc1 2f\n\t1:\n\tds_read_b32 %0, %2\n\ts_waitcnt lgkmcnt(0)\n\tv_readfirstlane_b32 %1, %0\n\ts_nop 0\n\ts_cmp_lt_u32 %1, 8\n\ts_cbranch_scc1 1b\n\t"
               "s_mov_b64 exec, 1\n\tbuffer_store_dword %3, %4, %5, %6 offen sc1\n\ts_mov_b64 exec, -1\n\t2:"
               : "=&v"(t), "=&s"(sv) : "v"(a), "v"(vt), "v"(vz), "s"(rWS), "s"(foff), "s"(w7) : "scc", "memory");
}
#define PIN(x) asm volatile("" : "+v"(x))
DI float qp(float r) { PIN(r); return r; }
DI float q_shl(u32 a) { return qp(blo(a)); }
DI float q_hi(u32 a) { return qp(bhi(a)); }
DI float q_fma(float a, float b, float c) { return qp(__builtin_fmaf(a, b, c)); }
DI float q_fmas(float a, float b, float c) { return qp(__builtin_fmaf(a, b, c)); }
DI float q_mul(float a, float b) { return qp(a * b); }
DI float q_mulk(float a) { return qp(a * -1.4426950408889634f); }
DI float q_add1(float a) { return qp(a + 1.f); }
DI float q_exp(float a) { return qp(__builtin_amdgcn_exp2f(a)); }
DI float q_rcp(float a) { return qp(__builtin_amdgcn_rcpf(a)); }
DI u32 q_cvt(float a, float b) { u32 r = pk(a, b); PIN(r); return r; }
DI float q_mov(float a) { return qp(a); }
DI u32 opq(u32 v) { asm volatile("" : "+v"(v)); return v; }

DI float sel(u64 m, float a, float b) { return __builtin_amdgcn_inverse_ballot_w64(m) ? a : b; }
DI v2u attend(v2u kp, v2u vp, v2u kc, v2u vc, v2u q, u64 m0, u64 m1, u32 hm, bool noprev) {
  const f32x4 z = {0.f, 0.f, 0.f, 0.f};
  const f32x4 sp = mfma16(kp, q, z);
  const f32x4 sc = mfma16(kc, q, z);
  const float K2 = 0.25f * 1.4426950408889634f;
  const u64 par = 0xaaaaaaaaaaaaaaaaull;
  float p0 = sel(par, sp[1], sp[0]), p1 = sel(par, sp[3], sp[2]);
  if (noprev) { p0 = -INFINITY; p1 = -INFINITY; }
  const float t0 = sel(m0, sel(par, sc[1], sc[0]), p0), t1 = sel(m1, sel(par, sc[3], sc[2]), p1);
  const float m = x32_max(x16_max(fmaxf(t0, t1)));
  const float mk = -m * K2;
  const float e0 = __builtin_amdgcn_exp2f(__builtin_fmaf(t0, K2, mk)), e1 = __builtin_amdgcn_exp2f(__builtin_fmaf(t1, K2, mk));
  const u32 w0 = pk(e0, e0) & hm, w1 = pk(e1, e1) & hm;
  v4u pf;
  pf.x = __builtin_amdgcn_inverse_ballot_w64(m0) ? 0u : w0; pf.y = __builtin_amdgcn_inverse_ballot_w64(m1) ? 0u : w1;
  pf.z = __builtin_amdgcn_inverse_ballot_w64(m0) ? w0 : 0u; pf.w = __builtin_amdgcn_inverse_ballot_w64(m1) ? w1 : 0u;
  const f32x4 o = mfma32(cat(vp, vc), pf, z);
  float sum = e0 + e1;
  sum = sw16_add(sum, sum); sum = sw32_add(sum, sum);
  const float inv = __builtin_amdgcn_rcpf(sum);
  return pk4(f32x4{qp(o.x * inv), qp(o.y * inv), qp(o.z * inv), qp(o.w * inv)});
}

template <int N> using IC = std::integral_constant<int, N>;
template <int I, int N, class F> DI void sfor(F&& f) { if constexpr (I < N) { f(IC<I>{}); sfor<I + 1, N>(f); } }

__global__ void __launch_bounds__(512, 2) fused_block(Params p) {
  extern __shared__ __attribute__((aligned(16))) char smem[];
  LAS char* L = (LAS char*)smem;
  const int tid = threadIdx.x, lane = tid & 63, w = __builtin_amdgcn_readfirstlane(tid >> 6);
  const int c16 = lane & 15, g4 = lane >> 4;
  const int b = blockIdx.x;
  int g, j;
  if (XMAP == 0) { g = (b & 7) * 4 + ((b >> 3) & 3); j = b >> 5; }
  else if (XMAP == 2) { const int x = b & 7, r = b >> 3; g = (x >> 1) * 8 + (r & 7); j = (x & 1) * 4 + (r >> 3); }
  else { const int x = b & 7, r = b >> 3; g = (x >> 2) * 16 + (r & 15); j = (x & 3) * 2 + (r >> 4); }
  const rsrc_t rX = __builtin_amdgcn_make_buffer_rsrc((void*)p.x, 0, S_ * D_ * 2, 0x00020000);
  const rsrc_t rQKV = __builtin_amdgcn_make_buffer_rsrc((void*)p.wqkv, 0, D_ * 3 * D_ * 2, 0x00020000);
  const rsrc_t rWO = __builtin_amdgcn_make_buffer_rsrc((void*)p.wo, 0, D_ * D_ * 2, 0x00020000);
  const rsrc_t rW1 = __builtin_amdgcn_make_buffer_rsrc((void*)p.w1, 0, D_ * F_ * 2, 0x00020000);
  const rsrc_t rW3 = __builtin_amdgcn_make_buffer_rsrc((void*)p.w3, 0, D_ * F_ * 2, 0x00020000);
  const rsrc_t rW2 = __builtin_amdgcn_make_buffer_rsrc((void*)p.w2, 0, F_ * D_ * 2, 0x00020000);
  const rsrc_t rC = __builtin_amdgcn_make_buffer_rsrc((void*)p.c, 0, D_ * 2, 0x00020000);
  const rsrc_t rMOD = __builtin_amdgcn_make_buffer_rsrc((void*)p.wmod, 0, D_ * 6 * D_ * 2, 0x00020000);
  const rsrc_t rWS = __builtin_amdgcn_make_buffer_rsrc((void*)p.ws, 0, WS_BYTES, 0x00020000);
  const rsrc_t rOUT = __builtin_amdgcn_make_buffer_rsrc((void*)p.out, 0, S_ * D_ * 2, 0x00020000);
  u64 tA_ = 0;
  STAMP(0);

  u32 ep;
  { const u32 vz = 0; asm volatile("buffer_load_dword %0, %1, %2, 0 offen sc1" : "=v"(ep) : "v"(vz), "s"(rWS)); }
  u32 vl16 = lane * 16; PIN(vl16);
  const int bm = b < 192 ? b : b - 192, mq = bm / 48, cl = bm - 48 * mq;
  v4u wm[4]; v2u cv; u32 ep0;
  if (w < 4) {
    const u32 vm = (tid >> 3) * 24576 + (tid & 7) * 16, s0 = mq * (128 * 6144) + cl * 128, s1 = s0 + 6144, s2 = s0 + 12288, s3 = s0 + 18432;
    LD16(wm[0], vm, rMOD, s0, 0); LD16(wm[1], vm, rMOD, s1, 0); LD16(wm[2], vm, rMOD, s2, 0); LD16(wm[3], vm, rMOD, s3, 0);
    const u32 vc = (tid >> 3) * 8, scq = mq * 256;
    LD8(cv, vc, rC, scq, 0);
    const u32 vz = 0; asm volatile("buffer_load_dword %0, %1, %2, 0 offen sc1" : "=v"(ep0) : "v"(vz), "s"(rWS));
  }
  if (tid < 16) *(LAS u32*)(L + L_CNT + tid * 4) = 0u;
  BAR();
  const int st_qd = lane >> 2, st_r = (st_qd & 3) + 4 * (st_qd >> 3);
  const int st_col = ((st_qd >> 2) & 1) * 64 + (((lane & 3) ^ ((w & 1) << 1)) << 4);
  const int rotp = g & 7;
  u32 vB1 = st_r * 3072 + st_col; PIN(vB1);
  const u32 mB1 = (w >> 2) * 12288 + (w & 3) * 1024, sB1 = (w >> 2) * 98304 + (w & 3) * 24576 + j * 128;
  auto stage_b1 = [&](auto PC, auto BC) {
    constexpr int pair = decltype(PC)::value, base = decltype(BC)::value;
    const u32 sp = ((pair + rotp) & 7) * 196608 + sB1;
    dma<base, 0>(mB1, vB1, rQKV, sp); dma<base + 4096, 1024>(mB1, vB1, rQKV, sp); dma<base + 8192, 2048>(mB1, vB1, rQKV, sp);
  };
  auto b1_piece = [&](auto PC, auto BC, auto TC) {
    constexpr int pair = decltype(PC)::value, base = decltype(BC)::value, ty = decltype(TC)::value;
    dma<base + ty * 4096, ty * 1024>(mB1, vB1, rQKV, ((pair + rotp) & 7) * 196608 + sB1);
  };
  const int hd = w & 3, blk2 = w >> 2, kq = w & 3, mh = w >> 2;
  const int cs0 = j < 4 ? 22 * j : 88 + 21 * (j - 4);
  const int cend = cs0 + (j < 4 ? 22 : 21);
  STAMP(1);
  const int T0 = 12 * (g >> 4) + 3 * (w & 3), rcb = min(cs0 + T0, 168);
  if (w < 4) {
    asm volatile("s_waitcnt vmcnt(0)" : "+v"(cv), "+v"(wm[0]), "+v"(wm[1]), "+v"(wm[2]), "+v"(wm[3]), "+v"(ep0)::"memory");
    const u32 tg = __builtin_amdgcn_readfirstlane(ep0) + 1u;
    const f32x4 cf = {blo(cv.x), bhi(cv.x), blo(cv.y), bhi(cv.y)};
    float sc[4];
#pragma unroll
    for (int r = 0; r < 4; ++r) sc[r] = cf[r] * __builtin_amdgcn_rcpf(1.f + __builtin_amdgcn_exp2f(cf[r] * -1.4426950408889634f));
    f32x2 v[4];
#pragma unroll
    for (int i = 0; i < 4; ++i) v[i] = (unp(wm[0][i]) * sc[0] + unp(wm[1][i]) * sc[1]) + (unp(wm[2][i]) * sc[2] + unp(wm[3][i]) * sc[3]);
    const float a0 = sw32_add(v[0].x, v[2].x), a1 = sw32_add(v[0].y, v[2].y), a2 = sw32_add(v[1].x, v[3].x), a3 = sw32_add(v[1].y, v[3].y);
    float t0 = sw16_add(a0, a2), t1 = sw16_add(a1, a3);
    t0 += dppf<0x128>(t0); t1 += dppf<0x128>(t1);
    f32x2 o = {t0, t1};
    *(LAS f32x2*)(L + L_RED + w * 256 + (lane & 7) * 32 + g4 * 8) = o;
    LGW();
    u32 old = 0;
    if (lane == 0) old = __hip_atomic_fetch_add((LAS u32*)(L + L_CNT + 28), 1u, __ATOMIC_RELAXED, __HIP_MEMORY_SCOPE_WORKGROUP);
    if (__builtin_amdgcn_readfirstlane(old) == 3u) {
      asm volatile("" ::: "memory");
      float s = 0.f;
#pragma unroll
      for (int i = 0; i < 4; ++i) s += *(LAS float*)(L + L_RED + i * 256 + lane * 4);
      v2u gr; gr.x = asu(s); gr.y = tg;
      if (b < 192) __builtin_amdgcn_raw_buffer_store_b64(gr, rWS, lane * 8, WS_MODG + (mq * 3072 + cl * 64) * 8, 16);
    }
  }
  STAMP(2);
  v4u xr[10];
  {
    const int r0 = 64 * g - 16 + w;
    const u32 s0 = (u32)max(r0, 0) * 1024u, s1 = (u32)max(r0 + 8, 0) * 1024u;
    LD16(xr[0], vl16, rX, s0, 0); LD16(xr[1], vl16, rX, s1, 0);
#pragma unroll
    for (int i = 2; i < 10; ++i) { const u32 so = (u32)(r0 + 8 * i) * 1024u; LD16(xr[i], vl16, rX, so, 0); }
  }
  stage_b1(IC<0>{}, IC<L_XY>{});
  int afo[4];
#pragma unroll
  for (int i = 0; i < 4; ++i) { afo[i] = blk2 * 32768 + c16 * 1024 + (((4 * i + g4) ^ c16) << 4); PIN(afo[i]); }
  const u64 am0 = __builtin_amdgcn_ballot_w64((c16 & 14) >= 4 * g4), am1 = __builtin_amdgcn_ballot_w64((c16 & 14) >= 4 * g4 + 2);
  u32 ahm = (lane & 1) ? 0xffff0000u : 0xffffu; PIN(ahm);
  u32 vOst = c16 * 16 + g4 * 256 + (hd & 1) * 8; PIN(vOst);
  int tB1 = g4 * 1024 + 64 * (c16 >> 2) + 8 * (lane & 3) + 32 * ((hd & 1) ^ (g4 & 1)) + 256 * (hd >> 1); PIN(tB1);
  int hw0 = w * 1024 + ((((lane - 8 * rotp) & 63) ^ w) << 4); PIN(hw0);
#define vfl ((opq(vl16) & 112u) << 3)
  STAMP(3);
  v4u bw[48];
  auto read_b1 = [&](int pair, int base, int off) {
#pragma unroll
    for (int kk = 0; kk < 2; ++kk)
#pragma unroll
      for (int ty = 0; ty < 3; ++ty) {
        LAS char* a = L + base + (off + kk * 12288 + ty * 4096);
        bw[(2 * pair + kk) * 3 + ty] = cat(trd(a), trd(a + 512));
      }
  };
  f32x2 xn[10][4];
  v4u ga_[4]; v2u pv;
  const u32 sMG = WS_MODG + w * 1024;
  {
    float u[10];
    sfor<0, 10>([&](auto I) {
      constexpr int i = decltype(I)::value;
      if constexpr (i == 0) asm volatile("s_waitcnt vmcnt(11)" : "+v"(xr[0]), "+v"(xr[1]), "+v"(ep)::"memory");
      if constexpr (i == 2) asm volatile("s_waitcnt vmcnt(9)" : "+v"(xr[2]), "+v"(xr[3])::"memory");
      if constexpr (i == 4) asm volatile("s_waitcnt vmcnt(10)" : "+v"(xr[4]), "+v"(xr[5])::"memory");
      if constexpr (i == 6) asm volatile("s_waitcnt vmcnt(8)" : "+v"(xr[6]), "+v"(xr[7])::"memory");
      if constexpr (i == 8) asm volatile("s_waitcnt vmcnt(9)" : "+v"(xr[8]), "+v"(xr[9])::"memory");
      float s = 0.f, q = 0.f;
#pragma unroll
      for (int e = 0; e < 4; ++e) { s = dot2(xr[i][e], 0x3f803f80u, s); q = dot2(xr[i][e], xr[i][e], q); }
      u[i] = sw32_add(s, q);
      PIN(u[i]);
      if constexpr (i == 3) stage_b1(IC<1>{}, IC<L_XY + 24576>{});
      if constexpr (i == 7) stage_b1(IC<2>{}, IC<L_XY + 49152>{});
      if constexpr (i == 9) {
        const u32 s0 = sMG, s1 = sMG + 24576, s2 = sMG + 49152, s3 = sMG + 73728;
        ld16s<0>(ga_[0], vl16, rWS, s0); ld16s<0>(ga_[1], vl16, rWS, s1); ld16s<0>(ga_[2], vl16, rWS, s2); ld16s<0>(ga_[3], vl16, rWS, s3);
      }
    });
    float t[5];
#pragma unroll
    for (int i = 0; i < 5; ++i) t[i] = row16_sum(sw16_add(u[i], u[i + 5]));
    float v = t[0];
#pragma unroll
    for (int i = 1; i < 5; ++i) v = c16 == i ? t[i] : v;
    auto r = __builtin_amdgcn_permlane32_swap(asu(v), asu(v), false, false);
    const float mean = asf(r[0]) * (1.f / 512.f);
    const float var = fmaxf(asf(r[1]) * (1.f / 512.f) - mean * mean, 0.f);
    float rs = __builtin_amdgcn_rsqf(var + 1e-5f), nm = -mean * rs;
    PIN(rs); PIN(nm);
    STAMP(4);
    { const u32 vo = ((lane >> 3) & 3) * 24576 + (lane & 7) * 8, so = WS_MODG + (1024 + 8 * b) * 8; LD8S(pv, vo, rWS, so, 0); }
    if (w >= 4) {
      const int u = lane >> 2, row = (u & 3) | ((u >> 1) & 4) | ((u << 1) & 8);
      const u32 vo = row * 2752 + (lane & 3) * 16, so = (g & 15) * (32 * 2752) + rcb * 16, mb = L_Z + 49152 + (w & 3) * 4096;
      dma<0, 0>(mb, vo, rW1, so); dma<1024, 16 * 2752>(mb, vo, rW1, so); dma<2048, 0>(mb, vo, rW3, so); dma<3072, 16 * 2752>(mb, vo, rW3, so);
    } else {
      const int u = lane >> 1, row = (u & 3) | ((u >> 1) & 4) | ((u << 1) & 8) | (u & 16);
      const u32 vo = row * 1024 + (lane & 1) * 16, so = g * 32768 + j * 128 + w * 32, mb = L_Z + 65536 + w * 2048;
      dma<0, 0>(mb, vo, rW2, so); dma<1024, 0>(mb, vo, rW2, so + (g < 11 ? 32 * 32768 : 0));
      const int rwo = (u & 7) | ((u >> 1) & 8) | ((u << 1) & 16);
      dma<0, 0>(76800 + w * 1024, rwo * 1024 + (lane & 1) * 16, rWO, (g & 15) * 32768 + j * 128 + w * 32);
    }
    sfor<0, 10>([&](auto I) {
      constexpr int i = decltype(I)::value;
      if constexpr (i == 2) stage_b1(IC<3>{}, IC<L_Z>{});
      if constexpr (i == 6) stage_b1(IC<4>{}, IC<L_Z + 24576>{});
      const float a = rdl(rs, (i / 5) * 16 + i % 5), c = rdl(nm, (i / 5) * 16 + i % 5);
#pragma unroll
      for (int e = 0; e < 4; ++e) { xn[i][e] = unp(xr[i][e]) * a + c; PIN(xn[i][e]); }
    });
  }
  const u32 tag = __builtin_amdgcn_readfirstlane(ep) + 1u;
  u32 m1; asm("s_lshr_b32 %0, 1, %1" : "=s"(m1) : "s"(w) : "scc");
#define RAISE(fo) raise_flag(rWS, (fo), tag, vl16, m1)
#define SIGNAL(idx, fo) signal_flag(rWS, L_CNT + 4 * (idx), (fo), tag)
  STAMP(5);
  asm volatile("s_waitcnt vmcnt(9)" : "+v"(ga_[0]), "+v"(ga_[1]), "+v"(ga_[2]), "+v"(ga_[3]), "+v"(pv)::"memory");
  auto gsum = [&](v4u (&a)[4], f32x2& o) -> u32 {
    o.x = (asf(a[0].x) + asf(a[1].x)) + (asf(a[2].x) + asf(a[3].x)); o.y = (asf(a[0].z) + asf(a[1].z)) + (asf(a[2].z) + asf(a[3].z));
    return ((a[0].y ^ tag) | (a[0].w ^ tag) | (a[1].y ^ tag) | (a[1].w ^ tag)) | ((a[2].y ^ tag) | (a[2].w ^ tag) | (a[3].y ^ tag) | (a[3].w ^ tag));
  };
  auto gather = [&](u32 voff, u32 soff, f32x2& o) {
    for (u32 sp = 0; sp < SPIN_CAP; ++sp) {
      asm volatile("" ::: "memory");
      v4u a[4];
#pragma unroll
      for (int q = 0; q < 4; ++q) a[q] = __builtin_amdgcn_raw_buffer_load_b128(rWS, voff, soff + q * 24576, 16);
      if (!__builtin_amdgcn_ballot_w64(gsum(a, o) != 0u)) break;
    }
  };
  const float one = w >= 4 ? 1.f : 0.f;
  {
    f32x2 mA;
    if (__builtin_amdgcn_ballot_w64(gsum(ga_, mA) != 0u)) gather(vl16, sMG, mA);
    *(LAS f32x2*)(L + L_MODA + w * 512 + (vl16 >> 1)) = mA + one;
  }
  BAR();
  if (w == 7) {
    const u32 vo = ((lane >> 3) & 3) * 24576 + (lane & 7) * 8, so = WS_MODG + (1024 + 8 * b) * 8;
    for (u32 sp = 0; sp < SPIN_CAP && __builtin_amdgcn_ballot_w64(pv.y != tag); ++sp) {
      asm volatile("" ::: "memory");
      pv = __builtin_amdgcn_raw_buffer_load_b64(rWS, vo, so, 16);
    }
    float sv = asf(pv.x);
    sv += dppf<0x128>(sv); sv = sw16_add(sv, sv);
    pv.x = asu(sv);
    __builtin_amdgcn_raw_buffer_store_b64(pv, rWS, (lane & 7) * 8, WS_MODF + (1024 + 8 * b) * 8, 16);
  }
  STAMP(6);
  int vma = L_MODA + 2 * vl16; PIN(vma);
  const f32x4 ms0 = *(LAS f32x4*)(L + vma), ms1 = *(LAS f32x4*)(L + vma + 16);
  const f32x4 ma0 = *(LAS f32x4*)(L + vma + 2048), ma1 = *(LAS f32x4*)(L + vma + 2064);
  int tz0 = tB1 + L_Z; PIN(tz0);
  { int t2 = tB1 + 49152; PIN(t2); read_b1(0, tB1, 0); read_b1(1, tB1, 24576); read_b1(2, t2, 0); }
  {
    VMW(6);
    if (w >= 4) {
      LAS char* a0 = L + L_Z + 49152 + (w & 3) * 4096 + ((lane >> 1) & 1) * 2048 + 64 * ((c16 >> 2) + 4 * (g4 & 1) + 16 * (g4 >> 1)) + 8 * (lane & 1);
#pragma unroll
      for (int ct = 0; ct < 3; ++ct) {
        const int c = min(cs0 + T0 + ct - rcb, 3) * 16;
        const v4u f = cat(trd(a0 + c), trd(a0 + c + 512));
        const u32 sf = WS_B4T + ((j * 16 + (g & 15)) * 24 + T0 + ct) * 1024;
        asm volatile("buffer_store_dwordx4 %0, %1, %2, %3 offen sc1\n\ts_nop 1" ::"v"(f), "v"(vl16), "s"(rWS), "s"(sf) : "memory");
      }
    }
    else {
      LAS char* a0 = L + L_Z + 65536 + w * 2048 + 32 * ((c16 >> 2) + 4 * (g4 & 1) + 16 * (g4 >> 1)) + 8 * (lane & 3);
#pragma unroll
      for (int i = 0; i < 2; ++i) {
        const v4u f = cat(trd(a0 + i * 1024), trd(a0 + i * 1024 + 256));
        const u32 sf = WS_B5T + ((j * 43 + g + (i && g < 11 ? 32 : 0)) * 4 + w) * 1024;
        asm volatile("buffer_store_dwordx4 %0, %1, %2, %3 offen sc1\n\ts_nop 1" ::"v"(f), "v"(vl16), "s"(rWS), "s"(sf) : "memory");
      }
      LAS char* a1 = L + 76800 + w * 1024 + 32 * ((c16 >> 2) + 4 * (g4 & 1) + 16 * (g4 >> 1)) + 8 * (lane & 3);
      const v4u f = cat(trd(a1), trd(a1 + 256));
      const u32 sf = WS_B3T + ((j * 16 + (g & 15)) * 4 + w) * 1024;
      asm volatile("buffer_store_dwordx4 %0, %1, %2, %3 offen sc1\n\ts_nop 1" ::"v"(f), "v"(vl16), "s"(rWS), "s"(sf) : "memory");
    }
  }
  BAR();
  STAMP(7);
  {
    int hw1 = (hw0 ^ 128) + 8192; PIN(hw1);
    sfor<0, 10>([&](auto I) {
      constexpr int i = decltype(I)::value;
      v4u o;
      o.x = pk2(xn[i][0] * ma0.xy + ms0.xy); o.y = pk2(xn[i][1] * ma0.zw + ms0.zw);
      o.z = pk2(xn[i][2] * ma1.xy + ms1.xy); o.w = pk2(xn[i][3] * ma1.zw + ms1.zw);
      if constexpr (i == 8) { hw0 += 65536; hw1 += 65536; }
      *(LAS v4u*)(L + ((i & 1) ? hw1 : hw0) + ((i >> 1) & 3) * 16384) = o;
      SCHED();
    });
  }
  VMW(0);
  BAR();
  RAISE(WS_RFLAG + (j * 32 + g) * 4);
  u32 flr;
  STAMP(8);
  __builtin_amdgcn_s_setprio(2);

  const f32x4 z4 = {0.f, 0.f, 0.f, 0.f};
#define AFRAG(off, ks) (*(LAS v4u*)(L + afo[(ks) & 3] + ((off) + ((ks) >> 2) * 256)))
  const int ch0 = cs0 + 3 * w;
  constexpr int ND = NODMA ? 0 : 1;
  v4u b4[48];
  const u32 sB4 = WS_B4T + (j * 16 * 24 + 3 * w) * 1024;
  auto load_b4 = [&](auto NC) {
    constexpr int n = decltype(NC)::value, ks = n / 3, ct = n % 3;
    const u32 so = sB4 + ((ks + 2 * rotp) & 15) * 24576;
    v4u& r = b4[n];
    if constexpr (ct == 0) LD16S(r, vl16, rWS, so, 0); else if constexpr (ct == 1) LD16S(r, vl16, rWS, so, 1024); else LD16S(r, vl16, rWS, so, 2048);
  };
  auto landed_b4 = [&](auto AC, auto BC) {
#pragma unroll
    for (int i = decltype(AC)::value; i < decltype(BC)::value; ++i) { v4u& r = b4[i]; PIN(r); }
  };
  v4u b3[16];
  auto load_b3 = [&](auto NC) { constexpr int n = decltype(NC)::value; ld16s<(n & 3) * 1024>(b3[n], vl16, rWS, WS_B3T + (j * 16 + 4 * kq + (n >> 2)) * 4096); };
  v4u gb_, gg_;
  u32 vgg, fl0, vRow;
  int pw, pr;
  f32x4 aKh = z4, aVh = z4, aQ = z4, aK = z4, aV = z4, bQ = z4, bK = z4, bV = z4;
  {
    v4u ah = AFRAG(0, 0), a1 = AFRAG(16384, 0), a2 = AFRAG(32768, 0);
    int l3, c3, g3; f32x2 mB, mG;
    auto job = [&](auto NC) {
      constexpr int n = decltype(NC)::value;
      if constexpr (n == 3) { l3 = opq(vl16) >> 4; c3 = l3 & 15; g3 = l3 >> 4; PIN(c3); PIN(g3); vgg = (vl16 < 512 ? 8192 : 20480 - 512) + 512 * j + vl16; PIN(vgg); }
      if constexpr (n == 7) { vRow = (8 * w + (l3 >> 3)) * 1024 + (l3 & 7) * 16; PIN(vRow); }
      if constexpr (n == 8) { pr = (8 * w + (l3 >> 3)) * 256 + (((2 * (l3 & 7)) ^ ((l3 >> 3) & 7)) << 4); PIN(pr); }
      if constexpr (n == 10) { pw = kq * 16384 + (32 * mh + c3) * 256 + ((g3 ^ (c3 & 3)) << 4) + ((c3 & 4) << 4); PIN(pw); }
      if constexpr (n == 31) { mB.x = asf(gb_.x) + one; PIN(mB.x); mB.y = asf(gb_.z) + one; PIN(mB.y); *(LAS f32x2*)(L + L_MODA + w * 512 + (vl16 >> 1)) = mB; }
      if constexpr (n == 32) { mG.x = asf(gg_.x); mG.y = asf(gg_.z); *(LAS f32x2*)(L + L_GAB + (vl16 >> 1)) = mG; }
      if constexpr (n == 34) { fl0 = (gb_.y ^ tag) | (gb_.w ^ tag) | (gg_.y ^ tag) | (gg_.w ^ tag); PIN(fl0); }
      if constexpr (n == 7) { const u32 s0 = WS_MODF + 12288 + w * 1024; LD16S(gb_, vl16, rWS, s0, 0); }
      if constexpr (n == 9) { const u32 s0 = WS_MODF; LD16S(gg_, vgg, rWS, s0, 0); }
      if constexpr (n < 6 && !(n & 1)) b1_piece(IC<5>{}, IC<L_Z + 49152>{}, IC<n / 2>{});
      if constexpr (n == 6) arrive(L_CNT + 8);
      if constexpr (n == 10) await8(L_CNT + 8);
      if constexpr (n >= 10 && n < 22 && !(n & 1)) b1_piece(IC<6 + (n - 10) / 6>{}, IC<L_Z + ((n - 10) / 6) * 24576>{}, IC<((n - 10) / 2) % 3>{});
      if constexpr (n == 21) { const u32 vr = (vl16 >> 2) & 124, sr = WS_RFLAG + j * 128; LD4S(flr, vr, rWS, sr); }
      if constexpr (n == 26) {
        asm volatile("s_waitcnt vmcnt(0)" : "+v"(gb_), "+v"(gg_), "+v"(flr)::"memory");
        arrive(L_CNT + 12);
        const u32 vr = (vl16 >> 2) & 124, sr = WS_RFLAG + j * 128; u32 cnt = SPIN_CAP;
        asm volatile("1:\n\tv_cmp_ne_u32 vcc, %5, %0\n\ts_cbranch_vccz 2f\n\tbuffer_load_dword %0, %2, %3, %4 offen sc1\n\ts_waitcnt vmcnt(0)\n\t"
                     "s_sub_u32 %1, %1, 1\n\ts_cbranch_scc0 1b\n\t2:" : "+v"(flr), "+s"(cnt) : "v"(vr), "s"(rWS), "s"(sr), "s"(tag) : "vcc", "scc", "memory");
      }
      if constexpr (n == 28) { await8(L_CNT + 12); int tz2 = tz0 + 49152; PIN(tz2); read_b1(5, tz2, 0); }
      if constexpr (n == 32) read_b1(6, tz0, 0);
      if constexpr (n == 37) read_b1(7, tz0, 24576);
      if constexpr (n >= 27 && n < 47 && (n & 1)) load_b3(IC<(n - 27) / 2>{});
    };
    sfor<0, 16>([&](auto KS) {
      constexpr int ks = decltype(KS)::value;
      KSTAMP(1, ks);
      if constexpr (ks == 0) read_b1(3, tz0, 0);
      if constexpr (ks == 1) read_b1(4, tz0, 24576);
      SCHED();
      v4u nh = ah, n1 = a1, n2 = a2;
      aKh = mfma32(bw[ks * 3 + 1], ah, aKh);
      aVh = mfma32(ah, bw[ks * 3 + 2], aVh);
      if constexpr (ks < 15) nh = AFRAG(0, ks + 1);
      job(IC<3 * ks>{});
      SCHED();
      aQ = mfma32(bw[ks * 3 + 0], a1, aQ);
      aK = mfma32(bw[ks * 3 + 1], a1, aK);
      aV = mfma32(a1, bw[ks * 3 + 2], aV);
      if constexpr (ks < 15) n1 = AFRAG(16384, ks + 1);
      job(IC<3 * ks + 1>{});
      SCHED();
      bQ = mfma32(bw[ks * 3 + 0], a2, bQ);
      bK = mfma32(bw[ks * 3 + 1], a2, bK);
      bV = mfma32(a2, bw[ks * 3 + 2], bV);
      if constexpr (ks < 15) n2 = AFRAG(32768, ks + 1);
      job(IC<3 * ks + 2>{});
      ah = nh; a1 = n1; a2 = n2;
      SCHED();
    });
  }
  __builtin_amdgcn_s_setprio(0);
  STAMP(9); KSTAMP(1, 16);
  {
    const u32 sOst = WS_O + ((g * 4 + 2 * blk2) * 16 + 2 * j + (hd >> 1)) * 1024;
    const v2u k1p = pk4(aK), v1p = pk4(aV);
    const v2u o0 = attend(pk4(aKh), pk4(aVh), k1p, v1p, pk4(aQ), am0, am1, ahm, g == 0 && blk2 == 0);
    const v2u o1 = attend(k1p, v1p, pk4(bK), pk4(bV), pk4(bQ), am0, am1, ahm, false);
    __builtin_amdgcn_raw_buffer_store_b64(o0, rWS, vOst, sOst, 16);
    __builtin_amdgcn_raw_buffer_store_b64(o1, rWS, vOst, sOst + 16384, 16);
  }
  if (__builtin_amdgcn_ballot_w64(fl0 != 0u)) {
    for (u32 sp = 0; sp < SPIN_CAP; ++sp) {
      asm volatile("" ::: "memory");
      gb_ = __builtin_amdgcn_raw_buffer_load_b128(rWS, vl16, WS_MODF + 12288 + w * 1024, 16);
      gg_ = __builtin_amdgcn_raw_buffer_load_b128(rWS, vgg, WS_MODF, 16);
      if (!__builtin_amdgcn_ballot_w64(((gb_.y ^ tag) | (gb_.w ^ tag) | (gg_.y ^ tag) | (gg_.w ^ tag)) != 0u)) break;
    }
    const f32x2 mB = {asf(gb_.x) + one, asf(gb_.z) + one}, mG = {asf(gg_.x), asf(gg_.z)};
    *(LAS f32x2*)(L + L_MODA + w * 512 + (vl16 >> 1)) = mB;
    *(LAS f32x2*)(L + L_GAB + (vl16 >> 1)) = mG;
  }
  STAMP(10);
  VMW(0);
  BAR();
  RAISE(flag_off(0, g, 0) + j * 128);
  STAMP(11);
  if (b == 0 && w == 7) {
    for (u32 sp = 0; sp < SPIN_CAP; ++sp) {
      asm volatile("" ::: "memory");
      const v4u v = __builtin_amdgcn_raw_buffer_load_b128(rWS, vl16, WS_RFLAG, 16);
      if (__all(v.x == tag && v.y == tag && v.z == tag && v.w == tag)) break;
    }
    __builtin_amdgcn_raw_buffer_store_b32(tag, rWS, 0, WS_EPOCH, 16);
  }

  v4u xv;
  { const u32 sx = g * 65536 + j * 128; LD16(xv, vRow, rX, sx, 0); }
  sfor<10, 16>(load_b3);
  const u64 care2 = 0x0101010101010101ull * (3u << (2 * kq));
  poll_flags(rWS, vfl, flag_off(0, g, 0), care2, tag);
  PIN(xv);
  v4u af[8];
  {
    const u32 so = WS_O + ((g * 4 + 2 * mh) * 16 + 4 * kq) * 1024, so1 = so + 16384;
    LD16S(af[0], vl16, rWS, so, 0); LD16S(af[1], vl16, rWS, so, 1024); LD16S(af[2], vl16, rWS, so, 2048); LD16S(af[3], vl16, rWS, so, 3072);
    LD16S(af[4], vl16, rWS, so1, 0); LD16S(af[5], vl16, rWS, so1, 1024); LD16S(af[6], vl16, rWS, so1, 2048); LD16S(af[7], vl16, rWS, so1, 3072);
  }
  STAMP(12);
  sfor<0, 2>([&](auto M) {
    constexpr int m = decltype(M)::value;
    asm volatile("s_waitcnt vmcnt(%c4)" : "+v"(af[4 * m]), "+v"(af[4 * m + 1]), "+v"(af[4 * m + 2]), "+v"(af[4 * m + 3]) : "i"(m == 0 ? 4 : 2) : "memory");
    if constexpr (m == 0) {
#pragma unroll
      for (int i = 0; i < 16; ++i) PIN(b3[i]);
    }
    f32x4 acc[4] = {z4, z4, z4, z4};
    sfor<0, 4>([&](auto SC) {
      constexpr int sx = decltype(SC)::value;
#pragma unroll
      for (int n = 0; n < 4; ++n) acc[n] = mfma32(b3[sx * 4 + n], af[4 * m + sx], acc[n]);
      if constexpr (sx & 1) load_b4(IC<2 * m + (sx >> 1)>{});
      SCHED();
    });
#pragma unroll
    for (int n = 0; n < 4; ++n) *(LAS f32x4*)(L + ((n & 1) ? (pw ^ 64) : pw) + (m * 4096 + (n >> 1) * 128)) = acc[n];
  });
  BAR();
  STAMP(13);
  v4u x1p;
  {
    f32x4 y0 = *(LAS f32x4*)(L + pr), y1 = *(LAS f32x4*)(L + (pr ^ 16));
#pragma unroll
    for (int q = 1; q < 4; ++q) { y0 += *(LAS f32x4*)(L + pr + q * 16384); y1 += *(LAS f32x4*)(L + (pr ^ 16) + q * 16384); }
    const int ga = L_GAB + ((vl16 & 112) << 1);
    const f32x4 g0 = *(LAS f32x4*)(L + ga), g1 = *(LAS f32x4*)(L + ga + 16);
    const f32x4 x0 = f32x4{blo(xv.x), bhi(xv.x), blo(xv.y), bhi(xv.y)} + g0 * y0, x1 = f32x4{blo(xv.z), bhi(xv.z), blo(xv.w), bhi(xv.w)} + g1 * y1;
    { const v4u o = cat(pk4(x0), pk4(x1)); x1p = o; const u32 so = WS_X1 + g * 65536 + j * 128;
      asm volatile("buffer_store_dwordx4 %0, %1, %2, %3 offen sc1\n\ts_nop 1" ::"v"(o), "v"(vRow), "s"(rWS), "s"(so) : "memory"); }
    const f32x4 sv = x0 + x1;
    float mean = (sv.x + sv.y) + (sv.z + sv.w);
    mean += dppf<0xB1>(mean); mean += dppf<0x4E>(mean); mean += dppf<0x141>(mean);
    mean *= 1.f / 64.f;
    const f32x4 d0 = x0 - mean, d1 = x1 - mean, dd = d0 * d0 + d1 * d1;
    float m2 = (dd.x + dd.y) + (dd.z + dd.w);
    m2 += dppf<0xB1>(m2); m2 += dppf<0x4E>(m2); m2 += dppf<0x141>(m2);
    v2u st; st.x = asu(mean); st.y = asu(m2);
    __builtin_amdgcn_raw_buffer_store_b64(st, rWS, (vRow >> 10) << 6, WS_STAT + g * 4096 + j * 8, 16);
  }
  STAMP(14);
  VMW(0);
  BAR();
  RAISE(flag_off(1, g, 0) + j * 128);
  sfor<4, 9>(load_b4);
  STAMP(15);

  u32 hmask[3];
#pragma unroll
  for (int ct = 0; ct < 3; ++ct)
    asm("s_cmp_lt_u32 %1, %2\n\ts_cselect_b32 %0, -1, 0" : "=s"(hmask[ct]) : "s"((u32)(ch0 + ct)), "s"((u32)cend) : "scc");
#pragma unroll
  for (int i = 0; i < 4; ++i) { afo[i] -= blk2 * 32768; PIN(afo[i]); }
  int vm2 = L_MODA + 2 * vl16; PIN(vm2);
  int h2w = w * 8192 + (((vl16 - 128 * rotp) & 1023) ^ ((8 * w & 15) << 4)); PIN(h2w);
  const f32x4 ls0 = *(LAS f32x4*)(L + vm2), ls1 = *(LAS f32x4*)(L + vm2 + 16), la0 = *(LAS f32x4*)(L + vm2 + 2048), la1 = *(LAS f32x4*)(L + vm2 + 2064);
  poll_flags(rWS, vfl, flag_off(1, g, 0), ~0ull, tag);
  landed_b4(IC<0>{}, IC<9>{});
  {
    v4u x1v[8]; v2u stv;
    const u32 so = WS_X1 + g * 65536 + w * 8192, so2 = so + 4096;
    { const u32 vs = vl16 >> 1, ss = WS_STAT + g * 4096 + w * 512; LD8S(stv, vs, rWS, ss, 0); }
    LD16S(x1v[0], vl16, rWS, so, 0); LD16S(x1v[1], vl16, rWS, so, 1024); LD16S(x1v[2], vl16, rWS, so, 2048); LD16S(x1v[3], vl16, rWS, so, 3072);
    LD16S(x1v[4], vl16, rWS, so2, 0); LD16S(x1v[5], vl16, rWS, so2, 1024); LD16S(x1v[6], vl16, rWS, so2, 2048); LD16S(x1v[7], vl16, rWS, so2, 3072);
    sfor<9, 12>(load_b4);
    asm volatile("s_waitcnt vmcnt(11)" : "+v"(stv)::"memory");
    const float mj = asf(stv.x);
    float mean = mj;
    mean += dppf<0xB1>(mean); mean += dppf<0x4E>(mean); mean += dppf<0x141>(mean);
    mean *= 0.125f;
    const float dm = mj - mean;
    float m2 = asf(stv.y) + 64.f * dm * dm;
    m2 += dppf<0xB1>(m2); m2 += dppf<0x4E>(m2); m2 += dppf<0x141>(m2);
    const float l2r = __builtin_amdgcn_rsqf(m2 * (1.f / 512.f) + 1e-5f), l2n = -mean * l2r;
    sfor<0, 8>([&](auto I) {
      constexpr int i = decltype(I)::value;
      v4u& xx = x1v[i];
      asm volatile("s_waitcnt vmcnt(%c1)" : "+v"(xx) : "i"(10 - i) : "memory");
      const float rs = rdl(l2r, 8 * i), nn = rdl(l2n, 8 * i);
      v4u o;
      o.x = pk2((unp(xx.x) * rs + nn) * la0.xy + ls0.xy); o.y = pk2((unp(xx.y) * rs + nn) * la0.zw + ls0.zw);
      o.z = pk2((unp(xx.z) * rs + nn) * la1.xy + ls1.xy); o.w = pk2((unp(xx.w) * rs + nn) * la1.zw + ls1.zw);
      *(LAS v4u*)(L + (h2w ^ (i << 4)) + i * 1024) = o;
    });
  }
  BAR();
  STAMP(16);
  __builtin_amdgcn_s_setprio(2);
  const int kb5 = 5 * w + min(w, 3);
  u64 care5 = 0x0101010101010101ull * ((0x80c06030180c0603ull >> (8 * w)) & 255u);
  asm volatile("" : "+s"(care5));
  v4u b5[24];
  const u32 sB5 = WS_B5T + (j * 43 + kb5) * 4096, d5 = (w + 5) >> 3;
  auto load_b5 = [&](auto NC) {
    constexpr int n = decltype(NC)::value, sx = n / 4, t = n % 4;
    ld16s<t * 1024>(b5[n], vl16, rWS, sB5 + (sx == 5 ? (5 - d5) * 4096 : sx * 4096));
  };
  f32x4 acc4[3][4];
#pragma unroll
  for (int ct = 0; ct < 3; ++ct)
#pragma unroll
    for (int m = 0; m < 4; ++m) acc4[ct][m] = z4;
  u32 vH; int pw5;
  {
    v4u a[4] = {AFRAG(0, 0), AFRAG(16384, 0), AFRAG(32768, 0), AFRAG(49152, 0)};
    sfor<0, 16>([&](auto KS) {
      constexpr int ks = decltype(KS)::value;
      KSTAMP(3, ks);
      if constexpr (ks >= 3 && ks < 13) { VMW(9); landed_b4(IC<3 * ks>{}, IC<3 * ks + 3>{}); }
      if constexpr (ks >= 13) { VMW(9); landed_b4(IC<3 * ks>{}, IC<3 * ks + 3>{}); }
      v4u nx[4] = {a[0], a[1], a[2], a[3]};
      sfor<0, 4>([&](auto MC) {
        constexpr int m = decltype(MC)::value;
        if constexpr (ks < 15) nx[m] = AFRAG(16384 * m, ks + 1);
        if constexpr (m < 3) { if constexpr (ks < 12) load_b4(IC<12 + 3 * ks + m>{}); else load_b5(IC<3 * (ks - 12) + m>{}); }
        SCHED();
#pragma unroll
        for (int ct = 0; ct < 3; ++ct) acc4[ct][m] = mfma32(b4[ks * 3 + ct], a[m], acc4[ct][m]);
        if constexpr (ks == 8 && m == 0) { vH = (vl16 & 255) + ((vl16 >> 5) & 8) + (vl16 >> 9) * (172 * 256); PIN(vH); }
        if constexpr (ks == 9 && m == 0) { const int l5 = opq(vl16) >> 4, c5 = l5 & 15, g5 = l5 >> 4;
          pw5 = w * 16384 + c5 * 256 + ((c5 & 4) << 4) + ((g5 ^ (c5 & 3)) << 4); PIN(pw5); }
        SCHED();
      });
#pragma unroll
      for (int m = 0; m < 4; ++m) a[m] = nx[m];
    });
  }
#pragma unroll
  for (int ct = 0; ct < 3; ++ct)
#pragma unroll
    for (int m = 0; m < 4; ++m) PIN(acc4[ct][m]);
  __builtin_amdgcn_s_setprio(0);
  STAMP(17); KSTAMP(3, 16);
#pragma unroll
  for (int ct = 0; ct < 3; ++ct)
#pragma unroll
    for (int mp = 0; mp < 2; ++mp) {
      f32x4 G, U, R;
#pragma unroll
      for (int e = 0; e < 4; ++e) {
        auto r = __builtin_amdgcn_permlane32_swap(asu(acc4[ct][2 * mp][e]), asu(acc4[ct][2 * mp + 1][e]), false, false);
        G[e] = asf(r[0]); U[e] = asf(r[1]);
      }
#pragma unroll
      for (int e = 0; e < 4; ++e) R[e] = qp(qp(G[e] * U[e]) * __builtin_amdgcn_rcpf(1.f + __builtin_amdgcn_exp2f(qp(G[e] * -1.4426950408889634f))));
      const v2u o = pk4(R);
      const u32 so = WS_HH + ((g * 4 + 2 * mp) * 172 + ch0 + ct) * 256;
      asm volatile("s_mov_b32 exec_lo, %4\n\ts_mov_b32 exec_hi, %4\n\tbuffer_store_dwordx2 %0, %1, %2, %3 offen sc1\n\ts_mov_b64 exec, -1"
                   ::"v"(o), "v"(vH), "s"(rWS), "s"(so), "s"(hmask[ct]) : "memory");
    }
  STAMP(18);
  sfor<12, 16>(load_b5);
  VMW(4);
  BAR();
  RAISE(flag_off(2, g, 0) + j * 128);
  STAMP(19);
  sfor<16, 22>(load_b5);
  poll_flags(rWS, vfl, flag_off(2, g, 0), care5, tag);
#pragma unroll
  for (int i = 0; i < 22; ++i) PIN(b5[i]);
  v4u hf[16];
  auto load_h = [&](auto NC) {
    constexpr int n = decltype(NC)::value, sx = n / 4, m = n % 4;
    ld16s<0>(hf[n & 15], vl16, rWS, WS_HH + ((g * 4 + m) * 172 + 4 * kb5) * 256 + (sx == 5 ? (5 - d5) * 1024 : sx * 1024));
  };
  sfor<0, 16>(load_h);
  sfor<22, 24>(load_b5);
  STAMP(20);
  {
    f32x4 acc[4][4];
#pragma unroll
    for (int m = 0; m < 4; ++m)
#pragma unroll
      for (int n = 0; n < 4; ++n) acc[m][n] = z4;
    sfor<0, 6>([&](auto SC) {
      constexpr int sx = decltype(SC)::value, r = (sx & 3) * 4;
      constexpr int nw = sx == 0 ? 14 : sx == 1 ? 14 : sx == 2 ? 14 : sx == 3 ? 10 : sx == 4 ? 4 : 0;
      asm volatile("s_waitcnt vmcnt(%c4)" : "+v"(hf[r]), "+v"(hf[r + 1]), "+v"(hf[r + 2]), "+v"(hf[r + 3]) : "i"(nw) : "memory");
      if constexpr (sx == 4) { PIN(b5[22]); PIN(b5[23]); }
      if constexpr (sx == 5) { if (w >= 3) { hf[r] = v4u{0u, 0u, 0u, 0u}; hf[r + 1] = v4u{0u, 0u, 0u, 0u}; hf[r + 2] = v4u{0u, 0u, 0u, 0u}; hf[r + 3] = v4u{0u, 0u, 0u, 0u}; } }
      sfor<0, 4>([&](auto MC) {
        constexpr int m = decltype(MC)::value;
#pragma unroll
        for (int n = 0; n < 4; ++n) acc[m][n] = mfma32(b5[sx * 4 + n], hf[r + m], acc[m][n]);
        SCHED();
        if constexpr (sx < 2) load_h(IC<16 + 4 * sx + m>{});
        SCHED();
      });
    });
#pragma unroll
    for (int m = 0; m < 4; ++m)
#pragma unroll
      for (int n = 0; n < 4; ++n) *(LAS f32x4*)(L + ((n & 1) ? (pw5 ^ 64) : pw5) + (m * 4096 + (n >> 1) * 128)) = acc[m][n];
  }
  BAR();
  STAMP(21);
  {
    const int l6 = opq(vl16) >> 4;
    const int p0 = opq((8 * w + (l6 >> 3)) * 256 + (((2 * (l6 & 7)) ^ ((l6 >> 3) & 7)) << 4)), p1 = opq(p0 + 65536);
    f32x4 y0 = *(LAS f32x4*)(L + p0), y1 = *(LAS f32x4*)(L + (p0 ^ 16));
#pragma unroll
    for (int q = 1; q < 8; ++q) { const int pb = q < 4 ? p0 : p1; y0 += *(LAS f32x4*)(L + pb + (q & 3) * 16384); y1 += *(LAS f32x4*)(L + (pb ^ 16) + (q & 3) * 16384); }
    const int gb = L_GAB + 256 + (l6 & 7) * 32;
    const f32x4 g0 = *(LAS f32x4*)(L + gb), g1 = *(LAS f32x4*)(L + gb + 16);
    const f32x4 o0 = f32x4{blo(x1p.x), bhi(x1p.x), blo(x1p.y), bhi(x1p.y)} + g0 * y0, o1 = f32x4{blo(x1p.z), bhi(x1p.z), blo(x1p.w), bhi(x1p.w)} + g1 * y1;
    const v4u o = cat(pk4(o0), pk4(o1));
    const u32 vo = (8 * w + (l6 >> 3)) * 1024 + (l6 & 7) * 16, so = g * 65536 + j * 128;
    asm volatile("buffer_store_dwordx4 %0, %1, %2, %3 offen sc1\n\ts_nop 1" ::"v"(o), "v"(vo), "s"(rOUT), "s"(so) : "memory");
  }
  STAMP(22);
  if (b == BSEL && w == 0 && MULT) { const u64 tp = *(volatile u64*)(p.ws + (20u << 20)); u64 t0 = __builtin_amdgcn_s_memrealtime(); const u64 d = MULT * (tA_ - tp); while (__builtin_amdgcn_s_memrealtime() - t0 < d && d < 20000) {} }
}

__global__ void __launch_bounds__(64) pre_ts(u64* out) { if (threadIdx.x == 0) *out = __builtin_amdgcn_s_memrealtime(); }
}

extern "C" void kernel_launch(void* const* d_in, const int* in_sizes, int n_in, void* d_out, int out_size, void* d_ws, size_t ws_size,
                              hipStream_t stream) {
  Params p;
  p.x = (const u16*)d_in[0]; p.wqkv = (const u16*)d_in[1]; p.wo = (const u16*)d_in[2];
  p.w1 = (const u16*)d_in[4]; p.w3 = (const u16*)d_in[5]; p.w2 = (const u16*)d_in[6];
  p.c = (const u16*)d_in[7]; p.wmod = (const u16*)d_in[8];
  p.out = (u16*)d_out; p.ws = (char*)d_ws;
  (void)hipFuncSetAttribute((const void*)fused_block, hipFuncAttributeMaxDynamicSharedMemorySize, L_TOTAL);
  pre_ts<<<1, 64, 0, stream>>>((u64*)(p.ws + (20u << 20)));
  fused_block<<<256, 512, L_TOTAL, stream>>>(p);
}
```

```cpp
#include <hip/hip_runtime.h>
#include <stdint.h>

#define KDBG 0
#define NODMA 0
#define CODEWARM 0
#define L2WARM 1
#define EARLYT 0
#define XMAP 2

#define DI __device__ __forceinline__
#define GAS __attribute__((address_space(1)))
#define LAS __attribute__((address_space(3)))
typedef unsigned u32;
typedef unsigned short u16;
typedef unsigned long long u64;
typedef u32 v4u __attribute__((ext_vector_type(4)));
typedef u32 v2u __attribute__((ext_vector_type(2)));
typedef float f32x4 __attribute__((ext_vector_type(4)));
typedef float f32x2 __attribute__((ext_vector_type(2)));
typedef __bf16 bf16x8 __attribute__((ext_vector_type(8)));
typedef __bf16 bf16x2 __attribute__((ext_vector_type(2)));
typedef short s16x4 __attribute__((ext_vector_type(4)));
typedef __amdgpu_buffer_rsrc_t rsrc_t;
typedef GAS u32 gu32;
typedef GAS u64 gu64;
#define RLX_AGENT __ATOMIC_RELAXED, __HIP_MEMORY_SCOPE_AGENT

namespace {
constexpr int S_ = 2048, D_ = 512, F_ = 1376;
constexpr u32 WS_EPOCH = 0;
constexpr u32 WS_FLAG = 32768;
constexpr u32 WS_STAT = 262144;
constexpr u32 WS_MODG = 524288;
constexpr u32 WS_MODF = 655360;
constexpr u32 WS_O = 1u << 20;
constexpr u32 WS_X1 = 3u << 20;
constexpr u32 WS_HH = 5u << 20;
constexpr u32 WS_RFLAG = 8192;
constexpr u32 WS_B4T = 11u << 20;
constexpr u32 WS_B5T = 14u << 20;
constexpr u32 WS_B3T = (15u << 20) + (512u << 10);
constexpr u32 WS_DBG = 16u << 20;
constexpr u32 WS_BYTES = 32u << 20;
constexpr int L_XY = 0;
constexpr int L_Z = 81920;
constexpr int L_MISC = 158720;
constexpr int L_TOTAL = 163840;
constexpr int L_RED = 73728;
constexpr int L_MODA = L_MISC;
constexpr int L_CNT = L_MISC + 4096;
constexpr int L_GAB = L_MISC + 4096 + 64;
constexpr int L_JUNK = L_MISC + 4096 + 64 + 512;
constexpr int L_STAMP = 156672;
constexpr u32 SPIN_CAP = 1u << 18;
constexpr u32 CODE_BYTES = 26112;

DI f32x4 mfma32(v4u a, v4u b, f32x4 c) {
  return __builtin_amdgcn_mfma_f32_16x16x32_bf16(__builtin_bit_cast(bf16x8, a), __builtin_bit_cast(bf16x8, b), c, 0, 0, 0);
}
DI f32x4 mfma16(v2u a, v2u b, f32x4 c) {
  return __builtin_amdgcn_mfma_f32_16x16x16bf16_1k(__builtin_bit_cast(s16x4, a), __builtin_bit_cast(s16x4, b), c, 0, 0, 0);
}
DI u32 pk(float lo, float hi) { f32x2 v = {lo, hi}; return __builtin_bit_cast(u32, __builtin_convertvector(v, bf16x2)); }
DI u32 pk2(f32x2 v) { return __builtin_bit_cast(u32, __builtin_convertvector(v, bf16x2)); }
DI v2u pk4(f32x4 v) { v2u r; r.x = pk(v.x, v.y); r.y = pk(v.z, v.w); return r; }
DI float asf(u32 u) { return __builtin_bit_cast(float, u); }
DI u32 asu(float f) { return __builtin_bit_cast(u32, f); }
DI float blo(u32 u) { return asf(u << 16); }
DI float bhi(u32 u) { return asf(u & 0xffff0000u); }
DI f32x2 unp(u32 u) { f32x2 r = {blo(u), bhi(u)}; return r; }
DI float dot2(u32 a, u32 b, float c) { return __builtin_amdgcn_fdot2_f32_bf16(__builtin_bit_cast(bf16x2, a), __builtin_bit_cast(bf16x2, b), c, false); }
DI v2u trd(LAS char* p) { return __builtin_bit_cast(v2u, __builtin_amdgcn_ds_read_tr16_b64_v4i16((LAS s16x4*)p)); }
DI v4u cat(v2u a, v2u b) { v4u r; r.x = a.x; r.y = a.y; r.z = b.x; r.w = b.y; return r; }
template <int MI, int SI> DI void dma(u32 mb, u32 voff, rsrc_t srd, u32 sb) {
  u32 t;
  asm volatile("s_add_u32 m0, %1, %c2\n\ts_add_u32 %0, %3, %c4\n\tbuffer_load_dwordx4 %5, %6, %0 offen lds"
               : "=&s"(t) : "s"(mb), "i"(MI), "s"(sb), "i"(SI), "v"(voff), "s"(srd) : "scc");
}
DI void touch(u32 voff, rsrc_t srd, u32 sb) {
  asm volatile("s_mov_b32 m0, %c0\n\ts_nop 0\n\tbuffer_load_dword %1, %2, %3 offen lds" ::"i"(L_JUNK), "v"(voff), "s"(srd), "s"(sb));
}
#define BAR() asm volatile("s_waitcnt lgkmcnt(0)\n\ts_barrier" ::: "memory")
#define VMW(n) asm volatile("s_waitcnt vmcnt(" #n ")" ::: "memory")
#define LGW() asm volatile("s_waitcnt lgkmcnt(0)" ::: "memory")
#define SCHED() __builtin_amdgcn_sched_barrier(0)
#define LD16(dst, voff, srd, soff, IMM) asm volatile("buffer_load_dwordx4 %0, %1, %2, %3 offen offset:" #IMM : "=v"(dst) : "v"(voff), "s"(srd), "s"(soff))
#define LD16S(dst, voff, srd, soff, IMM) asm volatile("buffer_load_dwordx4 %0, %1, %2, %3 offen offset:" #IMM " sc1" : "=v"(dst) : "v"(voff), "s"(srd), "s"(soff))
#define LD8(dst, voff, srd, soff, IMM) asm volatile("buffer_load_dwordx2 %0, %1, %2, %3 offen offset:" #IMM : "=v"(dst) : "v"(voff), "s"(srd), "s"(soff))
#define LD8S(dst, voff, srd, soff, IMM) asm volatile("buffer_load_dwordx2 %0, %1, %2, %3 offen offset:" #IMM " sc1" : "=v"(dst) : "v"(voff), "s"(srd), "s"(soff))
#define LD4(dst, voff, srd, soff) asm volatile("buffer_load_dword %0, %1, %2, %3 offen" : "=v"(dst) : "v"(voff), "s"(srd), "s"(soff))
#define LD4S(dst, voff, srd, soff) asm volatile("buffer_load_dword %0, %1, %2, %3 offen sc1" : "=v"(dst) : "v"(voff), "s"(srd), "s"(soff))

template <int IMM> DI void ld16s(v4u& r, u32 v, rsrc_t rs, u32 so) { asm volatile("buffer_load_dwordx4 %0, %1, %2, %3 offen offset:%c4 sc1" : "=v"(r) : "v"(v), "s"(rs), "s"(so), "i"(IMM)); }
template <int CTRL> DI float dppf(float v) { return asf(__builtin_amdgcn_update_dpp(0, asu(v), CTRL, 0xf, 0xf, true)); }
DI float row16_sum(float v) { v += dppf<0x128>(v); v += dppf<0x124>(v); v += dppf<0x122>(v); v += dppf<0x121>(v); return v; }
DI float sw32_add(float a, float b) { auto r = __builtin_amdgcn_permlane32_swap(asu(a), asu(b), false, false); return asf(r[0]) + asf(r[1]); }
DI float sw16_add(float a, float b) { auto r = __builtin_amdgcn_permlane16_swap(asu(a), asu(b), false, false); return asf(r[0]) + asf(r[1]); }
DI float x16_max(float v) { auto r = __builtin_amdgcn_permlane16_swap(asu(v), asu(v), false, false); return fmaxf(asf(r[0]), asf(r[1])); }
DI float x32_max(float v) { auto r = __builtin_amdgcn_permlane32_swap(asu(v), asu(v), false, false); return fmaxf(asf(r[0]), asf(r[1])); }
DI int fresh_lane() { int l; asm volatile("v_mbcnt_lo_u32_b32 %0, -1, 0\n\tv_mbcnt_hi_u32_b32 %0, -1, %0" : "=v"(l)); return l; }
DI float rdl(float v, int l) { return asf(__builtin_amdgcn_readlane(asu(v), l)); }

struct Params {
  const u16 *x, *wqkv, *wo, *w1, *w3, *w2, *c, *wmod;
  u16* out;
  char* ws;
};

#define LOOPSEL 0
#define STAMP(i) do {} while (0)
#define KSTAMP(lp, i) do {} while (0)
#define STAMP_DUMP() do {} while (0)

DI u32 flag_off(int phase, int g, int h) { return WS_FLAG + ((phase * 32 + g) * 2 + h) * 1024; }
DI void poll_flags(rsrc_t rWS, u32 vfl, u32 foff, u64 care, u32 tag) {
  for (u32 sp = 0; sp < SPIN_CAP; ++sp) {
    asm volatile("" ::: "memory");
    const u32 v = __builtin_amdgcn_raw_buffer_load_b32(rWS, vfl, foff, 16);
    if (!(__builtin_amdgcn_ballot_w64(v != tag) & care)) break;
  }
  VMW(0);
}
DI void raise_flag(rsrc_t rWS, u32 foff, u32 vtag, u32 vzero, u32 m1) {
  asm volatile("s_mov_b32 exec_hi, 0\n\ts_mov_b32 exec_lo, %4\n\tbuffer_store_dword %0, %1, %2, %3 offen sc1\n\ts_mov_b64 exec, -1" ::"v"(vtag), "v"(vzero), "s"(rWS), "s"(foff), "s"(m1) : "memory");
}
DI void signal_flag(rsrc_t rWS, u32 cnt_addr, u32 foff, u32 tag) {
  u32 t, a = cnt_addr, one = 1u, vt = tag, vz = 0u;
  asm volatile("s_mov_b64 exec, 1\n\tds_add_rtn_u32 %0, %1, %2\n\ts_waitcnt lgkmcnt(0)\n\tv_cmp_eq_u32 vcc, 7, %0\n\ts_and_b64 exec, exec, vcc\n\t"
               "buffer_store_dword %3, %4, %5, %6 offen sc1\n\ts_mov_b64 exec, -1"
               : "=&v"(t) : "v"(a), "v"(one), "v"(vt), "v"(vz), "s"(rWS), "s"(foff) : "vcc", "scc", "memory");
}
DI void arrive(u32 cnt_addr) {
  u32 a = cnt_addr, one = 1u;
  asm volatile("s_waitcnt lgkmcnt(0)\n\ts_mov_b64 exec, 1\n\tds_add_u32 %0, %1\n\ts_mov_b64 exec, -1" ::"v"(a), "v"(one) : "memory");
}
DI void await8(u32 cnt_addr) {
  u32 a = cnt_addr, t, sv;
  asm volatile("1:\n\tds_read_b32 %0, %2\n\ts_waitcnt lgkmcnt(0)\n\tv_readfirstlane_b32 %1, %0\n\ts_nop 0\n\ts_cmp_lt_u32 %1, 8\n\ts_cbranch_scc1 1b"
               : "=&v"(t), "=&s"(sv) : "v"(a) : "scc", "memory");
}
DI void raise_when8(rsrc_t rWS, u32 cnt_addr, u32 foff, u32 tag, u32 w7) {
  u32 a = cnt_addr, t, sv, vt = tag, vz = 0u;
  asm volatile("s_cmp_eq_u32 %7, 0\n\ts_cbranch_scc1 2f\n\t1:\n\tds_read_b32 %0, %2\n\ts_waitcnt lgkmcnt(0)\n\tv_readfirstlane_b32 %1, %0\n\ts_nop 0\n\ts_cmp_lt_u32 %1, 8\n\ts_cbranch_scc1 1b\n\t"
               "s_mov_b64 exec, 1\n\tbuffer_store_dword %3, %4, %5, %6 offen sc1\n\ts_mov_b64 exec, -1\n\t2:"
               : "=&v"(t), "=&s"(sv) : "v"(a), "v"(vt), "v"(vz), "s"(rWS), "s"(foff), "s"(w7) : "scc", "memory");
}
#define PIN(x) asm volatile("" : "+v"(x))
DI float qp(float r) { PIN(r); return r; }
DI float q_shl(u32 a) { return qp(blo(a)); }
DI float q_hi(u32 a) { return qp(bhi(a)); }
DI float q_fma(float a, float b, float c) { return qp(__builtin_fmaf(a, b, c)); }
DI float q_fmas(float a, float b, float c) { return qp(__builtin_fmaf(a, b, c)); }
DI float q_mul(float a, float b) { return qp(a * b); }
DI float q_mulk(float a) { return qp(a * -1.4426950408889634f); }
DI float q_add1(float a) { return qp(a + 1.f); }
DI float q_exp(float a) { return qp(__builtin_amdgcn_exp2f(a)); }
DI float q_rcp(float a) { return qp(__builtin_amdgcn_rcpf(a)); }
DI u32 q_cvt(float a, float b) { u32 r = pk(a, b); PIN(r); return r; }
DI float q_mov(float a) { return qp(a); }
DI u32 opq(u32 v) { asm volatile("" : "+v"(v)); return v; }

DI float sel(u64 m, float a, float b) { return __builtin_amdgcn_inverse_ballot_w64(m) ? a : b; }
DI v2u attend(v2u kp, v2u vp, v2u kc, v2u vc, v2u q, u64 m0, u64 m1, u32 hm, bool noprev) {
  const f32x4 z = {0.f, 0.f, 0.f, 0.f};
  const f32x4 sp = mfma16(kp, q, z);
  const f32x4 sc = mfma16(kc, q, z);
  const float K2 = 0.25f * 1.4426950408889634f;
  const u64 par = 0xaaaaaaaaaaaaaaaaull;
  float p0 = sel(par, sp[1], sp[0]), p1 = sel(par, sp[3], sp[2]);
  if (noprev) { p0 = -INFINITY; p1 = -INFINITY; }
  const float t0 = sel(m0, sel(par, sc[1], sc[0]), p0), t1 = sel(m1, sel(par, sc[3], sc[2]), p1);
  const float m = x32_max(x16_max(fmaxf(t0, t1)));
  const float mk = -m * K2;
  const float e0 = __builtin_amdgcn_exp2f(__builtin_fmaf(t0, K2, mk)), e1 = __builtin_amdgcn_exp2f(__builtin_fmaf(t1, K2, mk));
  const u32 w0 = pk(e0, e0) & hm, w1 = pk(e1, e1) & hm;
  v4u pf;
  pf.x = __builtin_amdgcn_inverse_ballot_w64(m0) ? 0u : w0; pf.y = __builtin_amdgcn_inverse_ballot_w64(m1) ? 0u : w1;
  pf.z = __builtin_amdgcn_inverse_ballot_w64(m0) ? w0 : 0u; pf.w = __builtin_amdgcn_inverse_ballot_w64(m1) ? w1 : 0u;
  const f32x4 o = mfma32(cat(vp, vc), pf, z);
  float sum = e0 + e1;
  sum = sw16_add(sum, sum); sum = sw32_add(sum, sum);
  const float inv = __builtin_amdgcn_rcpf(sum);
  return pk4(f32x4{qp(o.x * inv), qp(o.y * inv), qp(o.z * inv), qp(o.w * inv)});
}

template <int N> using IC = std::integral_constant<int, N>;
template <int I, int N, class F> DI void sfor(F&& f) { if constexpr (I < N) { f(IC<I>{}); sfor<I + 1, N>(f); } }

__global__ void __launch_bounds__(512, 2) fused_block(Params p) {
  extern __shared__ __attribute__((aligned(16))) char smem[];
  LAS char* L = (LAS char*)smem;
  const int tid = threadIdx.x, lane = tid & 63, w = __builtin_amdgcn_readfirstlane(tid >> 6);
  const int c16 = lane & 15, g4 = lane >> 4;
  const int b = blockIdx.x;
  int g, j;
  if (XMAP == 0) { g = (b & 7) * 4 + ((b >> 3) & 3); j = b >> 5; }
  else if (XMAP == 2) { const int x = b & 7, r = b >> 3; g = (x >> 1) * 8 + (r & 7); j = (x & 1) * 4 + (r >> 3); }
  else { const int x = b & 7, r = b >> 3; g = (x >> 2) * 16 + (r & 15); j = (x & 3) * 2 + (r >> 4); }
  const rsrc_t rX = __builtin_amdgcn_make_buffer_rsrc((void*)p.x, 0, S_ * D_ * 2, 0x00020000);
  const rsrc_t rQKV = __builtin_amdgcn_make_buffer_rsrc((void*)p.wqkv, 0, D_ * 3 * D_ * 2, 0x00020000);
  const rsrc_t rWO = __builtin_amdgcn_make_buffer_rsrc((void*)p.wo, 0, D_ * D_ * 2, 0x00020000);
  const rsrc_t rW1 = __builtin_amdgcn_make_buffer_rsrc((void*)p.w1, 0, D_ * F_ * 2, 0x00020000);
  const rsrc_t rW3 = __builtin_amdgcn_make_buffer_rsrc((void*)p.w3, 0, D_ * F_ * 2, 0x00020000);
  const rsrc_t rW2 = __builtin_amdgcn_make_buffer_rsrc((void*)p.w2, 0, F_ * D_ * 2, 0x00020000);
  const rsrc_t rC = __builtin_amdgcn_make_buffer_rsrc((void*)p.c, 0, D_ * 2, 0x00020000);
  const rsrc_t rMOD = __builtin_amdgcn_make_buffer_rsrc((void*)p.wmod, 0, D_ * 6 * D_ * 2, 0x00020000);
  const rsrc_t rWS = __builtin_amdgcn_make_buffer_rsrc((void*)p.ws, 0, WS_BYTES, 0x00020000);
  const rsrc_t rOUT = __builtin_amdgcn_make_buffer_rsrc((void*)p.out, 0, S_ * D_ * 2, 0x00020000);
  STAMP(0);

  u32 ep;
  { const u32 vz = 0; asm volatile("buffer_load_dword %0, %1, %2, 0 offen sc1" : "=v"(ep) : "v"(vz), "s"(rWS)); }
  u32 vl16 = lane * 16; PIN(vl16);
  const int bm = b < 192 ? b : b - 192, mq = bm / 48, cl = bm - 48 * mq;
  v4u wm[4]; v2u cv; u32 ep0;
  if (w < 4) {
    const u32 vm = (tid >> 3) * 24576 + (tid & 7) * 16, s0 = mq * (128 * 6144) + cl * 128, s1 = s0 + 6144, s2 = s0 + 12288, s3 = s0 + 18432;
    LD16(wm[0], vm, rMOD, s0, 0); LD16(wm[1], vm, rMOD, s1, 0); LD16(wm[2], vm, rMOD, s2, 0); LD16(wm[3], vm, rMOD, s3, 0);
    const u32 vc = (tid >> 3) * 8, scq = mq * 256;
    LD8(cv, vc, rC, scq, 0);
    const u32 vz = 0; asm volatile("buffer_load_dword %0, %1, %2, 0 offen sc1" : "=v"(ep0) : "v"(vz), "s"(rWS));
  }
  if (tid < 16) *(LAS u32*)(L + L_CNT + tid * 4) = 0u;
  BAR();
  const int st_qd = lane >> 2, st_r = (st_qd & 3) + 4 * (st_qd >> 3);
  const int st_col = ((st_qd >> 2) & 1) * 64 + (((lane & 3) ^ ((w & 1) << 1)) << 4);
  const int rotp = g & 7;
  u32 vB1 = st_r * 3072 + st_col; PIN(vB1);
  const u32 mB1 = (w >> 2) * 12288 + (w & 3) * 1024, sB1 = (w >> 2) * 98304 + (w & 3) * 24576 + j * 128;
  auto stage_b1 = [&](auto PC, auto BC) {
    constexpr int pair = decltype(PC)::value, base = decltype(BC)::value;
    const u32 sp = ((pair + rotp) & 7) * 196608 + sB1;
    dma<base, 0>(mB1, vB1, rQKV, sp); dma<base + 4096, 1024>(mB1, vB1, rQKV, sp); dma<base + 8192, 2048>(mB1, vB1, rQKV, sp);
  };
  auto b1_piece = [&](auto PC, auto BC, auto TC) {
    constexpr int pair = decltype(PC)::value, base = decltype(BC)::value, ty = decltype(TC)::value;
    dma<base + ty * 4096, ty * 1024>(mB1, vB1, rQKV, ((pair + rotp) & 7) * 196608 + sB1);
  };
  const int hd = w & 3, blk2 = w >> 2, kq = w & 3, mh = w >> 2;
  const int cs0 = j < 4 ? 22 * j : 88 + 21 * (j - 4);
  const int cend = cs0 + (j < 4 ? 22 : 21);
  STAMP(1);
  const int T0 = 12 * (g >> 4) + 3 * (w & 3), rcb = min(cs0 + T0, 168);
  if (w < 4) {
    asm volatile("s_waitcnt vmcnt(0)" : "+v"(cv), "+v"(wm[0]), "+v"(wm[1]), "+v"(wm[2]), "+v"(wm[3]), "+v"(ep0)::"memory");
    const u32 tg = __builtin_amdgcn_readfirstlane(ep0) + 1u;
    const f32x4 cf = {blo(cv.x), bhi(cv.x), blo(cv.y), bhi(cv.y)};
    float sc[4];
#pragma unroll
    for (int r = 0; r < 4; ++r) sc[r] = cf[r] * __builtin_amdgcn_rcpf(1.f + __builtin_amdgcn_exp2f(cf[r] * -1.4426950408889634f));
    f32x2 v[4];
#pragma unroll
    for (int i = 0; i < 4; ++i) v[i] = (unp(wm[0][i]) * sc[0] + unp(wm[1][i]) * sc[1]) + (unp(wm[2][i]) * sc[2] + unp(wm[3][i]) * sc[3]);
    const float a0 = sw32_add(v[0].x, v[2].x), a1 = sw32_add(v[0].y, v[2].y), a2 = sw32_add(v[1].x, v[3].x), a3 = sw32_add(v[1].y, v[3].y);
    float t0 = sw16_add(a0, a2), t1 = sw16_add(a1, a3);
    t0 += dppf<0x128>(t0); t1 += dppf<0x128>(t1);
    f32x2 o = {t0, t1};
    *(LAS f32x2*)(L + L_RED + w * 256 + (lane & 7) * 32 + g4 * 8) = o;
    LGW();
    u32 old = 0;
    if (lane == 0) old = __hip_atomic_fetch_add((LAS u32*)(L + L_CNT + 28), 1u, __ATOMIC_RELAXED, __HIP_MEMORY_SCOPE_WORKGROUP);
    if (__builtin_amdgcn_readfirstlane(old) == 3u) {
      asm volatile("" ::: "memory");
      float s = 0.f;
#pragma unroll
      for (int i = 0; i < 4; ++i) s += *(LAS float*)(L + L_RED + i * 256 + lane * 4);
      v2u gr; gr.x = asu(s); gr.y = tg;
      if (b < 192) __builtin_amdgcn_raw_buffer_store_b64(gr, rWS, lane * 8, WS_MODG + (mq * 3072 + cl * 64) * 8, 16);
    }
  }
  STAMP(2);
  v4u xr[10];
  const int rx2 = (0x4310 >> (4 * (j & 3))) & 7;
  {
    const int r0 = 64 * g - 16 + w;
#pragma unroll
    for (int i = 0; i < 10; ++i) {
      int bp = (i >> 1) + rx2; bp = bp >= 5 ? bp - 5 : bp;
      const u32 so = (u32)max(r0 + 16 * bp + 8 * (i & 1), 0) * 1024u;
      LD16(xr[i], vl16, rX, so, 0);
    }
  }
  stage_b1(IC<0>{}, IC<L_XY>{});
  int afo[4];
#pragma unroll
  for (int i = 0; i < 4; ++i) { afo[i] = blk2 * 32768 + c16 * 1024 + (((4 * i + g4) ^ c16) << 4); PIN(afo[i]); }
  const u64 am0 = __builtin_amdgcn_ballot_w64((c16 & 14) >= 4 * g4), am1 = __builtin_amdgcn_ballot_w64((c16 & 14) >= 4 * g4 + 2);
  u32 ahm = (lane & 1) ? 0xffff0000u : 0xffffu; PIN(ahm);
  u32 vOst = c16 * 16 + g4 * 256 + (hd & 1) * 8; PIN(vOst);
  int tB1 = g4 * 1024 + 64 * (c16 >> 2) + 8 * (lane & 3) + 32 * ((hd & 1) ^ (g4 & 1)) + 256 * (hd >> 1); PIN(tB1);
  int hw0 = w * 1024 + ((((lane - 8 * rotp) & 63) ^ w) << 4); PIN(hw0);
#define vfl ((opq(vl16) & 112u) << 3)
  STAMP(3);
  v4u bw[48];
  auto read_b1 = [&](int pair, int base, int off) {
#pragma unroll
    for (int kk = 0; kk < 2; ++kk)
#pragma unroll
      for (int ty = 0; ty < 3; ++ty) {
        LAS char* a = L + base + (off + kk * 12288 + ty * 4096);
        bw[(2 * pair + kk) * 3 + ty] = cat(trd(a), trd(a + 512));
      }
  };
  f32x2 xn[10][4];
  v4u ga_[4]; v2u pv;
  const u32 sMG = WS_MODG + w * 1024;
  {
    float u[10];
    sfor<0, 10>([&](auto I) {
      constexpr int i = decltype(I)::value;
      if constexpr (i == 0) asm volatile("s_waitcnt vmcnt(11)" : "+v"(xr[0]), "+v"(xr[1]), "+v"(ep)::"memory");
      if constexpr (i == 2) asm volatile("s_waitcnt vmcnt(9)" : "+v"(xr[2]), "+v"(xr[3])::"memory");
      if constexpr (i == 4) asm volatile("s_waitcnt vmcnt(10)" : "+v"(xr[4]), "+v"(xr[5])::"memory");
      if constexpr (i == 6) asm volatile("s_waitcnt vmcnt(8)" : "+v"(xr[6]), "+v"(xr[7])::"memory");
      if constexpr (i == 8) asm volatile("s_waitcnt vmcnt(9)" : "+v"(xr[8]), "+v"(xr[9])::"memory");
      float s = 0.f, q = 0.f;
#pragma unroll
      for (int e = 0; e < 4; ++e) { s = dot2(xr[i][e], 0x3f803f80u, s); q = dot2(xr[i][e], xr[i][e], q); }
      u[i] = sw32_add(s, q);
      PIN(u[i]);
      if constexpr (i == 3) stage_b1(IC<1>{}, IC<L_XY + 24576>{});
      if constexpr (i == 7) stage_b1(IC<2>{}, IC<L_XY + 49152>{});
      if constexpr (i == 9) {
        const u32 s0 = sMG, s1 = sMG + 24576, s2 = sMG + 49152, s3 = sMG + 73728;
        ld16s<0>(ga_[0], vl16, rWS, s0); ld16s<0>(ga_[1], vl16, rWS, s1); ld16s<0>(ga_[2], vl16, rWS, s2); ld16s<0>(ga_[3], vl16, rWS, s3);
      }
    });
    float t[5];
#pragma unroll
    for (int i = 0; i < 5; ++i) t[i] = row16_sum(sw16_add(u[i], u[i + 5]));
    float v = t[0];
#pragma unroll
    for (int i = 1; i < 5; ++i) v = c16 == i ? t[i] : v;
    auto r = __builtin_amdgcn_permlane32_swap(asu(v), asu(v), false, false);
    const float mean = asf(r[0]) * (1.f / 512.f);
    const float var = fmaxf(asf(r[1]) * (1.f / 512.f) - mean * mean, 0.f);
    float rs = __builtin_amdgcn_rsqf(var + 1e-5f), nm = -mean * rs;
    PIN(rs); PIN(nm);
    STAMP(4);
    { const u32 vo = ((lane >> 3) & 3) * 24576 + (lane & 7) * 8, so = WS_MODG + (1024 + 8 * b) * 8; LD8S(pv, vo, rWS, so, 0); }
    if (w >= 4) {
      const int u = lane >> 2, row = (u & 3) | ((u >> 1) & 4) | ((u << 1) & 8);
      const u32 vo = row * 2752 + (lane & 3) * 16, so = (g & 15) * (32 * 2752) + rcb * 16, mb = L_Z + 49152 + (w & 3) * 4096;
      dma<0, 0>(mb, vo, rW1, so); dma<1024, 16 * 2752>(mb, vo, rW1, so); dma<2048, 0>(mb, vo, rW3, so); dma<3072, 16 * 2752>(mb, vo, rW3, so);
    } else {
      const int u = lane >> 1, row = (u & 3) | ((u >> 1) & 4) | ((u << 1) & 8) | (u & 16);
      const u32 vo = row * 1024 + (lane & 1) * 16, so = g * 32768 + j * 128 + w * 32, mb = L_Z + 65536 + w * 2048;
      dma<0, 0>(mb, vo, rW2, so); dma<1024, 0>(mb, vo, rW2, so + (g < 11 ? 32 * 32768 : 0));
      const int rwo = (u & 7) | ((u >> 1) & 8) | ((u << 1) & 16);
      dma<0, 0>(76800 + w * 1024, rwo * 1024 + (lane & 1) * 16, rWO, (g & 15) * 32768 + j * 128 + w * 32);
    }
    sfor<0, 10>([&](auto I) {
      constexpr int i = decltype(I)::value;
      if constexpr (i == 2) stage_b1(IC<3>{}, IC<L_Z>{});
      if constexpr (i == 6) stage_b1(IC<4>{}, IC<L_Z + 24576>{});
      const float a = rdl(rs, (i / 5) * 16 + i % 5), c = rdl(nm, (i / 5) * 16 + i % 5);
#pragma unroll
      for (int e = 0; e < 4; ++e) { xn[i][e] = unp(xr[i][e]) * a + c; PIN(xn[i][e]); }
    });
  }
  const u32 tag = __builtin_amdgcn_readfirstlane(ep) + 1u;
  u32 m1; asm("s_lshr_b32 %0, 1, %1" : "=s"(m1) : "s"(w) : "scc");
#define RAISE(fo) raise_flag(rWS, (fo), tag, vl16, m1)
#define SIGNAL(idx, fo) signal_flag(rWS, L_CNT + 4 * (idx), (fo), tag)
  STAMP(5);
  asm volatile("s_waitcnt vmcnt(9)" : "+v"(ga_[0]), "+v"(ga_[1]), "+v"(ga_[2]), "+v"(ga_[3]), "+v"(pv)::"memory");
  auto gsum = [&](v4u (&a)[4], f32x2& o) -> u32 {
    o.x = (asf(a[0].x) + asf(a[1].x)) + (asf(a[2].x) + asf(a[3].x)); o.y = (asf(a[0].z) + asf(a[1].z)) + (asf(a[2].z) + asf(a[3].z));
    return ((a[0].y ^ tag) | (a[0].w ^ tag) | (a[1].y ^ tag) | (a[1].w ^ tag)) | ((a[2].y ^ tag) | (a[2].w ^ tag) | (a[3].y ^ tag) | (a[3].w ^ tag));
  };
  auto gather = [&](u32 voff, u32 soff, f32x2& o) {
    for (u32 sp = 0; sp < SPIN_CAP; ++sp) {
      asm volatile("" ::: "memory");
      v4u a[4];
#pragma unroll
      for (int q = 0; q < 4; ++q) a[q] = __builtin_amdgcn_raw_buffer_load_b128(rWS, voff, soff + q * 24576, 16);
      if (!__builtin_amdgcn_ballot_w64(gsum(a, o) != 0u)) break;
    }
  };
  const float one = w >= 4 ? 1.f : 0.f;
  {
    f32x2 mA;
    if (__builtin_amdgcn_ballot_w64(gsum(ga_, mA) != 0u)) gather(vl16, sMG, mA);
    *(LAS f32x2*)(L + L_MODA + w * 512 + (vl16 >> 1)) = mA + one;
  }
  BAR();
  if (w == 7) {
    const u32 vo = ((lane >> 3) & 3) * 24576 + (lane & 7) * 8, so = WS_MODG + (1024 + 8 * b) * 8;
    for (u32 sp = 0; sp < SPIN_CAP && __builtin_amdgcn_ballot_w64(pv.y != tag); ++sp) {
      asm volatile("" ::: "memory");
      pv = __builtin_amdgcn_raw_buffer_load_b64(rWS, vo, so, 16);
    }
    float sv = asf(pv.x);
    sv += dppf<0x128>(sv); sv = sw16_add(sv, sv);
    pv.x = asu(sv);
    __builtin_amdgcn_raw_buffer_store_b64(pv, rWS, (lane & 7) * 8, WS_MODF + (1024 + 8 * b) * 8, 16);
  }
  STAMP(6);
  int vma = L_MODA + 2 * vl16; PIN(vma);
  const f32x4 ms0 = *(LAS f32x4*)(L + vma), ms1 = *(LAS f32x4*)(L + vma + 16);
  const f32x4 ma0 = *(LAS f32x4*)(L + vma + 2048), ma1 = *(LAS f32x4*)(L + vma + 2064);
  int tz0 = tB1 + L_Z; PIN(tz0);
  { int t2 = tB1 + 49152; PIN(t2); read_b1(0, tB1, 0); read_b1(1, tB1, 24576); read_b1(2, t2, 0); }
  {
    VMW(6);
    if (w >= 4) {
      LAS char* a0 = L + L_Z + 49152 + (w & 3) * 4096 + ((lane >> 1) & 1) * 2048 + 64 * ((c16 >> 2) + 4 * (g4 & 1) + 16 * (g4 >> 1)) + 8 * (lane & 1);
#pragma unroll
      for (int ct = 0; ct < 3; ++ct) {
        const int c = min(cs0 + T0 + ct - rcb, 3) * 16;
        const v4u f = cat(trd(a0 + c), trd(a0 + c + 512));
        const u32 sf = WS_B4T + ((j * 16 + (g & 15)) * 24 + T0 + ct) * 1024;
        asm volatile("buffer_store_dwordx4 %0, %1, %2, %3 offen sc1\n\ts_nop 1" ::"v"(f), "v"(vl16), "s"(rWS), "s"(sf) : "memory");
      }
    }
    else {
      LAS char* a0 = L + L_Z + 65536 + w * 2048 + 32 * ((c16 >> 2) + 4 * (g4 & 1) + 16 * (g4 >> 1)) + 8 * (lane & 3);
#pragma unroll
      for (int i = 0; i < 2; ++i) {
        const v4u f = cat(trd(a0 + i * 1024), trd(a0 + i * 1024 + 256));
        const u32 sf = WS_B5T + ((j * 43 + g + (i && g < 11 ? 32 : 0)) * 4 + w) * 1024;
        asm volatile("buffer_store_dwordx4 %0, %1, %2, %3 offen sc1\n\ts_nop 1" ::"v"(f), "v"(vl16), "s"(rWS), "s"(sf) : "memory");
      }
      LAS char* a1 = L + 76800 + w * 1024 + 32 * ((c16 >> 2) + 4 * (g4 & 1) + 16 * (g4 >> 1)) + 8 * (lane & 3);
      const v4u f = cat(trd(a1), trd(a1 + 256));
      const u32 sf = WS_B3T + ((j * 16 + (g & 15)) * 4 + w) * 1024;
      asm volatile("buffer_store_dwordx4 %0, %1, %2, %3 offen sc1\n\ts_nop 1" ::"v"(f), "v"(vl16), "s"(rWS), "s"(sf) : "memory");
    }
  }
  BAR();
  STAMP(7);
  {
    int hw1 = (hw0 ^ 128) + 8192; PIN(hw1);
    sfor<0, 10>([&](auto I) {
      constexpr int i = decltype(I)::value;
      v4u o;
      o.x = pk2(xn[i][0] * ma0.xy + ms0.xy); o.y = pk2(xn[i][1] * ma0.zw + ms0.zw);
      o.z = pk2(xn[i][2] * ma1.xy + ms1.xy); o.w = pk2(xn[i][3] * ma1.zw + ms1.zw);
      int bp = (i >> 1) + rx2; bp = bp >= 5 ? bp - 5 : bp;
      *(LAS v4u*)(L + ((i & 1) ? hw1 : hw0) + bp * 16384) = o;
      SCHED();
    });
  }
  VMW(0);
  BAR();
  RAISE(WS_RFLAG + (j * 32 + g) * 4);
  u32 flr;
  STAMP(8);
  __builtin_amdgcn_s_setprio(2);

  const f32x4 z4 = {0.f, 0.f, 0.f, 0.f};
#define AFRAG(off, ks) (*(LAS v4u*)(L + afo[(ks) & 3] + ((off) + ((ks) >> 2) * 256)))
  const int ch0 = cs0 + 3 * w;
  constexpr int ND = NODMA ? 0 : 1;
  v4u b4[48];
  const u32 sB4 = WS_B4T + (j * 16 * 24 + 3 * w) * 1024;
  auto load_b4 = [&](auto NC) {
    constexpr int n = decltype(NC)::value, ks = n / 3, ct = n % 3;
    const u32 so = sB4 + ((ks + 2 * rotp) & 15) * 24576;
    v4u& r = b4[n];
    if constexpr (ct == 0) LD16S(r, vl16, rWS, so, 0); else if constexpr (ct == 1) LD16S(r, vl16, rWS, so, 1024); else LD16S(r, vl16, rWS, so, 2048);
  };
  auto landed_b4 = [&](auto AC, auto BC) {
#pragma unroll
    for (int i = decltype(AC)::value; i < decltype(BC)::value; ++i) { v4u& r = b4[i]; PIN(r); }
  };
  v4u b3[16];
  auto load_b3 = [&](auto NC) { constexpr int n = decltype(NC)::value; ld16s<(n & 3) * 1024>(b3[n], vl16, rWS, WS_B3T + (j * 16 + 4 * kq + (n >> 2)) * 4096); };
  v4u gb_, gg_;
  u32 vgg, fl0, vRow;
  int pw, pr;
  f32x4 aKh = z4, aVh = z4, aQ = z4, aK = z4, aV = z4, bQ = z4, bK = z4, bV = z4;
  {
    v4u ah = AFRAG(0, 0), a1 = AFRAG(16384, 0), a2 = AFRAG(32768, 0);
    int l3, c3, g3; f32x2 mB, mG;
    auto job = [&](auto NC) {
      constexpr int n = decltype(NC)::value;
      if constexpr (n == 3) { l3 = opq(vl16) >> 4; c3 = l3 & 15; g3 = l3 >> 4; PIN(c3); PIN(g3); vgg = (vl16 < 512 ? 8192 : 20480 - 512) + 512 * j + vl16; PIN(vgg); }
      if constexpr (n == 7) { vRow = (8 * w + (l3 >> 3)) * 1024 + (l3 & 7) * 16; PIN(vRow); }
      if constexpr (n == 8) { pr = (8 * w + (l3 >> 3)) * 256 + (((2 * (l3 & 7)) ^ ((l3 >> 3) & 7)) << 4); PIN(pr); }
      if constexpr (n == 10) { pw = kq * 16384 + (32 * mh + c3) * 256 + ((g3 ^ (c3 & 3)) << 4) + ((c3 & 4) << 4); PIN(pw); }
      if constexpr (n == 31) { mB.x = asf(gb_.x) + one; PIN(mB.x); mB.y = asf(gb_.z) + one; PIN(mB.y); *(LAS f32x2*)(L + L_MODA + w * 512 + (vl16 >> 1)) = mB; }
      if constexpr (n == 32) { mG.x = asf(gg_.x); mG.y = asf(gg_.z); *(LAS f32x2*)(L + L_GAB + (vl16 >> 1)) = mG; }
      if constexpr (n == 34) { fl0 = (gb_.y ^ tag) | (gb_.w ^ tag) | (gg_.y ^ tag) | (gg_.w ^ tag); PIN(fl0); }
      if constexpr (n == 7) { const u32 s0 = WS_MODF + 12288 + w * 1024; LD16S(gb_, vl16, rWS, s0, 0); }
      if constexpr (n == 9) { const u32 s0 = WS_MODF; LD16S(gg_, vgg, rWS, s0, 0); }
      if constexpr (n < 6 && !(n & 1)) b1_piece(IC<5>{}, IC<L_Z + 49152>{}, IC<n / 2>{});
      if constexpr (n == 6) arrive(L_CNT + 8);
      if constexpr (n == 10) await8(L_CNT + 8);
      if constexpr (n >= 10 && n < 22 && !(n & 1)) b1_piece(IC<6 + (n - 10) / 6>{}, IC<L_Z + ((n - 10) / 6) * 24576>{}, IC<((n - 10) / 2) % 3>{});
      if constexpr (n == 21) { const u32 vr = (vl16 >> 2) & 124, sr = WS_RFLAG + j * 128; LD4S(flr, vr, rWS, sr); }
      if constexpr (n == 26) {
        asm volatile("s_waitcnt vmcnt(0)" : "+v"(gb_), "+v"(gg_), "+v"(flr)::"memory");
        arrive(L_CNT + 12);
        const u32 vr = (vl16 >> 2) & 124, sr = WS_RFLAG + j * 128; u32 cnt = SPIN_CAP;
        asm volatile("1:\n\tv_cmp_ne_u32 vcc, %5, %0\n\ts_cbranch_vccz 2f\n\tbuffer_load_dword %0, %2, %3, %4 offen sc1\n\ts_waitcnt vmcnt(0)\n\t"
                     "s_sub_u32 %1, %1, 1\n\ts_cbranch_scc0 1b\n\t2:" : "+v"(flr), "+s"(cnt) : "v"(vr), "s"(rWS), "s"(sr), "s"(tag) : "vcc", "scc", "memory");
      }
      if constexpr (n == 28) { await8(L_CNT + 12); int tz2 = tz0 + 49152; PIN(tz2); read_b1(5, tz2, 0); }
      if constexpr (n == 32) read_b1(6, tz0, 0);
      if constexpr (n == 37) read_b1(7, tz0, 24576);
      if constexpr (n >= 27 && n < 47 && (n & 1)) load_b3(IC<(n - 27) / 2>{});
    };
    sfor<0, 16>([&](auto KS) {
      constexpr int ks = decltype(KS)::value;
      KSTAMP(1, ks);
      if constexpr (ks == 0) read_b1(3, tz0, 0);
      if constexpr (ks == 1) read_b1(4, tz0, 24576);
      SCHED();
      v4u nh = ah, n1 = a1, n2 = a2;
      aKh = mfma32(bw[ks * 3 + 1], ah, aKh);
      aVh = mfma32(ah, bw[ks * 3 + 2], aVh);
      if constexpr (ks < 15) nh = AFRAG(0, ks + 1);
      job(IC<3 * ks>{});
      SCHED();
      aQ = mfma32(bw[ks * 3 + 0], a1, aQ);
      aK = mfma32(bw[ks * 3 + 1], a1, aK);
      aV = mfma32(a1, bw[ks * 3 + 2], aV);
      if constexpr (ks < 15) n1 = AFRAG(16384, ks + 1);
      job(IC<3 * ks + 1>{});
      SCHED();
      bQ = mfma32(bw[ks * 3 + 0], a2, bQ);
      bK = mfma32(bw[ks * 3 + 1], a2, bK);
      bV = mfma32(a2, bw[ks * 3 + 2], bV);
      if constexpr (ks < 15) n2 = AFRAG(32768, ks + 1);
      job(IC<3 * ks + 2>{});
      ah = nh; a1 = n1; a2 = n2;
      SCHED();
    });
  }
  __builtin_amdgcn_s_setprio(0);
  STAMP(9); KSTAMP(1, 16);
  {
    const u32 sOst = WS_O + ((g * 4 + 2 * blk2) * 16 + 2 * j + (hd >> 1)) * 1024;
    const v2u k1p = pk4(aK), v1p = pk4(aV);
    const v2u o0 = attend(pk4(aKh), pk4(aVh), k1p, v1p, pk4(aQ), am0, am1, ahm, g == 0 && blk2 == 0);
    const v2u o1 = attend(k1p, v1p, pk4(bK), pk4(bV), pk4(bQ), am0, am1, ahm, false);
    __builtin_amdgcn_raw_buffer_store_b64(o0, rWS, vOst, sOst, 16);
    __builtin_amdgcn_raw_buffer_store_b64(o1, rWS, vOst, sOst + 16384, 16);
  }
  if (__builtin_amdgcn_ballot_w64(fl0 != 0u)) {
    for (u32 sp = 0; sp < SPIN_CAP; ++sp) {
      asm volatile("" ::: "memory");
      gb_ = __builtin_amdgcn_raw_buffer_load_b128(rWS, vl16, WS_MODF + 12288 + w * 1024, 16);
      gg_ = __builtin_amdgcn_raw_buffer_load_b128(rWS, vgg, WS_MODF, 16);
      if (!__builtin_amdgcn_ballot_w64(((gb_.y ^ tag) | (gb_.w ^ tag) | (gg_.y ^ tag) | (gg_.w ^ tag)) != 0u)) break;
    }
    const f32x2 mB = {asf(gb_.x) + one, asf(gb_.z) + one}, mG = {asf(gg_.x), asf(gg_.z)};
    *(LAS f32x2*)(L + L_MODA + w * 512 + (vl16 >> 1)) = mB;
    *(LAS f32x2*)(L + L_GAB + (vl16 >> 1)) = mG;
  }
  STAMP(10);
  VMW(0);
  BAR();
  RAISE(flag_off(0, g, 0) + j * 128);
  STAMP(11);
  if (b == 0 && w == 7) {
    for (u32 sp = 0; sp < SPIN_CAP; ++sp) {
      asm volatile("" ::: "memory");
      const v4u v = __builtin_amdgcn_raw_buffer_load_b128(rWS, vl16, WS_RFLAG, 16);
      if (__all(v.x == tag && v.y == tag && v.z == tag && v.w == tag)) break;
    }
    __builtin_amdgcn_raw_buffer_store_b32(tag, rWS, 0, WS_EPOCH, 16);
  }

  v4u xv;
  { const u32 sx = g * 65536 + j * 128; LD16(xv, vRow, rX, sx, 0); }
  sfor<10, 16>(load_b3);
  const u64 care2 = 0x0101010101010101ull * (3u << (2 * kq));
  poll_flags(rWS, vfl, flag_off(0, g, 0), care2, tag);
  PIN(xv);
  v4u af[8];
  {
    const u32 so = WS_O + ((g * 4 + 2 * mh) * 16 + 4 * kq) * 1024, so1 = so + 16384;
    LD16S(af[0], vl16, rWS, so, 0); LD16S(af[1], vl16, rWS, so, 1024); LD16S(af[2], vl16, rWS, so, 2048); LD16S(af[3], vl16, rWS, so, 3072);
    LD16S(af[4], vl16, rWS, so1, 0); LD16S(af[5], vl16, rWS, so1, 1024); LD16S(af[6], vl16, rWS, so1, 2048); LD16S(af[7], vl16, rWS, so1, 3072);
  }
  STAMP(12);
  sfor<0, 2>([&](auto M) {
    constexpr int m = decltype(M)::value;
    asm volatile("s_waitcnt vmcnt(%c4)" : "+v"(af[4 * m]), "+v"(af[4 * m + 1]), "+v"(af[4 * m + 2]), "+v"(af[4 * m + 3]) : "i"(m == 0 ? 4 : 2) : "memory");
    if constexpr (m == 0) {
#pragma unroll
      for (int i = 0; i < 16; ++i) PIN(b3[i]);
    }
    f32x4 acc[4] = {z4, z4, z4, z4};
    sfor<0, 4>([&](auto SC) {
      constexpr int sx = decltype(SC)::value;
#pragma unroll
      for (int n = 0; n < 4; ++n) acc[n] = mfma32(b3[sx * 4 + n], af[4 * m + sx], acc[n]);
      if constexpr (sx & 1) load_b4(IC<2 * m + (sx >> 1)>{});
      SCHED();
    });
#pragma unroll
    for (int n = 0; n < 4; ++n) *(LAS f32x4*)(L + ((n & 1) ? (pw ^ 64) : pw) + (m * 4096 + (n >> 1) * 128)) = acc[n];
  });
  BAR();
  STAMP(13);
  v4u x1p;
  {
    f32x4 y0 = *(LAS f32x4*)(L + pr), y1 = *(LAS f32x4*)(L + (pr ^ 16));
#pragma unroll
    for (int q = 1; q < 4; ++q) { y0 += *(LAS f32x4*)(L + pr + q * 16384); y1 += *(LAS f32x4*)(L + (pr ^ 16) + q * 16384); }
    const int ga = L_GAB + ((vl16 & 112) << 1);
    const f32x4 g0 = *(LAS f32x4*)(L + ga), g1 = *(LAS f32x4*)(L + ga + 16);
    const f32x4 x0 = f32x4{blo(xv.x), bhi(xv.x), blo(xv.y), bhi(xv.y)} + g0 * y0, x1 = f32x4{blo(xv.z), bhi(xv.z), blo(xv.w), bhi(xv.w)} + g1 * y1;
    { const v4u o = cat(pk4(x0), pk4(x1)); x1p = o; const u32 so = WS_X1 + g * 65536 + j * 128;
      asm volatile("buffer_store_dwordx4 %0, %1, %2, %3 offen sc1\n\ts_nop 1" ::"v"(o), "v"(vRow), "s"(rWS), "s"(so) : "memory"); }
    const f32x4 sv = x0 + x1;
    float mean = (sv.x + sv.y) + (sv.z + sv.w);
    mean += dppf<0xB1>(mean); mean += dppf<0x4E>(mean); mean += dppf<0x141>(mean);
    mean *= 1.f / 64.f;
    const f32x4 d0 = x0 - mean, d1 = x1 - mean, dd = d0 * d0 + d1 * d1;
    float m2 = (dd.x + dd.y) + (dd.z + dd.w);
    m2 += dppf<0xB1>(m2); m2 += dppf<0x4E>(m2); m2 += dppf<0x141>(m2);
    v2u st; st.x = asu(mean); st.y = asu(m2);
    __builtin_amdgcn_raw_buffer_store_b64(st, rWS, (vRow >> 10) << 6, WS_STAT + g * 4096 + j * 8, 16);
  }
  STAMP(14);
  VMW(0);
  BAR();
  RAISE(flag_off(1, g, 0) + j * 128);
  sfor<4, 9>(load_b4);
  STAMP(15);

  u32 hmask[3];
#pragma unroll
  for (int ct = 0; ct < 3; ++ct)
    asm("s_cmp_lt_u32 %1, %2\n\ts_cselect_b32 %0, -1, 0" : "=s"(hmask[ct]) : "s"((u32)(ch0 + ct)), "s"((u32)cend) : "scc");
#pragma unroll
  for (int i = 0; i < 4; ++i) { afo[i] -= blk2 * 32768; PIN(afo[i]); }
  int vm2 = L_MODA + 2 * vl16; PIN(vm2);
  int h2w = w * 8192 + (((vl16 - 128 * rotp) & 1023) ^ ((8 * w & 15) << 4)); PIN(h2w);
  const f32x4 ls0 = *(LAS f32x4*)(L + vm2), ls1 = *(LAS f32x4*)(L + vm2 + 16), la0 = *(LAS f32x4*)(L + vm2 + 2048), la1 = *(LAS f32x4*)(L + vm2 + 2064);
  poll_flags(rWS, vfl, flag_off(1, g, 0), ~0ull, tag);
  landed_b4(IC<0>{}, IC<9>{});
  {
    v4u x1v[8]; v2u stv;
    const u32 so = WS_X1 + g * 65536 + w * 8192, so2 = so + 4096;
    { const u32 vs = vl16 >> 1, ss = WS_STAT + g * 4096 + w * 512; LD8S(stv, vs, rWS, ss, 0); }
    LD16S(x1v[0], vl16, rWS, so, 0); LD16S(x1v[1], vl16, rWS, so, 1024); LD16S(x1v[2], vl16, rWS, so, 2048); LD16S(x1v[3], vl16, rWS, so, 3072);
    LD16S(x1v[4], vl16, rWS, so2, 0); LD16S(x1v[5], vl16, rWS, so2, 1024); LD16S(x1v[6], vl16, rWS, so2, 2048); LD16S(x1v[7], vl16, rWS, so2, 3072);
    sfor<9, 12>(load_b4);
    asm volatile("s_waitcnt vmcnt(11)" : "+v"(stv)::"memory");
    const float mj = asf(stv.x);
    float mean = mj;
    mean += dppf<0xB1>(mean); mean += dppf<0x4E>(mean); mean += dppf<0x141>(mean);
    mean *= 0.125f;
    const float dm = mj - mean;
    float m2 = asf(stv.y) + 64.f * dm * dm;
    m2 += dppf<0xB1>(m2); m2 += dppf<0x4E>(m2); m2 += dppf<0x141>(m2);
    const float l2r = __builtin_amdgcn_rsqf(m2 * (1.f / 512.f) + 1e-5f), l2n = -mean * l2r;
    sfor<0, 8>([&](auto I) {
      constexpr int i = decltype(I)::value;
      v4u& xx = x1v[i];
      asm volatile("s_waitcnt vmcnt(%c1)" : "+v"(xx) : "i"(10 - i) : "memory");
      const float rs = rdl(l2r, 8 * i), nn = rdl(l2n, 8 * i);
      v4u o;
      o.x = pk2((unp(xx.x) * rs + nn) * la0.xy + ls0.xy); o.y = pk2((unp(xx.y) * rs + nn) * la0.zw + ls0.zw);
      o.z = pk2((unp(xx.z) * rs + nn) * la1.xy + ls1.xy); o.w = pk2((unp(xx.w) * rs + nn) * la1.zw + ls1.zw);
      *(LAS v4u*)(L + (h2w ^ (i << 4)) + i * 1024) = o;
    });
  }
  BAR();
  STAMP(16);
  __builtin_amdgcn_s_setprio(2);
  const int kb5 = 5 * w + min(w, 3);
  u64 care5 = 0x0101010101010101ull * ((0x80c06030180c0603ull >> (8 * w)) & 255u);
  asm volatile("" : "+s"(care5));
  v4u b5[24];
  const u32 sB5 = WS_B5T + (j * 43 + kb5) * 4096, d5 = (w + 5) >> 3;
  auto load_b5 = [&](auto NC) {
    constexpr int n = decltype(NC)::value, sx = n / 4, t = n % 4;
    ld16s<t * 1024>(b5[n], vl16, rWS, sB5 + (sx == 5 ? (5 - d5) * 4096 : sx * 4096));
  };
  f32x4 acc4[3][4];
#pragma unroll
  for (int ct = 0; ct < 3; ++ct)
#pragma unroll
    for (int m = 0; m < 4; ++m) acc4[ct][m] = z4;
  u32 vH; int pw5;
  {
    v4u a[4] = {AFRAG(0, 0), AFRAG(16384, 0), AFRAG(32768, 0), AFRAG(49152, 0)};
    sfor<0, 16>([&](auto KS) {
      constexpr int ks = decltype(KS)::value;
      KSTAMP(3, ks);
      if constexpr (ks >= 3 && ks < 13) { VMW(9); landed_b4(IC<3 * ks>{}, IC<3 * ks + 3>{}); }
      if constexpr (ks >= 13) { VMW(9); landed_b4(IC<3 * ks>{}, IC<3 * ks + 3>{}); }
      v4u nx[4] = {a[0], a[1], a[2], a[3]};
      sfor<0, 4>([&](auto MC) {
        constexpr int m = decltype(MC)::value;
        if constexpr (ks < 15) nx[m] = AFRAG(16384 * m, ks + 1);
        if constexpr (m < 3) { if constexpr (ks < 12) load_b4(IC<12 + 3 * ks + m>{}); else load_b5(IC<3 * (ks - 12) + m>{}); }
        SCHED();
#pragma unroll
        for (int ct = 0; ct < 3; ++ct) acc4[ct][m] = mfma32(b4[ks * 3 + ct], a[m], acc4[ct][m]);
        if constexpr (ks == 8 && m == 0) { vH = (vl16 & 255) + ((vl16 >> 5) & 8) + (vl16 >> 9) * (172 * 256); PIN(vH); }
        if constexpr (ks == 9 && m == 0) { const int l5 = opq(vl16) >> 4, c5 = l5 & 15, g5 = l5 >> 4;
          pw5 = w * 16384 + c5 * 256 + ((c5 & 4) << 4) + ((g5 ^ (c5 & 3)) << 4); PIN(pw5); }
        SCHED();
      });
#pragma unroll
      for (int m = 0; m < 4; ++m) a[m] = nx[m];
    });
  }
#pragma unroll
  for (int ct = 0; ct < 3; ++ct)
#pragma unroll
    for (int m = 0; m < 4; ++m) PIN(acc4[ct][m]);
  __builtin_amdgcn_s_setprio(0);
  STAMP(17); KSTAMP(3, 16);
#pragma unroll
  for (int ct = 0; ct < 3; ++ct)
#pragma unroll
    for (int mp = 0; mp < 2; ++mp) {
      f32x4 G, U, R;
#pragma unroll
      for (int e = 0; e < 4; ++e) {
        auto r = __builtin_amdgcn_permlane32_swap(asu(acc4[ct][2 * mp][e]), asu(acc4[ct][2 * mp + 1][e]), false, false);
        G[e] = asf(r[0]); U[e] = asf(r[1]);
      }
#pragma unroll
      for (int e = 0; e < 4; ++e) R[e] = qp(qp(G[e] * U[e]) * __builtin_amdgcn_rcpf(1.f + __builtin_amdgcn_exp2f(qp(G[e] * -1.4426950408889634f))));
      const v2u o = pk4(R);
      const u32 so = WS_HH + ((g * 4 + 2 * mp) * 172 + ch0 + ct) * 256;
      asm volatile("s_mov_b32 exec_lo, %4\n\ts_mov_b32 exec_hi, %4\n\tbuffer_store_dwordx2 %0, %1, %2, %3 offen sc1\n\ts_mov_b64 exec, -1"
                   ::"v"(o), "v"(vH), "s"(rWS), "s"(so), "s"(hmask[ct]) : "memory");
    }
  STAMP(18);
  sfor<12, 16>(load_b5);
  VMW(4);
  BAR();
  RAISE(flag_off(2, g, 0) + j * 128);
  STAMP(19);
  sfor<16, 22>(load_b5);
  poll_flags(rWS, vfl, flag_off(2, g, 0), care5, tag);
#pragma unroll
  for (int i = 0; i < 22; ++i) PIN(b5[i]);
  v4u hf[16];
  auto load_h = [&](auto NC) {
    constexpr int n = decltype(NC)::value, sx = n / 4, m = n % 4;
    ld16s<0>(hf[n & 15], vl16, rWS, WS_HH + ((g * 4 + m) * 172 + 4 * kb5) * 256 + (sx == 5 ? (5 - d5) * 1024 : sx * 1024));
  };
  sfor<0, 16>(load_h);
  sfor<22, 24>(load_b5);
  STAMP(20);
  {
    f32x4 acc[4][4];
#pragma unroll
    for (int m = 0; m < 4; ++m)
#pragma unroll
      for (int n = 0; n < 4; ++n) acc[m][n] = z4;
    sfor<0, 6>([&](auto SC) {
      constexpr int sx = decltype(SC)::value, r = (sx & 3) * 4;
      constexpr int nw = sx == 0 ? 14 : sx == 1 ? 14 : sx == 2 ? 14 : sx == 3 ? 10 : sx == 4 ? 4 : 0;
      asm volatile("s_waitcnt vmcnt(%c4)" : "+v"(hf[r]), "+v"(hf[r + 1]), "+v"(hf[r + 2]), "+v"(hf[r + 3]) : "i"(nw) : "memory");
      if constexpr (sx == 4) { PIN(b5[22]); PIN(b5[23]); }
      if constexpr (sx == 5) { if (w >= 3) { hf[r] = v4u{0u, 0u, 0u, 0u}; hf[r + 1] = v4u{0u, 0u, 0u, 0u}; hf[r + 2] = v4u{0u, 0u, 0u, 0u}; hf[r + 3] = v4u{0u, 0u, 0u, 0u}; } }
      sfor<0, 4>([&](auto MC) {
        constexpr int m = decltype(MC)::value;
#pragma unroll
        for (int n = 0; n < 4; ++n) acc[m][n] = mfma32(b5[sx * 4 + n], hf[r + m], acc[m][n]);
        SCHED();
        if constexpr (sx < 2) load_h(IC<16 + 4 * sx + m>{});
        SCHED();
      });
    });
#pragma unroll
    for (int m = 0; m < 4; ++m)
#pragma unroll
      for (int n = 0; n < 4; ++n) *(LAS f32x4*)(L + ((n & 1) ? (pw5 ^ 64) : pw5) + (m * 4096 + (n >> 1) * 128)) = acc[m][n];
  }
  BAR();
  STAMP(21);
  {
    const int l6 = opq(vl16) >> 4;
    const int p0 = opq((8 * w + (l6 >> 3)) * 256 + (((2 * (l6 & 7)) ^ ((l6 >> 3) & 7)) << 4)), p1 = opq(p0 + 65536);
    f32x4 y0 = *(LAS f32x4*)(L + p0), y1 = *(LAS f32x4*)(L + (p0 ^ 16));
#pragma unroll
    for (int q = 1; q < 8; ++q) { const int pb = q < 4 ? p0 : p1; y0 += *(LAS f32x4*)(L + pb + (q & 3) * 16384); y1 += *(LAS f32x4*)(L + (pb ^ 16) + (q & 3) * 16384); }
    const int gb = L_GAB + 256 + (l6 & 7) * 32;
    const f32x4 g0 = *(LAS f32x4*)(L + gb), g1 = *(LAS f32x4*)(L + gb + 16);
    const f32x4 o0 = f32x4{blo(x1p.x), bhi(x1p.x), blo(x1p.y), bhi(x1p.y)} + g0 * y0, o1 = f32x4{blo(x1p.z), bhi(x1p.z), blo(x1p.w), bhi(x1p.w)} + g1 * y1;
    const v4u o = cat(pk4(o0), pk4(o1));
    const u32 vo = (8 * w + (l6 >> 3)) * 1024 + (l6 & 7) * 16, so = g * 65536 + j * 128;
    asm volatile("buffer_store_dwordx4 %0, %1, %2, %3 offen sc1\n\ts_nop 1" ::"v"(o), "v"(vo), "s"(rOUT), "s"(so) : "memory");
  }
  STAMP(22);
  STAMP_DUMP();
}
}

extern "C" void kernel_launch(void* const* d_in, const int* in_sizes, int n_in, void* d_out, int out_size, void* d_ws, size_t ws_size,
                              hipStream_t stream) {
  Params p;
  p.x = (const u16*)d_in[0]; p.wqkv = (const u16*)d_in[1]; p.wo = (const u16*)d_in[2];
  p.w1 = (const u16*)d_in[4]; p.w3 = (const u16*)d_in[5]; p.w2 = (const u16*)d_in[6];
  p.c = (const u16*)d_in[7]; p.wmod = (const u16*)d_in[8];
  p.out = (u16*)d_out; p.ws = (char*)d_ws;
  (void)hipFuncSetAttribute((const void*)fused_block, hipFuncAttributeMaxDynamicSharedMemorySize, L_TOTAL);
  fused_block<<<256, 512, L_TOTAL, stream>>>(p);
}
```

```cpp
#define SEG_A 21
#define SEG_B 22
#include <hip/hip_runtime.h>
#include <stdint.h>

#define KDBG 0
#define NODMA 0
#define CODEWARM 0
#define L2WARM 1
#define EARLYT 0
#define XMAP 2

#define DI __device__ __forceinline__
#define GAS __attribute__((address_space(1)))
#define LAS __attribute__((address_space(3)))
typedef unsigned u32;
typedef unsigned short u16;
typedef unsigned long long u64;
typedef u32 v4u __attribute__((ext_vector_type(4)));
typedef u32 v2u __attribute__((ext_vector_type(2)));
typedef float f32x4 __attribute__((ext_vector_type(4)));
typedef float f32x2 __attribute__((ext_vector_type(2)));
typedef __bf16 bf16x8 __attribute__((ext_vector_type(8)));
typedef __bf16 bf16x2 __attribute__((ext_vector_type(2)));
typedef short s16x4 __attribute__((ext_vector_type(4)));
typedef __amdgpu_buffer_rsrc_t rsrc_t;
typedef GAS u32 gu32;
typedef GAS u64 gu64;
#define RLX_AGENT __ATOMIC_RELAXED, __HIP_MEMORY_SCOPE_AGENT

namespace {
constexpr int S_ = 2048, D_ = 512, F_ = 1376;
constexpr u32 WS_EPOCH = 0;
constexpr u32 WS_FLAG = 32768;
constexpr u32 WS_STAT = 262144;
constexpr u32 WS_MODG = 524288;
constexpr u32 WS_MODF = 655360;
constexpr u32 WS_O = 1u << 20;
constexpr u32 WS_X1 = 3u << 20;
constexpr u32 WS_HH = 5u << 20;
constexpr u32 WS_RFLAG = 8192;
constexpr u32 WS_B4T = 11u << 20;
constexpr u32 WS_B5T = 14u << 20;
constexpr u32 WS_B3T = (15u << 20) + (512u << 10);
constexpr u32 WS_DBG = 16u << 20;
constexpr u32 WS_BYTES = 32u << 20;
constexpr int L_XY = 0;
constexpr int L_Z = 81920;
constexpr int L_MISC = 158720;
constexpr int L_TOTAL = 163840;
constexpr int L_RED = 73728;
constexpr int L_MODA = L_MISC;
constexpr int L_CNT = L_MISC + 4096;
constexpr int L_GAB = L_MISC + 4096 + 64;
constexpr int L_JUNK = L_MISC + 4096 + 64 + 512;
constexpr int L_STAMP = 156672;
constexpr u32 SPIN_CAP = 1u << 18;
constexpr u32 CODE_BYTES = 26112;

DI f32x4 mfma32(v4u a, v4u b, f32x4 c) {
  return __builtin_amdgcn_mfma_f32_16x16x32_bf16(__builtin_bit_cast(bf16x8, a), __builtin_bit_cast(bf16x8, b), c, 0, 0, 0);
}
DI f32x4 mfma16(v2u a, v2u b, f32x4 c) {
  return __builtin_amdgcn_mfma_f32_16x16x16bf16_1k(__builtin_bit_cast(s16x4, a), __builtin_bit_cast(s16x4, b), c, 0, 0, 0);
}
DI u32 pk(float lo, float hi) { f32x2 v = {lo, hi}; return __builtin_bit_cast(u32, __builtin_convertvector(v, bf16x2)); }
DI u32 pk2(f32x2 v) { return __builtin_bit_cast(u32, __builtin_convertvector(v, bf16x2)); }
DI v2u pk4(f32x4 v) { v2u r; r.x = pk(v.x, v.y); r.y = pk(v.z, v.w); return r; }
DI float asf(u32 u) { return __builtin_bit_cast(float, u); }
DI u32 asu(float f) { return __builtin_bit_cast(u32, f); }
DI float blo(u32 u) { return asf(u << 16); }
DI float bhi(u32 u) { return asf(u & 0xffff0000u); }
DI f32x2 unp(u32 u) { f32x2 r = {blo(u), bhi(u)}; return r; }
DI float dot2(u32 a, u32 b, float c) { return __builtin_amdgcn_fdot2_f32_bf16(__builtin_bit_cast(bf16x2, a), __builtin_bit_cast(bf16x2, b), c, false); }
DI v2u trd(LAS char* p) { return __builtin_bit_cast(v2u, __builtin_amdgcn_ds_read_tr16_b64_v4i16((LAS s16x4*)p)); }
DI v4u cat(v2u a, v2u b) { v4u r; r.x = a.x; r.y = a.y; r.z = b.x; r.w = b.y; return r; }
template <int MI, int SI> DI void dma(u32 mb, u32 voff, rsrc_t srd, u32 sb) {
  u32 t;
  asm volatile("s_add_u32 m0, %1, %c2\n\ts_add_u32 %0, %3, %c4\n\tbuffer_load_dwordx4 %5, %6, %0 offen lds"
               : "=&s"(t) : "s"(mb), "i"(MI), "s"(sb), "i"(SI), "v"(voff), "s"(srd) : "scc");
}
DI void touch(u32 voff, rsrc_t srd, u32 sb) {
  asm volatile("s_mov_b32 m0, %c0\n\ts_nop 0\n\tbuffer_load_dword %1, %2, %3 offen lds" ::"i"(L_JUNK), "v"(voff), "s"(srd), "s"(sb));
}
#define BAR() asm volatile("s_waitcnt lgkmcnt(0)\n\ts_barrier" ::: "memory")
#define VMW(n) asm volatile("s_waitcnt vmcnt(" #n ")" ::: "memory")
#define LGW() asm volatile("s_waitcnt lgkmcnt(0)" ::: "memory")
#define SCHED() __builtin_amdgcn_sched_barrier(0)
#define LD16(dst, voff, srd, soff, IMM) asm volatile("buffer_load_dwordx4 %0, %1, %2, %3 offen offset:" #IMM : "=v"(dst) : "v"(voff), "s"(srd), "s"(soff))
#define LD16S(dst, voff, srd, soff, IMM) asm volatile("buffer_load_dwordx4 %0, %1, %2, %3 offen offset:" #IMM " sc1" : "=v"(dst) : "v"(voff), "s"(srd), "s"(soff))
#define LD8(dst, voff, srd, soff, IMM) asm volatile("buffer_load_dwordx2 %0, %1, %2, %3 offen offset:" #IMM : "=v"(dst) : "v"(voff), "s"(srd), "s"(soff))
#define LD8S(dst, voff, srd, soff, IMM) asm volatile("buffer_load_dwordx2 %0, %1, %2, %3 offen offset:" #IMM " sc1" : "=v"(dst) : "v"(voff), "s"(srd), "s"(soff))
#define LD4(dst, voff, srd, soff) asm volatile("buffer_load_dword %0, %1, %2, %3 offen" : "=v"(dst) : "v"(voff), "s"(srd), "s"(soff))
#define LD4S(dst, voff, srd, soff) asm volatile("buffer_load_dword %0, %1, %2, %3 offen sc1" : "=v"(dst) : "v"(voff), "s"(srd), "s"(soff))

template <int IMM> DI void ld16s(v4u& r, u32 v, rsrc_t rs, u32 so) { asm volatile("buffer_load_dwordx4 %0, %1, %2, %3 offen offset:%c4 sc1" : "=v"(r) : "v"(v), "s"(rs), "s"(so), "i"(IMM)); }
template <int CTRL> DI float dppf(float v) { return asf(__builtin_amdgcn_update_dpp(0, asu(v), CTRL, 0xf, 0xf, true)); }
DI float row16_sum(float v) { v += dppf<0x128>(v); v += dppf<0x124>(v); v += dppf<0x122>(v); v += dppf<0x121>(v); return v; }
DI float sw32_add(float a, float b) { auto r = __builtin_amdgcn_permlane32_swap(asu(a), asu(b), false, false); return asf(r[0]) + asf(r[1]); }
DI float sw16_add(float a, float b) { auto r = __builtin_amdgcn_permlane16_swap(asu(a), asu(b), false, false); return asf(r[0]) + asf(r[1]); }
DI float x16_max(float v) { auto r = __builtin_amdgcn_permlane16_swap(asu(v), asu(v), false, false); return fmaxf(asf(r[0]), asf(r[1])); }
DI float x32_max(float v) { auto r = __builtin_amdgcn_permlane32_swap(asu(v), asu(v), false, false); return fmaxf(asf(r[0]), asf(r[1])); }
DI int fresh_lane() { int l; asm volatile("v_mbcnt_lo_u32_b32 %0, -1, 0\n\tv_mbcnt_hi_u32_b32 %0, -1, %0" : "=v"(l)); return l; }
DI float rdl(float v, int l) { return asf(__builtin_amdgcn_readlane(asu(v), l)); }

struct Params {
  const u16 *x, *wqkv, *wo, *w1, *w3, *w2, *c, *wmod;
  u16* out;
  char* ws;
};

#define LOOPSEL 0
#define STAMP(i) do { if constexpr ((i) == SEG_A) asm volatile("s_memrealtime %0\n\ts_waitcnt lgkmcnt(0)" : "=s"(tA_)::"memory"); \
    if constexpr ((i) == SEG_B) asm volatile("s_memrealtime %0\n\ts_waitcnt lgkmcnt(0)" : "=s"(tB_)::"memory"); } while (0)
#define KSTAMP(lp, i) do {} while (0)
#define STAMP_DUMP() do {} while (0)

DI u32 flag_off(int phase, int g, int h) { return WS_FLAG + ((phase * 32 + g) * 2 + h) * 1024; }
DI void poll_flags(rsrc_t rWS, u32 vfl, u32 foff, u64 care, u32 tag) {
  for (u32 sp = 0; sp < SPIN_CAP; ++sp) {
    asm volatile("" ::: "memory");
    const u32 v = __builtin_amdgcn_raw_buffer_load_b32(rWS, vfl, foff, 16);
    if (!(__builtin_amdgcn_ballot_w64(v != tag) & care)) break;
  }
  VMW(0);
}
DI void raise_flag(rsrc_t rWS, u32 foff, u32 vtag, u32 vzero, u32 m1) {
  asm volatile("s_mov_b32 exec_hi, 0\n\ts_mov_b32 exec_lo, %4\n\tbuffer_store_dword %0, %1, %2, %3 offen sc1\n\ts_mov_b64 exec, -1" ::"v"(vtag), "v"(vzero), "s"(rWS), "s"(foff), "s"(m1) : "memory");
}
DI void signal_flag(rsrc_t rWS, u32 cnt_addr, u32 foff, u32 tag) {
  u32 t, a = cnt_addr, one = 1u, vt = tag, vz = 0u;
  asm volatile("s_mov_b64 exec, 1\n\tds_add_rtn_u32 %0, %1, %2\n\ts_waitcnt lgkmcnt(0)\n\tv_cmp_eq_u32 vcc, 7, %0\n\ts_and_b64 exec, exec, vcc\n\t"
               "buffer_store_dword %3, %4, %5, %6 offen sc1\n\ts_mov_b64 exec, -1"
               : "=&v"(t) : "v"(a), "v"(one), "v"(vt), "v"(vz), "s"(rWS), "s"(foff) : "vcc", "scc", "memory");
}
DI void arrive(u32 cnt_addr) {
  u32 a = cnt_addr, one = 1u;
  asm volatile("s_waitcnt lgkmcnt(0)\n\ts_mov_b64 exec, 1\n\tds_add_u32 %0, %1\n\ts_mov_b64 exec, -1" ::"v"(a), "v"(one) : "memory");
}
DI void await8(u32 cnt_addr) {
  u32 a = cnt_addr, t, sv;
  asm volatile("1:\n\tds_read_b32 %0, %2\n\ts_waitcnt lgkmcnt(0)\n\tv_readfirstlane_b32 %1, %0\n\ts_nop 0\n\ts_cmp_lt_u32 %1, 8\n\ts_cbranch_scc1 1b"
               : "=&v"(t), "=&s"(sv) : "v"(a) : "scc", "memory");
}
DI void raise_when8(rsrc_t rWS, u32 cnt_addr, u32 foff, u32 tag, u32 w7) {
  u32 a = cnt_addr, t, sv, vt = tag, vz = 0u;
  asm volatile("s_cmp_eq_u32 %7, 0\n\ts_cbranch_scc1 2f\n\t1:\n\tds_read_b32 %0, %2\n\ts_waitcnt lgkmcnt(0)\n\tv_readfirstlane_b32 %1, %0\n\ts_nop 0\n\ts_cmp_lt_u32 %1, 8\n\ts_cbranch_scc1 1b\n\t"
               "s_mov_b64 exec, 1\n\tbuffer_store_dword %3, %4, %5, %6 offen sc1\n\ts_mov_b64 exec, -1\n\t2:"
               : "=&v"(t), "=&s"(sv) : "v"(a), "v"(vt), "v"(vz), "s"(rWS), "s"(foff), "s"(w7) : "scc", "memory");
}
#define PIN(x) asm volatile("" : "+v"(x))
DI float qp(float r) { PIN(r); return r; }
DI float q_shl(u32 a) { return qp(blo(a)); }
DI float q_hi(u32 a) { return qp(bhi(a)); }
DI float q_fma(float a, float b, float c) { return qp(__builtin_fmaf(a, b, c)); }
DI float q_fmas(float a, float b, float c) { return qp(__builtin_fmaf(a, b, c)); }
DI float q_mul(float a, float b) { return qp(a * b); }
DI float q_mulk(float a) { return qp(a * -1.4426950408889634f); }
DI float q_add1(float a) { return qp(a + 1.f); }
DI float q_exp(float a) { return qp(__builtin_amdgcn_exp2f(a)); }
DI float q_rcp(float a) { return qp(__builtin_amdgcn_rcpf(a)); }
DI u32 q_cvt(float a, float b) { u32 r = pk(a, b); PIN(r); return r; }
DI float q_mov(float a) { return qp(a); }
DI u32 opq(u32 v) { asm volatile("" : "+v"(v)); return v; }

DI float sel(u64 m, float a, float b) { return __builtin_amdgcn_inverse_ballot_w64(m) ? a : b; }
DI v2u attend(v2u kp, v2u vp, v2u kc, v2u vc, v2u q, u64 m0, u64 m1, u32 hm, bool noprev) {
  const f32x4 z = {0.f, 0.f, 0.f, 0.f};
  const f32x4 sp = mfma16(kp, q, z);
  const f32x4 sc = mfma16(kc, q, z);
  const float K2 = 0.25f * 1.4426950408889634f;
  const u64 par = 0xaaaaaaaaaaaaaaaaull;
  float p0 = sel(par, sp[1], sp[0]), p1 = sel(par, sp[3], sp[2]);
  if (noprev) { p0 = -INFINITY; p1 = -INFINITY; }
  const float t0 = sel(m0, sel(par, sc[1], sc[0]), p0), t1 = sel(m1, sel(par, sc[3], sc[2]), p1);
  const float m = x32_max(x16_max(fmaxf(t0, t1)));
  const float mk = -m * K2;
  const float e0 = __builtin_amdgcn_exp2f(__builtin_fmaf(t0, K2, mk)), e1 = __builtin_amdgcn_exp2f(__builtin_fmaf(t1, K2, mk));
  const u32 w0 = pk(e0, e0) & hm, w1 = pk(e1, e1) & hm;
  v4u pf;
  pf.x = __builtin_amdgcn_inverse_ballot_w64(m0) ? 0u : w0; pf.y = __builtin_amdgcn_inverse_ballot_w64(m1) ? 0u : w1;
  pf.z = __builtin_amdgcn_inverse_ballot_w64(m0) ? w0 : 0u; pf.w = __builtin_amdgcn_inverse_ballot_w64(m1) ? w1 : 0u;
  const f32x4 o = mfma32(cat(vp, vc), pf, z);
  float sum = e0 + e1;
  sum = sw16_add(sum, sum); sum = sw32_add(sum, sum);
  const float inv = __builtin_amdgcn_rcpf(sum);
  return pk4(f32x4{qp(o.x * inv), qp(o.y * inv), qp(o.z * inv), qp(o.w * inv)});
}

template <int N> using IC = std::integral_constant<int, N>;
template <int I, int N, class F> DI void sfor(F&& f) { if constexpr (I < N) { f(IC<I>{}); sfor<I + 1, N>(f); } }

__global__ void __launch_bounds__(512, 2) fused_block(Params p) {
  extern __shared__ __attribute__((aligned(16))) char smem[];
  LAS char* L = (LAS char*)smem;
  const int tid = threadIdx.x, lane = tid & 63, w = __builtin_amdgcn_readfirstlane(tid >> 6);
  const int c16 = lane & 15, g4 = lane >> 4;
  const int b = blockIdx.x;
  int g, j;
  if (XMAP == 0) { g = (b & 7) * 4 + ((b >> 3) & 3); j = b >> 5; }
  else if (XMAP == 2) { const int x = b & 7, r = b >> 3; g = (x >> 1) * 8 + (r & 7); j = (x & 1) * 4 + (r >> 3); }
  else { const int x = b & 7, r = b >> 3; g = (x >> 2) * 16 + (r & 15); j = (x & 3) * 2 + (r >> 4); }
  const rsrc_t rX = __builtin_amdgcn_make_buffer_rsrc((void*)p.x, 0, S_ * D_ * 2, 0x00020000);
  const rsrc_t rQKV = __builtin_amdgcn_make_buffer_rsrc((void*)p.wqkv, 0, D_ * 3 * D_ * 2, 0x00020000);
  const rsrc_t rWO = __builtin_amdgcn_make_buffer_rsrc((void*)p.wo, 0, D_ * D_ * 2, 0x00020000);
  const rsrc_t rW1 = __builtin_amdgcn_make_buffer_rsrc((void*)p.w1, 0, D_ * F_ * 2, 0x00020000);
  const rsrc_t rW3 = __builtin_amdgcn_make_buffer_rsrc((void*)p.w3, 0, D_ * F_ * 2, 0x00020000);
  const rsrc_t rW2 = __builtin_amdgcn_make_buffer_rsrc((void*)p.w2, 0, F_ * D_ * 2, 0x00020000);
  const rsrc_t rC = __builtin_amdgcn_make_buffer_rsrc((void*)p.c, 0, D_ * 2, 0x00020000);
  const rsrc_t rMOD = __builtin_amdgcn_make_buffer_rsrc((void*)p.wmod, 0, D_ * 6 * D_ * 2, 0x00020000);
  const rsrc_t rWS = __builtin_amdgcn_make_buffer_rsrc((void*)p.ws, 0, WS_BYTES, 0x00020000);
  const rsrc_t rOUT = __builtin_amdgcn_make_buffer_rsrc((void*)p.out, 0, S_ * D_ * 2, 0x00020000);
  u64 tA_ = 0, tB_ = 0;
  STAMP(0);

  u32 ep;
  { const u32 vz = 0; asm volatile("buffer_load_dword %0, %1, %2, 0 offen sc1" : "=v"(ep) : "v"(vz), "s"(rWS)); }
  u32 vl16 = lane * 16; PIN(vl16);
  const int bm = b < 192 ? b : b - 192, mq = bm / 48, cl = bm - 48 * mq;
  v4u wm[4]; v2u cv; u32 ep0;
  if (w < 4) {
    const u32 vm = (tid >> 3) * 24576 + (tid & 7) * 16, s0 = mq * (128 * 6144) + cl * 128, s1 = s0 + 6144, s2 = s0 + 12288, s3 = s0 + 18432;
    LD16(wm[0], vm, rMOD, s0, 0); LD16(wm[1], vm, rMOD, s1, 0); LD16(wm[2], vm, rMOD, s2, 0); LD16(wm[3], vm, rMOD, s3, 0);
    const u32 vc = (tid >> 3) * 8, scq = mq * 256;
    LD8(cv, vc, rC, scq, 0);
    const u32 vz = 0; asm volatile("buffer_load_dword %0, %1, %2, 0 offen sc1" : "=v"(ep0) : "v"(vz), "s"(rWS));
  }
  if (tid < 16) *(LAS u32*)(L + L_CNT + tid * 4) = 0u;
  BAR();
  const int st_qd = lane >> 2, st_r = (st_qd & 3) + 4 * (st_qd >> 3);
  const int st_col = ((st_qd >> 2) & 1) * 64 + (((lane & 3) ^ ((w & 1) << 1)) << 4);
  const int rotp = g & 7;
  u32 vB1 = st_r * 3072 + st_col; PIN(vB1);
  const u32 mB1 = (w >> 2) * 12288 + (w & 3) * 1024, sB1 = (w >> 2) * 98304 + (w & 3) * 24576 + j * 128;
  auto stage_b1 = [&](auto PC, auto BC) {
    constexpr int pair = decltype(PC)::value, base = decltype(BC)::value;
    const u32 sp = ((pair + rotp) & 7) * 196608 + sB1;
    dma<base, 0>(mB1, vB1, rQKV, sp); dma<base + 4096, 1024>(mB1, vB1, rQKV, sp); dma<base + 8192, 2048>(mB1, vB1, rQKV, sp);
  };
  auto b1_piece = [&](auto PC, auto BC, auto TC) {
    constexpr int pair = decltype(PC)::value, base = decltype(BC)::value, ty = decltype(TC)::value;
    dma<base + ty * 4096, ty * 1024>(mB1, vB1, rQKV, ((pair + rotp) & 7) * 196608 + sB1);
  };
  const int hd = w & 3, blk2 = w >> 2, kq = w & 3, mh = w >> 2;
  const int cs0 = j < 4 ? 22 * j : 88 + 21 * (j - 4);
  const int cend = cs0 + (j < 4 ? 22 : 21);
  STAMP(1);
  const int T0 = 12 * (g >> 4) + 3 * (w & 3), rcb = min(cs0 + T0, 168);
  if (w < 4) {
    asm volatile("s_waitcnt vmcnt(0)" : "+v"(cv), "+v"(wm[0]), "+v"(wm[1]), "+v"(wm[2]), "+v"(wm[3]), "+v"(ep0)::"memory");
    const u32 tg = __builtin_amdgcn_readfirstlane(ep0) + 1u;
    const f32x4 cf = {blo(cv.x), bhi(cv.x), blo(cv.y), bhi(cv.y)};
    float sc[4];
#pragma unroll
    for (int r = 0; r < 4; ++r) sc[r] = cf[r] * __builtin_amdgcn_rcpf(1.f + __builtin_amdgcn_exp2f(cf[r] * -1.4426950408889634f));
    f32x2 v[4];
#pragma unroll
    for (int i = 0; i < 4; ++i) v[i] = (unp(wm[0][i]) * sc[0] + unp(wm[1][i]) * sc[1]) + (unp(wm[2][i]) * sc[2] + unp(wm[3][i]) * sc[3]);
    const float a0 = sw32_add(v[0].x, v[2].x), a1 = sw32_add(v[0].y, v[2].y), a2 = sw32_add(v[1].x, v[3].x), a3 = sw32_add(v[1].y, v[3].y);
    float t0 = sw16_add(a0, a2), t1 = sw16_add(a1, a3);
    t0 += dppf<0x128>(t0); t1 += dppf<0x128>(t1);
    f32x2 o = {t0, t1};
    *(LAS f32x2*)(L + L_RED + w * 256 + (lane & 7) * 32 + g4 * 8) = o;
    LGW();
    u32 old = 0;
    if (lane == 0) old = __hip_atomic_fetch_add((LAS u32*)(L + L_CNT + 28), 1u, __ATOMIC_RELAXED, __HIP_MEMORY_SCOPE_WORKGROUP);
    if (__builtin_amdgcn_readfirstlane(old) == 3u) {
      asm volatile("" ::: "memory");
      float s = 0.f;
#pragma unroll
      for (int i = 0; i < 4; ++i) s += *(LAS float*)(L + L_RED + i * 256 + lane * 4);
      v2u gr; gr.x = asu(s); gr.y = tg;
      if (b < 192) __builtin_amdgcn_raw_buffer_store_b64(gr, rWS, lane * 8, WS_MODG + (mq * 3072 + cl * 64) * 8, 16);
    }
  }
  STAMP(2);
  v4u xr[10];
  {
    const int r0 = 64 * g - 16 + w;
    const u32 s0 = (u32)max(r0, 0) * 1024u, s1 = (u32)max(r0 + 8, 0) * 1024u;
    LD16(xr[0], vl16, rX, s0, 0); LD16(xr[1], vl16, rX, s1, 0);
#pragma unroll
    for (int i = 2; i < 10; ++i) { const u32 so = (u32)(r0 + 8 * i) * 1024u; LD16(xr[i], vl16, rX, so, 0); }
  }
  stage_b1(IC<0>{}, IC<L_XY>{});
  int afo[4];
#pragma unroll
  for (int i = 0; i < 4; ++i) { afo[i] = blk2 * 32768 + c16 * 1024 + (((4 * i + g4) ^ c16) << 4); PIN(afo[i]); }
  const u64 am0 = __builtin_amdgcn_ballot_w64((c16 & 14) >= 4 * g4), am1 = __builtin_amdgcn_ballot_w64((c16 & 14) >= 4 * g4 + 2);
  u32 ahm = (lane & 1) ? 0xffff0000u : 0xffffu; PIN(ahm);
  u32 vOst = c16 * 16 + g4 * 256 + (hd & 1) * 8; PIN(vOst);
  int tB1 = g4 * 1024 + 64 * (c16 >> 2) + 8 * (lane & 3) + 32 * ((hd & 1) ^ (g4 & 1)) + 256 * (hd >> 1); PIN(tB1);
  int hw0 = w * 1024 + ((((lane - 8 * rotp) & 63) ^ w) << 4); PIN(hw0);
#define vfl ((opq(vl16) & 112u) << 3)
  STAMP(3);
  v4u bw[48];
  auto read_b1 = [&](int pair, int base, int off) {
#pragma unroll
    for (int kk = 0; kk < 2; ++kk)
#pragma unroll
      for (int ty = 0; ty < 3; ++ty) {
        LAS char* a = L + base + (off + kk * 12288 + ty * 4096);
        bw[(2 * pair + kk) * 3 + ty] = cat(trd(a), trd(a + 512));
      }
  };
  f32x2 xn[10][4];
  v4u ga_[4]; v2u pv;
  const u32 sMG = WS_MODG + w * 1024;
  {
    float u[10];
    sfor<0, 10>([&](auto I) {
      constexpr int i = decltype(I)::value;
      if constexpr (i == 0) asm volatile("s_waitcnt vmcnt(11)" : "+v"(xr[0]), "+v"(xr[1]), "+v"(ep)::"memory");
      if constexpr (i == 2) asm volatile("s_waitcnt vmcnt(9)" : "+v"(xr[2]), "+v"(xr[3])::"memory");
      if constexpr (i == 4) asm volatile("s_waitcnt vmcnt(10)" : "+v"(xr[4]), "+v"(xr[5])::"memory");
      if constexpr (i == 6) asm volatile("s_waitcnt vmcnt(8)" : "+v"(xr[6]), "+v"(xr[7])::"memory");
      if constexpr (i == 8) asm volatile("s_waitcnt vmcnt(9)" : "+v"(xr[8]), "+v"(xr[9])::"memory");
      float s = 0.f, q = 0.f;
#pragma unroll
      for (int e = 0; e < 4; ++e) { s = dot2(xr[i][e], 0x3f803f80u, s); q = dot2(xr[i][e], xr[i][e], q); }
      u[i] = sw32_add(s, q);
      PIN(u[i]);
      if constexpr (i == 3) stage_b1(IC<1>{}, IC<L_XY + 24576>{});
      if constexpr (i == 7) stage_b1(IC<2>{}, IC<L_XY + 49152>{});
      if constexpr (i == 9) {
        const u32 s0 = sMG, s1 = sMG + 24576, s2 = sMG + 49152, s3 = sMG + 73728;
        ld16s<0>(ga_[0], vl16, rWS, s0); ld16s<0>(ga_[1], vl16, rWS, s1); ld16s<0>(ga_[2], vl16, rWS, s2); ld16s<0>(ga_[3], vl16, rWS, s3);
      }
    });
    float t[5];
#pragma unroll
    for (int i = 0; i < 5; ++i) t[i] = row16_sum(sw16_add(u[i], u[i + 5]));
    float v = t[0];
#pragma unroll
    for (int i = 1; i < 5; ++i) v = c16 == i ? t[i] : v;
    auto r = __builtin_amdgcn_permlane32_swap(asu(v), asu(v), false, false);
    const float mean = asf(r[0]) * (1.f / 512.f);
    const float var = fmaxf(asf(r[1]) * (1.f / 512.f) - mean * mean, 0.f);
    float rs = __builtin_amdgcn_rsqf(var + 1e-5f), nm = -mean * rs;
    PIN(rs); PIN(nm);
    STAMP(4);
    { const u32 vo = ((lane >> 3) & 3) * 24576 + (lane & 7) * 8, so = WS_MODG + (1024 + 8 * b) * 8; LD8S(pv, vo, rWS, so, 0); }
    if (w >= 4) {
      const int u = lane >> 2, row = (u & 3) | ((u >> 1) & 4) | ((u << 1) & 8);
      const u32 vo = row * 2752 + (lane & 3) * 16, so = (g & 15) * (32 * 2752) + rcb * 16, mb = L_Z + 49152 + (w & 3) * 4096;
      dma<0, 0>(mb, vo, rW1, so); dma<1024, 16 * 2752>(mb, vo, rW1, so); dma<2048, 0>(mb, vo, rW3, so); dma<3072, 16 * 2752>(mb, vo, rW3, so);
    } else {
      const int u = lane >> 1, row = (u & 3) | ((u >> 1) & 4) | ((u << 1) & 8) | (u & 16);
      const u32 vo = row * 1024 + (lane & 1) * 16, so = g * 32768 + j * 128 + w * 32, mb = L_Z + 65536 + w * 2048;
      dma<0, 0>(mb, vo, rW2, so); dma<1024, 0>(mb, vo, rW2, so + (g < 11 ? 32 * 32768 : 0));
      const int rwo = (u & 7) | ((u >> 1) & 8) | ((u << 1) & 16);
      dma<0, 0>(76800 + w * 1024, rwo * 1024 + (lane & 1) * 16, rWO, (g & 15) * 32768 + j * 128 + w * 32);
    }
    sfor<0, 10>([&](auto I) {
      constexpr int i = decltype(I)::value;
      if constexpr (i == 2) stage_b1(IC<3>{}, IC<L_Z>{});
      if constexpr (i == 6) stage_b1(IC<4>{}, IC<L_Z + 24576>{});
      const float a = rdl(rs, (i / 5) * 16 + i % 5), c = rdl(nm, (i / 5) * 16 + i % 5);
#pragma unroll
      for (int e = 0; e < 4; ++e) { xn[i][e] = unp(xr[i][e]) * a + c; PIN(xn[i][e]); }
    });
  }
  const u32 tag = __builtin_amdgcn_readfirstlane(ep) + 1u;
  u32 m1; asm("s_lshr_b32 %0, 1, %1" : "=s"(m1) : "s"(w) : "scc");
#define RAISE(fo) raise_flag(rWS, (fo), tag, vl16, m1)
#define SIGNAL(idx, fo) signal_flag(rWS, L_CNT + 4 * (idx), (fo), tag)
  STAMP(5);
  asm volatile("s_waitcnt vmcnt(9)" : "+v"(ga_[0]), "+v"(ga_[1]), "+v"(ga_[2]), "+v"(ga_[3]), "+v"(pv)::"memory");
  auto gsum = [&](v4u (&a)[4], f32x2& o) -> u32 {
    o.x = (asf(a[0].x) + asf(a[1].x)) + (asf(a[2].x) + asf(a[3].x)); o.y = (asf(a[0].z) + asf(a[1].z)) + (asf(a[2].z) + asf(a[3].z));
    return ((a[0].y ^ tag) | (a[0].w ^ tag) | (a[1].y ^ tag) | (a[1].w ^ tag)) | ((a[2].y ^ tag) | (a[2].w ^ tag) | (a[3].y ^ tag) | (a[3].w ^ tag));
  };
  auto gather = [&](u32 voff, u32 soff, f32x2& o) {
    for (u32 sp = 0; sp < SPIN_CAP; ++sp) {
      asm volatile("" ::: "memory");
      v4u a[4];
#pragma unroll
      for (int q = 0; q < 4; ++q) a[q] = __builtin_amdgcn_raw_buffer_load_b128(rWS, voff, soff + q * 24576, 16);
      if (!__builtin_amdgcn_ballot_w64(gsum(a, o) != 0u)) break;
    }
  };
  const float one = w >= 4 ? 1.f : 0.f;
  {
    f32x2 mA;
    if (__builtin_amdgcn_ballot_w64(gsum(ga_, mA) != 0u)) gather(vl16, sMG, mA);
    *(LAS f32x2*)(L + L_MODA + w * 512 + (vl16 >> 1)) = mA + one;
  }
  BAR();
  if (w == 7) {
    const u32 vo = ((lane >> 3) & 3) * 24576 + (lane & 7) * 8, so = WS_MODG + (1024 + 8 * b) * 8;
    for (u32 sp = 0; sp < SPIN_CAP && __builtin_amdgcn_ballot_w64(pv.y != tag); ++sp) {
      asm volatile("" ::: "memory");
      pv = __builtin_amdgcn_raw_buffer_load_b64(rWS, vo, so, 16);
    }
    float sv = asf(pv.x);
    sv += dppf<0x128>(sv); sv = sw16_add(sv, sv);
    pv.x = asu(sv);
    __builtin_amdgcn_raw_buffer_store_b64(pv, rWS, (lane & 7) * 8, WS_MODF + (1024 + 8 * b) * 8, 16);
  }
  STAMP(6);
  int vma = L_MODA + 2 * vl16; PIN(vma);
  const f32x4 ms0 = *(LAS f32x4*)(L + vma), ms1 = *(LAS f32x4*)(L + vma + 16);
  const f32x4 ma0 = *(LAS f32x4*)(L + vma + 2048), ma1 = *(LAS f32x4*)(L + vma + 2064);
  int tz0 = tB1 + L_Z; PIN(tz0);
  { int t2 = tB1 + 49152; PIN(t2); read_b1(0, tB1, 0); read_b1(1, tB1, 24576); read_b1(2, t2, 0); }
  {
    VMW(6);
    if (w >= 4) {
      LAS char* a0 = L + L_Z + 49152 + (w & 3) * 4096 + ((lane >> 1) & 1) * 2048 + 64 * ((c16 >> 2) + 4 * (g4 & 1) + 16 * (g4 >> 1)) + 8 * (lane & 1);
#pragma unroll
      for (int ct = 0; ct < 3; ++ct) {
        const int c = min(cs0 + T0 + ct - rcb, 3) * 16;
        const v4u f = cat(trd(a0 + c), trd(a0 + c + 512));
        const u32 sf = WS_B4T + ((j * 16 + (g & 15)) * 24 + T0 + ct) * 1024;
        asm volatile("buffer_store_dwordx4 %0, %1, %2, %3 offen sc1\n\ts_nop 1" ::"v"(f), "v"(vl16), "s"(rWS), "s"(sf) : "memory");
      }
    }
    else {
      LAS char* a0 = L + L_Z + 65536 + w * 2048 + 32 * ((c16 >> 2) + 4 * (g4 & 1) + 16 * (g4 >> 1)) + 8 * (lane & 3);
#pragma unroll
      for (int i = 0; i < 2; ++i) {
        const v4u f = cat(trd(a0 + i * 1024), trd(a0 + i * 1024 + 256));
        const u32 sf = WS_B5T + ((j * 43 + g + (i && g < 11 ? 32 : 0)) * 4 + w) * 1024;
        asm volatile("buffer_store_dwordx4 %0, %1, %2, %3 offen sc1\n\ts_nop 1" ::"v"(f), "v"(vl16), "s"(rWS), "s"(sf) : "memory");
      }
      LAS char* a1 = L + 76800 + w * 1024 + 32 * ((c16 >> 2) + 4 * (g4 & 1) + 16 * (g4 >> 1)) + 8 * (lane & 3);
      const v4u f = cat(trd(a1), trd(a1 + 256));
      const u32 sf = WS_B3T + ((j * 16 + (g & 15)) * 4 + w) * 1024;
      asm volatile("buffer_store_dwordx4 %0, %1, %2, %3 offen sc1\n\ts_nop 1" ::"v"(f), "v"(vl16), "s"(rWS), "s"(sf) : "memory");
    }
  }
  BAR();
  STAMP(7);
  {
    int hw1 = (hw0 ^ 128) + 8192; PIN(hw1);
    sfor<0, 10>([&](auto I) {
      constexpr int i = decltype(I)::value;
      v4u o;
      o.x = pk2(xn[i][0] * ma0.xy + ms0.xy); o.y = pk2(xn[i][1] * ma0.zw + ms0.zw);
      o.z = pk2(xn[i][2] * ma1.xy + ms1.xy); o.w = pk2(xn[i][3] * ma1.zw + ms1.zw);
      if constexpr (i == 8) { hw0 += 65536; hw1 += 65536; }
      *(LAS v4u*)(L + ((i & 1) ? hw1 : hw0) + ((i >> 1) & 3) * 16384) = o;
      SCHED();
    });
  }
  VMW(0);
  BAR();
  RAISE(WS_RFLAG + (j * 32 + g) * 4);
  u32 flr;
  STAMP(8);
  __builtin_amdgcn_s_setprio(2);

  const f32x4 z4 = {0.f, 0.f, 0.f, 0.f};
#define AFRAG(off, ks) (*(LAS v4u*)(L + afo[(ks) & 3] + ((off) + ((ks) >> 2) * 256)))
  const int ch0 = cs0 + 3 * w;
  constexpr int ND = NODMA ? 0 : 1;
  v4u b4[48];
  const u32 sB4 = WS_B4T + (j * 16 * 24 + 3 * w) * 1024;
  auto load_b4 = [&](auto NC) {
    constexpr int n = decltype(NC)::value, ks = n / 3, ct = n % 3;
    const u32 so = sB4 + ((ks + 2 * rotp) & 15) * 24576;
    v4u& r = b4[n];
    if constexpr (ct == 0) LD16S(r, vl16, rWS, so, 0); else if constexpr (ct == 1) LD16S(r, vl16, rWS, so, 1024); else LD16S(r, vl16, rWS, so, 2048);
  };
  auto landed_b4 = [&](auto AC, auto BC) {
#pragma unroll
    for (int i = decltype(AC)::value; i < decltype(BC)::value; ++i) { v4u& r = b4[i]; PIN(r); }
  };
  v4u b3[16];
  auto load_b3 = [&](auto NC) { constexpr int n = decltype(NC)::value; ld16s<(n & 3) * 1024>(b3[n], vl16, rWS, WS_B3T + (j * 16 + 4 * kq + (n >> 2)) * 4096); };
  v4u gb_, gg_;
  u32 vgg, fl0, vRow;
  int pw, pr;
  f32x4 aKh = z4, aVh = z4, aQ = z4, aK = z4, aV = z4, bQ = z4, bK = z4, bV = z4;
  {
    v4u ah = AFRAG(0, 0), a1 = AFRAG(16384, 0), a2 = AFRAG(32768, 0);
    int l3, c3, g3; f32x2 mB, mG;
    auto job = [&](auto NC) {
      constexpr int n = decltype(NC)::value;
      if constexpr (n == 3) { l3 = opq(vl16) >> 4; c3 = l3 & 15; g3 = l3 >> 4; PIN(c3); PIN(g3); vgg = (vl16 < 512 ? 8192 : 20480 - 512) + 512 * j + vl16; PIN(vgg); }
      if constexpr (n == 7) { vRow = (8 * w + (l3 >> 3)) * 1024 + (l3 & 7) * 16; PIN(vRow); }
      if constexpr (n == 8) { pr = (8 * w + (l3 >> 3)) * 256 + (((2 * (l3 & 7)) ^ ((l3 >> 3) & 7)) << 4); PIN(pr); }
      if constexpr (n == 10) { pw = kq * 16384 + (32 * mh + c3) * 256 + ((g3 ^ (c3 & 3)) << 4) + ((c3 & 4) << 4); PIN(pw); }
      if constexpr (n == 31) { mB.x = asf(gb_.x) + one; PIN(mB.x); mB.y = asf(gb_.z) + one; PIN(mB.y); *(LAS f32x2*)(L + L_MODA + w * 512 + (vl16 >> 1)) = mB; }
      if constexpr (n == 32) { mG.x = asf(gg_.x); mG.y = asf(gg_.z); *(LAS f32x2*)(L + L_GAB + (vl16 >> 1)) = mG; }
      if constexpr (n == 34) { fl0 = (gb_.y ^ tag) | (gb_.w ^ tag) | (gg_.y ^ tag) | (gg_.w ^ tag); PIN(fl0); }
      if constexpr (n == 7) { const u32 s0 = WS_MODF + 12288 + w * 1024; LD16S(gb_, vl16, rWS, s0, 0); }
      if constexpr (n == 9) { const u32 s0 = WS_MODF; LD16S(gg_, vgg, rWS, s0, 0); }
      if constexpr (n < 6 && !(n & 1)) b1_piece(IC<5>{}, IC<L_Z + 49152>{}, IC<n / 2>{});
      if constexpr (n == 6) arrive(L_CNT + 8);
      if constexpr (n == 10) await8(L_CNT + 8);
      if constexpr (n >= 10 && n < 22 && !(n & 1)) b1_piece(IC<6 + (n - 10) / 6>{}, IC<L_Z + ((n - 10) / 6) * 24576>{}, IC<((n - 10) / 2) % 3>{});
      if constexpr (n == 21) { const u32 vr = (vl16 >> 2) & 124, sr = WS_RFLAG + j * 128; LD4S(flr, vr, rWS, sr); }
      if constexpr (n == 26) {
        asm volatile("s_waitcnt vmcnt(0)" : "+v"(gb_), "+v"(gg_), "+v"(flr)::"memory");
        arrive(L_CNT + 12);
        const u32 vr = (vl16 >> 2) & 124, sr = WS_RFLAG + j * 128; u32 cnt = SPIN_CAP;
        asm volatile("1:\n\tv_cmp_ne_u32 vcc, %5, %0\n\ts_cbranch_vccz 2f\n\tbuffer_load_dword %0, %2, %3, %4 offen sc1\n\ts_waitcnt vmcnt(0)\n\t"
                     "s_sub_u32 %1, %1, 1\n\ts_cbranch_scc0 1b\n\t2:" : "+v"(flr), "+s"(cnt) : "v"(vr), "s"(rWS), "s"(sr), "s"(tag) : "vcc", "scc", "memory");
      }
      if constexpr (n == 28) { await8(L_CNT + 12); int tz2 = tz0 + 49152; PIN(tz2); read_b1(5, tz2, 0); }
      if constexpr (n == 32) read_b1(6, tz0, 0);
      if constexpr (n == 37) read_b1(7, tz0, 24576);
      if constexpr (n >= 27 && n < 47 && (n & 1)) load_b3(IC<(n - 27) / 2>{});
    };
    sfor<0, 16>([&](auto KS) {
      constexpr int ks = decltype(KS)::value;
      KSTAMP(1, ks);
      if constexpr (ks == 0) read_b1(3, tz0, 0);
      if constexpr (ks == 1) read_b1(4, tz0, 24576);
      SCHED();
      v4u nh = ah, n1 = a1, n2 = a2;
      aKh = mfma32(bw[ks * 3 + 1], ah, aKh);
      aVh = mfma32(ah, bw[ks * 3 + 2], aVh);
      if constexpr (ks < 15) nh = AFRAG(0, ks + 1);
      job(IC<3 * ks>{});
      SCHED();
      aQ = mfma32(bw[ks * 3 + 0], a1, aQ);
      aK = mfma32(bw[ks * 3 + 1], a1, aK);
      aV = mfma32(a1, bw[ks * 3 + 2], aV);
      if constexpr (ks < 15) n1 = AFRAG(16384, ks + 1);
      job(IC<3 * ks + 1>{});
      SCHED();
      bQ = mfma32(bw[ks * 3 + 0], a2, bQ);
      bK = mfma32(bw[ks * 3 + 1], a2, bK);
      bV = mfma32(a2, bw[ks * 3 + 2], bV);
      if constexpr (ks < 15) n2 = AFRAG(32768, ks + 1);
      job(IC<3 * ks + 2>{});
      ah = nh; a1 = n1; a2 = n2;
      SCHED();
    });
  }
  __builtin_amdgcn_s_setprio(0);
  STAMP(9); KSTAMP(1, 16);
  {
    const u32 sOst = WS_O + ((g * 4 + 2 * blk2) * 16 + 2 * j + (hd >> 1)) * 1024;
    const v2u k1p = pk4(aK), v1p = pk4(aV);
    const v2u o0 = attend(pk4(aKh), pk4(aVh), k1p, v1p, pk4(aQ), am0, am1, ahm, g == 0 && blk2 == 0);
    const v2u o1 = attend(k1p, v1p, pk4(bK), pk4(bV), pk4(bQ), am0, am1, ahm, false);
    __builtin_amdgcn_raw_buffer_store_b64(o0, rWS, vOst, sOst, 16);
    __builtin_amdgcn_raw_buffer_store_b64(o1, rWS, vOst, sOst + 16384, 16);
  }
  if (__builtin_amdgcn_ballot_w64(fl0 != 0u)) {
    for (u32 sp = 0; sp < SPIN_CAP; ++sp) {
      asm volatile("" ::: "memory");
      gb_ = __builtin_amdgcn_raw_buffer_load_b128(rWS, vl16, WS_MODF + 12288 + w * 1024, 16);
      gg_ = __builtin_amdgcn_raw_buffer_load_b128(rWS, vgg, WS_MODF, 16);
      if (!__builtin_amdgcn_ballot_w64(((gb_.y ^ tag) | (gb_.w ^ tag) | (gg_.y ^ tag) | (gg_.w ^ tag)) != 0u)) break;
    }
    const f32x2 mB = {asf(gb_.x) + one, asf(gb_.z) + one}, mG = {asf(gg_.x), asf(gg_.z)};
    *(LAS f32x2*)(L + L_MODA + w * 512 + (vl16 >> 1)) = mB;
    *(LAS f32x2*)(L + L_GAB + (vl16 >> 1)) = mG;
  }
  STAMP(10);
  VMW(0);
  BAR();
  RAISE(flag_off(0, g, 0) + j * 128);
  STAMP(11);
  if (b == 0 && w == 7) {
    for (u32 sp = 0; sp < SPIN_CAP; ++sp) {
      asm volatile("" ::: "memory");
      const v4u v = __builtin_amdgcn_raw_buffer_load_b128(rWS, vl16, WS_RFLAG, 16);
      if (__all(v.x == tag && v.y == tag && v.z == tag && v.w == tag)) break;
    }
    __builtin_amdgcn_raw_buffer_store_b32(tag, rWS, 0, WS_EPOCH, 16);
  }

  v4u xv;
  { const u32 sx = g * 65536 + j * 128; LD16(xv, vRow, rX, sx, 0); }
  sfor<10, 16>(load_b3);
  const u64 care2 = 0x0101010101010101ull * (3u << (2 * kq));
  poll_flags(rWS, vfl, flag_off(0, g, 0), care2, tag);
  PIN(xv);
  v4u af[8];
  {
    const u32 so = WS_O + ((g * 4 + 2 * mh) * 16 + 4 * kq) * 1024, so1 = so + 16384;
    LD16S(af[0], vl16, rWS, so, 0); LD16S(af[1], vl16, rWS, so, 1024); LD16S(af[2], vl16, rWS, so, 2048); LD16S(af[3], vl16, rWS, so, 3072);
    LD16S(af[4], vl16, rWS, so1, 0); LD16S(af[5], vl16, rWS, so1, 1024); LD16S(af[6], vl16, rWS, so1, 2048); LD16S(af[7], vl16, rWS, so1, 3072);
  }
  STAMP(12);
  sfor<0, 2>([&](auto M) {
    constexpr int m = decltype(M)::value;
    asm volatile("s_waitcnt vmcnt(%c4)" : "+v"(af[4 * m]), "+v"(af[4 * m + 1]), "+v"(af[4 * m + 2]), "+v"(af[4 * m + 3]) : "i"(m == 0 ? 4 : 2) : "memory");
    if constexpr (m == 0) {
#pragma unroll
      for (int i = 0; i < 16; ++i) PIN(b3[i]);
    }
    f32x4 acc[4] = {z4, z4, z4, z4};
    sfor<0, 4>([&](auto SC) {
      constexpr int sx = decltype(SC)::value;
#pragma unroll
      for (int n = 0; n < 4; ++n) acc[n] = mfma32(b3[sx * 4 + n], af[4 * m + sx], acc[n]);
      if constexpr (sx & 1) load_b4(IC<2 * m + (sx >> 1)>{});
      SCHED();
    });
#pragma unroll
    for (int n = 0; n < 4; ++n) *(LAS f32x4*)(L + ((n & 1) ? (pw ^ 64) : pw) + (m * 4096 + (n >> 1) * 128)) = acc[n];
  });
  BAR();
  STAMP(13);
  v4u x1p;
  {
    f32x4 y0 = *(LAS f32x4*)(L + pr), y1 = *(LAS f32x4*)(L + (pr ^ 16));
#pragma unroll
    for (int q = 1; q < 4; ++q) { y0 += *(LAS f32x4*)(L + pr + q * 16384); y1 += *(LAS f32x4*)(L + (pr ^ 16) + q * 16384); }
    const int ga = L_GAB + ((vl16 & 112) << 1);
    const f32x4 g0 = *(LAS f32x4*)(L + ga), g1 = *(LAS f32x4*)(L + ga + 16);
    const f32x4 x0 = f32x4{blo(xv.x), bhi(xv.x), blo(xv.y), bhi(xv.y)} + g0 * y0, x1 = f32x4{blo(xv.z), bhi(xv.z), blo(xv.w), bhi(xv.w)} + g1 * y1;
    { const v4u o = cat(pk4(x0), pk4(x1)); x1p = o; const u32 so = WS_X1 + g * 65536 + j * 128;
      asm volatile("buffer_store_dwordx4 %0, %1, %2, %3 offen sc1\n\ts_nop 1" ::"v"(o), "v"(vRow), "s"(rWS), "s"(so) : "memory"); }
    const f32x4 sv = x0 + x1;
    float mean = (sv.x + sv.y) + (sv.z + sv.w);
    mean += dppf<0xB1>(mean); mean += dppf<0x4E>(mean); mean += dppf<0x141>(mean);
    mean *= 1.f / 64.f;
    const f32x4 d0 = x0 - mean, d1 = x1 - mean, dd = d0 * d0 + d1 * d1;
    float m2 = (dd.x + dd.y) + (dd.z + dd.w);
    m2 += dppf<0xB1>(m2); m2 += dppf<0x4E>(m2); m2 += dppf<0x141>(m2);
    v2u st; st.x = asu(mean); st.y = asu(m2);
    __builtin_amdgcn_raw_buffer_store_b64(st, rWS, (vRow >> 10) << 6, WS_STAT + g * 4096 + j * 8, 16);
  }
  STAMP(14);
  VMW(0);
  BAR();
  RAISE(flag_off(1, g, 0) + j * 128);
  sfor<4, 9>(load_b4);
  STAMP(15);

  u32 hmask[3];
#pragma unroll
  for (int ct = 0; ct < 3; ++ct)
    asm("s_cmp_lt_u32 %1, %2\n\ts_cselect_b32 %0, -1, 0" : "=s"(hmask[ct]) : "s"((u32)(ch0 + ct)), "s"((u32)cend) : "scc");
#pragma unroll
  for (int i = 0; i < 4; ++i) { afo[i] -= blk2 * 32768; PIN(afo[i]); }
  int vm2 = L_MODA + 2 * vl16; PIN(vm2);
  int h2w = w * 8192 + (((vl16 - 128 * rotp) & 1023) ^ ((8 * w & 15) << 4)); PIN(h2w);
  const f32x4 ls0 = *(LAS f32x4*)(L + vm2), ls1 = *(LAS f32x4*)(L + vm2 + 16), la0 = *(LAS f32x4*)(L + vm2 + 2048), la1 = *(LAS f32x4*)(L + vm2 + 2064);
  poll_flags(rWS, vfl, flag_off(1, g, 0), ~0ull, tag);
  landed_b4(IC<0>{}, IC<9>{});
  {
    v4u x1v[8]; v2u stv;
    const u32 so = WS_X1 + g * 65536 + w * 8192, so2 = so + 4096;
    { const u32 vs = vl16 >> 1, ss = WS_STAT + g * 4096 + w * 512; LD8S(stv, vs, rWS, ss, 0); }
    LD16S(x1v[0], vl16, rWS, so, 0); LD16S(x1v[1], vl16, rWS, so, 1024); LD16S(x1v[2], vl16, rWS, so, 2048); LD16S(x1v[3], vl16, rWS, so, 3072);
    LD16S(x1v[4], vl16, rWS, so2, 0); LD16S(x1v[5], vl16, rWS, so2, 1024); LD16S(x1v[6], vl16, rWS, so2, 2048); LD16S(x1v[7], vl16, rWS, so2, 3072);
    sfor<9, 12>(load_b4);
    asm volatile("s_waitcnt vmcnt(11)" : "+v"(stv)::"memory");
    const float mj = asf(stv.x);
    float mean = mj;
    mean += dppf<0xB1>(mean); mean += dppf<0x4E>(mean); mean += dppf<0x141>(mean);
    mean *= 0.125f;
    const float dm = mj - mean;
    float m2 = asf(stv.y) + 64.f * dm * dm;
    m2 += dppf<0xB1>(m2); m2 += dppf<0x4E>(m2); m2 += dppf<0x141>(m2);
    const float l2r = __builtin_amdgcn_rsqf(m2 * (1.f / 512.f) + 1e-5f), l2n = -mean * l2r;
    sfor<0, 8>([&](auto I) {
      constexpr int i = decltype(I)::value;
      v4u& xx = x1v[i];
      asm volatile("s_waitcnt vmcnt(%c1)" : "+v"(xx) : "i"(10 - i) : "memory");
      const float rs = rdl(l2r, 8 * i), nn = rdl(l2n, 8 * i);
      v4u o;
      o.x = pk2((unp(xx.x) * rs + nn) * la0.xy + ls0.xy); o.y = pk2((unp(xx.y) * rs + nn) * la0.zw + ls0.zw);
      o.z = pk2((unp(xx.z) * rs + nn) * la1.xy + ls1.xy); o.w = pk2((unp(xx.w) * rs + nn) * la1.zw + ls1.zw);
      *(LAS v4u*)(L + (h2w ^ (i << 4)) + i * 1024) = o;
    });
  }
  BAR();
  STAMP(16);
  __builtin_amdgcn_s_setprio(2);
  const int kb5 = 5 * w + min(w, 3);
  u64 care5 = 0x0101010101010101ull * ((0x80c06030180c0603ull >> (8 * w)) & 255u);
  asm volatile("" : "+s"(care5));
  v4u b5[24];
  const u32 sB5 = WS_B5T + (j * 43 + kb5) * 4096, d5 = (w + 5) >> 3;
  auto load_b5 = [&](auto NC) {
    constexpr int n = decltype(NC)::value, sx = n / 4, t = n % 4;
    ld16s<t * 1024>(b5[n], vl16, rWS, sB5 + (sx == 5 ? (5 - d5) * 4096 : sx * 4096));
  };
  f32x4 acc4[3][4];
#pragma unroll
  for (int ct = 0; ct < 3; ++ct)
#pragma unroll
    for (int m = 0; m < 4; ++m) acc4[ct][m] = z4;
  u32 vH; int pw5;
  {
    v4u a[4] = {AFRAG(0, 0), AFRAG(16384, 0), AFRAG(32768, 0), AFRAG(49152, 0)};
    sfor<0, 16>([&](auto KS) {
      constexpr int ks = decltype(KS)::value;
      KSTAMP(3, ks);
      if constexpr (ks >= 3 && ks < 13) { VMW(9); landed_b4(IC<3 * ks>{}, IC<3 * ks + 3>{}); }
      if constexpr (ks >= 13) { VMW(9); landed_b4(IC<3 * ks>{}, IC<3 * ks + 3>{}); }
      v4u nx[4] = {a[0], a[1], a[2], a[3]};
      sfor<0, 4>([&](auto MC) {
        constexpr int m = decltype(MC)::value;
        if constexpr (ks < 15) nx[m] = AFRAG(16384 * m, ks + 1);
        if constexpr (m < 3) { if constexpr (ks < 12) load_b4(IC<12 + 3 * ks + m>{}); else load_b5(IC<3 * (ks - 12) + m>{}); }
        SCHED();
#pragma unroll
        for (int ct = 0; ct < 3; ++ct) acc4[ct][m] = mfma32(b4[ks * 3 + ct], a[m], acc4[ct][m]);
        if constexpr (ks == 8 && m == 0) { vH = (vl16 & 255) + ((vl16 >> 5) & 8) + (vl16 >> 9) * (172 * 256); PIN(vH); }
        if constexpr (ks == 9 && m == 0) { const int l5 = opq(vl16) >> 4, c5 = l5 & 15, g5 = l5 >> 4;
          pw5 = w * 16384 + c5 * 256 + ((c5 & 4) << 4) + ((g5 ^ (c5 & 3)) << 4); PIN(pw5); }
        SCHED();
      });
#pragma unroll
      for (int m = 0; m < 4; ++m) a[m] = nx[m];
    });
  }
#pragma unroll
  for (int ct = 0; ct < 3; ++ct)
#pragma unroll
    for (int m = 0; m < 4; ++m) PIN(acc4[ct][m]);
  __builtin_amdgcn_s_setprio(0);
  STAMP(17); KSTAMP(3, 16);
#pragma unroll
  for (int ct = 0; ct < 3; ++ct)
#pragma unroll
    for (int mp = 0; mp < 2; ++mp) {
      f32x4 G, U, R;
#pragma unroll
      for (int e = 0; e < 4; ++e) {
        auto r = __builtin_amdgcn_permlane32_swap(asu(acc4[ct][2 * mp][e]), asu(acc4[ct][2 * mp + 1][e]), false, false);
        G[e] = asf(r[0]); U[e] = asf(r[1]);
      }
#pragma unroll
      for (int e = 0; e < 4; ++e) R[e] = qp(qp(G[e] * U[e]) * __builtin_amdgcn_rcpf(1.f + __builtin_amdgcn_exp2f(qp(G[e] * -1.4426950408889634f))));
      const v2u o = pk4(R);
      const u32 so = WS_HH + ((g * 4 + 2 * mp) * 172 + ch0 + ct) * 256;
      asm volatile("s_mov_b32 exec_lo, %4\n\ts_mov_b32 exec_hi, %4\n\tbuffer_store_dwordx2 %0, %1, %2, %3 offen sc1\n\ts_mov_b64 exec, -1"
                   ::"v"(o), "v"(vH), "s"(rWS), "s"(so), "s"(hmask[ct]) : "memory");
    }
  STAMP(18);
  sfor<12, 16>(load_b5);
  VMW(4);
  BAR();
  RAISE(flag_off(2, g, 0) + j * 128);
  STAMP(19);
  sfor<16, 22>(load_b5);
  poll_flags(rWS, vfl, flag_off(2, g, 0), care5, tag);
#pragma unroll
  for (int i = 0; i < 22; ++i) PIN(b5[i]);
  v4u hf[16];
  auto load_h = [&](auto NC) {
    constexpr int n = decltype(NC)::value, sx = n / 4, m = n % 4;
    ld16s<0>(hf[n & 15], vl16, rWS, WS_HH + ((g * 4 + m) * 172 + 4 * kb5) * 256 + (sx == 5 ? (5 - d5) * 1024 : sx * 1024));
  };
  sfor<0, 16>(load_h);
  sfor<22, 24>(load_b5);
  STAMP(20);
  {
    f32x4 acc[4][4];
#pragma unroll
    for (int m = 0; m < 4; ++m)
#pragma unroll
      for (int n = 0; n < 4; ++n) acc[m][n] = z4;
    sfor<0, 6>([&](auto SC) {
      constexpr int sx = decltype(SC)::value, r = (sx & 3) * 4;
      constexpr int nw = sx == 0 ? 14 : sx == 1 ? 14 : sx == 2 ? 14 : sx == 3 ? 10 : sx == 4 ? 4 : 0;
      asm volatile("s_waitcnt vmcnt(%c4)" : "+v"(hf[r]), "+v"(hf[r + 1]), "+v"(hf[r + 2]), "+v"(hf[r + 3]) : "i"(nw) : "memory");
      if constexpr (sx == 4) { PIN(b5[22]); PIN(b5[23]); }
      if constexpr (sx == 5) { if (w >= 3) { hf[r] = v4u{0u, 0u, 0u, 0u}; hf[r + 1] = v4u{0u, 0u, 0u, 0u}; hf[r + 2] = v4u{0u, 0u, 0u, 0u}; hf[r + 3] = v4u{0u, 0u, 0u, 0u}; } }
      sfor<0, 4>([&](auto MC) {
        constexpr int m = decltype(MC)::value;
#pragma unroll
        for (int n = 0; n < 4; ++n) acc[m][n] = mfma32(b5[sx * 4 + n], hf[r + m], acc[m][n]);
        SCHED();
        if constexpr (sx < 2) load_h(IC<16 + 4 * sx + m>{});
        SCHED();
      });
    });
#pragma unroll
    for (int m = 0; m < 4; ++m)
#pragma unroll
      for (int n = 0; n < 4; ++n) *(LAS f32x4*)(L + ((n & 1) ? (pw5 ^ 64) : pw5) + (m * 4096 + (n >> 1) * 128)) = acc[m][n];
  }
  BAR();
  STAMP(21);
  {
    const int l6 = opq(vl16) >> 4;
    const int p0 = opq((8 * w + (l6 >> 3)) * 256 + (((2 * (l6 & 7)) ^ ((l6 >> 3) & 7)) << 4)), p1 = opq(p0 + 65536);
    f32x4 y0 = *(LAS f32x4*)(L + p0), y1 = *(LAS f32x4*)(L + (p0 ^ 16));
#pragma unroll
    for (int q = 1; q < 8; ++q) { const int pb = q < 4 ? p0 : p1; y0 += *(LAS f32x4*)(L + pb + (q & 3) * 16384); y1 += *(LAS f32x4*)(L + (pb ^ 16) + (q & 3) * 16384); }
    const int gb = L_GAB + 256 + (l6 & 7) * 32;
    const f32x4 g0 = *(LAS f32x4*)(L + gb), g1 = *(LAS f32x4*)(L + gb + 16);
    const f32x4 o0 = f32x4{blo(x1p.x), bhi(x1p.x), blo(x1p.y), bhi(x1p.y)} + g0 * y0, o1 = f32x4{blo(x1p.z), bhi(x1p.z), blo(x1p.w), bhi(x1p.w)} + g1 * y1;
    const v4u o = cat(pk4(o0), pk4(o1));
    const u32 vo = (8 * w + (l6 >> 3)) * 1024 + (l6 & 7) * 16, so = g * 65536 + j * 128;
    asm volatile("buffer_store_dwordx4 %0, %1, %2, %3 offen sc1\n\ts_nop 1" ::"v"(o), "v"(vo), "s"(rOUT), "s"(so) : "memory");
  }
  STAMP(22);
  if (b == 40 && w == 0) { u64 t0 = __builtin_amdgcn_s_memrealtime(); const u64 d = 10 * (tB_ - tA_); while (__builtin_amdgcn_s_memrealtime() - t0 < d && d < 20000) {} }
}
}

extern "C" void kernel_launch(void* const* d_in, const int* in_sizes, int n_in, void* d_out, int out_size, void* d_ws, size_t ws_size,
                              hipStream_t stream) {
  Params p;
  p.x = (const u16*)d_in[0]; p.wqkv = (const u16*)d_in[1]; p.wo = (const u16*)d_in[2];
  p.w1 = (const u16*)d_in[4]; p.w3 = (const u16*)d_in[5]; p.w2 = (const u16*)d_in[6];
  p.c = (const u16*)d_in[7]; p.wmod = (const u16*)d_in[8];
  p.out = (u16*)d_out; p.ws = (char*)d_ws;
  (void)hipFuncSetAttribute((const void*)fused_block, hipFuncAttributeMaxDynamicSharedMemorySize, L_TOTAL);
  fused_block<<<256, 512, L_TOTAL, stream>>>(p);
}
```

```cpp
#include <hip/hip_runtime.h>
#include <stdint.h>

#define KDBG 0
#define NODMA 0
#define CODEWARM 0
#define L2WARM 1
#define EARLYT 0
#define XMAP 2

#define DI __device__ __forceinline__
#define GAS __attribute__((address_space(1)))
#define LAS __attribute__((address_space(3)))
typedef unsigned u32;
typedef unsigned short u16;
typedef unsigned long long u64;
typedef u32 v4u __attribute__((ext_vector_type(4)));
typedef u32 v2u __attribute__((ext_vector_type(2)));
typedef float f32x4 __attribute__((ext_vector_type(4)));
typedef float f32x2 __attribute__((ext_vector_type(2)));
typedef __bf16 bf16x8 __attribute__((ext_vector_type(8)));
typedef __bf16 bf16x2 __attribute__((ext_vector_type(2)));
typedef short s16x4 __attribute__((ext_vector_type(4)));
typedef __amdgpu_buffer_rsrc_t rsrc_t;
typedef GAS u32 gu32;
typedef GAS u64 gu64;
#define RLX_AGENT __ATOMIC_RELAXED, __HIP_MEMORY_SCOPE_AGENT

namespace {
constexpr int S_ = 2048, D_ = 512, F_ = 1376;
constexpr u32 WS_EPOCH = 0;
constexpr u32 WS_FLAG = 32768;
constexpr u32 WS_STAT = 262144;
constexpr u32 WS_MODG = 524288;
constexpr u32 WS_MODF = 655360;
constexpr u32 WS_O = 1u << 20;
constexpr u32 WS_X1 = 3u << 20;
constexpr u32 WS_HH = 5u << 20;
constexpr u32 WS_RFLAG = 8192;
constexpr u32 WS_B4T = 11u << 20;
constexpr u32 WS_B5T = 14u << 20;
constexpr u32 WS_B3T = (15u << 20) + (512u << 10);
constexpr u32 WS_DBG = 16u << 20;
constexpr u32 WS_BYTES = 32u << 20;
constexpr int L_XY = 0;
constexpr int L_Z = 81920;
constexpr int L_MISC = 158720;
constexpr int L_TOTAL = 163840;
constexpr int L_RED = 73728;
constexpr int L_MODA = L_MISC;
constexpr int L_CNT = L_MISC + 4096;
constexpr int L_GAB = L_MISC + 4096 + 64;
constexpr int L_JUNK = L_MISC + 4096 + 64 + 512;
constexpr int L_STAMP = 156672;
constexpr u32 SPIN_CAP = 1u << 18;
constexpr u32 CODE_BYTES = 26112;

DI f32x4 mfma32(v4u a, v4u b, f32x4 c) {
  return __builtin_amdgcn_mfma_f32_16x16x32_bf16(__builtin_bit_cast(bf16x8, a), __builtin_bit_cast(bf16x8, b), c, 0, 0, 0);
}
DI f32x4 mfma16(v2u a, v2u b, f32x4 c) {
  return __builtin_amdgcn_mfma_f32_16x16x16bf16_1k(__builtin_bit_cast(s16x4, a), __builtin_bit_cast(s16x4, b), c, 0, 0, 0);
}
DI u32 pk(float lo, float hi) { f32x2 v = {lo, hi}; return __builtin_bit_cast(u32, __builtin_convertvector(v, bf16x2)); }
DI u32 pk2(f32x2 v) { return __builtin_bit_cast(u32, __builtin_convertvector(v, bf16x2)); }
DI v2u pk4(f32x4 v) { v2u r; r.x = pk(v.x, v.y); r.y = pk(v.z, v.w); return r; }
DI float asf(u32 u) { return __builtin_bit_cast(float, u); }
DI u32 asu(float f) { return __builtin_bit_cast(u32, f); }
DI float blo(u32 u) { return asf(u << 16); }
DI float bhi(u32 u) { return asf(u & 0xffff0000u); }
DI f32x2 unp(u32 u) { f32x2 r = {blo(u), bhi(u)}; return r; }
DI float dot2(u32 a, u32 b, float c) { return __builtin_amdgcn_fdot2_f32_bf16(__builtin_bit_cast(bf16x2, a), __builtin_bit_cast(bf16x2, b), c, false); }
DI v2u trd(LAS char* p) { return __builtin_bit_cast(v2u, __builtin_amdgcn_ds_read_tr16_b64_v4i16((LAS s16x4*)p)); }
DI v4u cat(v2u a, v2u b) { v4u r; r.x = a.x; r.y = a.y; r.z = b.x; r.w = b.y; return r; }
template <int MI, int SI> DI void dma(u32 mb, u32 voff, rsrc_t srd, u32 sb) {
  u32 t;
  asm volatile("s_add_u32 m0, %1, %c2\n\ts_add_u32 %0, %3, %c4\n\tbuffer_load_dwordx4 %5, %6, %0 offen lds"
               : "=&s"(t) : "s"(mb), "i"(MI), "s"(sb), "i"(SI), "v"(voff), "s"(srd) : "scc");
}
DI void touch(u32 voff, rsrc_t srd, u32 sb) {
  asm volatile("s_mov_b32 m0, %c0\n\ts_nop 0\n\tbuffer_load_dword %1, %2, %3 offen lds" ::"i"(L_JUNK), "v"(voff), "s"(srd), "s"(sb));
}
#define BAR() asm volatile("s_waitcnt lgkmcnt(0)\n\ts_barrier" ::: "memory")
#define VMW(n) asm volatile("s_waitcnt vmcnt(" #n ")" ::: "memory")
#define LGW() asm volatile("s_waitcnt lgkmcnt(0)" ::: "memory")
#define SCHED() __builtin_amdgcn_sched_barrier(0)
#define LD16(dst, voff, srd, soff, IMM) asm volatile("buffer_load_dwordx4 %0, %1, %2, %3 offen offset:" #IMM : "=v"(dst) : "v"(voff), "s"(srd), "s"(soff))
#define LD16S(dst, voff, srd, soff, IMM) asm volatile("buffer_load_dwordx4 %0, %1, %2, %3 offen offset:" #IMM " sc1" : "=v"(dst) : "v"(voff), "s"(srd), "s"(soff))
#define LD8(dst, voff, srd, soff, IMM) asm volatile("buffer_load_dwordx2 %0, %1, %2, %3 offen offset:" #IMM : "=v"(dst) : "v"(voff), "s"(srd), "s"(soff))
#define LD8S(dst, voff, srd, soff, IMM) asm volatile("buffer_load_dwordx2 %0, %1, %2, %3 offen offset:" #IMM " sc1" : "=v"(dst) : "v"(voff), "s"(srd), "s"(soff))
#define LD4(dst, voff, srd, soff) asm volatile("buffer_load_dword %0, %1, %2, %3 offen" : "=v"(dst) : "v"(voff), "s"(srd), "s"(soff))
#define LD4S(dst, voff, srd, soff) asm volatile("buffer_load_dword %0, %1, %2, %3 offen sc1" : "=v"(dst) : "v"(voff), "s"(srd), "s"(soff))

template <int IMM> DI void ld16s(v4u& r, u32 v, rsrc_t rs, u32 so) { asm volatile("buffer_load_dwordx4 %0, %1, %2, %3 offen offset:%c4 sc1" : "=v"(r) : "v"(v), "s"(rs), "s"(so), "i"(IMM)); }
template <int CTRL> DI float dppf(float v) { return asf(__builtin_amdgcn_update_dpp(0, asu(v), CTRL, 0xf, 0xf, true)); }
DI float row16_sum(float v) { v += dppf<0x128>(v); v += dppf<0x124>(v); v += dppf<0x122>(v); v += dppf<0x121>(v); return v; }
DI float sw32_add(float a, float b) { auto r = __builtin_amdgcn_permlane32_swap(asu(a), asu(b), false, false); return asf(r[0]) + asf(r[1]); }
DI float sw16_add(float a, float b) { auto r = __builtin_amdgcn_permlane16_swap(asu(a), asu(b), false, false); return asf(r[0]) + asf(r[1]); }
DI float x16_max(float v) { auto r = __builtin_amdgcn_permlane16_swap(asu(v), asu(v), false, false); return fmaxf(asf(r[0]), asf(r[1])); }
DI float x32_max(float v) { auto r = __builtin_amdgcn_permlane32_swap(asu(v), asu(v), false, false); return fmaxf(asf(r[0]), asf(r[1])); }
DI int fresh_lane() { int l; asm volatile("v_mbcnt_lo_u32_b32 %0, -1, 0\n\tv_mbcnt_hi_u32_b32 %0, -1, %0" : "=v"(l)); return l; }
DI float rdl(float v, int l) { return asf(__builtin_amdgcn_readlane(asu(v), l)); }

struct Params {
  const u16 *x, *wqkv, *wo, *w1, *w3, *w2, *c, *wmod;
  u16* out;
  char* ws;
};

#define LOOPSEL 0
#define STAMP(i) do {} while (0)
#define KSTAMP(lp, i) do {} while (0)
#define STAMP_DUMP() do {} while (0)

DI u32 flag_off(int phase, int g, int h) { return WS_FLAG + ((phase * 32 + g) * 2 + h) * 1024; }
DI void poll_flags(rsrc_t rWS, u32 vfl, u32 foff, u64 care, u32 tag) {
  for (u32 sp = 0; sp < SPIN_CAP; ++sp) {
    asm volatile("" ::: "memory");
    const u32 v = __builtin_amdgcn_raw_buffer_load_b32(rWS, vfl, foff, 16);
    if (!(__builtin_amdgcn_ballot_w64(v != tag) & care)) break;
  }
  VMW(0);
}
DI void raise_flag(rsrc_t rWS, u32 foff, u32 vtag, u32 vzero, u32 m1) {
  asm volatile("s_mov_b32 exec_hi, 0\n\ts_mov_b32 exec_lo, %4\n\tbuffer_store_dword %0, %1, %2, %3 offen sc1\n\ts_mov_b64 exec, -1" ::"v"(vtag), "v"(vzero), "s"(rWS), "s"(foff), "s"(m1) : "memory");
}
DI void signal_flag(rsrc_t rWS, u32 cnt_addr, u32 foff, u32 tag) {
  u32 t, a = cnt_addr, one = 1u, vt = tag, vz = 0u;
  asm volatile("s_mov_b64 exec, 1\n\tds_add_rtn_u32 %0, %1, %2\n\ts_waitcnt lgkmcnt(0)\n\tv_cmp_eq_u32 vcc, 7, %0\n\ts_and_b64 exec, exec, vcc\n\t"
               "buffer_store_dword %3, %4, %5, %6 offen sc1\n\ts_mov_b64 exec, -1"
               : "=&v"(t) : "v"(a), "v"(one), "v"(vt), "v"(vz), "s"(rWS), "s"(foff) : "vcc", "scc", "memory");
}
DI void arrive(u32 cnt_addr) {
  u32 a = cnt_addr, one = 1u;
  asm volatile("s_waitcnt lgkmcnt(0)\n\ts_mov_b64 exec, 1\n\tds_add_u32 %0, %1\n\ts_mov_b64 exec, -1" ::"v"(a), "v"(one) : "memory");
}
DI void await8(u32 cnt_addr) {
  u32 a = cnt_addr, t, sv;
  asm volatile("1:\n\tds_read_b32 %0, %2\n\ts_waitcnt lgkmcnt(0)\n\tv_readfirstlane_b32 %1, %0\n\ts_nop 0\n\ts_cmp_lt_u32 %1, 8\n\ts_cbranch_scc1 1b"
               : "=&v"(t), "=&s"(sv) : "v"(a) : "scc", "memory");
}
DI void raise_when8(rsrc_t rWS, u32 cnt_addr, u32 foff, u32 tag, u32 w7) {
  u32 a = cnt_addr, t, sv, vt = tag, vz = 0u;
  asm volatile("s_cmp_eq_u32 %7, 0\n\ts_cbranch_scc1 2f\n\t1:\n\tds_read_b32 %0, %2\n\ts_waitcnt lgkmcnt(0)\n\tv_readfirstlane_b32 %1, %0\n\ts_nop 0\n\ts_cmp_lt_u32 %1, 8\n\ts_cbranch_scc1 1b\n\t"
               "s_mov_b64 exec, 1\n\tbuffer_store_dword %3, %4, %5, %6 offen sc1\n\ts_mov_b64 exec, -1\n\t2:"
               : "=&v"(t), "=&s"(sv) : "v"(a), "v"(vt), "v"(vz), "s"(rWS), "s"(foff), "s"(w7) : "scc", "memory");
}
#define PIN(x) asm volatile("" : "+v"(x))
DI float qp(float r) { PIN(r); return r; }
DI float q_shl(u32 a) { return qp(blo(a)); }
DI float q_hi(u32 a) { return qp(bhi(a)); }
DI float q_fma(float a, float b, float c) { return qp(__builtin_fmaf(a, b, c)); }
DI float q_fmas(float a, float b, float c) { return qp(__builtin_fmaf(a, b, c)); }
DI float q_mul(float a, float b) { return qp(a * b); }
DI float q_mulk(float a) { return qp(a * -1.4426950408889634f); }
DI float q_add1(float a) { return qp(a + 1.f); }
DI float q_exp(float a) { return qp(__builtin_amdgcn_exp2f(a)); }
DI float q_rcp(float a) { return qp(__builtin_amdgcn_rcpf(a)); }
DI u32 q_cvt(float a, float b) { u32 r = pk(a, b); PIN(r); return r; }
DI float q_mov(float a) { return qp(a); }
DI u32 opq(u32 v) { asm volatile("" : "+v"(v)); return v; }

DI float sel(u64 m, float a, float b) { return __builtin_amdgcn_inverse_ballot_w64(m) ? a : b; }
DI v2u attend(v2u kp, v2u vp, v2u kc, v2u vc, v2u q, u64 m0, u64 m1, u32 hm, bool noprev) {
  const f32x4 z = {0.f, 0.f, 0.f, 0.f};
  const f32x4 sp = mfma16(kp, q, z);
  const f32x4 sc = mfma16(kc, q, z);
  const float K2 = 0.25f * 1.4426950408889634f;
  const u64 par = 0xaaaaaaaaaaaaaaaaull;
  float p0 = sel(par, sp[1], sp[0]), p1 = sel(par, sp[3], sp[2]);
  if (noprev) { p0 = -INFINITY; p1 = -INFINITY; }
  const float t0 = sel(m0, sel(par, sc[1], sc[0]), p0), t1 = sel(m1, sel(par, sc[3], sc[2]), p1);
  const float m = x32_max(x16_max(fmaxf(t0, t1)));
  const float mk = -m * K2;
  const float e0 = __builtin_amdgcn_exp2f(__builtin_fmaf(t0, K2, mk)), e1 = __builtin_amdgcn_exp2f(__builtin_fmaf(t1, K2, mk));
  const u32 w0 = pk(e0, e0) & hm, w1 = pk(e1, e1) & hm;
  v4u pf;
  pf.x = __builtin_amdgcn_inverse_ballot_w64(m0) ? 0u : w0; pf.y = __builtin_amdgcn_inverse_ballot_w64(m1) ? 0u : w1;
  pf.z = __builtin_amdgcn_inverse_ballot_w64(m0) ? w0 : 0u; pf.w = __builtin_amdgcn_inverse_ballot_w64(m1) ? w1 : 0u;
  const f32x4 o = mfma32(cat(vp, vc), pf, z);
  float sum = e0 + e1;
  sum = sw16_add(sum, sum); sum = sw32_add(sum, sum);
  const float inv = __builtin_amdgcn_rcpf(sum);
  return pk4(f32x4{qp(o.x * inv), qp(o.y * inv), qp(o.z * inv), qp(o.w * inv)});
}

template <int N> using IC = std::integral_constant<int, N>;
template <int I, int N, class F> DI void sfor(F&& f) { if constexpr (I < N) { f(IC<I>{}); sfor<I + 1, N>(f); } }

__global__ void __launch_bounds__(512, 2) fused_block(Params p) {
  extern __shared__ __attribute__((aligned(16))) char smem[];
  LAS char* L = (LAS char*)smem;
  const int tid = threadIdx.x, lane = tid & 63, w = __builtin_amdgcn_readfirstlane(tid >> 6);
  const int c16 = lane & 15, g4 = lane >> 4;
  const int b = blockIdx.x;
  int g, j;
  if (XMAP == 0) { g = (b & 7) * 4 + ((b >> 3) & 3); j = b >> 5; }
  else if (XMAP == 2) { const int x = b & 7, r = b >> 3; g = (x >> 1) * 8 + (r & 7); j = (x & 1) * 4 + (r >> 3); }
  else { const int x = b & 7, r = b >> 3; g = (x >> 2) * 16 + (r & 15); j = (x & 3) * 2 + (r >> 4); }
  const rsrc_t rX = __builtin_amdgcn_make_buffer_rsrc((void*)p.x, 0, S_ * D_ * 2, 0x00020000);
  const rsrc_t rQKV = __builtin_amdgcn_make_buffer_rsrc((void*)p.wqkv, 0, D_ * 3 * D_ * 2, 0x00020000);
  const rsrc_t rWO = __builtin_amdgcn_make_buffer_rsrc((void*)p.wo, 0, D_ * D_ * 2, 0x00020000);
  const rsrc_t rW1 = __builtin_amdgcn_make_buffer_rsrc((void*)p.w1, 0, D_ * F_ * 2, 0x00020000);
  const rsrc_t rW3 = __builtin_amdgcn_make_buffer_rsrc((void*)p.w3, 0, D_ * F_ * 2, 0x00020000);
  const rsrc_t rW2 = __builtin_amdgcn_make_buffer_rsrc((void*)p.w2, 0, F_ * D_ * 2, 0x00020000);
  const rsrc_t rC = __builtin_amdgcn_make_buffer_rsrc((void*)p.c, 0, D_ * 2, 0x00020000);
  const rsrc_t rMOD = __builtin_amdgcn_make_buffer_rsrc((void*)p.wmod, 0, D_ * 6 * D_ * 2, 0x00020000);
  const rsrc_t rWS = __builtin_amdgcn_make_buffer_rsrc((void*)p.ws, 0, WS_BYTES, 0x00020000);
  const rsrc_t rOUT = __builtin_amdgcn_make_buffer_rsrc((void*)p.out, 0, S_ * D_ * 2, 0x00020000);
  STAMP(0);

  u32 ep;
  { const u32 vz = 0; asm volatile("buffer_load_dword %0, %1, %2, 0 offen sc1" : "=v"(ep) : "v"(vz), "s"(rWS)); }
  u32 vl16 = lane * 16; PIN(vl16);
  const int bm = b < 192 ? b : b - 192, mq = bm / 48, cl = bm - 48 * mq;
  v4u wm[4]; v2u cv; u32 ep0;
  if (w < 4) {
    const u32 vm = (tid >> 3) * 24576 + (tid & 7) * 16, s0 = mq * (128 * 6144) + cl * 128, s1 = s0 + 6144, s2 = s0 + 12288, s3 = s0 + 18432;
    LD16(wm[0], vm, rMOD, s0, 0); LD16(wm[1], vm, rMOD, s1, 0); LD16(wm[2], vm, rMOD, s2, 0); LD16(wm[3], vm, rMOD, s3, 0);
    const u32 vc = (tid >> 3) * 8, scq = mq * 256;
    LD8(cv, vc, rC, scq, 0);
    const u32 vz = 0; asm volatile("buffer_load_dword %0, %1, %2, 0 offen sc1" : "=v"(ep0) : "v"(vz), "s"(rWS));
  }
  if (tid < 16) *(LAS u32*)(L + L_CNT + tid * 4) = 0u;
  BAR();
  const int st_qd = lane >> 2, st_r = (st_qd & 3) + 4 * (st_qd >> 3);
  const int st_col = ((st_qd >> 2) & 1) * 64 + (((lane & 3) ^ ((w & 1) << 1)) << 4);
  const int rotp = g & 7;
  u32 vB1 = st_r * 3072 + st_col; PIN(vB1);
  const u32 mB1 = (w >> 2) * 12288 + (w & 3) * 1024, sB1 = (w >> 2) * 98304 + (w & 3) * 24576 + j * 128;
  auto stage_b1 = [&](auto PC, auto BC) {
    constexpr int pair = decltype(PC)::value, base = decltype(BC)::value;
    const u32 sp = ((pair + rotp) & 7) * 196608 + sB1;
    dma<base, 0>(mB1, vB1, rQKV, sp); dma<base + 4096, 1024>(mB1, vB1, rQKV, sp); dma<base + 8192, 2048>(mB1, vB1, rQKV, sp);
  };
  auto b1_piece = [&](auto PC, auto BC, auto TC) {
    constexpr int pair = decltype(PC)::value, base = decltype(BC)::value, ty = decltype(TC)::value;
    dma<base + ty * 4096, ty * 1024>(mB1, vB1, rQKV, ((pair + rotp) & 7) * 196608 + sB1);
  };
  const int hd = w & 3, blk2 = w >> 2, kq = w & 3, mh = w >> 2;
  const int cs0 = j < 4 ? 22 * j : 88 + 21 * (j - 4);
  const int cend = cs0 + (j < 4 ? 22 : 21);
  v4u xr[10];
  const int rx2 = (0x4310 >> (4 * (j & 3))) & 7;
  auto load_x = [&](int i0, int i1) {
    const int r0 = 64 * g - 16 + w;
#pragma unroll
    for (int i = i0; i < i1; ++i) {
      int bp = (i >> 1) + rx2; bp = bp >= 5 ? bp - 5 : bp;
      const u32 so = (u32)max(r0 + 16 * bp + 8 * (i & 1), 0) * 1024u;
      LD16(xr[i], vl16, rX, so, 0);
    }
  };
  load_x(0, 5);
  STAMP(1);
  const int T0 = 12 * (g >> 4) + 3 * (w & 3), rcb = min(cs0 + T0, 168);
  if (w < 4) {
    asm volatile("s_waitcnt vmcnt(5)" : "+v"(cv), "+v"(wm[0]), "+v"(wm[1]), "+v"(wm[2]), "+v"(wm[3]), "+v"(ep0)::"memory");
    const u32 tg = __builtin_amdgcn_readfirstlane(ep0) + 1u;
    const f32x4 cf = {blo(cv.x), bhi(cv.x), blo(cv.y), bhi(cv.y)};
    float sc[4];
#pragma unroll
    for (int r = 0; r < 4; ++r) sc[r] = cf[r] * __builtin_amdgcn_rcpf(1.f + __builtin_amdgcn_exp2f(cf[r] * -1.4426950408889634f));
    f32x2 v[4];
#pragma unroll
    for (int i = 0; i < 4; ++i) v[i] = (unp(wm[0][i]) * sc[0] + unp(wm[1][i]) * sc[1]) + (unp(wm[2][i]) * sc[2] + unp(wm[3][i]) * sc[3]);
    const float a0 = sw32_add(v[0].x, v[2].x), a1 = sw32_add(v[0].y, v[2].y), a2 = sw32_add(v[1].x, v[3].x), a3 = sw32_add(v[1].y, v[3].y);
    float t0 = sw16_add(a0, a2), t1 = sw16_add(a1, a3);
    t0 += dppf<0x128>(t0); t1 += dppf<0x128>(t1);
    f32x2 o = {t0, t1};
    *(LAS f32x2*)(L + L_RED + w * 256 + (lane & 7) * 32 + g4 * 8) = o;
    LGW();
    u32 old = 0;
    if (lane == 0) old = __hip_atomic_fetch_add((LAS u32*)(L + L_CNT + 28), 1u, __ATOMIC_RELAXED, __HIP_MEMORY_SCOPE_WORKGROUP);
    if (__builtin_amdgcn_readfirstlane(old) == 3u) {
      asm volatile("" ::: "memory");
      float s = 0.f;
#pragma unroll
      for (int i = 0; i < 4; ++i) s += *(LAS float*)(L + L_RED + i * 256 + lane * 4);
      v2u gr; gr.x = asu(s); gr.y = tg;
      if (b < 192) __builtin_amdgcn_raw_buffer_store_b64(gr, rWS, lane * 8, WS_MODG + (mq * 3072 + cl * 64) * 8, 16);
    }
  }
  STAMP(2);
  load_x(5, 10);
  stage_b1(IC<0>{}, IC<L_XY>{});
  int afo[4];
#pragma unroll
  for (int i = 0; i < 4; ++i) { afo[i] = blk2 * 32768 + c16 * 1024 + (((4 * i + g4) ^ c16) << 4); PIN(afo[i]); }
  const u64 am0 = __builtin_amdgcn_ballot_w64((c16 & 14) >= 4 * g4), am1 = __builtin_amdgcn_ballot_w64((c16 & 14) >= 4 * g4 + 2);
  u32 ahm = (lane & 1) ? 0xffff0000u : 0xffffu; PIN(ahm);
  u32 vOst = c16 * 16 + g4 * 256 + (hd & 1) * 8; PIN(vOst);
  int tB1 = g4 * 1024 + 64 * (c16 >> 2) + 8 * (lane & 3) + 32 * ((hd & 1) ^ (g4 & 1)) + 256 * (hd >> 1); PIN(tB1);
  int hw0 = w * 1024 + ((((lane - 8 * rotp) & 63) ^ w) << 4); PIN(hw0);
#define vfl ((opq(vl16) & 112u) << 3)
  STAMP(3);
  v4u bw[48];
  auto read_b1 = [&](int pair, int base, int off) {
#pragma unroll
    for (int kk = 0; kk < 2; ++kk)
#pragma unroll
      for (int ty = 0; ty < 3; ++ty) {
        LAS char* a = L + base + (off + kk * 12288 + ty * 4096);
        bw[(2 * pair + kk) * 3 + ty] = cat(trd(a), trd(a + 512));
      }
  };
  f32x2 xn[10][4];
  v4u ga_[4]; v2u pv;
  const u32 sMG = WS_MODG + w * 1024;
  {
    float u[10];
    sfor<0, 10>([&](auto I) {
      constexpr int i = decltype(I)::value;
      if constexpr (i == 0) asm volatile("s_waitcnt vmcnt(11)" : "+v"(xr[0]), "+v"(xr[1]), "+v"(ep)::"memory");
      if constexpr (i == 2) asm volatile("s_waitcnt vmcnt(9)" : "+v"(xr[2]), "+v"(xr[3])::"memory");
      if constexpr (i == 4) asm volatile("s_waitcnt vmcnt(10)" : "+v"(xr[4]), "+v"(xr[5])::"memory");
      if constexpr (i == 6) asm volatile("s_waitcnt vmcnt(8)" : "+v"(xr[6]), "+v"(xr[7])::"memory");
      if constexpr (i == 8) asm volatile("s_waitcnt vmcnt(9)" : "+v"(xr[8]), "+v"(xr[9])::"memory");
      float s = 0.f, q = 0.f;
#pragma unroll
      for (int e = 0; e < 4; ++e) { s = dot2(xr[i][e], 0x3f803f80u, s); q = dot2(xr[i][e], xr[i][e], q); }
      u[i] = sw32_add(s, q);
      PIN(u[i]);
      if constexpr (i == 3) stage_b1(IC<1>{}, IC<L_XY + 24576>{});
      if constexpr (i == 7) stage_b1(IC<2>{}, IC<L_XY + 49152>{});
      if constexpr (i == 9) {
        const u32 s0 = sMG, s1 = sMG + 24576, s2 = sMG + 49152, s3 = sMG + 73728;
        ld16s<0>(ga_[0], vl16, rWS, s0); ld16s<0>(ga_[1], vl16, rWS, s1); ld16s<0>(ga_[2], vl16, rWS, s2); ld16s<0>(ga_[3], vl16, rWS, s3);
      }
    });
    float t[5];
#pragma unroll
    for (int i = 0; i < 5; ++i) t[i] = row16_sum(sw16_add(u[i], u[i + 5]));
    float v = t[0];
#pragma unroll
    for (int i = 1; i < 5; ++i) v = c16 == i ? t[i] : v;
    auto r = __builtin_amdgcn_permlane32_swap(asu(v), asu(v), false, false);
    const float mean = asf(r[0]) * (1.f / 512.f);
    const float var = fmaxf(asf(r[1]) * (1.f / 512.f) - mean * mean, 0.f);
    float rs = __builtin_amdgcn_rsqf(var + 1e-5f), nm = -mean * rs;
    PIN(rs); PIN(nm);
    STAMP(4);
    { const u32 vo = ((lane >> 3) & 3) * 24576 + (lane & 7) * 8, so = WS_MODG + (1024 + 8 * b) * 8; LD8S(pv, vo, rWS, so, 0); }
    if (w >= 4) {
      const int u = lane >> 2, row = (u & 3) | ((u >> 1) & 4) | ((u << 1) & 8);
      const u32 vo = row * 2752 + (lane & 3) * 16, so = (g & 15) * (32 * 2752) + rcb * 16, mb = L_Z + 49152 + (w & 3) * 4096;
      dma<0, 0>(mb, vo, rW1, so); dma<1024, 16 * 2752>(mb, vo, rW1, so); dma<2048, 0>(mb, vo, rW3, so); dma<3072, 16 * 2752>(mb, vo, rW3, so);
    } else {
      const int u = lane >> 1, row = (u & 3) | ((u >> 1) & 4) | ((u << 1) & 8) | (u & 16);
      const u32 vo = row * 1024 + (lane & 1) * 16, so = g * 32768 + j * 128 + w * 32, mb = L_Z + 65536 + w * 2048;
      dma<0, 0>(mb, vo, rW2, so); dma<1024, 0>(mb, vo, rW2, so + (g < 11 ? 32 * 32768 : 0));
      const int rwo = (u & 7) | ((u >> 1) & 8) | ((u << 1) & 16);
      dma<0, 0>(76800 + w * 1024, rwo * 1024 + (lane & 1) * 16, rWO, (g & 15) * 32768 + j * 128 + w * 32);
    }
    sfor<0, 10>([&](auto I) {
      constexpr int i = decltype(I)::value;
      if constexpr (i == 2) stage_b1(IC<3>{}, IC<L_Z>{});
      if constexpr (i == 6) stage_b1(IC<4>{}, IC<L_Z + 24576>{});
      const float a = rdl(rs, (i / 5) * 16 + i % 5), c = rdl(nm, (i / 5) * 16 + i % 5);
#pragma unroll
      for (int e = 0; e < 4; ++e) { xn[i][e] = unp(xr[i][e]) * a + c; PIN(xn[i][e]); }
    });
  }
  const u32 tag = __builtin_amdgcn_readfirstlane(ep) + 1u;
  u32 m1; asm("s_lshr_b32 %0, 1, %1" : "=s"(m1) : "s"(w) : "scc");
#define RAISE(fo) raise_flag(rWS, (fo), tag, vl16, m1)
#define SIGNAL(idx, fo) signal_flag(rWS, L_CNT + 4 * (idx), (fo), tag)
  STAMP(5);
  asm volatile("s_waitcnt vmcnt(9)" : "+v"(ga_[0]), "+v"(ga_[1]), "+v"(ga_[2]), "+v"(ga_[3]), "+v"(pv)::"memory");
  auto gsum = [&](v4u (&a)[4], f32x2& o) -> u32 {
    o.x = (asf(a[0].x) + asf(a[1].x)) + (asf(a[2].x) + asf(a[3].x)); o.y = (asf(a[0].z) + asf(a[1].z)) + (asf(a[2].z) + asf(a[3].z));
    return ((a[0].y ^ tag) | (a[0].w ^ tag) | (a[1].y ^ tag) | (a[1].w ^ tag)) | ((a[2].y ^ tag) | (a[2].w ^ tag) | (a[3].y ^ tag) | (a[3].w ^ tag));
  };
  auto gather = [&](u32 voff, u32 soff, f32x2& o) {
    for (u32 sp = 0; sp < SPIN_CAP; ++sp) {
      asm volatile("" ::: "memory");
      v4u a[4];
#pragma unroll
      for (int q = 0; q < 4; ++q) a[q] = __builtin_amdgcn_raw_buffer_load_b128(rWS, voff, soff + q * 24576, 16);
      if (!__builtin_amdgcn_ballot_w64(gsum(a, o) != 0u)) break;
    }
  };
  const float one = w >= 4 ? 1.f : 0.f;
  {
    f32x2 mA;
    if (__builtin_amdgcn_ballot_w64(gsum(ga_, mA) != 0u)) gather(vl16, sMG, mA);
    *(LAS f32x2*)(L + L_MODA + w * 512 + (vl16 >> 1)) = mA + one;
  }
  BAR();
  if (w == 7) {
    const u32 vo = ((lane >> 3) & 3) * 24576 + (lane & 7) * 8, so = WS_MODG + (1024 + 8 * b) * 8;
    for (u32 sp = 0; sp < SPIN_CAP && __builtin_amdgcn_ballot_w64(pv.y != tag); ++sp) {
      asm volatile("" ::: "memory");
      pv = __builtin_amdgcn_raw_buffer_load_b64(rWS, vo, so, 16);
    }
    float sv = asf(pv.x);
    sv += dppf<0x128>(sv); sv = sw16_add(sv, sv);
    pv.x = asu(sv);
    __builtin_amdgcn_raw_buffer_store_b64(pv, rWS, (lane & 7) * 8, WS_MODF + (1024 + 8 * b) * 8, 16);
  }
  STAMP(6);
  int vma = L_MODA + 2 * vl16; PIN(vma);
  const f32x4 ms0 = *(LAS f32x4*)(L + vma), ms1 = *(LAS f32x4*)(L + vma + 16);
  const f32x4 ma0 = *(LAS f32x4*)(L + vma + 2048), ma1 = *(LAS f32x4*)(L + vma + 2064);
  int tz0 = tB1 + L_Z; PIN(tz0);
  { int t2 = tB1 + 49152; PIN(t2); read_b1(0, tB1, 0); read_b1(1, tB1, 24576); read_b1(2, t2, 0); }
  {
    VMW(6);
    if (w >= 4) {
      LAS char* a0 = L + L_Z + 49152 + (w & 3) * 4096 + ((lane >> 1) & 1) * 2048 + 64 * ((c16 >> 2) + 4 * (g4 & 1) + 16 * (g4 >> 1)) + 8 * (lane & 1);
#pragma unroll
      for (int ct = 0; ct < 3; ++ct) {
        const int c = min(cs0 + T0 + ct - rcb, 3) * 16;
        const v4u f = cat(trd(a0 + c), trd(a0 + c + 512));
        const u32 sf = WS_B4T + ((j * 16 + (g & 15)) * 24 + T0 + ct) * 1024;
        asm volatile("buffer_store_dwordx4 %0, %1, %2, %3 offen sc1\n\ts_nop 1" ::"v"(f), "v"(vl16), "s"(rWS), "s"(sf) : "memory");
      }
    }
    else {
      LAS char* a0 = L + L_Z + 65536 + w * 2048 + 32 * ((c16 >> 2) + 4 * (g4 & 1) + 16 * (g4 >> 1)) + 8 * (lane & 3);
#pragma unroll
      for (int i = 0; i < 2; ++i) {
        const v4u f = cat(trd(a0 + i * 1024), trd(a0 + i * 1024 + 256));
        const u32 sf = WS_B5T + ((j * 43 + g + (i && g < 11 ? 32 : 0)) * 4 + w) * 1024;
        asm volatile("buffer_store_dwordx4 %0, %1, %2, %3 offen sc1\n\ts_nop 1" ::"v"(f), "v"(vl16), "s"(rWS), "s"(sf) : "memory");
      }
      LAS char* a1 = L + 76800 + w * 1024 + 32 * ((c16 >> 2) + 4 * (g4 & 1) + 16 * (g4 >> 1)) + 8 * (lane & 3);
      const v4u f = cat(trd(a1), trd(a1 + 256));
      const u32 sf = WS_B3T + ((j * 16 + (g & 15)) * 4 + w) * 1024;
      asm volatile("buffer_store_dwordx4 %0, %1, %2, %3 offen sc1\n\ts_nop 1" ::"v"(f), "v"(vl16), "s"(rWS), "s"(sf) : "memory");
    }
  }
  BAR();
  STAMP(7);
  {
    int hw1 = (hw0 ^ 128) + 8192; PIN(hw1);
    sfor<0, 10>([&](auto I) {
      constexpr int i = decltype(I)::value;
      v4u o;
      o.x = pk2(xn[i][0] * ma0.xy + ms0.xy); o.y = pk2(xn[i][1] * ma0.zw + ms0.zw);
      o.z = pk2(xn[i][2] * ma1.xy + ms1.xy); o.w = pk2(xn[i][3] * ma1.zw + ms1.zw);
      int bp = (i >> 1) + rx2; bp = bp >= 5 ? bp - 5 : bp;
      *(LAS v4u*)(L + ((i & 1) ? hw1 : hw0) + bp * 16384) = o;
      SCHED();
    });
  }
  VMW(0);
  BAR();
  RAISE(WS_RFLAG + (j * 32 + g) * 4);
  u32 flr;
  STAMP(8);
  __builtin_amdgcn_s_setprio(2);

  const f32x4 z4 = {0.f, 0.f, 0.f, 0.f};
#define AFRAG(off, ks) (*(LAS v4u*)(L + afo[(ks) & 3] + ((off) + ((ks) >> 2) * 256)))
  const int ch0 = cs0 + 3 * w;
  constexpr int ND = NODMA ? 0 : 1;
  v4u b4[48];
  const u32 sB4 = WS_B4T + (j * 16 * 24 + 3 * w) * 1024;
  auto load_b4 = [&](auto NC) {
    constexpr int n = decltype(NC)::value, ks = n / 3, ct = n % 3;
    const u32 so = sB4 + ((ks + 2 * rotp) & 15) * 24576;
    v4u& r = b4[n];
    if constexpr (ct == 0) LD16S(r, vl16, rWS, so, 0); else if constexpr (ct == 1) LD16S(r, vl16, rWS, so, 1024); else LD16S(r, vl16, rWS, so, 2048);
  };
  auto landed_b4 = [&](auto AC, auto BC) {
#pragma unroll
    for (int i = decltype(AC)::value; i < decltype(BC)::value; ++i) { v4u& r = b4[i]; PIN(r); }
  };
  v4u b3[16];
  auto load_b3 = [&](auto NC) { constexpr int n = decltype(NC)::value; ld16s<(n & 3) * 1024>(b3[n], vl16, rWS, WS_B3T + (j * 16 + 4 * kq + (n >> 2)) * 4096); };
  v4u gb_, gg_;
  u32 vgg, fl0, vRow;
  int pw, pr;
  f32x4 aKh = z4, aVh = z4, aQ = z4, aK = z4, aV = z4, bQ = z4, bK = z4, bV = z4;
  {
    v4u ah = AFRAG(0, 0), a1 = AFRAG(16384, 0), a2 = AFRAG(32768, 0);
    int l3, c3, g3; f32x2 mB, mG;
    auto job = [&](auto NC) {
      constexpr int n = decltype(NC)::value;
      if constexpr (n == 3) { l3 = opq(vl16) >> 4; c3 = l3 & 15; g3 = l3 >> 4; PIN(c3); PIN(g3); vgg = (vl16 < 512 ? 8192 : 20480 - 512) + 512 * j + vl16; PIN(vgg); }
      if constexpr (n == 7) { vRow = (8 * w + (l3 >> 3)) * 1024 + (l3 & 7) * 16; PIN(vRow); }
      if constexpr (n == 8) { pr = (8 * w + (l3 >> 3)) * 256 + (((2 * (l3 & 7)) ^ ((l3 >> 3) & 7)) << 4); PIN(pr); }
      if constexpr (n == 10) { pw = kq * 16384 + (32 * mh + c3) * 256 + ((g3 ^ (c3 & 3)) << 4) + ((c3 & 4) << 4); PIN(pw); }
      if constexpr (n == 31) { mB.x = asf(gb_.x) + one; PIN(mB.x); mB.y = asf(gb_.z) + one; PIN(mB.y); *(LAS f32x2*)(L + L_MODA + w * 512 + (vl16 >> 1)) = mB; }
      if constexpr (n == 32) { mG.x = asf(gg_.x); mG.y = asf(gg_.z); *(LAS f32x2*)(L + L_GAB + (vl16 >> 1)) = mG; }
      if constexpr (n == 34) { fl0 = (gb_.y ^ tag) | (gb_.w ^ tag) | (gg_.y ^ tag) | (gg_.w ^ tag); PIN(fl0); }
      if constexpr (n == 7) { const u32 s0 = WS_MODF + 12288 + w * 1024; LD16S(gb_, vl16, rWS, s0, 0); }
      if constexpr (n == 9) { const u32 s0 = WS_MODF; LD16S(gg_, vgg, rWS, s0, 0); }
      if constexpr (n < 6 && !(n & 1)) b1_piece(IC<5>{}, IC<L_Z + 49152>{}, IC<n / 2>{});
      if constexpr (n == 6) arrive(L_CNT + 8);
      if constexpr (n == 10) await8(L_CNT + 8);
      if constexpr (n >= 10 && n < 22 && !(n & 1)) b1_piece(IC<6 + (n - 10) / 6>{}, IC<L_Z + ((n - 10) / 6) * 24576>{}, IC<((n - 10) / 2) % 3>{});
      if constexpr (n == 21) { const u32 vr = (vl16 >> 2) & 124, sr = WS_RFLAG + j * 128; LD4S(flr, vr, rWS, sr); }
      if constexpr (n == 26) {
        asm volatile("s_waitcnt vmcnt(0)" : "+v"(gb_), "+v"(gg_), "+v"(flr)::"memory");
        arrive(L_CNT + 12);
        const u32 vr = (vl16 >> 2) & 124, sr = WS_RFLAG + j * 128; u32 cnt = SPIN_CAP;
        asm volatile("1:\n\tv_cmp_ne_u32 vcc, %5, %0\n\ts_cbranch_vccz 2f\n\tbuffer_load_dword %0, %2, %3, %4 offen sc1\n\ts_waitcnt vmcnt(0)\n\t"
                     "s_sub_u32 %1, %1, 1\n\ts_cbranch_scc0 1b\n\t2:" : "+v"(flr), "+s"(cnt) : "v"(vr), "s"(rWS), "s"(sr), "s"(tag) : "vcc", "scc", "memory");
      }
      if constexpr (n == 28) { await8(L_CNT + 12); int tz2 = tz0 + 49152; PIN(tz2); read_b1(5, tz2, 0); }
      if constexpr (n == 32) read_b1(6, tz0, 0);
      if constexpr (n == 37) read_b1(7, tz0, 24576);
      if constexpr (n >= 27 && n < 47 && (n & 1)) load_b3(IC<(n - 27) / 2>{});
    };
    sfor<0, 16>([&](auto KS) {
      constexpr int ks = decltype(KS)::value;
      KSTAMP(1, ks);
      if constexpr (ks == 0) read_b1(3, tz0, 0);
      if constexpr (ks == 1) read_b1(4, tz0, 24576);
      SCHED();
      v4u nh = ah, n1 = a1, n2 = a2;
      aKh = mfma32(bw[ks * 3 + 1], ah, aKh);
      aVh = mfma32(ah, bw[ks * 3 + 2], aVh);
      if constexpr (ks < 15) nh = AFRAG(0, ks + 1);
      job(IC<3 * ks>{});
      SCHED();
      aQ = mfma32(bw[ks * 3 + 0], a1, aQ);
      aK = mfma32(bw[ks * 3 + 1], a1, aK);
      aV = mfma32(a1, bw[ks * 3 + 2], aV);
      if constexpr (ks < 15) n1 = AFRAG(16384, ks + 1);
      job(IC<3 * ks + 1>{});
      SCHED();
      bQ = mfma32(bw[ks * 3 + 0], a2, bQ);
      bK = mfma32(bw[ks * 3 + 1], a2, bK);
      bV = mfma32(a2, bw[ks * 3 + 2], bV);
      if constexpr (ks < 15) n2 = AFRAG(32768, ks + 1);
      job(IC<3 * ks + 2>{});
      ah = nh; a1 = n1; a2 = n2;
      SCHED();
    });
  }
  __builtin_amdgcn_s_setprio(0);
  STAMP(9); KSTAMP(1, 16);
  {
    const u32 sOst = WS_O + ((g * 4 + 2 * blk2) * 16 + 2 * j + (hd >> 1)) * 1024;
    const v2u k1p = pk4(aK), v1p = pk4(aV);
    const v2u o0 = attend(pk4(aKh), pk4(aVh), k1p, v1p, pk4(aQ), am0, am1, ahm, g == 0 && blk2 == 0);
    const v2u o1 = attend(k1p, v1p, pk4(bK), pk4(bV), pk4(bQ), am0, am1, ahm, false);
    __builtin_amdgcn_raw_buffer_store_b64(o0, rWS, vOst, sOst, 16);
    __builtin_amdgcn_raw_buffer_store_b64(o1, rWS, vOst, sOst + 16384, 16);
  }
  if (__builtin_amdgcn_ballot_w64(fl0 != 0u)) {
    for (u32 sp = 0; sp < SPIN_CAP; ++sp) {
      asm volatile("" ::: "memory");
      gb_ = __builtin_amdgcn_raw_buffer_load_b128(rWS, vl16, WS_MODF + 12288 + w * 1024, 16);
      gg_ = __builtin_amdgcn_raw_buffer_load_b128(rWS, vgg, WS_MODF, 16);
      if (!__builtin_amdgcn_ballot_w64(((gb_.y ^ tag) | (gb_.w ^ tag) | (gg_.y ^ tag) | (gg_.w ^ tag)) != 0u)) break;
    }
    const f32x2 mB = {asf(gb_.x) + one, asf(gb_.z) + one}, mG = {asf(gg_.x), asf(gg_.z)};
    *(LAS f32x2*)(L + L_MODA + w * 512 + (vl16 >> 1)) = mB;
    *(LAS f32x2*)(L + L_GAB + (vl16 >> 1)) = mG;
  }
  STAMP(10);
  VMW(0);
  BAR();
  RAISE(flag_off(0, g, 0) + j * 128);
  STAMP(11);
  if (b == 0 && w == 7) {
    for (u32 sp = 0; sp < SPIN_CAP; ++sp) {
      asm volatile("" ::: "memory");
      const v4u v = __builtin_amdgcn_raw_buffer_load_b128(rWS, vl16, WS_RFLAG, 16);
      if (__all(v.x == tag && v.y == tag && v.z == tag && v.w == tag)) break;
    }
    __builtin_amdgcn_raw_buffer_store_b32(tag, rWS, 0, WS_EPOCH, 16);
  }

  v4u xv;
  { const u32 sx = g * 65536 + j * 128; LD16(xv, vRow, rX, sx, 0); }
  sfor<10, 16>(load_b3);
  const u64 care2 = 0x0101010101010101ull * (3u << (2 * kq));
  poll_flags(rWS, vfl, flag_off(0, g, 0), care2, tag);
  PIN(xv);
  v4u af[8];
  {
    const u32 so = WS_O + ((g * 4 + 2 * mh) * 16 + 4 * kq) * 1024, so1 = so + 16384;
    LD16S(af[0], vl16, rWS, so, 0); LD16S(af[1], vl16, rWS, so, 1024); LD16S(af[2], vl16, rWS, so, 2048); LD16S(af[3], vl16, rWS, so, 3072);
    LD16S(af[4], vl16, rWS, so1, 0); LD16S(af[5], vl16, rWS, so1, 1024); LD16S(af[6], vl16, rWS, so1, 2048); LD16S(af[7], vl16, rWS, so1, 3072);
  }
  STAMP(12);
  sfor<0, 2>([&](auto M) {
    constexpr int m = decltype(M)::value;
    asm volatile("s_waitcnt vmcnt(%c4)" : "+v"(af[4 * m]), "+v"(af[4 * m + 1]), "+v"(af[4 * m + 2]), "+v"(af[4 * m + 3]) : "i"(m == 0 ? 4 : 2) : "memory");
    if constexpr (m == 0) {
#pragma unroll
      for (int i = 0; i < 16; ++i) PIN(b3[i]);
    }
    f32x4 acc[4] = {z4, z4, z4, z4};
    sfor<0, 4>([&](auto SC) {
      constexpr int sx = decltype(SC)::value;
#pragma unroll
      for (int n = 0; n < 4; ++n) acc[n] = mfma32(b3[sx * 4 + n], af[4 * m + sx], acc[n]);
      if constexpr (sx & 1) load_b4(IC<2 * m + (sx >> 1)>{});
      SCHED();
    });
#pragma unroll
    for (int n = 0; n < 4; ++n) *(LAS f32x4*)(L + ((n & 1) ? (pw ^ 64) : pw) + (m * 4096 + (n >> 1) * 128)) = acc[n];
  });
  BAR();
  STAMP(13);
  v4u x1p;
  {
    f32x4 y0 = *(LAS f32x4*)(L + pr), y1 = *(LAS f32x4*)(L + (pr ^ 16));
#pragma unroll
    for (int q = 1; q < 4; ++q) { y0 += *(LAS f32x4*)(L + pr + q * 16384); y1 += *(LAS f32x4*)(L + (pr ^ 16) + q * 16384); }
    const int ga = L_GAB + ((vl16 & 112) << 1);
    const f32x4 g0 = *(LAS f32x4*)(L + ga), g1 = *(LAS f32x4*)(L + ga + 16);
    const f32x4 x0 = f32x4{blo(xv.x), bhi(xv.x), blo(xv.y), bhi(xv.y)} + g0 * y0, x1 = f32x4{blo(xv.z), bhi(xv.z), blo(xv.w), bhi(xv.w)} + g1 * y1;
    { const v4u o = cat(pk4(x0), pk4(x1)); x1p = o; const u32 so = WS_X1 + g * 65536 + j * 128;
      asm volatile("buffer_store_dwordx4 %0, %1, %2, %3 offen sc1\n\ts_nop 1" ::"v"(o), "v"(vRow), "s"(rWS), "s"(so) : "memory"); }
    const f32x4 sv = x0 + x1;
    float mean = (sv.x + sv.y) + (sv.z + sv.w);
    mean += dppf<0xB1>(mean); mean += dppf<0x4E>(mean); mean += dppf<0x141>(mean);
    mean *= 1.f / 64.f;
    const f32x4 d0 = x0 - mean, d1 = x1 - mean, dd = d0 * d0 + d1 * d1;
    float m2 = (dd.x + dd.y) + (dd.z + dd.w);
    m2 += dppf<0xB1>(m2); m2 += dppf<0x4E>(m2); m2 += dppf<0x141>(m2);
    v2u st; st.x = asu(mean); st.y = asu(m2);
    __builtin_amdgcn_raw_buffer_store_b64(st, rWS, (vRow >> 10) << 6, WS_STAT + g * 4096 + j * 8, 16);
  }
  STAMP(14);
  VMW(0);
  BAR();
  RAISE(flag_off(1, g, 0) + j * 128);
  sfor<4, 9>(load_b4);
  STAMP(15);

  u32 hmask[3];
#pragma unroll
  for (int ct = 0; ct < 3; ++ct)
    asm("s_cmp_lt_u32 %1, %2\n\ts_cselect_b32 %0, -1, 0" : "=s"(hmask[ct]) : "s"((u32)(ch0 + ct)), "s"((u32)cend) : "scc");
#pragma unroll
  for (int i = 0; i < 4; ++i) { afo[i] -= blk2 * 32768; PIN(afo[i]); }
  int vm2 = L_MODA + 2 * vl16; PIN(vm2);
  int h2w = w * 8192 + (((vl16 - 128 * rotp) & 1023) ^ ((8 * w & 15) << 4)); PIN(h2w);
  const f32x4 ls0 = *(LAS f32x4*)(L + vm2), ls1 = *(LAS f32x4*)(L + vm2 + 16), la0 = *(LAS f32x4*)(L + vm2 + 2048), la1 = *(LAS f32x4*)(L + vm2 + 2064);
  poll_flags(rWS, vfl, flag_off(1, g, 0), ~0ull, tag);
  landed_b4(IC<0>{}, IC<9>{});
  {
    v4u x1v[8]; v2u stv;
    const u32 so = WS_X1 + g * 65536 + w * 8192, so2 = so + 4096;
    { const u32 vs = vl16 >> 1, ss = WS_STAT + g * 4096 + w * 512; LD8S(stv, vs, rWS, ss, 0); }
    LD16S(x1v[0], vl16, rWS, so, 0); LD16S(x1v[1], vl16, rWS, so, 1024); LD16S(x1v[2], vl16, rWS, so, 2048); LD16S(x1v[3], vl16, rWS, so, 3072);
    LD16S(x1v[4], vl16, rWS, so2, 0); LD16S(x1v[5], vl16, rWS, so2, 1024); LD16S(x1v[6], vl16, rWS, so2, 2048); LD16S(x1v[7], vl16, rWS, so2, 3072);
    sfor<9, 12>(load_b4);
    asm volatile("s_waitcnt vmcnt(11)" : "+v"(stv)::"memory");
    const float mj = asf(stv.x);
    float mean = mj;
    mean += dppf<0xB1>(mean); mean += dppf<0x4E>(mean); mean += dppf<0x141>(mean);
    mean *= 0.125f;
    const float dm = mj - mean;
    float m2 = asf(stv.y) + 64.f * dm * dm;
    m2 += dppf<0xB1>(m2); m2 += dppf<0x4E>(m2); m2 += dppf<0x141>(m2);
    const float l2r = __builtin_amdgcn_rsqf(m2 * (1.f / 512.f) + 1e-5f), l2n = -mean * l2r;
    sfor<0, 8>([&](auto I) {
      constexpr int i = decltype(I)::value;
      v4u& xx = x1v[i];
      asm volatile("s_waitcnt vmcnt(%c1)" : "+v"(xx) : "i"(10 - i) : "memory");
      const float rs = rdl(l2r, 8 * i), nn = rdl(l2n, 8 * i);
      v4u o;
      o.x = pk2((unp(xx.x) * rs + nn) * la0.xy + ls0.xy); o.y = pk2((unp(xx.y) * rs + nn) * la0.zw + ls0.zw);
      o.z = pk2((unp(xx.z) * rs + nn) * la1.xy + ls1.xy); o.w = pk2((unp(xx.w) * rs + nn) * la1.zw + ls1.zw);
      *(LAS v4u*)(L + (h2w ^ (i << 4)) + i * 1024) = o;
    });
  }
  BAR();
  STAMP(16);
  __builtin_amdgcn_s_setprio(2);
  const int kb5 = 5 * w + min(w, 3);
  u64 care5 = 0x0101010101010101ull * ((0x80c06030180c0603ull >> (8 * w)) & 255u);
  asm volatile("" : "+s"(care5));
  v4u b5[24];
  const u32 sB5 = WS_B5T + (j * 43 + kb5) * 4096, d5 = (w + 5) >> 3;
  auto load_b5 = [&](auto NC) {
    constexpr int n = decltype(NC)::value, sx = n / 4, t = n % 4;
    ld16s<t * 1024>(b5[n], vl16, rWS, sB5 + (sx == 5 ? (5 - d5) * 4096 : sx * 4096));
  };
  f32x4 acc4[3][4];
#pragma unroll
  for (int ct = 0; ct < 3; ++ct)
#pragma unroll
    for (int m = 0; m < 4; ++m) acc4[ct][m] = z4;
  u32 vH; int pw5;
  {
    v4u a[4] = {AFRAG(0, 0), AFRAG(16384, 0), AFRAG(32768, 0), AFRAG(49152, 0)};
    sfor<0, 16>([&](auto KS) {
      constexpr int ks = decltype(KS)::value;
      KSTAMP(3, ks);
      if constexpr (ks >= 3 && ks < 13) { VMW(9); landed_b4(IC<3 * ks>{}, IC<3 * ks + 3>{}); }
      if constexpr (ks >= 13) { VMW(9); landed_b4(IC<3 * ks>{}, IC<3 * ks + 3>{}); }
      v4u nx[4] = {a[0], a[1], a[2], a[3]};
      sfor<0, 4>([&](auto MC) {
        constexpr int m = decltype(MC)::value;
        if constexpr (ks < 15) nx[m] = AFRAG(16384 * m, ks + 1);
        if constexpr (m < 3) { if constexpr (ks < 12) load_b4(IC<12 + 3 * ks + m>{}); else load_b5(IC<3 * (ks - 12) + m>{}); }
        SCHED();
#pragma unroll
        for (int ct = 0; ct < 3; ++ct) acc4[ct][m] = mfma32(b4[ks * 3 + ct], a[m], acc4[ct][m]);
        if constexpr (ks == 8 && m == 0) { vH = (vl16 & 255) + ((vl16 >> 5) & 8) + (vl16 >> 9) * (172 * 256); PIN(vH); }
        if constexpr (ks == 9 && m == 0) { const int l5 = opq(vl16) >> 4, c5 = l5 & 15, g5 = l5 >> 4;
          pw5 = w * 16384 + c5 * 256 + ((c5 & 4) << 4) + ((g5 ^ (c5 & 3)) << 4); PIN(pw5); }
        SCHED();
      });
#pragma unroll
      for (int m = 0; m < 4; ++m) a[m] = nx[m];
    });
  }
#pragma unroll
  for (int ct = 0; ct < 3; ++ct)
#pragma unroll
    for (int m = 0; m < 4; ++m) PIN(acc4[ct][m]);
  __builtin_amdgcn_s_setprio(0);
  STAMP(17); KSTAMP(3, 16);
#pragma unroll
  for (int ct = 0; ct < 3; ++ct)
#pragma unroll
    for (int mp = 0; mp < 2; ++mp) {
      f32x4 G, U, R;
#pragma unroll
      for (int e = 0; e < 4; ++e) {
        auto r = __builtin_amdgcn_permlane32_swap(asu(acc4[ct][2 * mp][e]), asu(acc4[ct][2 * mp + 1][e]), false, false);
        G[e] = asf(r[0]); U[e] = asf(r[1]);
      }
#pragma unroll
      for (int e = 0; e < 4; ++e) R[e] = qp(qp(G[e] * U[e]) * __builtin_amdgcn_rcpf(1.f + __builtin_amdgcn_exp2f(qp(G[e] * -1.4426950408889634f))));
      const v2u o = pk4(R);
      const u32 so = WS_HH + ((g * 4 + 2 * mp) * 172 + ch0 + ct) * 256;
      asm volatile("s_mov_b32 exec_lo, %4\n\ts_mov_b32 exec_hi, %4\n\tbuffer_store_dwordx2 %0, %1, %2, %3 offen sc1\n\ts_mov_b64 exec, -1"
                   ::"v"(o), "v"(vH), "s"(rWS), "s"(so), "s"(hmask[ct]) : "memory");
    }
  STAMP(18);
  sfor<12, 16>(load_b5);
  VMW(4);
  BAR();
  RAISE(flag_off(2, g, 0) + j * 128);
  STAMP(19);
  sfor<16, 22>(load_b5);
  poll_flags(rWS, vfl, flag_off(2, g, 0), care5, tag);
#pragma unroll
  for (int i = 0; i < 22; ++i) PIN(b5[i]);
  v4u hf[16];
  auto load_h = [&](auto NC) {
    constexpr int n = decltype(NC)::value, sx = n / 4, m = n % 4;
    ld16s<0>(hf[n & 15], vl16, rWS, WS_HH + ((g * 4 + m) * 172 + 4 * kb5) * 256 + (sx == 5 ? (5 - d5) * 1024 : sx * 1024));
  };
  sfor<0, 16>(load_h);
  sfor<22, 24>(load_b5);
  STAMP(20);
  {
    f32x4 acc[4][4];
#pragma unroll
    for (int m = 0; m < 4; ++m)
#pragma unroll
      for (int n = 0; n < 4; ++n) acc[m][n] = z4;
    sfor<0, 6>([&](auto SC) {
      constexpr int sx = decltype(SC)::value, r = (sx & 3) * 4;
      constexpr int nw = sx == 0 ? 14 : sx == 1 ? 14 : sx == 2 ? 14 : sx == 3 ? 10 : sx == 4 ? 4 : 0;
      asm volatile("s_waitcnt vmcnt(%c4)" : "+v"(hf[r]), "+v"(hf[r + 1]), "+v"(hf[r + 2]), "+v"(hf[r + 3]) : "i"(nw) : "memory");
      if constexpr (sx == 4) { PIN(b5[22]); PIN(b5[23]); }
      if constexpr (sx == 5) { if (w >= 3) { hf[r] = v4u{0u, 0u, 0u, 0u}; hf[r + 1] = v4u{0u, 0u, 0u, 0u}; hf[r + 2] = v4u{0u, 0u, 0u, 0u}; hf[r + 3] = v4u{0u, 0u, 0u, 0u}; } }
      sfor<0, 4>([&](auto MC) {
        constexpr int m = decltype(MC)::value;
#pragma unroll
        for (int n = 0; n < 4; ++n) acc[m][n] = mfma32(b5[sx * 4 + n], hf[r + m], acc[m][n]);
        SCHED();
        if constexpr (sx < 2) load_h(IC<16 + 4 * sx + m>{});
        SCHED();
      });
    });
#pragma unroll
    for (int m = 0; m < 4; ++m)
#pragma unroll
      for (int n = 0; n < 4; ++n) *(LAS f32x4*)(L + ((n & 1) ? (pw5 ^ 64) : pw5) + (m * 4096 + (n >> 1) * 128)) = acc[m][n];
  }
  BAR();
  STAMP(21);
  {
    const int l6 = opq(vl16) >> 4;
    const int p0 = opq((8 * w + (l6 >> 3)) * 256 + (((2 * (l6 & 7)) ^ ((l6 >> 3) & 7)) << 4)), p1 = opq(p0 + 65536);
    f32x4 y0 = *(LAS f32x4*)(L + p0), y1 = *(LAS f32x4*)(L + (p0 ^ 16));
#pragma unroll
    for (int q = 1; q < 8; ++q) { const int pb = q < 4 ? p0 : p1; y0 += *(LAS f32x4*)(L + pb + (q & 3) * 16384); y1 += *(LAS f32x4*)(L + (pb ^ 16) + (q & 3) * 16384); }
    const int gb = L_GAB + 256 + (l6 & 7) * 32;
    const f32x4 g0 = *(LAS f32x4*)(L + gb), g1 = *(LAS f32x4*)(L + gb + 16);
    const f32x4 o0 = f32x4{blo(x1p.x), bhi(x1p.x), blo(x1p.y), bhi(x1p.y)} + g0 * y0, o1 = f32x4{blo(x1p.z), bhi(x1p.z), blo(x1p.w), bhi(x1p.w)} + g1 * y1;
    const v4u o = cat(pk4(o0), pk4(o1));
    const u32 vo = (8 * w + (l6 >> 3)) * 1024 + (l6 & 7) * 16, so = g * 65536 + j * 128;
    asm volatile("buffer_store_dwordx4 %0, %1, %2, %3 offen sc1\n\ts_nop 1" ::"v"(o), "v"(vo), "s"(rOUT), "s"(so) : "memory");
  }
  STAMP(22);
  STAMP_DUMP();
}
}

extern "C" void kernel_launch(void* const* d_in, const int* in_sizes, int n_in, void* d_out, int out_size, void* d_ws, size_t ws_size,
                              hipStream_t stream) {
  Params p;
  p.x = (const u16*)d_in[0]; p.wqkv = (const u16*)d_in[1]; p.wo = (const u16*)d_in[2];
  p.w1 = (const u16*)d_in[4]; p.w3 = (const u16*)d_in[5]; p.w2 = (const u16*)d_in[6];
  p.c = (const u16*)d_in[7]; p.wmod = (const u16*)d_in[8];
  p.out = (u16*)d_out; p.ws = (char*)d_ws;
  (void)hipFuncSetAttribute((const void*)fused_block, hipFuncAttributeMaxDynamicSharedMemorySize, L_TOTAL);
  fused_block<<<256, 512, L_TOTAL, stream>>>(p);
}
```

```cpp
#define SEG_A 9
#define SEG_B 10
#define WSEL 4
#include <hip/hip_runtime.h>
#include <stdint.h>

#define KDBG 0
#define NODMA 0
#define CODEWARM 0
#define L2WARM 1
#define EARLYT 0
#define XMAP 2

#define DI __device__ __forceinline__
#define GAS __attribute__((address_space(1)))
#define LAS __attribute__((address_space(3)))
typedef unsigned u32;
typedef unsigned short u16;
typedef unsigned long long u64;
typedef u32 v4u __attribute__((ext_vector_type(4)));
typedef u32 v2u __attribute__((ext_vector_type(2)));
typedef float f32x4 __attribute__((ext_vector_type(4)));
typedef float f32x2 __attribute__((ext_vector_type(2)));
typedef __bf16 bf16x8 __attribute__((ext_vector_type(8)));
typedef __bf16 bf16x2 __attribute__((ext_vector_type(2)));
typedef short s16x4 __attribute__((ext_vector_type(4)));
typedef __amdgpu_buffer_rsrc_t rsrc_t;
typedef GAS u32 gu32;
typedef GAS u64 gu64;
#define RLX_AGENT __ATOMIC_RELAXED, __HIP_MEMORY_SCOPE_AGENT

namespace {
constexpr int S_ = 2048, D_ = 512, F_ = 1376;
constexpr u32 WS_EPOCH = 0;
constexpr u32 WS_FLAG = 32768;
constexpr u32 WS_STAT = 262144;
constexpr u32 WS_MODG = 524288;
constexpr u32 WS_MODF = 655360;
constexpr u32 WS_O = 1u << 20;
constexpr u32 WS_X1 = 3u << 20;
constexpr u32 WS_HH = 5u << 20;
constexpr u32 WS_RFLAG = 8192;
constexpr u32 WS_B4T = 11u << 20;
constexpr u32 WS_B5T = 14u << 20;
constexpr u32 WS_B3T = (15u << 20) + (512u << 10);
constexpr u32 WS_DBG = 16u << 20;
constexpr u32 WS_BYTES = 32u << 20;
constexpr int L_XY = 0;
constexpr int L_Z = 81920;
constexpr int L_MISC = 158720;
constexpr int L_TOTAL = 163840;
constexpr int L_RED = 73728;
constexpr int L_MODA = L_MISC;
constexpr int L_CNT = L_MISC + 4096;
constexpr int L_GAB = L_MISC + 4096 + 64;
constexpr int L_JUNK = L_MISC + 4096 + 64 + 512;
constexpr int L_STAMP = 156672;
constexpr u32 SPIN_CAP = 1u << 18;
constexpr u32 CODE_BYTES = 26112;

DI f32x4 mfma32(v4u a, v4u b, f32x4 c) {
  return __builtin_amdgcn_mfma_f32_16x16x32_bf16(__builtin_bit_cast(bf16x8, a), __builtin_bit_cast(bf16x8, b), c, 0, 0, 0);
}
DI f32x4 mfma16(v2u a, v2u b, f32x4 c) {
  return __builtin_amdgcn_mfma_f32_16x16x16bf16_1k(__builtin_bit_cast(s16x4, a), __builtin_bit_cast(s16x4, b), c, 0, 0, 0);
}
DI u32 pk(float lo, float hi) { f32x2 v = {lo, hi}; return __builtin_bit_cast(u32, __builtin_convertvector(v, bf16x2)); }
DI u32 pk2(f32x2 v) { return __builtin_bit_cast(u32, __builtin_convertvector(v, bf16x2)); }
DI v2u pk4(f32x4 v) { v2u r; r.x = pk(v.x, v.y); r.y = pk(v.z, v.w); return r; }
DI float asf(u32 u) { return __builtin_bit_cast(float, u); }
DI u32 asu(float f) { return __builtin_bit_cast(u32, f); }
DI float blo(u32 u) { return asf(u << 16); }
DI float bhi(u32 u) { return asf(u & 0xffff0000u); }
DI f32x2 unp(u32 u) { f32x2 r = {blo(u), bhi(u)}; return r; }
DI float dot2(u32 a, u32 b, float c) { return __builtin_amdgcn_fdot2_f32_bf16(__builtin_bit_cast(bf16x2, a), __builtin_bit_cast(bf16x2, b), c, false); }
DI v2u trd(LAS char* p) { return __builtin_bit_cast(v2u, __builtin_amdgcn_ds_read_tr16_b64_v4i16((LAS s16x4*)p)); }
DI v4u cat(v2u a, v2u b) { v4u r; r.x = a.x; r.y = a.y; r.z = b.x; r.w = b.y; return r; }
template <int MI, int SI> DI void dma(u32 mb, u32 voff, rsrc_t srd, u32 sb) {
  u32 t;
  asm volatile("s_add_u32 m0, %1, %c2\n\ts_add_u32 %0, %3, %c4\n\tbuffer_load_dwordx4 %5, %6, %0 offen lds"
               : "=&s"(t) : "s"(mb), "i"(MI), "s"(sb), "i"(SI), "v"(voff), "s"(srd) : "scc");
}
DI void touch(u32 voff, rsrc_t srd, u32 sb) {
  asm volatile("s_mov_b32 m0, %c0\n\ts_nop 0\n\tbuffer_load_dword %1, %2, %3 offen lds" ::"i"(L_JUNK), "v"(voff), "s"(srd), "s"(sb));
}
#define BAR() asm volatile("s_waitcnt lgkmcnt(0)\n\ts_barrier" ::: "memory")
#define VMW(n) asm volatile("s_waitcnt vmcnt(" #n ")" ::: "memory")
#define LGW() asm volatile("s_waitcnt lgkmcnt(0)" ::: "memory")
#define SCHED() __builtin_amdgcn_sched_barrier(0)
#define LD16(dst, voff, srd, soff, IMM) asm volatile("buffer_load_dwordx4 %0, %1, %2, %3 offen offset:" #IMM : "=v"(dst) : "v"(voff), "s"(srd), "s"(soff))
#define LD16S(dst, voff, srd, soff, IMM) asm volatile("buffer_load_dwordx4 %0, %1, %2, %3 offen offset:" #IMM " sc1" : "=v"(dst) : "v"(voff), "s"(srd), "s"(soff))
#define LD8(dst, voff, srd, soff, IMM) asm volatile("buffer_load_dwordx2 %0, %1, %2, %3 offen offset:" #IMM : "=v"(dst) : "v"(voff), "s"(srd), "s"(soff))
#define LD8S(dst, voff, srd, soff, IMM) asm volatile("buffer_load_dwordx2 %0, %1, %2, %3 offen offset:" #IMM " sc1" : "=v"(dst) : "v"(voff), "s"(srd), "s"(soff))
#define LD4(dst, voff, srd, soff) asm volatile("buffer_load_dword %0, %1, %2, %3 offen" : "=v"(dst) : "v"(voff), "s"(srd), "s"(soff))
#define LD4S(dst, voff, srd, soff) asm volatile("buffer_load_dword %0, %1, %2, %3 offen sc1" : "=v"(dst) : "v"(voff), "s"(srd), "s"(soff))

template <int IMM> DI void ld16s(v4u& r, u32 v, rsrc_t rs, u32 so) { asm volatile("buffer_load_dwordx4 %0, %1, %2, %3 offen offset:%c4 sc1" : "=v"(r) : "v"(v), "s"(rs), "s"(so), "i"(IMM)); }
template <int CTRL> DI float dppf(float v) { return asf(__builtin_amdgcn_update_dpp(0, asu(v), CTRL, 0xf, 0xf, true)); }
DI float row16_sum(float v) { v += dppf<0x128>(v); v += dppf<0x124>(v); v += dppf<0x122>(v); v += dppf<0x121>(v); return v; }
DI float sw32_add(float a, float b) { auto r = __builtin_amdgcn_permlane32_swap(asu(a), asu(b), false, false); return asf(r[0]) + asf(r[1]); }
DI float sw16_add(float a, float b) { auto r = __builtin_amdgcn_permlane16_swap(asu(a), asu(b), false, false); return asf(r[0]) + asf(r[1]); }
DI float x16_max(float v) { auto r = __builtin_amdgcn_permlane16_swap(asu(v), asu(v), false, false); return fmaxf(asf(r[0]), asf(r[1])); }
DI float x32_max(float v) { auto r = __builtin_amdgcn_permlane32_swap(asu(v), asu(v), false, false); return fmaxf(asf(r[0]), asf(r[1])); }
DI int fresh_lane() { int l; asm volatile("v_mbcnt_lo_u32_b32 %0, -1, 0\n\tv_mbcnt_hi_u32_b32 %0, -1, %0" : "=v"(l)); return l; }
DI float rdl(float v, int l) { return asf(__builtin_amdgcn_readlane(asu(v), l)); }

struct Params {
  const u16 *x, *wqkv, *wo, *w1, *w3, *w2, *c, *wmod;
  u16* out;
  char* ws;
};

#define LOOPSEL 0
#define STAMP(i) do { if constexpr ((i) == SEG_A) asm volatile("s_memrealtime %0\n\ts_waitcnt lgkmcnt(0)" : "=s"(tA_)::"memory"); \
    if constexpr ((i) == SEG_B) asm volatile("s_memrealtime %0\n\ts_waitcnt lgkmcnt(0)" : "=s"(tB_)::"memory"); } while (0)
#define KSTAMP(lp, i) do {} while (0)
#define STAMP_DUMP() do {} while (0)

DI u32 flag_off(int phase, int g, int h) { return WS_FLAG + ((phase * 32 + g) * 2 + h) * 1024; }
DI void poll_flags(rsrc_t rWS, u32 vfl, u32 foff, u64 care, u32 tag) {
  for (u32 sp = 0; sp < SPIN_CAP; ++sp) {
    asm volatile("" ::: "memory");
    const u32 v = __builtin_amdgcn_raw_buffer_load_b32(rWS, vfl, foff, 16);
    if (!(__builtin_amdgcn_ballot_w64(v != tag) & care)) break;
  }
  VMW(0);
}
DI void raise_flag(rsrc_t rWS, u32 foff, u32 vtag, u32 vzero, u32 m1) {
  asm volatile("s_mov_b32 exec_hi, 0\n\ts_mov_b32 exec_lo, %4\n\tbuffer_store_dword %0, %1, %2, %3 offen sc1\n\ts_mov_b64 exec, -1" ::"v"(vtag), "v"(vzero), "s"(rWS), "s"(foff), "s"(m1) : "memory");
}
DI void signal_flag(rsrc_t rWS, u32 cnt_addr, u32 foff, u32 tag) {
  u32 t, a = cnt_addr, one = 1u, vt = tag, vz = 0u;
  asm volatile("s_mov_b64 exec, 1\n\tds_add_rtn_u32 %0, %1, %2\n\ts_waitcnt lgkmcnt(0)\n\tv_cmp_eq_u32 vcc, 7, %0\n\ts_and_b64 exec, exec, vcc\n\t"
               "buffer_store_dword %3, %4, %5, %6 offen sc1\n\ts_mov_b64 exec, -1"
               : "=&v"(t) : "v"(a), "v"(one), "v"(vt), "v"(vz), "s"(rWS), "s"(foff) : "vcc", "scc", "memory");
}
DI void arrive(u32 cnt_addr) {
  u32 a = cnt_addr, one = 1u;
  asm volatile("s_waitcnt lgkmcnt(0)\n\ts_mov_b64 exec, 1\n\tds_add_u32 %0, %1\n\ts_mov_b64 exec, -1" ::"v"(a), "v"(one) : "memory");
}
DI void await8(u32 cnt_addr) {
  u32 a = cnt_addr, t, sv;
  asm volatile("1:\n\tds_read_b32 %0, %2\n\ts_waitcnt lgkmcnt(0)\n\tv_readfirstlane_b32 %1, %0\n\ts_nop 0\n\ts_cmp_lt_u32 %1, 8\n\ts_cbranch_scc1 1b"
               : "=&v"(t), "=&s"(sv) : "v"(a) : "scc", "memory");
}
DI void raise_when8(rsrc_t rWS, u32 cnt_addr, u32 foff, u32 tag, u32 w7) {
  u32 a = cnt_addr, t, sv, vt = tag, vz = 0u;
  asm volatile("s_cmp_eq_u32 %7, 0\n\ts_cbranch_scc1 2f\n\t1:\n\tds_read_b32 %0, %2\n\ts_waitcnt lgkmcnt(0)\n\tv_readfirstlane_b32 %1, %0\n\ts_nop 0\n\ts_cmp_lt_u32 %1, 8\n\ts_cbranch_scc1 1b\n\t"
               "s_mov_b64 exec, 1\n\tbuffer_store_dword %3, %4, %5, %6 offen sc1\n\ts_mov_b64 exec, -1\n\t2:"
               : "=&v"(t), "=&s"(sv) : "v"(a), "v"(vt), "v"(vz), "s"(rWS), "s"(foff), "s"(w7) : "scc", "memory");
}
#define PIN(x) asm volatile("" : "+v"(x))
DI float qp(float r) { PIN(r); return r; }
DI float q_shl(u32 a) { return qp(blo(a)); }
DI float q_hi(u32 a) { return qp(bhi(a)); }
DI float q_fma(float a, float b, float c) { return qp(__builtin_fmaf(a, b, c)); }
DI float q_fmas(float a, float b, float c) { return qp(__builtin_fmaf(a, b, c)); }
DI float q_mul(float a, float b) { return qp(a * b); }
DI float q_mulk(float a) { return qp(a * -1.4426950408889634f); }
DI float q_add1(float a) { return qp(a + 1.f); }
DI float q_exp(float a) { return qp(__builtin_amdgcn_exp2f(a)); }
DI float q_rcp(float a) { return qp(__builtin_amdgcn_rcpf(a)); }
DI u32 q_cvt(float a, float b) { u32 r = pk(a, b); PIN(r); return r; }
DI float q_mov(float a) { return qp(a); }
DI u32 opq(u32 v) { asm volatile("" : "+v"(v)); return v; }

DI float sel(u64 m, float a, float b) { return __builtin_amdgcn_inverse_ballot_w64(m) ? a : b; }
DI v2u attend(v2u kp, v2u vp, v2u kc, v2u vc, v2u q, u64 m0, u64 m1, u32 hm, bool noprev) {
  const f32x4 z = {0.f, 0.f, 0.f, 0.f};
  const f32x4 sp = mfma16(kp, q, z);
  const f32x4 sc = mfma16(kc, q, z);
  const float K2 = 0.25f * 1.4426950408889634f;
  const u64 par = 0xaaaaaaaaaaaaaaaaull;
  float p0 = sel(par, sp[1], sp[0]), p1 = sel(par, sp[3], sp[2]);
  if (noprev) { p0 = -INFINITY; p1 = -INFINITY; }
  const float t0 = sel(m0, sel(par, sc[1], sc[0]), p0), t1 = sel(m1, sel(par, sc[3], sc[2]), p1);
  const float m = x32_max(x16_max(fmaxf(t0, t1)));
  const float mk = -m * K2;
  const float e0 = __builtin_amdgcn_exp2f(__builtin_fmaf(t0, K2, mk)), e1 = __builtin_amdgcn_exp2f(__builtin_fmaf(t1, K2, mk));
  const u32 w0 = pk(e0, e0) & hm, w1 = pk(e1, e1) & hm;
  v4u pf;
  pf.x = __builtin_amdgcn_inverse_ballot_w64(m0) ? 0u : w0; pf.y = __builtin_amdgcn_inverse_ballot_w64(m1) ? 0u : w1;
  pf.z = __builtin_amdgcn_inverse_ballot_w64(m0) ? w0 : 0u; pf.w = __builtin_amdgcn_inverse_ballot_w64(m1) ? w1 : 0u;
  const f32x4 o = mfma32(cat(vp, vc), pf, z);
  float sum = e0 + e1;
  sum = sw16_add(sum, sum); sum = sw32_add(sum, sum);
  const float inv = __builtin_amdgcn_rcpf(sum);
  return pk4(f32x4{qp(o.x * inv), qp(o.y * inv), qp(o.z * inv), qp(o.w * inv)});
}

template <int N> using IC = std::integral_constant<int, N>;
template <int I, int N, class F> DI void sfor(F&& f) { if constexpr (I < N) { f(IC<I>{}); sfor<I + 1, N>(f); } }

__global__ void __launch_bounds__(512, 2) fused_block(Params p) {
  extern __shared__ __attribute__((aligned(16))) char smem[];
  LAS char* L = (LAS char*)smem;
  const int tid = threadIdx.x, lane = tid & 63, w = __builtin_amdgcn_readfirstlane(tid >> 6);
  const int c16 = lane & 15, g4 = lane >> 4;
  const int b = blockIdx.x;
  int g, j;
  if (XMAP == 0) { g = (b & 7) * 4 + ((b >> 3) & 3); j = b >> 5; }
  else if (XMAP == 2) { const int x = b & 7, r = b >> 3; g = (x >> 1) * 8 + (r & 7); j = (x & 1) * 4 + (r >> 3); }
  else { const int x = b & 7, r = b >> 3; g = (x >> 2) * 16 + (r & 15); j = (x & 3) * 2 + (r >> 4); }
  const rsrc_t rX = __builtin_amdgcn_make_buffer_rsrc((void*)p.x, 0, S_ * D_ * 2, 0x00020000);
  const rsrc_t rQKV = __builtin_amdgcn_make_buffer_rsrc((void*)p.wqkv, 0, D_ * 3 * D_ * 2, 0x00020000);
  const rsrc_t rWO = __builtin_amdgcn_make_buffer_rsrc((void*)p.wo, 0, D_ * D_ * 2, 0x00020000);
  const rsrc_t rW1 = __builtin_amdgcn_make_buffer_rsrc((void*)p.w1, 0, D_ * F_ * 2, 0x00020000);
  const rsrc_t rW3 = __builtin_amdgcn_make_buffer_rsrc((void*)p.w3, 0, D_ * F_ * 2, 0x00020000);
  const rsrc_t rW2 = __builtin_amdgcn_make_buffer_rsrc((void*)p.w2, 0, F_ * D_ * 2, 0x00020000);
  const rsrc_t rC = __builtin_amdgcn_make_buffer_rsrc((void*)p.c, 0, D_ * 2, 0x00020000);
  const rsrc_t rMOD = __builtin_amdgcn_make_buffer_rsrc((void*)p.wmod, 0, D_ * 6 * D_ * 2, 0x00020000);
  const rsrc_t rWS = __builtin_amdgcn_make_buffer_rsrc((void*)p.ws, 0, WS_BYTES, 0x00020000);
  const rsrc_t rOUT = __builtin_amdgcn_make_buffer_rsrc((void*)p.out, 0, S_ * D_ * 2, 0x00020000);
  u64 tA_ = 0, tB_ = 0;
  STAMP(0);

  u32 ep;
  { const u32 vz = 0; asm volatile("buffer_load_dword %0, %1, %2, 0 offen sc1" : "=v"(ep) : "v"(vz), "s"(rWS)); }
  u32 vl16 = lane * 16; PIN(vl16);
  const int bm = b < 192 ? b : b - 192, mq = bm / 48, cl = bm - 48 * mq;
  v4u wm[4]; v2u cv; u32 ep0;
  if (w < 4) {
    const u32 vm = (tid >> 3) * 24576 + (tid & 7) * 16, s0 = mq * (128 * 6144) + cl * 128, s1 = s0 + 6144, s2 = s0 + 12288, s3 = s0 + 18432;
    LD16(wm[0], vm, rMOD, s0, 0); LD16(wm[1], vm, rMOD, s1, 0); LD16(wm[2], vm, rMOD, s2, 0); LD16(wm[3], vm, rMOD, s3, 0);
    const u32 vc = (tid >> 3) * 8, scq = mq * 256;
    LD8(cv, vc, rC, scq, 0);
    const u32 vz = 0; asm volatile("buffer_load_dword %0, %1, %2, 0 offen sc1" : "=v"(ep0) : "v"(vz), "s"(rWS));
  }
  if (tid < 16) *(LAS u32*)(L + L_CNT + tid * 4) = 0u;
  BAR();
  const int st_qd = lane >> 2, st_r = (st_qd & 3) + 4 * (st_qd >> 3);
  const int st_col = ((st_qd >> 2) & 1) * 64 + (((lane & 3) ^ ((w & 1) << 1)) << 4);
  const int rotp = g & 7;
  u32 vB1 = st_r * 3072 + st_col; PIN(vB1);
  const u32 mB1 = (w >> 2) * 12288 + (w & 3) * 1024, sB1 = (w >> 2) * 98304 + (w & 3) * 24576 + j * 128;
  auto stage_b1 = [&](auto PC, auto BC) {
    constexpr int pair = decltype(PC)::value, base = decltype(BC)::value;
    const u32 sp = ((pair + rotp) & 7) * 196608 + sB1;
    dma<base, 0>(mB1, vB1, rQKV, sp); dma<base + 4096, 1024>(mB1, vB1, rQKV, sp); dma<base + 8192, 2048>(mB1, vB1, rQKV, sp);
  };
  auto b1_piece = [&](auto PC, auto BC, auto TC) {
    constexpr int pair = decltype(PC)::value, base = decltype(BC)::value, ty = decltype(TC)::value;
    dma<base + ty * 4096, ty * 1024>(mB1, vB1, rQKV, ((pair + rotp) & 7) * 196608 + sB1);
  };
  const int hd = w & 3, blk2 = w >> 2, kq = w & 3, mh = w >> 2;
  const int cs0 = j < 4 ? 22 * j : 88 + 21 * (j - 4);
  const int cend = cs0 + (j < 4 ? 22 : 21);
  v4u xr[10];
  const int rx2 = (0x4310 >> (4 * (j & 3))) & 7;
  auto load_x = [&](int i0, int i1) {
    const int r0 = 64 * g - 16 + w;
#pragma unroll
    for (int i = i0; i < i1; ++i) {
      int bp = (i >> 1) + rx2; bp = bp >= 5 ? bp - 5 : bp;
      const u32 so = (u32)max(r0 + 16 * bp + 8 * (i & 1), 0) * 1024u;
      LD16(xr[i], vl16, rX, so, 0);
    }
  };
  load_x(0, 5);
  STAMP(1);
  const int T0 = 12 * (g >> 4) + 3 * (w & 3), rcb = min(cs0 + T0, 168);
  if (w < 4) {
    asm volatile("s_waitcnt vmcnt(5)" : "+v"(cv), "+v"(wm[0]), "+v"(wm[1]), "+v"(wm[2]), "+v"(wm[3]), "+v"(ep0)::"memory");
    const u32 tg = __builtin_amdgcn_readfirstlane(ep0) + 1u;
    const f32x4 cf = {blo(cv.x), bhi(cv.x), blo(cv.y), bhi(cv.y)};
    float sc[4];
#pragma unroll
    for (int r = 0; r < 4; ++r) sc[r] = cf[r] * __builtin_amdgcn_rcpf(1.f + __builtin_amdgcn_exp2f(cf[r] * -1.4426950408889634f));
    f32x2 v[4];
#pragma unroll
    for (int i = 0; i < 4; ++i) v[i] = (unp(wm[0][i]) * sc[0] + unp(wm[1][i]) * sc[1]) + (unp(wm[2][i]) * sc[2] + unp(wm[3][i]) * sc[3]);
    const float a0 = sw32_add(v[0].x, v[2].x), a1 = sw32_add(v[0].y, v[2].y), a2 = sw32_add(v[1].x, v[3].x), a3 = sw32_add(v[1].y, v[3].y);
    float t0 = sw16_add(a0, a2), t1 = sw16_add(a1, a3);
    t0 += dppf<0x128>(t0); t1 += dppf<0x128>(t1);
    f32x2 o = {t0, t1};
    *(LAS f32x2*)(L + L_RED + w * 256 + (lane & 7) * 32 + g4 * 8) = o;
    LGW();
    u32 old = 0;
    if (lane == 0) old = __hip_atomic_fetch_add((LAS u32*)(L + L_CNT + 28), 1u, __ATOMIC_RELAXED, __HIP_MEMORY_SCOPE_WORKGROUP);
    if (__builtin_amdgcn_readfirstlane(old) == 3u) {
      asm volatile("" ::: "memory");
      float s = 0.f;
#pragma unroll
      for (int i = 0; i < 4; ++i) s += *(LAS float*)(L + L_RED + i * 256 + lane * 4);
      v2u gr; gr.x = asu(s); gr.y = tg;
      if (b < 192) __builtin_amdgcn_raw_buffer_store_b64(gr, rWS, lane * 8, WS_MODG + (mq * 3072 + cl * 64) * 8, 16);
    }
  }
  STAMP(2);
  load_x(5, 10);
  stage_b1(IC<0>{}, IC<L_XY>{});
  int afo[4];
#pragma unroll
  for (int i = 0; i < 4; ++i) { afo[i] = blk2 * 32768 + c16 * 1024 + (((4 * i + g4) ^ c16) << 4); PIN(afo[i]); }
  const u64 am0 = __builtin_amdgcn_ballot_w64((c16 & 14) >= 4 * g4), am1 = __builtin_amdgcn_ballot_w64((c16 & 14) >= 4 * g4 + 2);
  u32 ahm = (lane & 1) ? 0xffff0000u : 0xffffu; PIN(ahm);
  u32 vOst = c16 * 16 + g4 * 256 + (hd & 1) * 8; PIN(vOst);
  int tB1 = g4 * 1024 + 64 * (c16 >> 2) + 8 * (lane & 3) + 32 * ((hd & 1) ^ (g4 & 1)) + 256 * (hd >> 1); PIN(tB1);
  int hw0 = w * 1024 + ((((lane - 8 * rotp) & 63) ^ w) << 4); PIN(hw0);
#define vfl ((opq(vl16) & 112u) << 3)
  STAMP(3);
  v4u bw[48];
  auto read_b1 = [&](int pair, int base, int off) {
#pragma unroll
    for (int kk = 0; kk < 2; ++kk)
#pragma unroll
      for (int ty = 0; ty < 3; ++ty) {
        LAS char* a = L + base + (off + kk * 12288 + ty * 4096);
        bw[(2 * pair + kk) * 3 + ty] = cat(trd(a), trd(a + 512));
      }
  };
  f32x2 xn[10][4];
  v4u ga_[4]; v2u pv;
  const u32 sMG = WS_MODG + w * 1024;
  {
    float u[10];
    sfor<0, 10>([&](auto I) {
      constexpr int i = decltype(I)::value;
      if constexpr (i == 0) asm volatile("s_waitcnt vmcnt(11)" : "+v"(xr[0]), "+v"(xr[1]), "+v"(ep)::"memory");
      if constexpr (i == 2) asm volatile("s_waitcnt vmcnt(9)" : "+v"(xr[2]), "+v"(xr[3])::"memory");
      if constexpr (i == 4) asm volatile("s_waitcnt vmcnt(10)" : "+v"(xr[4]), "+v"(xr[5])::"memory");
      if constexpr (i == 6) asm volatile("s_waitcnt vmcnt(8)" : "+v"(xr[6]), "+v"(xr[7])::"memory");
      if constexpr (i == 8) asm volatile("s_waitcnt vmcnt(9)" : "+v"(xr[8]), "+v"(xr[9])::"memory");
      float s = 0.f, q = 0.f;
#pragma unroll
      for (int e = 0; e < 4; ++e) { s = dot2(xr[i][e], 0x3f803f80u, s); q = dot2(xr[i][e], xr[i][e], q); }
      u[i] = sw32_add(s, q);
      PIN(u[i]);
      if constexpr (i == 3) stage_b1(IC<1>{}, IC<L_XY + 24576>{});
      if constexpr (i == 7) stage_b1(IC<2>{}, IC<L_XY + 49152>{});
      if constexpr (i == 9) {
        const u32 s0 = sMG, s1 = sMG + 24576, s2 = sMG + 49152, s3 = sMG + 73728;
        ld16s<0>(ga_[0], vl16, rWS, s0); ld16s<0>(ga_[1], vl16, rWS, s1); ld16s<0>(ga_[2], vl16, rWS, s2); ld16s<0>(ga_[3], vl16, rWS, s3);
      }
    });
    float t[5];
#pragma unroll
    for (int i = 0; i < 5; ++i) t[i] = row16_sum(sw16_add(u[i], u[i + 5]));
    float v = t[0];
#pragma unroll
    for (int i = 1; i < 5; ++i) v = c16 == i ? t[i] : v;
    auto r = __builtin_amdgcn_permlane32_swap(asu(v), asu(v), false, false);
    const float mean = asf(r[0]) * (1.f / 512.f);
    const float var = fmaxf(asf(r[1]) * (1.f / 512.f) - mean * mean, 0.f);
    float rs = __builtin_amdgcn_rsqf(var + 1e-5f), nm = -mean * rs;
    PIN(rs); PIN(nm);
    STAMP(4);
    { const u32 vo = ((lane >> 3) & 3) * 24576 + (lane & 7) * 8, so = WS_MODG + (1024 + 8 * b) * 8; LD8S(pv, vo, rWS, so, 0); }
    if (w >= 4) {
      const int u = lane >> 2, row = (u & 3) | ((u >> 1) & 4) | ((u << 1) & 8);
      const u32 vo = row * 2752 + (lane & 3) * 16, so = (g & 15) * (32 * 2752) + rcb * 16, mb = L_Z + 49152 + (w & 3) * 4096;
      dma<0, 0>(mb, vo, rW1, so); dma<1024, 16 * 2752>(mb, vo, rW1, so); dma<2048, 0>(mb, vo, rW3, so); dma<3072, 16 * 2752>(mb, vo, rW3, so);
    } else {
      const int u = lane >> 1, row = (u & 3) | ((u >> 1) & 4) | ((u << 1) & 8) | (u & 16);
      const u32 vo = row * 1024 + (lane & 1) * 16, so = g * 32768 + j * 128 + w * 32, mb = L_Z + 65536 + w * 2048;
      dma<0, 0>(mb, vo, rW2, so); dma<1024, 0>(mb, vo, rW2, so + (g < 11 ? 32 * 32768 : 0));
      const int rwo = (u & 7) | ((u >> 1) & 8) | ((u << 1) & 16);
      dma<0, 0>(76800 + w * 1024, rwo * 1024 + (lane & 1) * 16, rWO, (g & 15) * 32768 + j * 128 + w * 32);
    }
    sfor<0, 10>([&](auto I) {
      constexpr int i = decltype(I)::value;
      if constexpr (i == 2) stage_b1(IC<3>{}, IC<L_Z>{});
      if constexpr (i == 6) stage_b1(IC<4>{}, IC<L_Z + 24576>{});
      const float a = rdl(rs, (i / 5) * 16 + i % 5), c = rdl(nm, (i / 5) * 16 + i % 5);
#pragma unroll
      for (int e = 0; e < 4; ++e) { xn[i][e] = unp(xr[i][e]) * a + c; PIN(xn[i][e]); }
    });
  }
  const u32 tag = __builtin_amdgcn_readfirstlane(ep) + 1u;
  u32 m1; asm("s_lshr_b32 %0, 1, %1" : "=s"(m1) : "s"(w) : "scc");
#define RAISE(fo) raise_flag(rWS, (fo), tag, vl16, m1)
#define SIGNAL(idx, fo) signal_flag(rWS, L_CNT + 4 * (idx), (fo), tag)
  STAMP(5);
  asm volatile("s_waitcnt vmcnt(9)" : "+v"(ga_[0]), "+v"(ga_[1]), "+v"(ga_[2]), "+v"(ga_[3]), "+v"(pv)::"memory");
  auto gsum = [&](v4u (&a)[4], f32x2& o) -> u32 {
    o.x = (asf(a[0].x) + asf(a[1].x)) + (asf(a[2].x) + asf(a[3].x)); o.y = (asf(a[0].z) + asf(a[1].z)) + (asf(a[2].z) + asf(a[3].z));
    return ((a[0].y ^ tag) | (a[0].w ^ tag) | (a[1].y ^ tag) | (a[1].w ^ tag)) | ((a[2].y ^ tag) | (a[2].w ^ tag) | (a[3].y ^ tag) | (a[3].w ^ tag));
  };
  auto gather = [&](u32 voff, u32 soff, f32x2& o) {
    for (u32 sp = 0; sp < SPIN_CAP; ++sp) {
      asm volatile("" ::: "memory");
      v4u a[4];
#pragma unroll
      for (int q = 0; q < 4; ++q) a[q] = __builtin_amdgcn_raw_buffer_load_b128(rWS, voff, soff + q * 24576, 16);
      if (!__builtin_amdgcn_ballot_w64(gsum(a, o) != 0u)) break;
    }
  };
  const float one = w >= 4 ? 1.f : 0.f;
  {
    f32x2 mA;
    if (__builtin_amdgcn_ballot_w64(gsum(ga_, mA) != 0u)) gather(vl16, sMG, mA);
    *(LAS f32x2*)(L + L_MODA + w * 512 + (vl16 >> 1)) = mA + one;
  }
  BAR();
  if (w == 7) {
    const u32 vo = ((lane >> 3) & 3) * 24576 + (lane & 7) * 8, so = WS_MODG + (1024 + 8 * b) * 8;
    for (u32 sp = 0; sp < SPIN_CAP && __builtin_amdgcn_ballot_w64(pv.y != tag); ++sp) {
      asm volatile("" ::: "memory");
      pv = __builtin_amdgcn_raw_buffer_load_b64(rWS, vo, so, 16);
    }
    float sv = asf(pv.x);
    sv += dppf<0x128>(sv); sv = sw16_add(sv, sv);
    pv.x = asu(sv);
    __builtin_amdgcn_raw_buffer_store_b64(pv, rWS, (lane & 7) * 8, WS_MODF + (1024 + 8 * b) * 8, 16);
  }
  STAMP(6);
  int vma = L_MODA + 2 * vl16; PIN(vma);
  const f32x4 ms0 = *(LAS f32x4*)(L + vma), ms1 = *(LAS f32x4*)(L + vma + 16);
  const f32x4 ma0 = *(LAS f32x4*)(L + vma + 2048), ma1 = *(LAS f32x4*)(L + vma + 2064);
  int tz0 = tB1 + L_Z; PIN(tz0);
  { int t2 = tB1 + 49152; PIN(t2); read_b1(0, tB1, 0); read_b1(1, tB1, 24576); read_b1(2, t2, 0); }
  {
    VMW(6);
    if (w >= 4) {
      LAS char* a0 = L + L_Z + 49152 + (w & 3) * 4096 + ((lane >> 1) & 1) * 2048 + 64 * ((c16 >> 2) + 4 * (g4 & 1) + 16 * (g4 >> 1)) + 8 * (lane & 1);
#pragma unroll
      for (int ct = 0; ct < 3; ++ct) {
        const int c = min(cs0 + T0 + ct - rcb, 3) * 16;
        const v4u f = cat(trd(a0 + c), trd(a0 + c + 512));
        const u32 sf = WS_B4T + ((j * 16 + (g & 15)) * 24 + T0 + ct) * 1024;
        asm volatile("buffer_store_dwordx4 %0, %1, %2, %3 offen sc1\n\ts_nop 1" ::"v"(f), "v"(vl16), "s"(rWS), "s"(sf) : "memory");
      }
    }
    else {
      LAS char* a0 = L + L_Z + 65536 + w * 2048 + 32 * ((c16 >> 2) + 4 * (g4 & 1) + 16 * (g4 >> 1)) + 8 * (lane & 3);
#pragma unroll
      for (int i = 0; i < 2; ++i) {
        const v4u f = cat(trd(a0 + i * 1024), trd(a0 + i * 1024 + 256));
        const u32 sf = WS_B5T + ((j * 43 + g + (i && g < 11 ? 32 : 0)) * 4 + w) * 1024;
        asm volatile("buffer_store_dwordx4 %0, %1, %2, %3 offen sc1\n\ts_nop 1" ::"v"(f), "v"(vl16), "s"(rWS), "s"(sf) : "memory");
      }
      LAS char* a1 = L + 76800 + w * 1024 + 32 * ((c16 >> 2) + 4 * (g4 & 1) + 16 * (g4 >> 1)) + 8 * (lane & 3);
      const v4u f = cat(trd(a1), trd(a1 + 256));
      const u32 sf = WS_B3T + ((j * 16 + (g & 15)) * 4 + w) * 1024;
      asm volatile("buffer_store_dwordx4 %0, %1, %2, %3 offen sc1\n\ts_nop 1" ::"v"(f), "v"(vl16), "s"(rWS), "s"(sf) : "memory");
    }
  }
  BAR();
  STAMP(7);
  {
    int hw1 = (hw0 ^ 128) + 8192; PIN(hw1);
    sfor<0, 10>([&](auto I) {
      constexpr int i = decltype(I)::value;
      v4u o;
      o.x = pk2(xn[i][0] * ma0.xy + ms0.xy); o.y = pk2(xn[i][1] * ma0.zw + ms0.zw);
      o.z = pk2(xn[i][2] * ma1.xy + ms1.xy); o.w = pk2(xn[i][3] * ma1.zw + ms1.zw);
      int bp = (i >> 1) + rx2; bp = bp >= 5 ? bp - 5 : bp;
      *(LAS v4u*)(L + ((i & 1) ? hw1 : hw0) + bp * 16384) = o;
      SCHED();
    });
  }
  VMW(0);
  BAR();
  RAISE(WS_RFLAG + (j * 32 + g) * 4);
  u32 flr;
  STAMP(8);
  __builtin_amdgcn_s_setprio(2);

  const f32x4 z4 = {0.f, 0.f, 0.f, 0.f};
#define AFRAG(off, ks) (*(LAS v4u*)(L + afo[(ks) & 3] + ((off) + ((ks) >> 2) * 256)))
  const int ch0 = cs0 + 3 * w;
  constexpr int ND = NODMA ? 0 : 1;
  v4u b4[48];
  const u32 sB4 = WS_B4T + (j * 16 * 24 + 3 * w) * 1024;
  auto load_b4 = [&](auto NC) {
    constexpr int n = decltype(NC)::value, ks = n / 3, ct = n % 3;
    const u32 so = sB4 + ((ks + 2 * rotp) & 15) * 24576;
    v4u& r = b4[n];
    if constexpr (ct == 0) LD16S(r, vl16, rWS, so, 0); else if constexpr (ct == 1) LD16S(r, vl16, rWS, so, 1024); else LD16S(r, vl16, rWS, so, 2048);
  };
  auto landed_b4 = [&](auto AC, auto BC) {
#pragma unroll
    for (int i = decltype(AC)::value; i < decltype(BC)::value; ++i) { v4u& r = b4[i]; PIN(r); }
  };
  v4u b3[16];
  auto load_b3 = [&](auto NC) { constexpr int n = decltype(NC)::value; ld16s<(n & 3) * 1024>(b3[n], vl16, rWS, WS_B3T + (j * 16 + 4 * kq + (n >> 2)) * 4096); };
  v4u gb_, gg_;
  u32 vgg, fl0, vRow;
  int pw, pr;
  f32x4 aKh = z4, aVh = z4, aQ = z4, aK = z4, aV = z4, bQ = z4, bK = z4, bV = z4;
  {
    v4u ah = AFRAG(0, 0), a1 = AFRAG(16384, 0), a2 = AFRAG(32768, 0);
    int l3, c3, g3; f32x2 mB, mG;
    auto job = [&](auto NC) {
      constexpr int n = decltype(NC)::value;
      { u64& tA_r = tA_; u64& tB_r = tB_; (void)tA_r; (void)tB_r; }
      if constexpr (n == 25) STAMP(31);
      if constexpr (n == 27) STAMP(32);
      if constexpr (n == 30) STAMP(33);
      if constexpr (n == 3) { l3 = opq(vl16) >> 4; c3 = l3 & 15; g3 = l3 >> 4; PIN(c3); PIN(g3); vgg = (vl16 < 512 ? 8192 : 20480 - 512) + 512 * j + vl16; PIN(vgg); }
      if constexpr (n == 7) { vRow = (8 * w + (l3 >> 3)) * 1024 + (l3 & 7) * 16; PIN(vRow); }
      if constexpr (n == 8) { pr = (8 * w + (l3 >> 3)) * 256 + (((2 * (l3 & 7)) ^ ((l3 >> 3) & 7)) << 4); PIN(pr); }
      if constexpr (n == 10) { pw = kq * 16384 + (32 * mh + c3) * 256 + ((g3 ^ (c3 & 3)) << 4) + ((c3 & 4) << 4); PIN(pw); }
      if constexpr (n == 31) { mB.x = asf(gb_.x) + one; PIN(mB.x); mB.y = asf(gb_.z) + one; PIN(mB.y); *(LAS f32x2*)(L + L_MODA + w * 512 + (vl16 >> 1)) = mB; }
      if constexpr (n == 32) { mG.x = asf(gg_.x); mG.y = asf(gg_.z); *(LAS f32x2*)(L + L_GAB + (vl16 >> 1)) = mG; }
      if constexpr (n == 34) { fl0 = (gb_.y ^ tag) | (gb_.w ^ tag) | (gg_.y ^ tag) | (gg_.w ^ tag); PIN(fl0); }
      if constexpr (n == 7) { const u32 s0 = WS_MODF + 12288 + w * 1024; LD16S(gb_, vl16, rWS, s0, 0); }
      if constexpr (n == 9) { const u32 s0 = WS_MODF; LD16S(gg_, vgg, rWS, s0, 0); }
      if constexpr (n < 6 && !(n & 1)) b1_piece(IC<5>{}, IC<L_Z + 49152>{}, IC<n / 2>{});
      if constexpr (n == 6) arrive(L_CNT + 8);
      if constexpr (n == 10) await8(L_CNT + 8);
      if constexpr (n >= 10 && n < 22 && !(n & 1)) b1_piece(IC<6 + (n - 10) / 6>{}, IC<L_Z + ((n - 10) / 6) * 24576>{}, IC<((n - 10) / 2) % 3>{});
      if constexpr (n == 21) { const u32 vr = (vl16 >> 2) & 124, sr = WS_RFLAG + j * 128; LD4S(flr, vr, rWS, sr); }
      if constexpr (n == 26) {
        asm volatile("s_waitcnt vmcnt(0)" : "+v"(gb_), "+v"(gg_), "+v"(flr)::"memory");
        arrive(L_CNT + 12);
        const u32 vr = (vl16 >> 2) & 124, sr = WS_RFLAG + j * 128; u32 cnt = SPIN_CAP;
        asm volatile("1:\n\tv_cmp_ne_u32 vcc, %5, %0\n\ts_cbranch_vccz 2f\n\tbuffer_load_dword %0, %2, %3, %4 offen sc1\n\ts_waitcnt vmcnt(0)\n\t"
                     "s_sub_u32 %1, %1, 1\n\ts_cbranch_scc0 1b\n\t2:" : "+v"(flr), "+s"(cnt) : "v"(vr), "s"(rWS), "s"(sr), "s"(tag) : "vcc", "scc", "memory");
      }
      if constexpr (n == 28) { await8(L_CNT + 12); int tz2 = tz0 + 49152; PIN(tz2); read_b1(5, tz2, 0); }
      if constexpr (n == 32) read_b1(6, tz0, 0);
      if constexpr (n == 37) read_b1(7, tz0, 24576);
      if constexpr (n >= 27 && n < 47 && (n & 1)) load_b3(IC<(n - 27) / 2>{});
    };
    sfor<0, 16>([&](auto KS) {
      constexpr int ks = decltype(KS)::value;
      KSTAMP(1, ks);
      if constexpr (ks == 0) read_b1(3, tz0, 0);
      if constexpr (ks == 1) read_b1(4, tz0, 24576);
      SCHED();
      v4u nh = ah, n1 = a1, n2 = a2;
      aKh = mfma32(bw[ks * 3 + 1], ah, aKh);
      aVh = mfma32(ah, bw[ks * 3 + 2], aVh);
      if constexpr (ks < 15) nh = AFRAG(0, ks + 1);
      job(IC<3 * ks>{});
      SCHED();
      aQ = mfma32(bw[ks * 3 + 0], a1, aQ);
      aK = mfma32(bw[ks * 3 + 1], a1, aK);
      aV = mfma32(a1, bw[ks * 3 + 2], aV);
      if constexpr (ks < 15) n1 = AFRAG(16384, ks + 1);
      job(IC<3 * ks + 1>{});
      SCHED();
      bQ = mfma32(bw[ks * 3 + 0], a2, bQ);
      bK = mfma32(bw[ks * 3 + 1], a2, bK);
      bV = mfma32(a2, bw[ks * 3 + 2], bV);
      if constexpr (ks < 15) n2 = AFRAG(32768, ks + 1);
      job(IC<3 * ks + 2>{});
      ah = nh; a1 = n1; a2 = n2;
      SCHED();
    });
  }
  __builtin_amdgcn_s_setprio(0);
  STAMP(9); KSTAMP(1, 16);
  {
    const u32 sOst = WS_O + ((g * 4 + 2 * blk2) * 16 + 2 * j + (hd >> 1)) * 1024;
    const v2u k1p = pk4(aK), v1p = pk4(aV);
    const v2u o0 = attend(pk4(aKh), pk4(aVh), k1p, v1p, pk4(aQ), am0, am1, ahm, g == 0 && blk2 == 0);
    const v2u o1 = attend(k1p, v1p, pk4(bK), pk4(bV), pk4(bQ), am0, am1, ahm, false);
    __builtin_amdgcn_raw_buffer_store_b64(o0, rWS, vOst, sOst, 16);
    __builtin_amdgcn_raw_buffer_store_b64(o1, rWS, vOst, sOst + 16384, 16);
  }
  if (__builtin_amdgcn_ballot_w64(fl0 != 0u)) {
    for (u32 sp = 0; sp < SPIN_CAP; ++sp) {
      asm volatile("" ::: "memory");
      gb_ = __builtin_amdgcn_raw_buffer_load_b128(rWS, vl16, WS_MODF + 12288 + w * 1024, 16);
      gg_ = __builtin_amdgcn_raw_buffer_load_b128(rWS, vgg, WS_MODF, 16);
      if (!__builtin_amdgcn_ballot_w64(((gb_.y ^ tag) | (gb_.w ^ tag) | (gg_.y ^ tag) | (gg_.w ^ tag)) != 0u)) break;
    }
    const f32x2 mB = {asf(gb_.x) + one, asf(gb_.z) + one}, mG = {asf(gg_.x), asf(gg_.z)};
    *(LAS f32x2*)(L + L_MODA + w * 512 + (vl16 >> 1)) = mB;
    *(LAS f32x2*)(L + L_GAB + (vl16 >> 1)) = mG;
  }
  STAMP(10);
  VMW(0);
  BAR();
  RAISE(flag_off(0, g, 0) + j * 128);
  STAMP(11);
  if (b == 0 && w == 7) {
    for (u32 sp = 0; sp < SPIN_CAP; ++sp) {
      asm volatile("" ::: "memory");
      const v4u v = __builtin_amdgcn_raw_buffer_load_b128(rWS, vl16, WS_RFLAG, 16);
      if (__all(v.x == tag && v.y == tag && v.z == tag && v.w == tag)) break;
    }
    __builtin_amdgcn_raw_buffer_store_b32(tag, rWS, 0, WS_EPOCH, 16);
  }

  v4u xv;
  { const u32 sx = g * 65536 + j * 128; LD16(xv, vRow, rX, sx, 0); }
  sfor<10, 16>(load_b3);
  const u64 care2 = 0x0101010101010101ull * (3u << (2 * kq));
  poll_flags(rWS, vfl, flag_off(0, g, 0), care2, tag);
  PIN(xv);
  v4u af[8];
  {
    const u32 so = WS_O + ((g * 4 + 2 * mh) * 16 + 4 * kq) * 1024, so1 = so + 16384;
    LD16S(af[0], vl16, rWS, so, 0); LD16S(af[1], vl16, rWS, so, 1024); LD16S(af[2], vl16, rWS, so, 2048); LD16S(af[3], vl16, rWS, so, 3072);
    LD16S(af[4], vl16, rWS, so1, 0); LD16S(af[5], vl16, rWS, so1, 1024); LD16S(af[6], vl16, rWS, so1, 2048); LD16S(af[7], vl16, rWS, so1, 3072);
  }
  STAMP(12);
  sfor<0, 2>([&](auto M) {
    constexpr int m = decltype(M)::value;
    asm volatile("s_waitcnt vmcnt(%c4)" : "+v"(af[4 * m]), "+v"(af[4 * m + 1]), "+v"(af[4 * m + 2]), "+v"(af[4 * m + 3]) : "i"(m == 0 ? 4 : 2) : "memory");
    if constexpr (m == 0) {
#pragma unroll
      for (int i = 0; i < 16; ++i) PIN(b3[i]);
    }
    f32x4 acc[4] = {z4, z4, z4, z4};
    sfor<0, 4>([&](auto SC) {
      constexpr int sx = decltype(SC)::value;
#pragma unroll
      for (int n = 0; n < 4; ++n) acc[n] = mfma32(b3[sx * 4 + n], af[4 * m + sx], acc[n]);
      if constexpr (sx & 1) load_b4(IC<2 * m + (sx >> 1)>{});
      SCHED();
    });
#pragma unroll
    for (int n = 0; n < 4; ++n) *(LAS f32x4*)(L + ((n & 1) ? (pw ^ 64) : pw) + (m * 4096 + (n >> 1) * 128)) = acc[n];
  });
  BAR();
  STAMP(13);
  v4u x1p;
  {
    f32x4 y0 = *(LAS f32x4*)(L + pr), y1 = *(LAS f32x4*)(L + (pr ^ 16));
#pragma unroll
    for (int q = 1; q < 4; ++q) { y0 += *(LAS f32x4*)(L + pr + q * 16384); y1 += *(LAS f32x4*)(L + (pr ^ 16) + q * 16384); }
    const int ga = L_GAB + ((vl16 & 112) << 1);
    const f32x4 g0 = *(LAS f32x4*)(L + ga), g1 = *(LAS f32x4*)(L + ga + 16);
    const f32x4 x0 = f32x4{blo(xv.x), bhi(xv.x), blo(xv.y), bhi(xv.y)} + g0 * y0, x1 = f32x4{blo(xv.z), bhi(xv.z), blo(xv.w), bhi(xv.w)} + g1 * y1;
    { const v4u o = cat(pk4(x0), pk4(x1)); x1p = o; const u32 so = WS_X1 + g * 65536 + j * 128;
      asm volatile("buffer_store_dwordx4 %0, %1, %2, %3 offen sc1\n\ts_nop 1" ::"v"(o), "v"(vRow), "s"(rWS), "s"(so) : "memory"); }
    const f32x4 sv = x0 + x1;
    float mean = (sv.x + sv.y) + (sv.z + sv.w);
    mean += dppf<0xB1>(mean); mean += dppf<0x4E>(mean); mean += dppf<0x141>(mean);
    mean *= 1.f / 64.f;
    const f32x4 d0 = x0 - mean, d1 = x1 - mean, dd = d0 * d0 + d1 * d1;
    float m2 = (dd.x + dd.y) + (dd.z + dd.w);
    m2 += dppf<0xB1>(m2); m2 += dppf<0x4E>(m2); m2 += dppf<0x141>(m2);
    v2u st; st.x = asu(mean); st.y = asu(m2);
    __builtin_amdgcn_raw_buffer_store_b64(st, rWS, (vRow >> 10) << 6, WS_STAT + g * 4096 + j * 8, 16);
  }
  STAMP(14);
  VMW(0);
  BAR();
  RAISE(flag_off(1, g, 0) + j * 128);
  sfor<4, 9>(load_b4);
  STAMP(15);

  u32 hmask[3];
#pragma unroll
  for (int ct = 0; ct < 3; ++ct)
    asm("s_cmp_lt_u32 %1, %2\n\ts_cselect_b32 %0, -1, 0" : "=s"(hmask[ct]) : "s"((u32)(ch0 + ct)), "s"((u32)cend) : "scc");
#pragma unroll
  for (int i = 0; i < 4; ++i) { afo[i] -= blk2 * 32768; PIN(afo[i]); }
  int vm2 = L_MODA + 2 * vl16; PIN(vm2);
  int h2w = w * 8192 + (((vl16 - 128 * rotp) & 1023) ^ ((8 * w & 15) << 4)); PIN(h2w);
  const f32x4 ls0 = *(LAS f32x4*)(L + vm2), ls1 = *(LAS f32x4*)(L + vm2 + 16), la0 = *(LAS f32x4*)(L + vm2 + 2048), la1 = *(LAS f32x4*)(L + vm2 + 2064);
  poll_flags(rWS, vfl, flag_off(1, g, 0), ~0ull, tag);
  landed_b4(IC<0>{}, IC<9>{});
  {
    v4u x1v[8]; v2u stv;
    const u32 so = WS_X1 + g * 65536 + w * 8192, so2 = so + 4096;
    { const u32 vs = vl16 >> 1, ss = WS_STAT + g * 4096 + w * 512; LD8S(stv, vs, rWS, ss, 0); }
    LD16S(x1v[0], vl16, rWS, so, 0); LD16S(x1v[1], vl16, rWS, so, 1024); LD16S(x1v[2], vl16, rWS, so, 2048); LD16S(x1v[3], vl16, rWS, so, 3072);
    LD16S(x1v[4], vl16, rWS, so2, 0); LD16S(x1v[5], vl16, rWS, so2, 1024); LD16S(x1v[6], vl16, rWS, so2, 2048); LD16S(x1v[7], vl16, rWS, so2, 3072);
    sfor<9, 12>(load_b4);
    asm volatile("s_waitcnt vmcnt(11)" : "+v"(stv)::"memory");
    const float mj = asf(stv.x);
    float mean = mj;
    mean += dppf<0xB1>(mean); mean += dppf<0x4E>(mean); mean += dppf<0x141>(mean);
    mean *= 0.125f;
    const float dm = mj - mean;
    float m2 = asf(stv.y) + 64.f * dm * dm;
    m2 += dppf<0xB1>(m2); m2 += dppf<0x4E>(m2); m2 += dppf<0x141>(m2);
    const float l2r = __builtin_amdgcn_rsqf(m2 * (1.f / 512.f) + 1e-5f), l2n = -mean * l2r;
    sfor<0, 8>([&](auto I) {
      constexpr int i = decltype(I)::value;
      v4u& xx = x1v[i];
      asm volatile("s_waitcnt vmcnt(%c1)" : "+v"(xx) : "i"(10 - i) : "memory");
      const float rs = rdl(l2r, 8 * i), nn = rdl(l2n, 8 * i);
      v4u o;
      o.x = pk2((unp(xx.x) * rs + nn) * la0.xy + ls0.xy); o.y = pk2((unp(xx.y) * rs + nn) * la0.zw + ls0.zw);
      o.z = pk2((unp(xx.z) * rs + nn) * la1.xy + ls1.xy); o.w = pk2((unp(xx.w) * rs + nn) * la1.zw + ls1.zw);
      *(LAS v4u*)(L + (h2w ^ (i << 4)) + i * 1024) = o;
    });
  }
  BAR();
  STAMP(16);
  __builtin_amdgcn_s_setprio(2);
  const int kb5 = 5 * w + min(w, 3);
  u64 care5 = 0x0101010101010101ull * ((0x80c06030180c0603ull >> (8 * w)) & 255u);
  asm volatile("" : "+s"(care5));
  v4u b5[24];
  const u32 sB5 = WS_B5T + (j * 43 + kb5) * 4096, d5 = (w + 5) >> 3;
  auto load_b5 = [&](auto NC) {
    constexpr int n = decltype(NC)::value, sx = n / 4, t = n % 4;
    ld16s<t * 1024>(b5[n], vl16, rWS, sB5 + (sx == 5 ? (5 - d5) * 4096 : sx * 4096));
  };
  f32x4 acc4[3][4];
#pragma unroll
  for (int ct = 0; ct < 3; ++ct)
#pragma unroll
    for (int m = 0; m < 4; ++m) acc4[ct][m] = z4;
  u32 vH; int pw5;
  {
    v4u a[4] = {AFRAG(0, 0), AFRAG(16384, 0), AFRAG(32768, 0), AFRAG(49152, 0)};
    sfor<0, 16>([&](auto KS) {
      constexpr int ks = decltype(KS)::value;
      KSTAMP(3, ks);
      if constexpr (ks >= 3 && ks < 13) { VMW(9); landed_b4(IC<3 * ks>{}, IC<3 * ks + 3>{}); }
      if constexpr (ks >= 13) { VMW(9); landed_b4(IC<3 * ks>{}, IC<3 * ks + 3>{}); }
      v4u nx[4] = {a[0], a[1], a[2], a[3]};
      sfor<0, 4>([&](auto MC) {
        constexpr int m = decltype(MC)::value;
        if constexpr (ks < 15) nx[m] = AFRAG(16384 * m, ks + 1);
        if constexpr (m < 3) { if constexpr (ks < 12) load_b4(IC<12 + 3 * ks + m>{}); else load_b5(IC<3 * (ks - 12) + m>{}); }
        SCHED();
#pragma unroll
        for (int ct = 0; ct < 3; ++ct) acc4[ct][m] = mfma32(b4[ks * 3 + ct], a[m], acc4[ct][m]);
        if constexpr (ks == 8 && m == 0) { vH = (vl16 & 255) + ((vl16 >> 5) & 8) + (vl16 >> 9) * (172 * 256); PIN(vH); }
        if constexpr (ks == 9 && m == 0) { const int l5 = opq(vl16) >> 4, c5 = l5 & 15, g5 = l5 >> 4;
          pw5 = w * 16384 + c5 * 256 + ((c5 & 4) << 4) + ((g5 ^ (c5 & 3)) << 4); PIN(pw5); }
        SCHED();
      });
#pragma unroll
      for (int m = 0; m < 4; ++m) a[m] = nx[m];
    });
  }
#pragma unroll
  for (int ct = 0; ct < 3; ++ct)
#pragma unroll
    for (int m = 0; m < 4; ++m) PIN(acc4[ct][m]);
  __builtin_amdgcn_s_setprio(0);
  STAMP(17); KSTAMP(3, 16);
#pragma unroll
  for (int ct = 0; ct < 3; ++ct)
#pragma unroll
    for (int mp = 0; mp < 2; ++mp) {
      f32x4 G, U, R;
#pragma unroll
      for (int e = 0; e < 4; ++e) {
        auto r = __builtin_amdgcn_permlane32_swap(asu(acc4[ct][2 * mp][e]), asu(acc4[ct][2 * mp + 1][e]), false, false);
        G[e] = asf(r[0]); U[e] = asf(r[1]);
      }
#pragma unroll
      for (int e = 0; e < 4; ++e) R[e] = qp(qp(G[e] * U[e]) * __builtin_amdgcn_rcpf(1.f + __builtin_amdgcn_exp2f(qp(G[e] * -1.4426950408889634f))));
      const v2u o = pk4(R);
      const u32 so = WS_HH + ((g * 4 + 2 * mp) * 172 + ch0 + ct) * 256;
      asm volatile("s_mov_b32 exec_lo, %4\n\ts_mov_b32 exec_hi, %4\n\tbuffer_store_dwordx2 %0, %1, %2, %3 offen sc1\n\ts_mov_b64 exec, -1"
                   ::"v"(o), "v"(vH), "s"(rWS), "s"(so), "s"(hmask[ct]) : "memory");
    }
  STAMP(18);
  sfor<12, 16>(load_b5);
  VMW(4);
  BAR();
  RAISE(flag_off(2, g, 0) + j * 128);
  STAMP(19);
  sfor<16, 22>(load_b5);
  poll_flags(rWS, vfl, flag_off(2, g, 0), care5, tag);
#pragma unroll
  for (int i = 0; i < 22; ++i) PIN(b5[i]);
  v4u hf[16];
  auto load_h = [&](auto NC) {
    constexpr int n = decltype(NC)::value, sx = n / 4, m = n % 4;
    ld16s<0>(hf[n & 15], vl16, rWS, WS_HH + ((g * 4 + m) * 172 + 4 * kb5) * 256 + (sx == 5 ? (5 - d5) * 1024 : sx * 1024));
  };
  sfor<0, 16>(load_h);
  sfor<22, 24>(load_b5);
  STAMP(20);
  {
    f32x4 acc[4][4];
#pragma unroll
    for (int m = 0; m < 4; ++m)
#pragma unroll
      for (int n = 0; n < 4; ++n) acc[m][n] = z4;
    sfor<0, 6>([&](auto SC) {
      constexpr int sx = decltype(SC)::value, r = (sx & 3) * 4;
      constexpr int nw = sx == 0 ? 14 : sx == 1 ? 14 : sx == 2 ? 14 : sx == 3 ? 10 : sx == 4 ? 4 : 0;
      asm volatile("s_waitcnt vmcnt(%c4)" : "+v"(hf[r]), "+v"(hf[r + 1]), "+v"(hf[r + 2]), "+v"(hf[r + 3]) : "i"(nw) : "memory");
      if constexpr (sx == 4) { PIN(b5[22]); PIN(b5[23]); }
      if constexpr (sx == 5) { if (w >= 3) { hf[r] = v4u{0u, 0u, 0u, 0u}; hf[r + 1] = v4u{0u, 0u, 0u, 0u}; hf[r + 2] = v4u{0u, 0u, 0u, 0u}; hf[r + 3] = v4u{0u, 0u, 0u, 0u}; } }
      sfor<0, 4>([&](auto MC) {
        constexpr int m = decltype(MC)::value;
#pragma unroll
        for (int n = 0; n < 4; ++n) acc[m][n] = mfma32(b5[sx * 4 + n], hf[r + m], acc[m][n]);
        SCHED();
        if constexpr (sx < 2) load_h(IC<16 + 4 * sx + m>{});
        SCHED();
      });
    });
#pragma unroll
    for (int m = 0; m < 4; ++m)
#pragma unroll
      for (int n = 0; n < 4; ++n) *(LAS f32x4*)(L + ((n & 1) ? (pw5 ^ 64) : pw5) + (m * 4096 + (n >> 1) * 128)) = acc[m][n];
  }
  BAR();
  STAMP(21);
  {
    const int l6 = opq(vl16) >> 4;
    const int p0 = opq((8 * w + (l6 >> 3)) * 256 + (((2 * (l6 & 7)) ^ ((l6 >> 3) & 7)) << 4)), p1 = opq(p0 + 65536);
    f32x4 y0 = *(LAS f32x4*)(L + p0), y1 = *(LAS f32x4*)(L + (p0 ^ 16));
#pragma unroll
    for (int q = 1; q < 8; ++q) { const int pb = q < 4 ? p0 : p1; y0 += *(LAS f32x4*)(L + pb + (q & 3) * 16384); y1 += *(LAS f32x4*)(L + (pb ^ 16) + (q & 3) * 16384); }
    const int gb = L_GAB + 256 + (l6 & 7) * 32;
    const f32x4 g0 = *(LAS f32x4*)(L + gb), g1 = *(LAS f32x4*)(L + gb + 16);
    const f32x4 o0 = f32x4{blo(x1p.x), bhi(x1p.x), blo(x1p.y), bhi(x1p.y)} + g0 * y0, o1 = f32x4{blo(x1p.z), bhi(x1p.z), blo(x1p.w), bhi(x1p.w)} + g1 * y1;
    const v4u o = cat(pk4(o0), pk4(o1));
    const u32 vo = (8 * w + (l6 >> 3)) * 1024 + (l6 & 7) * 16, so = g * 65536 + j * 128;
    asm volatile("buffer_store_dwordx4 %0, %1, %2, %3 offen sc1\n\ts_nop 1" ::"v"(o), "v"(vo), "s"(rOUT), "s"(so) : "memory");
  }
  STAMP(22);
  if (b == 40 && w == WSEL) { u64 t0 = __builtin_amdgcn_s_memrealtime(); const u64 d = 10 * (tB_ - tA_); while (__builtin_amdgcn_s_memrealtime() - t0 < d && d < 20000) {} }
}
}

extern "C" void kernel_launch(void* const* d_in, const int* in_sizes, int n_in, void* d_out, int out_size, void* d_ws, size_t ws_size,
                              hipStream_t stream) {
  Params p;
  p.x = (const u16*)d_in[0]; p.wqkv = (const u16*)d_in[1]; p.wo = (const u16*)d_in[2];
  p.w1 = (const u16*)d_in[4]; p.w3 = (const u16*)d_in[5]; p.w2 = (const u16*)d_in[6];
  p.c = (const u16*)d_in[7]; p.wmod = (const u16*)d_in[8];
  p.out = (u16*)d_out; p.ws = (char*)d_ws;
  (void)hipFuncSetAttribute((const void*)fused_block, hipFuncAttributeMaxDynamicSharedMemorySize, L_TOTAL);
  fused_block<<<256, 512, L_TOTAL, stream>>>(p);
}
```

```cpp
#include <hip/hip_runtime.h>
#include <stdint.h>

#define KDBG 0
#define NODMA 0
#define CODEWARM 0
#define L2WARM 1
#define EARLYT 0
#define XMAP 2

#define DI __device__ __forceinline__
#define GAS __attribute__((address_space(1)))
#define LAS __attribute__((address_space(3)))
typedef unsigned u32;
typedef unsigned short u16;
typedef unsigned long long u64;
typedef u32 v4u __attribute__((ext_vector_type(4)));
typedef u32 v2u __attribute__((ext_vector_type(2)));
typedef float f32x4 __attribute__((ext_vector_type(4)));
typedef float f32x2 __attribute__((ext_vector_type(2)));
typedef __bf16 bf16x8 __attribute__((ext_vector_type(8)));
typedef __bf16 bf16x2 __attribute__((ext_vector_type(2)));
typedef short s16x4 __attribute__((ext_vector_type(4)));
typedef __amdgpu_buffer_rsrc_t rsrc_t;
typedef GAS u32 gu32;
typedef GAS u64 gu64;
#define RLX_AGENT __ATOMIC_RELAXED, __HIP_MEMORY_SCOPE_AGENT

namespace {
constexpr int S_ = 2048, D_ = 512, F_ = 1376;
constexpr u32 WS_EPOCH = 0;
constexpr u32 WS_FLAG = 32768;
constexpr u32 WS_STAT = 262144;
constexpr u32 WS_MODG = 524288;
constexpr u32 WS_MODF = 655360;
constexpr u32 WS_O = 1u << 20;
constexpr u32 WS_X1 = 3u << 20;
constexpr u32 WS_HH = 5u << 20;
constexpr u32 WS_RFLAG = 8192;
constexpr u32 WS_B4T = 11u << 20;
constexpr u32 WS_B5T = 14u << 20;
constexpr u32 WS_B3T = (15u << 20) + (512u << 10);
constexpr u32 WS_DBG = 16u << 20;
constexpr u32 WS_BYTES = 32u << 20;
constexpr int L_XY = 0;
constexpr int L_Z = 81920;
constexpr int L_MISC = 158720;
constexpr int L_TOTAL = 163840;
constexpr int L_RED = 73728;
constexpr int L_MODA = L_MISC;
constexpr int L_CNT = L_MISC + 4096;
constexpr int L_GAB = L_MISC + 4096 + 64;
constexpr int L_JUNK = L_MISC + 4096 + 64 + 512;
constexpr int L_STAMP = 156672;
constexpr u32 SPIN_CAP = 1u << 18;
constexpr u32 CODE_BYTES = 26112;

DI f32x4 mfma32(v4u a, v4u b, f32x4 c) {
  return __builtin_amdgcn_mfma_f32_16x16x32_bf16(__builtin_bit_cast(bf16x8, a), __builtin_bit_cast(bf16x8, b), c, 0, 0, 0);
}
DI f32x4 mfma16(v2u a, v2u b, f32x4 c) {
  return __builtin_amdgcn_mfma_f32_16x16x16bf16_1k(__builtin_bit_cast(s16x4, a), __builtin_bit_cast(s16x4, b), c, 0, 0, 0);
}
DI u32 pk(float lo, float hi) { f32x2 v = {lo, hi}; return __builtin_bit_cast(u32, __builtin_convertvector(v, bf16x2)); }
DI u32 pk2(f32x2 v) { return __builtin_bit_cast(u32, __builtin_convertvector(v, bf16x2)); }
DI v2u pk4(f32x4 v) { v2u r; r.x = pk(v.x, v.y); r.y = pk(v.z, v.w); return r; }
DI float asf(u32 u) { return __builtin_bit_cast(float, u); }
DI u32 asu(float f) { return __builtin_bit_cast(u32, f); }
DI float blo(u32 u) { return asf(u << 16); }
DI float bhi(u32 u) { return asf(u & 0xffff0000u); }
DI f32x2 unp(u32 u) { f32x2 r = {blo(u), bhi(u)}; return r; }
DI float dot2(u32 a, u32 b, float c) { return __builtin_amdgcn_fdot2_f32_bf16(__builtin_bit_cast(bf16x2, a), __builtin_bit_cast(bf16x2, b), c, false); }
DI v2u trd(LAS char* p) { return __builtin_bit_cast(v2u, __builtin_amdgcn_ds_read_tr16_b64_v4i16((LAS s16x4*)p)); }
DI v4u cat(v2u a, v2u b) { v4u r; r.x = a.x; r.y = a.y; r.z = b.x; r.w = b.y; return r; }
template <int MI, int SI> DI void dma(u32 mb, u32 voff, rsrc_t srd, u32 sb) {
  u32 t;
  asm volatile("s_add_u32 m0, %1, %c2\n\ts_add_u32 %0, %3, %c4\n\tbuffer_load_dwordx4 %5, %6, %0 offen lds"
               : "=&s"(t) : "s"(mb), "i"(MI), "s"(sb), "i"(SI), "v"(voff), "s"(srd) : "scc");
}
DI void touch(u32 voff, rsrc_t srd, u32 sb) {
  asm volatile("s_mov_b32 m0, %c0\n\ts_nop 0\n\tbuffer_load_dword %1, %2, %3 offen lds" ::"i"(L_JUNK), "v"(voff), "s"(srd), "s"(sb));
}
#define BAR() asm volatile("s_waitcnt lgkmcnt(0)\n\ts_barrier" ::: "memory")
#define VMW(n) asm volatile("s_waitcnt vmcnt(" #n ")" ::: "memory")
#define LGW() asm volatile("s_waitcnt lgkmcnt(0)" ::: "memory")
#define SCHED() __builtin_amdgcn_sched_barrier(0)
#define LD16(dst, voff, srd, soff, IMM) asm volatile("buffer_load_dwordx4 %0, %1, %2, %3 offen offset:" #IMM : "=v"(dst) : "v"(voff), "s"(srd), "s"(soff))
#define LD16S(dst, voff, srd, soff, IMM) asm volatile("buffer_load_dwordx4 %0, %1, %2, %3 offen offset:" #IMM " sc1" : "=v"(dst) : "v"(voff), "s"(srd), "s"(soff))
#define LD8(dst, voff, srd, soff, IMM) asm volatile("buffer_load_dwordx2 %0, %1, %2, %3 offen offset:" #IMM : "=v"(dst) : "v"(voff), "s"(srd), "s"(soff))
#define LD8S(dst, voff, srd, soff, IMM) asm volatile("buffer_load_dwordx2 %0, %1, %2, %3 offen offset:" #IMM " sc1" : "=v"(dst) : "v"(voff), "s"(srd), "s"(soff))
#define LD4(dst, voff, srd, soff) asm volatile("buffer_load_dword %0, %1, %2, %3 offen" : "=v"(dst) : "v"(voff), "s"(srd), "s"(soff))
#define LD4S(dst, voff, srd, soff) asm volatile("buffer_load_dword %0, %1, %2, %3 offen sc1" : "=v"(dst) : "v"(voff), "s"(srd), "s"(soff))

template <int IMM> DI void ld16s(v4u& r, u32 v, rsrc_t rs, u32 so) { asm volatile("buffer_load_dwordx4 %0, %1, %2, %3 offen offset:%c4 sc1" : "=v"(r) : "v"(v), "s"(rs), "s"(so), "i"(IMM)); }
template <int CTRL> DI float dppf(float v) { return asf(__builtin_amdgcn_update_dpp(0, asu(v), CTRL, 0xf, 0xf, true)); }
DI float row16_sum(float v) { v += dppf<0x128>(v); v += dppf<0x124>(v); v += dppf<0x122>(v); v += dppf<0x121>(v); return v; }
DI float sw32_add(float a, float b) { auto r = __builtin_amdgcn_permlane32_swap(asu(a), asu(b), false, false); return asf(r[0]) + asf(r[1]); }
DI float sw16_add(float a, float b) { auto r = __builtin_amdgcn_permlane16_swap(asu(a), asu(b), false, false); return asf(r[0]) + asf(r[1]); }
DI float x16_max(float v) { auto r = __builtin_amdgcn_permlane16_swap(asu(v), asu(v), false, false); return fmaxf(asf(r[0]), asf(r[1])); }
DI float x32_max(float v) { auto r = __builtin_amdgcn_permlane32_swap(asu(v), asu(v), false, false); return fmaxf(asf(r[0]), asf(r[1])); }
DI int fresh_lane() { int l; asm volatile("v_mbcnt_lo_u32_b32 %0, -1, 0\n\tv_mbcnt_hi_u32_b32 %0, -1, %0" : "=v"(l)); return l; }
DI float rdl(float v, int l) { return asf(__builtin_amdgcn_readlane(asu(v), l)); }

struct Params {
  const u16 *x, *wqkv, *wo, *w1, *w3, *w2, *c, *wmod;
  u16* out;
  char* ws;
};

#define LOOPSEL 0
#define STAMP(i) do {} while (0)
#define KSTAMP(lp, i) do {} while (0)
#define STAMP_DUMP() do {} while (0)

DI u32 flag_off(int phase, int g, int h) { return WS_FLAG + ((phase * 32 + g) * 2 + h) * 1024; }
DI void poll_flags(rsrc_t rWS, u32 vfl, u32 foff, u64 care, u32 tag) {
  for (u32 sp = 0; sp < SPIN_CAP; ++sp) {
    asm volatile("" ::: "memory");
    const u32 v = __builtin_amdgcn_raw_buffer_load_b32(rWS, vfl, foff, 16);
    if (!(__builtin_amdgcn_ballot_w64(v != tag) & care)) break;
  }
  VMW(0);
}
DI void raise_flag(rsrc_t rWS, u32 foff, u32 vtag, u32 vzero, u32 m1) {
  asm volatile("s_mov_b32 exec_hi, 0\n\ts_mov_b32 exec_lo, %4\n\tbuffer_store_dword %0, %1, %2, %3 offen sc1\n\ts_mov_b64 exec, -1" ::"v"(vtag), "v"(vzero), "s"(rWS), "s"(foff), "s"(m1) : "memory");
}
DI void signal_flag(rsrc_t rWS, u32 cnt_addr, u32 foff, u32 tag) {
  u32 t, a = cnt_addr, one = 1u, vt = tag, vz = 0u;
  asm volatile("s_mov_b64 exec, 1\n\tds_add_rtn_u32 %0, %1, %2\n\ts_waitcnt lgkmcnt(0)\n\tv_cmp_eq_u32 vcc, 7, %0\n\ts_and_b64 exec, exec, vcc\n\t"
               "buffer_store_dword %3, %4, %5, %6 offen sc1\n\ts_mov_b64 exec, -1"
               : "=&v"(t) : "v"(a), "v"(one), "v"(vt), "v"(vz), "s"(rWS), "s"(foff) : "vcc", "scc", "memory");
}
DI void arrive(u32 cnt_addr) {
  u32 a = cnt_addr, one = 1u;
  asm volatile("s_waitcnt lgkmcnt(0)\n\ts_mov_b64 exec, 1\n\tds_add_u32 %0, %1\n\ts_mov_b64 exec, -1" ::"v"(a), "v"(one) : "memory");
}
DI void await8(u32 cnt_addr) {
  u32 a = cnt_addr, t, sv;
  asm volatile("1:\n\tds_read_b32 %0, %2\n\ts_waitcnt lgkmcnt(0)\n\tv_readfirstlane_b32 %1, %0\n\ts_nop 0\n\ts_cmp_lt_u32 %1, 8\n\ts_cbranch_scc1 1b"
               : "=&v"(t), "=&s"(sv) : "v"(a) : "scc", "memory");
}
DI void raise_when8(rsrc_t rWS, u32 cnt_addr, u32 foff, u32 tag, u32 w7) {
  u32 a = cnt_addr, t, sv, vt = tag, vz = 0u;
  asm volatile("s_cmp_eq_u32 %7, 0\n\ts_cbranch_scc1 2f\n\t1:\n\tds_read_b32 %0, %2\n\ts_waitcnt lgkmcnt(0)\n\tv_readfirstlane_b32 %1, %0\n\ts_nop 0\n\ts_cmp_lt_u32 %1, 8\n\ts_cbranch_scc1 1b\n\t"
               "s_mov_b64 exec, 1\n\tbuffer_store_dword %3, %4, %5, %6 offen sc1\n\ts_mov_b64 exec, -1\n\t2:"
               : "=&v"(t), "=&s"(sv) : "v"(a), "v"(vt), "v"(vz), "s"(rWS), "s"(foff), "s"(w7) : "scc", "memory");
}
#define PIN(x) asm volatile("" : "+v"(x))
DI float qp(float r) { PIN(r); return r; }
DI float q_shl(u32 a) { return qp(blo(a)); }
DI float q_hi(u32 a) { return qp(bhi(a)); }
DI float q_fma(float a, float b, float c) { return qp(__builtin_fmaf(a, b, c)); }
DI float q_fmas(float a, float b, float c) { return qp(__builtin_fmaf(a, b, c)); }
DI float q_mul(float a, float b) { return qp(a * b); }
DI float q_mulk(float a) { return qp(a * -1.4426950408889634f); }
DI float q_add1(float a) { return qp(a + 1.f); }
DI float q_exp(float a) { return qp(__builtin_amdgcn_exp2f(a)); }
DI float q_rcp(float a) { return qp(__builtin_amdgcn_rcpf(a)); }
DI u32 q_cvt(float a, float b) { u32 r = pk(a, b); PIN(r); return r; }
DI float q_mov(float a) { return qp(a); }
DI u32 opq(u32 v) { asm volatile("" : "+v"(v)); return v; }

DI float sel(u64 m, float a, float b) { return __builtin_amdgcn_inverse_ballot_w64(m) ? a : b; }
DI v2u attend(v2u kp, v2u vp, v2u kc, v2u vc, v2u q, u64 m0, u64 m1, u32 hm, bool noprev) {
  const f32x4 z = {0.f, 0.f, 0.f, 0.f};
  const f32x4 sp = mfma16(kp, q, z);
  const f32x4 sc = mfma16(kc, q, z);
  const float K2 = 0.25f * 1.4426950408889634f;
  const u64 par = 0xaaaaaaaaaaaaaaaaull;
  float p0 = sel(par, sp[1], sp[0]), p1 = sel(par, sp[3], sp[2]);
  if (noprev) { p0 = -INFINITY; p1 = -INFINITY; }
  const float t0 = sel(m0, sel(par, sc[1], sc[0]), p0), t1 = sel(m1, sel(par, sc[3], sc[2]), p1);
  const float m = x32_max(x16_max(fmaxf(t0, t1)));
  const float mk = -m * K2;
  const float e0 = __builtin_amdgcn_exp2f(__builtin_fmaf(t0, K2, mk)), e1 = __builtin_amdgcn_exp2f(__builtin_fmaf(t1, K2, mk));
  const u32 w0 = pk(e0, e0) & hm, w1 = pk(e1, e1) & hm;
  v4u pf;
  pf.x = __builtin_amdgcn_inverse_ballot_w64(m0) ? 0u : w0; pf.y = __builtin_amdgcn_inverse_ballot_w64(m1) ? 0u : w1;
  pf.z = __builtin_amdgcn_inverse_ballot_w64(m0) ? w0 : 0u; pf.w = __builtin_amdgcn_inverse_ballot_w64(m1) ? w1 : 0u;
  const f32x4 o = mfma32(cat(vp, vc), pf, z);
  float sum = e0 + e1;
  sum = sw16_add(sum, sum); sum = sw32_add(sum, sum);
  const float inv = __builtin_amdgcn_rcpf(sum);
  return pk4(f32x4{qp(o.x * inv), qp(o.y * inv), qp(o.z * inv), qp(o.w * inv)});
}

template <int N> using IC = std::integral_constant<int, N>;
template <int I, int N, class F> DI void sfor(F&& f) { if constexpr (I < N) { f(IC<I>{}); sfor<I + 1, N>(f); } }

__global__ void __launch_bounds__(512, 2) fused_block(Params p) {
  extern __shared__ __attribute__((aligned(16))) char smem[];
  LAS char* L = (LAS char*)smem;
  const int tid = threadIdx.x, lane = tid & 63, w = __builtin_amdgcn_readfirstlane(tid >> 6);
  const int c16 = lane & 15, g4 = lane >> 4;
  const int b = blockIdx.x;
  int g, j;
  if (XMAP == 0) { g = (b & 7) * 4 + ((b >> 3) & 3); j = b >> 5; }
  else if (XMAP == 2) { const int x = b & 7, r = b >> 3; g = (x >> 1) * 8 + (r & 7); j = (x & 1) * 4 + (r >> 3); }
  else { const int x = b & 7, r = b >> 3; g = (x >> 2) * 16 + (r & 15); j = (x & 3) * 2 + (r >> 4); }
  const rsrc_t rX = __builtin_amdgcn_make_buffer_rsrc((void*)p.x, 0, S_ * D_ * 2, 0x00020000);
  const rsrc_t rQKV = __builtin_amdgcn_make_buffer_rsrc((void*)p.wqkv, 0, D_ * 3 * D_ * 2, 0x00020000);
  const rsrc_t rWO = __builtin_amdgcn_make_buffer_rsrc((void*)p.wo, 0, D_ * D_ * 2, 0x00020000);
  const rsrc_t rW1 = __builtin_amdgcn_make_buffer_rsrc((void*)p.w1, 0, D_ * F_ * 2, 0x00020000);
  const rsrc_t rW3 = __builtin_amdgcn_make_buffer_rsrc((void*)p.w3, 0, D_ * F_ * 2, 0x00020000);
  const rsrc_t rW2 = __builtin_amdgcn_make_buffer_rsrc((void*)p.w2, 0, F_ * D_ * 2, 0x00020000);
  const rsrc_t rC = __builtin_amdgcn_make_buffer_rsrc((void*)p.c, 0, D_ * 2, 0x00020000);
  const rsrc_t rMOD = __builtin_amdgcn_make_buffer_rsrc((void*)p.wmod, 0, D_ * 6 * D_ * 2, 0x00020000);
  const rsrc_t rWS = __builtin_amdgcn_make_buffer_rsrc((void*)p.ws, 0, WS_BYTES, 0x00020000);
  const rsrc_t rOUT = __builtin_amdgcn_make_buffer_rsrc((void*)p.out, 0, S_ * D_ * 2, 0x00020000);
  STAMP(0);

  u32 ep;
  { const u32 vz = 0; asm volatile("buffer_load_dword %0, %1, %2, 0 offen sc1" : "=v"(ep) : "v"(vz), "s"(rWS)); }
  u32 vl16 = lane * 16; PIN(vl16);
  const int bm = b < 192 ? b : b - 192, mq = bm / 48, cl = bm - 48 * mq;
  v4u wm[4]; v2u cv; u32 ep0;
  if (w < 4) {
    const u32 vm = (tid >> 3) * 24576 + (tid & 7) * 16, s0 = mq * (128 * 6144) + cl * 128, s1 = s0 + 6144, s2 = s0 + 12288, s3 = s0 + 18432;
    LD16(wm[0], vm, rMOD, s0, 0); LD16(wm[1], vm, rMOD, s1, 0); LD16(wm[2], vm, rMOD, s2, 0); LD16(wm[3], vm, rMOD, s3, 0);
    const u32 vc = (tid >> 3) * 8, scq = mq * 256;
    LD8(cv, vc, rC, scq, 0);
    const u32 vz = 0; asm volatile("buffer_load_dword %0, %1, %2, 0 offen sc1" : "=v"(ep0) : "v"(vz), "s"(rWS));
  }
  if (tid < 16) *(LAS u32*)(L + L_CNT + tid * 4) = 0u;
  BAR();
  const int st_qd = lane >> 2, st_r = (st_qd & 3) + 4 * (st_qd >> 3);
  const int st_col = ((st_qd >> 2) & 1) * 64 + (((lane & 3) ^ ((w & 1) << 1)) << 4);
  const int rotp = g & 7;
  u32 vB1 = st_r * 3072 + st_col; PIN(vB1);
  const u32 mB1 = (w >> 2) * 12288 + (w & 3) * 1024, sB1 = (w >> 2) * 98304 + (w & 3) * 24576 + j * 128;
  auto stage_b1 = [&](auto PC, auto BC) {
    constexpr int pair = decltype(PC)::value, base = decltype(BC)::value;
    const u32 sp = ((pair + rotp) & 7) * 196608 + sB1;
    dma<base, 0>(mB1, vB1, rQKV, sp); dma<base + 4096, 1024>(mB1, vB1, rQKV, sp); dma<base + 8192, 2048>(mB1, vB1, rQKV, sp);
  };
  auto b1_piece = [&](auto PC, auto BC, auto TC) {
    constexpr int pair = decltype(PC)::value, base = decltype(BC)::value, ty = decltype(TC)::value;
    dma<base + ty * 4096, ty * 1024>(mB1, vB1, rQKV, ((pair + rotp) & 7) * 196608 + sB1);
  };
  const int hd = w & 3, blk2 = w >> 2, kq = w & 3, mh = w >> 2;
  const int cs0 = j < 4 ? 22 * j : 88 + 21 * (j - 4);
  const int cend = cs0 + (j < 4 ? 22 : 21);
  v4u xr[10];
  const int rx2 = (0x4310 >> (4 * (j & 3))) & 7;
  auto load_x = [&](int i0, int i1) {
    const int r0 = 64 * g - 16 + w;
#pragma unroll
    for (int i = i0; i < i1; ++i) {
      int bp = (i >> 1) + rx2; bp = bp >= 5 ? bp - 5 : bp;
      const u32 so = (u32)max(r0 + 16 * bp + 8 * (i & 1), 0) * 1024u;
      LD16(xr[i], vl16, rX, so, 0);
    }
  };
  load_x(0, 5);
  STAMP(1);
  const int T0 = 12 * (g >> 4) + 3 * (w & 3), rcb = min(cs0 + T0, 168);
  if (w < 4) {
    asm volatile("s_waitcnt vmcnt(5)" : "+v"(cv), "+v"(wm[0]), "+v"(wm[1]), "+v"(wm[2]), "+v"(wm[3]), "+v"(ep0)::"memory");
    const u32 tg = __builtin_amdgcn_readfirstlane(ep0) + 1u;
    const f32x4 cf = {blo(cv.x), bhi(cv.x), blo(cv.y), bhi(cv.y)};
    float sc[4];
#pragma unroll
    for (int r = 0; r < 4; ++r) sc[r] = cf[r] * __builtin_amdgcn_rcpf(1.f + __builtin_amdgcn_exp2f(cf[r] * -1.4426950408889634f));
    f32x2 v[4];
#pragma unroll
    for (int i = 0; i < 4; ++i) v[i] = (unp(wm[0][i]) * sc[0] + unp(wm[1][i]) * sc[1]) + (unp(wm[2][i]) * sc[2] + unp(wm[3][i]) * sc[3]);
    const float a0 = sw32_add(v[0].x, v[2].x), a1 = sw32_add(v[0].y, v[2].y), a2 = sw32_add(v[1].x, v[3].x), a3 = sw32_add(v[1].y, v[3].y);
    float t0 = sw16_add(a0, a2), t1 = sw16_add(a1, a3);
    t0 += dppf<0x128>(t0); t1 += dppf<0x128>(t1);
    f32x2 o = {t0, t1};
    *(LAS f32x2*)(L + L_RED + w * 256 + (lane & 7) * 32 + g4 * 8) = o;
    LGW();
    u32 old = 0;
    if (lane == 0) old = __hip_atomic_fetch_add((LAS u32*)(L + L_CNT + 28), 1u, __ATOMIC_RELAXED, __HIP_MEMORY_SCOPE_WORKGROUP);
    if (__builtin_amdgcn_readfirstlane(old) == 3u) {
      asm volatile("" ::: "memory");
      float s = 0.f;
#pragma unroll
      for (int i = 0; i < 4; ++i) s += *(LAS float*)(L + L_RED + i * 256 + lane * 4);
      v2u gr; gr.x = asu(s); gr.y = tg;
      if (b < 192) __builtin_amdgcn_raw_buffer_store_b64(gr, rWS, lane * 8, WS_MODG + (mq * 3072 + cl * 64) * 8, 16);
    }
  }
  STAMP(2);
  load_x(5, 10);
  stage_b1(IC<0>{}, IC<L_XY>{});
  int afo[4];
#pragma unroll
  for (int i = 0; i < 4; ++i) { afo[i] = blk2 * 32768 + c16 * 1024 + (((4 * i + g4) ^ c16) << 4); PIN(afo[i]); }
  const u64 am0 = __builtin_amdgcn_ballot_w64((c16 & 14) >= 4 * g4), am1 = __builtin_amdgcn_ballot_w64((c16 & 14) >= 4 * g4 + 2);
  u32 ahm = (lane & 1) ? 0xffff0000u : 0xffffu; PIN(ahm);
  u32 vOst = c16 * 16 + (2 * (hd & 1) + (g4 >> 1)) * 256 + (g4 & 1) * 16384; PIN(vOst);
  int tB1 = g4 * 1024 + 64 * (c16 >> 2) + 8 * (lane & 3) + 32 * ((hd & 1) ^ (g4 & 1)) + 256 * (hd >> 1); PIN(tB1);
  int hw0 = w * 1024 + ((((lane - 8 * rotp) & 63) ^ w) << 4); PIN(hw0);
#define vfl ((opq(vl16) & 112u) << 3)
  STAMP(3);
  v4u bw[48];
  auto read_b1 = [&](int pair, int base, int off) {
#pragma unroll
    for (int kk = 0; kk < 2; ++kk)
#pragma unroll
      for (int ty = 0; ty < 3; ++ty) {
        LAS char* a = L + base + (off + kk * 12288 + ty * 4096);
        bw[(2 * pair + kk) * 3 + ty] = cat(trd(a), trd(a + 512));
      }
  };
  f32x2 xn[10][4];
  v4u ga_[4]; v2u pv;
  const u32 sMG = WS_MODG + w * 1024;
  {
    float u[10];
    sfor<0, 10>([&](auto I) {
      constexpr int i = decltype(I)::value;
      if constexpr (i == 0) asm volatile("s_waitcnt vmcnt(11)" : "+v"(xr[0]), "+v"(xr[1]), "+v"(ep)::"memory");
      if constexpr (i == 2) asm volatile("s_waitcnt vmcnt(9)" : "+v"(xr[2]), "+v"(xr[3])::"memory");
      if constexpr (i == 4) asm volatile("s_waitcnt vmcnt(10)" : "+v"(xr[4]), "+v"(xr[5])::"memory");
      if constexpr (i == 6) asm volatile("s_waitcnt vmcnt(8)" : "+v"(xr[6]), "+v"(xr[7])::"memory");
      if constexpr (i == 8) asm volatile("s_waitcnt vmcnt(9)" : "+v"(xr[8]), "+v"(xr[9])::"memory");
      float s = 0.f, q = 0.f;
#pragma unroll
      for (int e = 0; e < 4; ++e) { s = dot2(xr[i][e], 0x3f803f80u, s); q = dot2(xr[i][e], xr[i][e], q); }
      u[i] = sw32_add(s, q);
      PIN(u[i]);
      if constexpr (i == 3) stage_b1(IC<1>{}, IC<L_XY + 24576>{});
      if constexpr (i == 7) stage_b1(IC<2>{}, IC<L_XY + 49152>{});
      if constexpr (i == 9) {
        const u32 s0 = sMG, s1 = sMG + 24576, s2 = sMG + 49152, s3 = sMG + 73728;
        ld16s<0>(ga_[0], vl16, rWS, s0); ld16s<0>(ga_[1], vl16, rWS, s1); ld16s<0>(ga_[2], vl16, rWS, s2); ld16s<0>(ga_[3], vl16, rWS, s3);
      }
    });
    float t[5];
#pragma unroll
    for (int i = 0; i < 5; ++i) t[i] = row16_sum(sw16_add(u[i], u[i + 5]));
    float v = t[0];
#pragma unroll
    for (int i = 1; i < 5; ++i) v = c16 == i ? t[i] : v;
    auto r = __builtin_amdgcn_permlane32_swap(asu(v), asu(v), false, false);
    const float mean = asf(r[0]) * (1.f / 512.f);
    const float var = fmaxf(asf(r[1]) * (1.f / 512.f) - mean * mean, 0.f);
    float rs = __builtin_amdgcn_rsqf(var + 1e-5f), nm = -mean * rs;
    PIN(rs); PIN(nm);
    STAMP(4);
    { const u32 vo = ((lane >> 3) & 3) * 24576 + (lane & 7) * 8, so = WS_MODG + (1024 + 8 * b) * 8; LD8S(pv, vo, rWS, so, 0); }
    if (w >= 4) {
      const int u = lane >> 2, row = (u & 3) | ((u >> 1) & 4) | ((u << 1) & 8);
      const u32 vo = row * 2752 + (lane & 3) * 16, so = (g & 15) * (32 * 2752) + rcb * 16, mb = L_Z + 49152 + (w & 3) * 4096;
      dma<0, 0>(mb, vo, rW1, so); dma<1024, 16 * 2752>(mb, vo, rW1, so); dma<2048, 0>(mb, vo, rW3, so); dma<3072, 16 * 2752>(mb, vo, rW3, so);
    } else {
      const int u = lane >> 1, row = (u & 3) | ((u >> 1) & 4) | ((u << 1) & 8) | (u & 16);
      const u32 vo = row * 1024 + (lane & 1) * 16, so = g * 32768 + j * 128 + w * 32, mb = L_Z + 65536 + w * 2048;
      dma<0, 0>(mb, vo, rW2, so); dma<1024, 0>(mb, vo, rW2, so + (g < 11 ? 32 * 32768 : 0));
      dma<0, 0>(76800 + w * 1024, vo, rWO, (g & 15) * 32768 + j * 128 + w * 32);
    }
    sfor<0, 10>([&](auto I) {
      constexpr int i = decltype(I)::value;
      if constexpr (i == 2) stage_b1(IC<3>{}, IC<L_Z>{});
      if constexpr (i == 6) stage_b1(IC<4>{}, IC<L_Z + 24576>{});
      const float a = rdl(rs, (i / 5) * 16 + i % 5), c = rdl(nm, (i / 5) * 16 + i % 5);
#pragma unroll
      for (int e = 0; e < 4; ++e) { xn[i][e] = unp(xr[i][e]) * a + c; PIN(xn[i][e]); }
    });
  }
  const u32 tag = __builtin_amdgcn_readfirstlane(ep) + 1u;
  u32 m1; asm("s_lshr_b32 %0, 1, %1" : "=s"(m1) : "s"(w) : "scc");
#define RAISE(fo) raise_flag(rWS, (fo), tag, vl16, m1)
#define SIGNAL(idx, fo) signal_flag(rWS, L_CNT + 4 * (idx), (fo), tag)
  STAMP(5);
  asm volatile("s_waitcnt vmcnt(9)" : "+v"(ga_[0]), "+v"(ga_[1]), "+v"(ga_[2]), "+v"(ga_[3]), "+v"(pv)::"memory");
  auto gsum = [&](v4u (&a)[4], f32x2& o) -> u32 {
    o.x = (asf(a[0].x) + asf(a[1].x)) + (asf(a[2].x) + asf(a[3].x)); o.y = (asf(a[0].z) + asf(a[1].z)) + (asf(a[2].z) + asf(a[3].z));
    return ((a[0].y ^ tag) | (a[0].w ^ tag) | (a[1].y ^ tag) | (a[1].w ^ tag)) | ((a[2].y ^ tag) | (a[2].w ^ tag) | (a[3].y ^ tag) | (a[3].w ^ tag));
  };
  auto gather = [&](u32 voff, u32 soff, f32x2& o) {
    for (u32 sp = 0; sp < SPIN_CAP; ++sp) {
      asm volatile("" ::: "memory");
      v4u a[4];
#pragma unroll
      for (int q = 0; q < 4; ++q) a[q] = __builtin_amdgcn_raw_buffer_load_b128(rWS, voff, soff + q * 24576, 16);
      if (!__builtin_amdgcn_ballot_w64(gsum(a, o) != 0u)) break;
    }
  };
  const float one = w >= 4 ? 1.f : 0.f;
  {
    f32x2 mA;
    if (__builtin_amdgcn_ballot_w64(gsum(ga_, mA) != 0u)) gather(vl16, sMG, mA);
    *(LAS f32x2*)(L + L_MODA + w * 512 + (vl16 >> 1)) = mA + one;
  }
  BAR();
  if (w == 7) {
    const u32 vo = ((lane >> 3) & 3) * 24576 + (lane & 7) * 8, so = WS_MODG + (1024 + 8 * b) * 8;
    for (u32 sp = 0; sp < SPIN_CAP && __builtin_amdgcn_ballot_w64(pv.y != tag); ++sp) {
      asm volatile("" ::: "memory");
      pv = __builtin_amdgcn_raw_buffer_load_b64(rWS, vo, so, 16);
    }
    float sv = asf(pv.x);
    sv += dppf<0x128>(sv); sv = sw16_add(sv, sv);
    pv.x = asu(sv);
    __builtin_amdgcn_raw_buffer_store_b64(pv, rWS, (lane & 7) * 8, WS_MODF + (1024 + 8 * b) * 8, 16);
  }
  STAMP(6);
  int vma = L_MODA + 2 * vl16; PIN(vma);
  const f32x4 ms0 = *(LAS f32x4*)(L + vma), ms1 = *(LAS f32x4*)(L + vma + 16);
  const f32x4 ma0 = *(LAS f32x4*)(L + vma + 2048), ma1 = *(LAS f32x4*)(L + vma + 2064);
  int tz0 = tB1 + L_Z; PIN(tz0);
  { int t2 = tB1 + 49152; PIN(t2); read_b1(0, tB1, 0); read_b1(1, tB1, 24576); read_b1(2, t2, 0); }
  {
    VMW(6);
    if (w >= 4) {
      LAS char* a0 = L + L_Z + 49152 + (w & 3) * 4096 + ((lane >> 1) & 1) * 2048 + 64 * ((c16 >> 2) + 4 * (g4 & 1) + 16 * (g4 >> 1)) + 8 * (lane & 1);
#pragma unroll
      for (int ct = 0; ct < 3; ++ct) {
        const int c = min(cs0 + T0 + ct - rcb, 3) * 16;
        const v4u f = cat(trd(a0 + c), trd(a0 + c + 512));
        const u32 sf = WS_B4T + ((j * 16 + (g & 15)) * 24 + T0 + ct) * 1024;
        asm volatile("buffer_store_dwordx4 %0, %1, %2, %3 offen sc1\n\ts_nop 1" ::"v"(f), "v"(vl16), "s"(rWS), "s"(sf) : "memory");
      }
    }
    else {
      LAS char* a0 = L + L_Z + 65536 + w * 2048 + 32 * ((c16 >> 2) + 4 * (g4 & 1) + 16 * (g4 >> 1)) + 8 * (lane & 3);
#pragma unroll
      for (int i = 0; i < 2; ++i) {
        const v4u f = cat(trd(a0 + i * 1024), trd(a0 + i * 1024 + 256));
        const u32 sf = WS_B5T + ((j * 43 + g + (i && g < 11 ? 32 : 0)) * 4 + w) * 1024;
        asm volatile("buffer_store_dwordx4 %0, %1, %2, %3 offen sc1\n\ts_nop 1" ::"v"(f), "v"(vl16), "s"(rWS), "s"(sf) : "memory");
      }
      LAS char* a1 = L + 76800 + w * 1024 + 32 * ((c16 >> 2) + 4 * (g4 & 1) + 16 * (g4 >> 1)) + 8 * (lane & 3);
      const v4u f = cat(trd(a1), trd(a1 + 256));
      const u32 sf = WS_B3T + ((j * 16 + (g & 15)) * 4 + w) * 1024;
      asm volatile("buffer_store_dwordx4 %0, %1, %2, %3 offen sc1\n\ts_nop 1" ::"v"(f), "v"(vl16), "s"(rWS), "s"(sf) : "memory");
    }
  }
  BAR();
  STAMP(7);
  {
    int hw1 = (hw0 ^ 128) + 8192; PIN(hw1);
    sfor<0, 10>([&](auto I) {
      constexpr int i = decltype(I)::value;
      v4u o;
      o.x = pk2(xn[i][0] * ma0.xy + ms0.xy); o.y = pk2(xn[i][1] * ma0.zw + ms0.zw);
      o.z = pk2(xn[i][2] * ma1.xy + ms1.xy); o.w = pk2(xn[i][3] * ma1.zw + ms1.zw);
      int bp = (i >> 1) + rx2; bp = bp >= 5 ? bp - 5 : bp;
      *(LAS v4u*)(L + ((i & 1) ? hw1 : hw0) + bp * 16384) = o;
      SCHED();
    });
  }
  VMW(0);
  BAR();
  RAISE(WS_RFLAG + (j * 32 + g) * 4);
  u32 flr;
  STAMP(8);
  __builtin_amdgcn_s_setprio(2);

  const f32x4 z4 = {0.f, 0.f, 0.f, 0.f};
#define AFRAG(off, ks) (*(LAS v4u*)(L + afo[(ks) & 3] + ((off) + ((ks) >> 2) * 256)))
  const int ch0 = cs0 + 3 * w;
  constexpr int ND = NODMA ? 0 : 1;
  v4u b4[48];
  const u32 sB4 = WS_B4T + (j * 16 * 24 + 3 * w) * 1024;
  auto load_b4 = [&](auto NC) {
    constexpr int n = decltype(NC)::value, ks = n / 3, ct = n % 3;
    const u32 so = sB4 + ((ks + 2 * rotp) & 15) * 24576;
    v4u& r = b4[n];
    if constexpr (ct == 0) LD16S(r, vl16, rWS, so, 0); else if constexpr (ct == 1) LD16S(r, vl16, rWS, so, 1024); else LD16S(r, vl16, rWS, so, 2048);
  };
  auto landed_b4 = [&](auto AC, auto BC) {
#pragma unroll
    for (int i = decltype(AC)::value; i < decltype(BC)::value; ++i) { v4u& r = b4[i]; PIN(r); }
  };
  v4u b3[16];
  auto load_b3 = [&](auto NC) { constexpr int n = decltype(NC)::value; ld16s<(n & 3) * 1024>(b3[n], vl16, rWS, WS_B3T + (j * 16 + 4 * kq + (n >> 2)) * 4096); };
  v4u gb_, gg_;
  u32 vgg, fl0, vRow;
  int pw, pr;
  f32x4 aKh = z4, aVh = z4, aQ = z4, aK = z4, aV = z4, bQ = z4, bK = z4, bV = z4;
  {
    v4u ah = AFRAG(0, 0), a1 = AFRAG(16384, 0), a2 = AFRAG(32768, 0);
    int l3, c3, g3; f32x2 mB, mG;
    auto job = [&](auto NC) {
      constexpr int n = decltype(NC)::value;
      if constexpr (n == 3) { l3 = opq(vl16) >> 4; c3 = l3 & 15; g3 = l3 >> 4; PIN(c3); PIN(g3); vgg = (vl16 < 512 ? 8192 : 20480 - 512) + 512 * j + vl16; PIN(vgg); }
      if constexpr (n == 7) { vRow = (8 * w + (l3 >> 3)) * 1024 + (l3 & 7) * 16; PIN(vRow); }
      if constexpr (n == 8) { pr = (8 * w + (l3 >> 3)) * 256 + (((2 * (l3 & 7)) ^ ((l3 >> 3) & 7)) << 4); PIN(pr); }
      if constexpr (n == 10) { pw = kq * 16384 + (32 * mh + c3) * 256 + ((g3 ^ (c3 & 3)) << 4) + ((c3 & 4) << 4); PIN(pw); }
      if constexpr (n == 31) { mB.x = asf(gb_.x) + one; PIN(mB.x); mB.y = asf(gb_.z) + one; PIN(mB.y); *(LAS f32x2*)(L + L_MODA + w * 512 + (vl16 >> 1)) = mB; }
      if constexpr (n == 32) { mG.x = asf(gg_.x); mG.y = asf(gg_.z); *(LAS f32x2*)(L + L_GAB + (vl16 >> 1)) = mG; }
      if constexpr (n == 34) { fl0 = (gb_.y ^ tag) | (gb_.w ^ tag) | (gg_.y ^ tag) | (gg_.w ^ tag); PIN(fl0); }
      if constexpr (n == 7) { const u32 s0 = WS_MODF + 12288 + w * 1024; LD16S(gb_, vl16, rWS, s0, 0); }
      if constexpr (n == 9) { const u32 s0 = WS_MODF; LD16S(gg_, vgg, rWS, s0, 0); }
      if constexpr (n < 6 && !(n & 1)) b1_piece(IC<5>{}, IC<L_Z + 49152>{}, IC<n / 2>{});
      if constexpr (n == 6) arrive(L_CNT + 8);
      if constexpr (n == 10) await8(L_CNT + 8);
      if constexpr (n >= 10 && n < 22 && !(n & 1)) b1_piece(IC<6 + (n - 10) / 6>{}, IC<L_Z + ((n - 10) / 6) * 24576>{}, IC<((n - 10) / 2) % 3>{});
      if constexpr (n == 21) { const u32 vr = (vl16 >> 2) & 124, sr = WS_RFLAG + j * 128; LD4S(flr, vr, rWS, sr); }
      if constexpr (n == 26) {
        asm volatile("s_waitcnt vmcnt(0)" : "+v"(gb_), "+v"(gg_), "+v"(flr)::"memory");
        arrive(L_CNT + 12);
        const u32 vr = (vl16 >> 2) & 124, sr = WS_RFLAG + j * 128; u32 cnt = SPIN_CAP;
        asm volatile("1:\n\tv_cmp_ne_u32 vcc, %5, %0\n\ts_cbranch_vccz 2f\n\tbuffer_load_dword %0, %2, %3, %4 offen sc1\n\ts_waitcnt vmcnt(0)\n\t"
                     "s_sub_u32 %1, %1, 1\n\ts_cbranch_scc0 1b\n\t2:" : "+v"(flr), "+s"(cnt) : "v"(vr), "s"(rWS), "s"(sr), "s"(tag) : "vcc", "scc", "memory");
      }
      if constexpr (n == 28) { await8(L_CNT + 12); int tz2 = tz0 + 49152; PIN(tz2); read_b1(5, tz2, 0); }
      if constexpr (n == 32) read_b1(6, tz0, 0);
      if constexpr (n == 37) read_b1(7, tz0, 24576);
      if constexpr (n >= 27 && n < 47 && (n & 1)) load_b3(IC<(n - 27) / 2>{});
    };
    sfor<0, 16>([&](auto KS) {
      constexpr int ks = decltype(KS)::value;
      KSTAMP(1, ks);
      if constexpr (ks == 0) read_b1(3, tz0, 0);
      if constexpr (ks == 1) read_b1(4, tz0, 24576);
      SCHED();
      v4u nh = ah, n1 = a1, n2 = a2;
      aKh = mfma32(bw[ks * 3 + 1], ah, aKh);
      aVh = mfma32(ah, bw[ks * 3 + 2], aVh);
      if constexpr (ks < 15) nh = AFRAG(0, ks + 1);
      job(IC<3 * ks>{});
      SCHED();
      aQ = mfma32(bw[ks * 3 + 0], a1, aQ);
      aK = mfma32(bw[ks * 3 + 1], a1, aK);
      aV = mfma32(a1, bw[ks * 3 + 2], aV);
      if constexpr (ks < 15) n1 = AFRAG(16384, ks + 1);
      job(IC<3 * ks + 1>{});
      SCHED();
      bQ = mfma32(bw[ks * 3 + 0], a2, bQ);
      bK = mfma32(bw[ks * 3 + 1], a2, bK);
      bV = mfma32(a2, bw[ks * 3 + 2], bV);
      if constexpr (ks < 15) n2 = AFRAG(32768, ks + 1);
      job(IC<3 * ks + 2>{});
      ah = nh; a1 = n1; a2 = n2;
      SCHED();
    });
  }
  __builtin_amdgcn_s_setprio(0);
  STAMP(9); KSTAMP(1, 16);
  {
    const u32 sOst = WS_O + ((g * 4 + 2 * blk2) * 16 + 2 * j + (hd >> 1)) * 1024;
    const v2u k1p = pk4(aK), v1p = pk4(aV);
    const v2u o0 = attend(pk4(aKh), pk4(aVh), k1p, v1p, pk4(aQ), am0, am1, ahm, g == 0 && blk2 == 0);
    const v2u o1 = attend(k1p, v1p, pk4(bK), pk4(bV), pk4(bQ), am0, am1, ahm, false);
    const auto sx = __builtin_amdgcn_permlane16_swap(o0.x, o1.x, false, false), sy = __builtin_amdgcn_permlane16_swap(o0.y, o1.y, false, false);
    const v4u ov = {sx[0], sy[0], sx[1], sy[1]};
    asm volatile("buffer_store_dwordx4 %0, %1, %2, %3 offen sc1\n\ts_nop 1" ::"v"(ov), "v"(vOst), "s"(rWS), "s"(sOst) : "memory");
  }
  if (__builtin_amdgcn_ballot_w64(fl0 != 0u)) {
    for (u32 sp = 0; sp < SPIN_CAP; ++sp) {
      asm volatile("" ::: "memory");
      gb_ = __builtin_amdgcn_raw_buffer_load_b128(rWS, vl16, WS_MODF + 12288 + w * 1024, 16);
      gg_ = __builtin_amdgcn_raw_buffer_load_b128(rWS, vgg, WS_MODF, 16);
      if (!__builtin_amdgcn_ballot_w64(((gb_.y ^ tag) | (gb_.w ^ tag) | (gg_.y ^ tag) | (gg_.w ^ tag)) != 0u)) break;
    }
    const f32x2 mB = {asf(gb_.x) + one, asf(gb_.z) + one}, mG = {asf(gg_.x), asf(gg_.z)};
    *(LAS f32x2*)(L + L_MODA + w * 512 + (vl16 >> 1)) = mB;
    *(LAS f32x2*)(L + L_GAB + (vl16 >> 1)) = mG;
  }
  STAMP(10);
  VMW(0);
  BAR();
  RAISE(flag_off(0, g, 0) + j * 128);
  STAMP(11);
  if (b == 0 && w == 7) {
    for (u32 sp = 0; sp < SPIN_CAP; ++sp) {
      asm volatile("" ::: "memory");
      const v4u v = __builtin_amdgcn_raw_buffer_load_b128(rWS, vl16, WS_RFLAG, 16);
      if (__all(v.x == tag && v.y == tag && v.z == tag && v.w == tag)) break;
    }
    __builtin_amdgcn_raw_buffer_store_b32(tag, rWS, 0, WS_EPOCH, 16);
  }

  v4u xv;
  { const u32 sx = g * 65536 + j * 128; LD16(xv, vRow, rX, sx, 0); }
  sfor<10, 16>(load_b3);
  const u64 care2 = 0x0101010101010101ull * (3u << (2 * kq));
  poll_flags(rWS, vfl, flag_off(0, g, 0), care2, tag);
  PIN(xv);
  v4u af[8];
  {
    const u32 so = WS_O + ((g * 4 + 2 * mh) * 16 + 4 * kq) * 1024, so1 = so + 16384;
    LD16S(af[0], vl16, rWS, so, 0); LD16S(af[1], vl16, rWS, so, 1024); LD16S(af[2], vl16, rWS, so, 2048); LD16S(af[3], vl16, rWS, so, 3072);
    LD16S(af[4], vl16, rWS, so1, 0); LD16S(af[5], vl16, rWS, so1, 1024); LD16S(af[6], vl16, rWS, so1, 2048); LD16S(af[7], vl16, rWS, so1, 3072);
  }
  STAMP(12);
  sfor<0, 2>([&](auto M) {
    constexpr int m = decltype(M)::value;
    asm volatile("s_waitcnt vmcnt(%c4)" : "+v"(af[4 * m]), "+v"(af[4 * m + 1]), "+v"(af[4 * m + 2]), "+v"(af[4 * m + 3]) : "i"(m == 0 ? 4 : 2) : "memory");
    if constexpr (m == 0) {
#pragma unroll
      for (int i = 0; i < 16; ++i) PIN(b3[i]);
    }
    f32x4 acc[4] = {z4, z4, z4, z4};
    sfor<0, 4>([&](auto SC) {
      constexpr int sx = decltype(SC)::value;
#pragma unroll
      for (int n = 0; n < 4; ++n) acc[n] = mfma32(b3[sx * 4 + n], af[4 * m + sx], acc[n]);
      if constexpr (sx & 1) load_b4(IC<2 * m + (sx >> 1)>{});
      SCHED();
    });
#pragma unroll
    for (int n = 0; n < 4; ++n) *(LAS f32x4*)(L + ((n & 1) ? (pw ^ 64) : pw) + (m * 4096 + (n >> 1) * 128)) = acc[n];
  });
  BAR();
  STAMP(13);
  v4u x1p;
  {
    f32x4 y0 = *(LAS f32x4*)(L + pr), y1 = *(LAS f32x4*)(L + (pr ^ 16));
#pragma unroll
    for (int q = 1; q < 4; ++q) { y0 += *(LAS f32x4*)(L + pr + q * 16384); y1 += *(LAS f32x4*)(L + (pr ^ 16) + q * 16384); }
    const int ga = L_GAB + ((vl16 & 112) << 1);
    const f32x4 g0 = *(LAS f32x4*)(L + ga), g1 = *(LAS f32x4*)(L + ga + 16);
    const f32x4 x0 = f32x4{blo(xv.x), bhi(xv.x), blo(xv.y), bhi(xv.y)} + g0 * y0, x1 = f32x4{blo(xv.z), bhi(xv.z), blo(xv.w), bhi(xv.w)} + g1 * y1;
    { const v4u o = cat(pk4(x0), pk4(x1)); x1p = o; const u32 so = WS_X1 + g * 65536 + j * 128;
      asm volatile("buffer_store_dwordx4 %0, %1, %2, %3 offen sc1\n\ts_nop 1" ::"v"(o), "v"(vRow), "s"(rWS), "s"(so) : "memory"); }
    const f32x4 sv = x0 + x1;
    float mean = (sv.x + sv.y) + (sv.z + sv.w);
    mean += dppf<0xB1>(mean); mean += dppf<0x4E>(mean); mean += dppf<0x141>(mean);
    mean *= 1.f / 64.f;
    const f32x4 d0 = x0 - mean, d1 = x1 - mean, dd = d0 * d0 + d1 * d1;
    float m2 = (dd.x + dd.y) + (dd.z + dd.w);
    m2 += dppf<0xB1>(m2); m2 += dppf<0x4E>(m2); m2 += dppf<0x141>(m2);
    v2u st; st.x = asu(mean); st.y = asu(m2);
    __builtin_amdgcn_raw_buffer_store_b64(st, rWS, (vRow >> 10) << 3, WS_STAT + g * 4096 + j * 512, 16);
  }
  STAMP(14);
  VMW(0);
  BAR();
  RAISE(flag_off(1, g, 0) + j * 128);
  sfor<4, 9>(load_b4);
  STAMP(15);

  u32 hmask[3];
#pragma unroll
  for (int ct = 0; ct < 3; ++ct)
    asm("s_cmp_lt_u32 %1, %2\n\ts_cselect_b32 %0, -1, 0" : "=s"(hmask[ct]) : "s"((u32)(ch0 + ct)), "s"((u32)cend) : "scc");
#pragma unroll
  for (int i = 0; i < 4; ++i) { afo[i] -= blk2 * 32768; PIN(afo[i]); }
  int vm2 = L_MODA + 2 * vl16; PIN(vm2);
  int h2w = w * 8192 + (((vl16 - 128 * rotp) & 1023) ^ ((8 * w & 15) << 4)); PIN(h2w);
  const f32x4 ls0 = *(LAS f32x4*)(L + vm2), ls1 = *(LAS f32x4*)(L + vm2 + 16), la0 = *(LAS f32x4*)(L + vm2 + 2048), la1 = *(LAS f32x4*)(L + vm2 + 2064);
  poll_flags(rWS, vfl, flag_off(1, g, 0), ~0ull, tag);
  landed_b4(IC<0>{}, IC<9>{});
  {
    v4u x1v[8]; v2u stv;
    const u32 so = WS_X1 + g * 65536 + w * 8192, so2 = so + 4096;
    { const u32 vs = ((vl16 & 112) << 5) + ((vl16 >> 7) << 3), ss = WS_STAT + g * 4096 + w * 64; LD8S(stv, vs, rWS, ss, 0); }
    LD16S(x1v[0], vl16, rWS, so, 0); LD16S(x1v[1], vl16, rWS, so, 1024); LD16S(x1v[2], vl16, rWS, so, 2048); LD16S(x1v[3], vl16, rWS, so, 3072);
    LD16S(x1v[4], vl16, rWS, so2, 0); LD16S(x1v[5], vl16, rWS, so2, 1024); LD16S(x1v[6], vl16, rWS, so2, 2048); LD16S(x1v[7], vl16, rWS, so2, 3072);
    sfor<9, 12>(load_b4);
    asm volatile("s_waitcnt vmcnt(11)" : "+v"(stv)::"memory");
    const float mj = asf(stv.x);
    float mean = mj;
    mean += dppf<0xB1>(mean); mean += dppf<0x4E>(mean); mean += dppf<0x141>(mean);
    mean *= 0.125f;
    const float dm = mj - mean;
    float m2 = asf(stv.y) + 64.f * dm * dm;
    m2 += dppf<0xB1>(m2); m2 += dppf<0x4E>(m2); m2 += dppf<0x141>(m2);
    const float l2r = __builtin_amdgcn_rsqf(m2 * (1.f / 512.f) + 1e-5f), l2n = -mean * l2r;
    sfor<0, 8>([&](auto I) {
      constexpr int i = decltype(I)::value;
      v4u& xx = x1v[i];
      asm volatile("s_waitcnt vmcnt(%c1)" : "+v"(xx) : "i"(10 - i) : "memory");
      const float rs = rdl(l2r, 8 * i), nn = rdl(l2n, 8 * i);
      v4u o;
      o.x = pk2((unp(xx.x) * rs + nn) * la0.xy + ls0.xy); o.y = pk2((unp(xx.y) * rs + nn) * la0.zw + ls0.zw);
      o.z = pk2((unp(xx.z) * rs + nn) * la1.xy + ls1.xy); o.w = pk2((unp(xx.w) * rs + nn) * la1.zw + ls1.zw);
      *(LAS v4u*)(L + (h2w ^ (i << 4)) + i * 1024) = o;
    });
  }
  BAR();
  STAMP(16);
  __builtin_amdgcn_s_setprio(2);
  const int kb5 = 5 * w + min(w, 3);
  u64 care5 = 0x0101010101010101ull * ((0x80c06030180c0603ull >> (8 * w)) & 255u);
  asm volatile("" : "+s"(care5));
  v4u b5[24];
  const u32 sB5 = WS_B5T + (j * 43 + kb5) * 4096, d5 = (w + 5) >> 3;
  auto load_b5 = [&](auto NC) {
    constexpr int n = decltype(NC)::value, sx = n / 4, t = n % 4;
    ld16s<t * 1024>(b5[n], vl16, rWS, sB5 + (sx == 5 ? (5 - d5) * 4096 : sx * 4096));
  };
  f32x4 acc4[3][4];
#pragma unroll
  for (int ct = 0; ct < 3; ++ct)
#pragma unroll
    for (int m = 0; m < 4; ++m) acc4[ct][m] = z4;
  u32 vH; int pw5;
  {
    v4u a[4] = {AFRAG(0, 0), AFRAG(16384, 0), AFRAG(32768, 0), AFRAG(49152, 0)};
    sfor<0, 16>([&](auto KS) {
      constexpr int ks = decltype(KS)::value;
      KSTAMP(3, ks);
      if constexpr (ks >= 3 && ks < 13) { VMW(9); landed_b4(IC<3 * ks>{}, IC<3 * ks + 3>{}); }
      if constexpr (ks >= 13) { VMW(9); landed_b4(IC<3 * ks>{}, IC<3 * ks + 3>{}); }
      v4u nx[4] = {a[0], a[1], a[2], a[3]};
      sfor<0, 4>([&](auto MC) {
        constexpr int m = decltype(MC)::value;
        if constexpr (ks < 15) nx[m] = AFRAG(16384 * m, ks + 1);
        if constexpr (m < 3) { if constexpr (ks < 12) load_b4(IC<12 + 3 * ks + m>{}); else load_b5(IC<3 * (ks - 12) + m>{}); }
        SCHED();
#pragma unroll
        for (int ct = 0; ct < 3; ++ct) acc4[ct][m] = mfma32(b4[ks * 3 + ct], a[m], acc4[ct][m]);
        if constexpr (ks == 8 && m == 0) { vH = (vl16 & 255) + ((vl16 >> 5) & 8) + (vl16 >> 9) * (172 * 256); PIN(vH); }
        if constexpr (ks == 9 && m == 0) { const int l5 = opq(vl16) >> 4, c5 = l5 & 15, g5 = l5 >> 4;
          pw5 = w * 16384 + c5 * 256 + ((c5 & 4) << 4) + ((g5 ^ (c5 & 3)) << 4); PIN(pw5); }
        SCHED();
      });
#pragma unroll
      for (int m = 0; m < 4; ++m) a[m] = nx[m];
    });
  }
#pragma unroll
  for (int ct = 0; ct < 3; ++ct)
#pragma unroll
    for (int m = 0; m < 4; ++m) PIN(acc4[ct][m]);
  __builtin_amdgcn_s_setprio(0);
  STAMP(17); KSTAMP(3, 16);
#pragma unroll
  for (int ct = 0; ct < 3; ++ct)
#pragma unroll
    for (int mp = 0; mp < 2; ++mp) {
      f32x4 G, U, R;
#pragma unroll
      for (int e = 0; e < 4; ++e) {
        auto r = __builtin_amdgcn_permlane32_swap(asu(acc4[ct][2 * mp][e]), asu(acc4[ct][2 * mp + 1][e]), false, false);
        G[e] = asf(r[0]); U[e] = asf(r[1]);
      }
#pragma unroll
      for (int e = 0; e < 4; ++e) R[e] = qp(qp(G[e] * U[e]) * __builtin_amdgcn_rcpf(1.f + __builtin_amdgcn_exp2f(qp(G[e] * -1.4426950408889634f))));
      const v2u o = pk4(R);
      const u32 so = WS_HH + ((g * 4 + 2 * mp) * 172 + ch0 + ct) * 256;
      asm volatile("s_mov_b32 exec_lo, %4\n\ts_mov_b32 exec_hi, %4\n\tbuffer_store_dwordx2 %0, %1, %2, %3 offen sc1\n\ts_mov_b64 exec, -1"
                   ::"v"(o), "v"(vH), "s"(rWS), "s"(so), "s"(hmask[ct]) : "memory");
    }
  STAMP(18);
  sfor<12, 16>(load_b5);
  VMW(4);
  BAR();
  RAISE(flag_off(2, g, 0) + j * 128);
  STAMP(19);
  sfor<16, 22>(load_b5);
  poll_flags(rWS, vfl, flag_off(2, g, 0), care5, tag);
#pragma unroll
  for (int i = 0; i < 22; ++i) PIN(b5[i]);
  v4u hf[16];
  auto load_h = [&](auto NC) {
    constexpr int n = decltype(NC)::value, sx = n / 4, m = n % 4;
    ld16s<0>(hf[n & 15], vl16, rWS, WS_HH + ((g * 4 + m) * 172 + 4 * kb5) * 256 + (sx == 5 ? (5 - d5) * 1024 : sx * 1024));
  };
  sfor<0, 16>(load_h);
  sfor<22, 24>(load_b5);
  STAMP(20);
  {
    f32x4 acc[4][4];
#pragma unroll
    for (int m = 0; m < 4; ++m)
#pragma unroll
      for (int n = 0; n < 4; ++n) acc[m][n] = z4;
    sfor<0, 6>([&](auto SC) {
      constexpr int sx = decltype(SC)::value, r = (sx & 3) * 4;
      constexpr int nw = sx == 0 ? 14 : sx == 1 ? 14 : sx == 2 ? 14 : sx == 3 ? 10 : sx == 4 ? 4 : 0;
      asm volatile("s_waitcnt vmcnt(%c4)" : "+v"(hf[r]), "+v"(hf[r + 1]), "+v"(hf[r + 2]), "+v"(hf[r + 3]) : "i"(nw) : "memory");
      if constexpr (sx == 4) { PIN(b5[22]); PIN(b5[23]); }
      if constexpr (sx == 5) { if (w >= 3) { hf[r] = v4u{0u, 0u, 0u, 0u}; hf[r + 1] = v4u{0u, 0u, 0u, 0u}; hf[r + 2] = v4u{0u, 0u, 0u, 0u}; hf[r + 3] = v4u{0u, 0u, 0u, 0u}; } }
      sfor<0, 4>([&](auto MC) {
        constexpr int m = decltype(MC)::value;
#pragma unroll
        for (int n = 0; n < 4; ++n) acc[m][n] = mfma32(b5[sx * 4 + n], hf[r + m], acc[m][n]);
        SCHED();
        if constexpr (sx < 2) load_h(IC<16 + 4 * sx + m>{});
        SCHED();
      });
    });
#pragma unroll
    for (int m = 0; m < 4; ++m)
#pragma unroll
      for (int n = 0; n < 4; ++n) *(LAS f32x4*)(L + ((n & 1) ? (pw5 ^ 64) : pw5) + (m * 4096 + (n >> 1) * 128)) = acc[m][n];
  }
  BAR();
  STAMP(21);
  {
    const int l6 = opq(vl16) >> 4;
    const int p0 = opq((8 * w + (l6 >> 3)) * 256 + (((2 * (l6 & 7)) ^ ((l6 >> 3) & 7)) << 4)), p1 = opq(p0 + 65536);
    f32x4 y0 = *(LAS f32x4*)(L + p0), y1 = *(LAS f32x4*)(L + (p0 ^ 16));
#pragma unroll
    for (int q = 1; q < 8; ++q) { const int pb = q < 4 ? p0 : p1; y0 += *(LAS f32x4*)(L + pb + (q & 3) * 16384); y1 += *(LAS f32x4*)(L + (pb ^ 16) + (q & 3) * 16384); }
    const int gb = L_GAB + 256 + (l6 & 7) * 32;
    const f32x4 g0 = *(LAS f32x4*)(L + gb), g1 = *(LAS f32x4*)(L + gb + 16);
    const f32x4 o0 = f32x4{blo(x1p.x), bhi(x1p.x), blo(x1p.y), bhi(x1p.y)} + g0 * y0, o1 = f32x4{blo(x1p.z), bhi(x1p.z), blo(x1p.w), bhi(x1p.w)} + g1 * y1;
    const v4u o = cat(pk4(o0), pk4(o1));
    const u32 vo = (8 * w + (l6 >> 3)) * 1024 + (l6 & 7) * 16, so = g * 65536 + j * 128;
    asm volatile("buffer_store_dwordx4 %0, %1, %2, %3 offen sc1\n\ts_nop 1" ::"v"(o), "v"(vo), "s"(rOUT), "s"(so) : "memory");
  }
  STAMP(22);
  STAMP_DUMP();
}
}

extern "C" void kernel_launch(void* const* d_in, const int* in_sizes, int n_in, void* d_out, int out_size, void* d_ws, size_t ws_size,
                              hipStream_t stream) {
  Params p;
  p.x = (const u16*)d_in[0]; p.wqkv = (const u16*)d_in[1]; p.wo = (const u16*)d_in[2];
  p.w1 = (const u16*)d_in[4]; p.w3 = (const u16*)d_in[5]; p.w2 = (const u16*)d_in[6];
  p.c = (const u16*)d_in[7]; p.wmod = (const u16*)d_in[8];
  p.out = (u16*)d_out; p.ws = (char*)d_ws;
  (void)hipFuncSetAttribute((const void*)fused_block, hipFuncAttributeMaxDynamicSharedMemorySize, L_TOTAL);
  fused_block<<<256, 512, L_TOTAL, stream>>>(p);
}
```

```cpp
#include <hip/hip_runtime.h>
#include <stdint.h>

#define KDBG 0
#define NODMA 0
#define CODEWARM 0
#define L2WARM 1
#define EARLYT 0
#define XMAP 2

#define DI __device__ __forceinline__
#define GAS __attribute__((address_space(1)))
#define LAS __attribute__((address_space(3)))
typedef unsigned u32;
typedef unsigned short u16;
typedef unsigned long long u64;
typedef u32 v4u __attribute__((ext_vector_type(4)));
typedef u32 v2u __attribute__((ext_vector_type(2)));
typedef float f32x4 __attribute__((ext_vector_type(4)));
typedef float f32x2 __attribute__((ext_vector_type(2)));
typedef __bf16 bf16x8 __attribute__((ext_vector_type(8)));
typedef __bf16 bf16x2 __attribute__((ext_vector_type(2)));
typedef short s16x4 __attribute__((ext_vector_type(4)));
typedef __amdgpu_buffer_rsrc_t rsrc_t;
typedef GAS u32 gu32;
typedef GAS u64 gu64;
#define RLX_AGENT __ATOMIC_RELAXED, __HIP_MEMORY_SCOPE_AGENT

namespace {
constexpr int S_ = 2048, D_ = 512, F_ = 1376;
constexpr u32 WS_EPOCH = 0;
constexpr u32 WS_FLAG = 32768;
constexpr u32 WS_STAT = 262144;
constexpr u32 WS_MODG = 524288;
constexpr u32 WS_MODF = 655360;
constexpr u32 WS_O = 1u << 20;
constexpr u32 WS_X1 = 3u << 20;
constexpr u32 WS_HH = 5u << 20;
constexpr u32 WS_RFLAG = 8192;
constexpr u32 WS_B4T = 11u << 20;
constexpr u32 WS_B5T = 14u << 20;
constexpr u32 WS_B3T = (15u << 20) + (512u << 10);
constexpr u32 WS_DBG = 16u << 20;
constexpr u32 WS_BYTES = 32u << 20;
constexpr int L_XY = 0;
constexpr int L_Z = 81920;
constexpr int L_MISC = 158720;
constexpr int L_TOTAL = 163840;
constexpr int L_RED = 73728;
constexpr int L_MODA = L_MISC;
constexpr int L_CNT = L_MISC + 4096;
constexpr int L_GAB = L_MISC + 4096 + 64;
constexpr int L_JUNK = L_MISC + 4096 + 64 + 512;
constexpr int L_STAMP = 156672;
constexpr u32 SPIN_CAP = 1u << 18;
constexpr u32 CODE_BYTES = 26112;

DI f32x4 mfma32(v4u a, v4u b, f32x4 c) {
  return __builtin_amdgcn_mfma_f32_16x16x32_bf16(__builtin_bit_cast(bf16x8, a), __builtin_bit_cast(bf16x8, b), c, 0, 0, 0);
}
DI f32x4 mfma16(v2u a, v2u b, f32x4 c) {
  return __builtin_amdgcn_mfma_f32_16x16x16bf16_1k(__builtin_bit_cast(s16x4, a), __builtin_bit_cast(s16x4, b), c, 0, 0, 0);
}
DI u32 pk(float lo, float hi) { f32x2 v = {lo, hi}; return __builtin_bit_cast(u32, __builtin_convertvector(v, bf16x2)); }
DI u32 pk2(f32x2 v) { return __builtin_bit_cast(u32, __builtin_convertvector(v, bf16x2)); }
DI v2u pk4(f32x4 v) { v2u r; r.x = pk(v.x, v.y); r.y = pk(v.z, v.w); return r; }
DI float asf(u32 u) { return __builtin_bit_cast(float, u); }
DI u32 asu(float f) { return __builtin_bit_cast(u32, f); }
DI float blo(u32 u) { return asf(u << 16); }
DI float bhi(u32 u) { return asf(u & 0xffff0000u); }
DI f32x2 unp(u32 u) { f32x2 r = {blo(u), bhi(u)}; return r; }
DI float dot2(u32 a, u32 b, float c) { return __builtin_amdgcn_fdot2_f32_bf16(__builtin_bit_cast(bf16x2, a), __builtin_bit_cast(bf16x2, b), c, false); }
DI v2u trd(LAS char* p) { return __builtin_bit_cast(v2u, __builtin_amdgcn_ds_read_tr16_b64_v4i16((LAS s16x4*)p)); }
DI v4u cat(v2u a, v2u b) { v4u r; r.x = a.x; r.y = a.y; r.z = b.x; r.w = b.y; return r; }
template <int MI, int SI> DI void dma(u32 mb, u32 voff, rsrc_t srd, u32 sb) {
  u32 t;
  asm volatile("s_add_u32 m0, %1, %c2\n\ts_add_u32 %0, %3, %c4\n\tbuffer_load_dwordx4 %5, %6, %0 offen lds"
               : "=&s"(t) : "s"(mb), "i"(MI), "s"(sb), "i"(SI), "v"(voff), "s"(srd) : "scc");
}
DI void touch(u32 voff, rsrc_t srd, u32 sb) {
  asm volatile("s_mov_b32 m0, %c0\n\ts_nop 0\n\tbuffer_load_dword %1, %2, %3 offen sc1 lds" ::"i"(L_JUNK), "v"(voff), "s"(srd), "s"(sb));
}
#define BAR() asm volatile("s_waitcnt lgkmcnt(0)\n\ts_barrier" ::: "memory")
#define VMW(n) asm volatile("s_waitcnt vmcnt(" #n ")" ::: "memory")
#define LGW() asm volatile("s_waitcnt lgkmcnt(0)" ::: "memory")
#define SCHED() __builtin_amdgcn_sched_barrier(0)
#define LD16(dst, voff, srd, soff, IMM) asm volatile("buffer_load_dwordx4 %0, %1, %2, %3 offen offset:" #IMM : "=v"(dst) : "v"(voff), "s"(srd), "s"(soff))
#define LD16S(dst, voff, srd, soff, IMM) asm volatile("buffer_load_dwordx4 %0, %1, %2, %3 offen offset:" #IMM " sc1" : "=v"(dst) : "v"(voff), "s"(srd), "s"(soff))
#define LD8(dst, voff, srd, soff, IMM) asm volatile("buffer_load_dwordx2 %0, %1, %2, %3 offen offset:" #IMM : "=v"(dst) : "v"(voff), "s"(srd), "s"(soff))
#define LD8S(dst, voff, srd, soff, IMM) asm volatile("buffer_load_dwordx2 %0, %1, %2, %3 offen offset:" #IMM " sc1" : "=v"(dst) : "v"(voff), "s"(srd), "s"(soff))
#define LD4(dst, voff, srd, soff) asm volatile("buffer_load_dword %0, %1, %2, %3 offen" : "=v"(dst) : "v"(voff), "s"(srd), "s"(soff))
#define LD4S(dst, voff, srd, soff) asm volatile("buffer_load_dword %0, %1, %2, %3 offen sc1" : "=v"(dst) : "v"(voff), "s"(srd), "s"(soff))

template <int IMM> DI void ld16s(v4u& r, u32 v, rsrc_t rs, u32 so) { asm volatile("buffer_load_dwordx4 %0, %1, %2, %3 offen offset:%c4 sc1" : "=v"(r) : "v"(v), "s"(rs), "s"(so), "i"(IMM)); }
template <int CTRL> DI float dppf(float v) { return asf(__builtin_amdgcn_update_dpp(0, asu(v), CTRL, 0xf, 0xf, true)); }
DI float row16_sum(float v) { v += dppf<0x128>(v); v += dppf<0x124>(v); v += dppf<0x122>(v); v += dppf<0x121>(v); return v; }
DI float sw32_add(float a, float b) { auto r = __builtin_amdgcn_permlane32_swap(asu(a), asu(b), false, false); return asf(r[0]) + asf(r[1]); }
DI float sw16_add(float a, float b) { auto r = __builtin_amdgcn_permlane16_swap(asu(a), asu(b), false, false); return asf(r[0]) + asf(r[1]); }
DI float x16_max(float v) { auto r = __builtin_amdgcn_permlane16_swap(asu(v), asu(v), false, false); return fmaxf(asf(r[0]), asf(r[1])); }
DI float x32_max(float v) { auto r = __builtin_amdgcn_permlane32_swap(asu(v), asu(v), false, false); return fmaxf(asf(r[0]), asf(r[1])); }
DI int fresh_lane() { int l; asm volatile("v_mbcnt_lo_u32_b32 %0, -1, 0\n\tv_mbcnt_hi_u32_b32 %0, -1, %0" : "=v"(l)); return l; }
DI float rdl(float v, int l) { return asf(__builtin_amdgcn_readlane(asu(v), l)); }

struct Params {
  const u16 *x, *wqkv, *wo, *w1, *w3, *w2, *c, *wmod;
  u16* out;
  char* ws;
};

#define LOOPSEL 0
#define STAMP(i) do {} while (0)
#define KSTAMP(lp, i) do {} while (0)
#define STAMP_DUMP() do {} while (0)

DI u32 flag_off(int phase, int g, int h) { return WS_FLAG + ((phase * 32 + g) * 2 + h) * 1024; }
DI void poll_flags(rsrc_t rWS, u32 vfl, u32 foff, u64 care, u32 tag) {
  for (u32 sp = 0; sp < SPIN_CAP; ++sp) {
    asm volatile("" ::: "memory");
    const u32 v = __builtin_amdgcn_raw_buffer_load_b32(rWS, vfl, foff, 16);
    if (!(__builtin_amdgcn_ballot_w64(v != tag) & care)) break;
  }
  VMW(0);
}
DI void raise_flag(rsrc_t rWS, u32 foff, u32 vtag, u32 vzero, u32 m1) {
  asm volatile("s_mov_b32 exec_hi, 0\n\ts_mov_b32 exec_lo, %4\n\tbuffer_store_dword %0, %1, %2, %3 offen sc1\n\ts_mov_b64 exec, -1" ::"v"(vtag), "v"(vzero), "s"(rWS), "s"(foff), "s"(m1) : "memory");
}
DI void signal_flag(rsrc_t rWS, u32 cnt_addr, u32 foff, u32 tag) {
  u32 t, a = cnt_addr, one = 1u, vt = tag, vz = 0u;
  asm volatile("s_mov_b64 exec, 1\n\tds_add_rtn_u32 %0, %1, %2\n\ts_waitcnt lgkmcnt(0)\n\tv_cmp_eq_u32 vcc, 7, %0\n\ts_and_b64 exec, exec, vcc\n\t"
               "buffer_store_dword %3, %4, %5, %6 offen sc1\n\ts_mov_b64 exec, -1"
               : "=&v"(t) : "v"(a), "v"(one), "v"(vt), "v"(vz), "s"(rWS), "s"(foff) : "vcc", "scc", "memory");
}
DI void arrive(u32 cnt_addr) {
  u32 a = cnt_addr, one = 1u;
  asm volatile("s_waitcnt lgkmcnt(0)\n\ts_mov_b64 exec, 1\n\tds_add_u32 %0, %1\n\ts_mov_b64 exec, -1" ::"v"(a), "v"(one) : "memory");
}
DI void await8(u32 cnt_addr) {
  u32 a = cnt_addr, t, sv;
  asm volatile("1:\n\tds_read_b32 %0, %2\n\ts_waitcnt lgkmcnt(0)\n\tv_readfirstlane_b32 %1, %0\n\ts_nop 0\n\ts_cmp_lt_u32 %1, 8\n\ts_cbranch_scc1 1b"
               : "=&v"(t), "=&s"(sv) : "v"(a) : "scc", "memory");
}
DI void raise_when8(rsrc_t rWS, u32 cnt_addr, u32 foff, u32 tag, u32 w7) {
  u32 a = cnt_addr, t, sv, vt = tag, vz = 0u;
  asm volatile("s_cmp_eq_u32 %7, 0\n\ts_cbranch_scc1 2f\n\t1:\n\tds_read_b32 %0, %2\n\ts_waitcnt lgkmcnt(0)\n\tv_readfirstlane_b32 %1, %0\n\ts_nop 0\n\ts_cmp_lt_u32 %1, 8\n\ts_cbranch_scc1 1b\n\t"
               "s_mov_b64 exec, 1\n\tbuffer_store_dword %3, %4, %5, %6 offen sc1\n\ts_mov_b64 exec, -1\n\t2:"
               : "=&v"(t), "=&s"(sv) : "v"(a), "v"(vt), "v"(vz), "s"(rWS), "s"(foff), "s"(w7) : "scc", "memory");
}
#define PIN(x) asm volatile("" : "+v"(x))
DI float qp(float r) { PIN(r); return r; }
DI float q_shl(u32 a) { return qp(blo(a)); }
DI float q_hi(u32 a) { return qp(bhi(a)); }
DI float q_fma(float a, float b, float c) { return qp(__builtin_fmaf(a, b, c)); }
DI float q_fmas(float a, float b, float c) { return qp(__builtin_fmaf(a, b, c)); }
DI float q_mul(float a, float b) { return qp(a * b); }
DI float q_mulk(float a) { return qp(a * -1.4426950408889634f); }
DI float q_add1(float a) { return qp(a + 1.f); }
DI float q_exp(float a) { return qp(__builtin_amdgcn_exp2f(a)); }
DI float q_rcp(float a) { return qp(__builtin_amdgcn_rcpf(a)); }
DI u32 q_cvt(float a, float b) { u32 r = pk(a, b); PIN(r); return r; }
DI float q_mov(float a) { return qp(a); }
DI u32 opq(u32 v) { asm volatile("" : "+v"(v)); return v; }

DI float sel(u64 m, float a, float b) { return __builtin_amdgcn_inverse_ballot_w64(m) ? a : b; }
DI v2u attend(v2u kp, v2u vp, v2u kc, v2u vc, v2u q, u64 m0, u64 m1, u32 hm, bool noprev) {
  const f32x4 z = {0.f, 0.f, 0.f, 0.f};
  const f32x4 sp = mfma16(kp, q, z);
  const f32x4 sc = mfma16(kc, q, z);
  const float K2 = 0.25f * 1.4426950408889634f;
  const u64 par = 0xaaaaaaaaaaaaaaaaull;
  float p0 = sel(par, sp[1], sp[0]), p1 = sel(par, sp[3], sp[2]);
  if (noprev) { p0 = -INFINITY; p1 = -INFINITY; }
  const float t0 = sel(m0, sel(par, sc[1], sc[0]), p0), t1 = sel(m1, sel(par, sc[3], sc[2]), p1);
  const float m = x32_max(x16_max(fmaxf(t0, t1)));
  const float mk = -m * K2;
  const float e0 = __builtin_amdgcn_exp2f(__builtin_fmaf(t0, K2, mk)), e1 = __builtin_amdgcn_exp2f(__builtin_fmaf(t1, K2, mk));
  const u32 w0 = pk(e0, e0) & hm, w1 = pk(e1, e1) & hm;
  v4u pf;
  pf.x = __builtin_amdgcn_inverse_ballot_w64(m0) ? 0u : w0; pf.y = __builtin_amdgcn_inverse_ballot_w64(m1) ? 0u : w1;
  pf.z = __builtin_amdgcn_inverse_ballot_w64(m0) ? w0 : 0u; pf.w = __builtin_amdgcn_inverse_ballot_w64(m1) ? w1 : 0u;
  const f32x4 o = mfma32(cat(vp, vc), pf, z);
  float sum = e0 + e1;
  sum = sw16_add(sum, sum); sum = sw32_add(sum, sum);
  const float inv = __builtin_amdgcn_rcpf(sum);
  return pk4(f32x4{qp(o.x * inv), qp(o.y * inv), qp(o.z * inv), qp(o.w * inv)});
}

template <int N> using IC = std::integral_constant<int, N>;
template <int I, int N, class F> DI void sfor(F&& f) { if constexpr (I < N) { f(IC<I>{}); sfor<I + 1, N>(f); } }

__global__ void __launch_bounds__(512, 2) fused_block(Params p) {
  extern __shared__ __attribute__((aligned(16))) char smem[];
  LAS char* L = (LAS char*)smem;
  const int tid = threadIdx.x, lane = tid & 63, w = __builtin_amdgcn_readfirstlane(tid >> 6);
  const int c16 = lane & 15, g4 = lane >> 4;
  const int b = blockIdx.x;
  int g, j;
  if (XMAP == 0) { g = (b & 7) * 4 + ((b >> 3) & 3); j = b >> 5; }
  else if (XMAP == 2) { const int x = b & 7, r = b >> 3; g = (x >> 1) * 8 + (r & 7); j = (x & 1) * 4 + (r >> 3); }
  else { const int x = b & 7, r = b >> 3; g = (x >> 2) * 16 + (r & 15); j = (x & 3) * 2 + (r >> 4); }
  const rsrc_t rX = __builtin_amdgcn_make_buffer_rsrc((void*)p.x, 0, S_ * D_ * 2, 0x00020000);
  const rsrc_t rQKV = __builtin_amdgcn_make_buffer_rsrc((void*)p.wqkv, 0, D_ * 3 * D_ * 2, 0x00020000);
  const rsrc_t rWO = __builtin_amdgcn_make_buffer_rsrc((void*)p.wo, 0, D_ * D_ * 2, 0x00020000);
  const rsrc_t rW1 = __builtin_amdgcn_make_buffer_rsrc((void*)p.w1, 0, D_ * F_ * 2, 0x00020000);
  const rsrc_t rW3 = __builtin_amdgcn_make_buffer_rsrc((void*)p.w3, 0, D_ * F_ * 2, 0x00020000);
  const rsrc_t rW2 = __builtin_amdgcn_make_buffer_rsrc((void*)p.w2, 0, F_ * D_ * 2, 0x00020000);
  const rsrc_t rC = __builtin_amdgcn_make_buffer_rsrc((void*)p.c, 0, D_ * 2, 0x00020000);
  const rsrc_t rMOD = __builtin_amdgcn_make_buffer_rsrc((void*)p.wmod, 0, D_ * 6 * D_ * 2, 0x00020000);
  const rsrc_t rWS = __builtin_amdgcn_make_buffer_rsrc((void*)p.ws, 0, WS_BYTES, 0x00020000);
  const rsrc_t rOUT = __builtin_amdgcn_make_buffer_rsrc((void*)p.out, 0, S_ * D_ * 2, 0x00020000);
  STAMP(0);

  u32 ep;
  { const u32 vz = 0; asm volatile("buffer_load_dword %0, %1, %2, 0 offen sc1" : "=v"(ep) : "v"(vz), "s"(rWS)); }
  u32 vl16 = lane * 16; PIN(vl16);
  const int bm = b < 192 ? b : b - 192, mq = bm / 48, cl = bm - 48 * mq;
  v4u wm[4]; v2u cv; u32 ep0;
  if (w < 4) {
    const u32 vm = (tid >> 3) * 24576 + (tid & 7) * 16, s0 = mq * (128 * 6144) + cl * 128, s1 = s0 + 6144, s2 = s0 + 12288, s3 = s0 + 18432;
    LD16(wm[0], vm, rMOD, s0, 0); LD16(wm[1], vm, rMOD, s1, 0); LD16(wm[2], vm, rMOD, s2, 0); LD16(wm[3], vm, rMOD, s3, 0);
    const u32 vc = (tid >> 3) * 8, scq = mq * 256;
    LD8(cv, vc, rC, scq, 0);
    const u32 vz = 0; asm volatile("buffer_load_dword %0, %1, %2, 0 offen sc1" : "=v"(ep0) : "v"(vz), "s"(rWS));
  }
  if (tid < 16) *(LAS u32*)(L + L_CNT + tid * 4) = 0u;
  BAR();
  const int st_qd = lane >> 2, st_r = (st_qd & 3) + 4 * (st_qd >> 3);
  const int st_col = ((st_qd >> 2) & 1) * 64 + (((lane & 3) ^ ((w & 1) << 1)) << 4);
  const int rotp = g & 7;
  u32 vB1 = st_r * 3072 + st_col; PIN(vB1);
  const u32 mB1 = (w >> 2) * 12288 + (w & 3) * 1024, sB1 = (w >> 2) * 98304 + (w & 3) * 24576 + j * 128;
  auto stage_b1 = [&](auto PC, auto BC) {
    constexpr int pair = decltype(PC)::value, base = decltype(BC)::value;
    const u32 sp = ((pair + rotp) & 7) * 196608 + sB1;
    dma<base, 0>(mB1, vB1, rQKV, sp); dma<base + 4096, 1024>(mB1, vB1, rQKV, sp); dma<base + 8192, 2048>(mB1, vB1, rQKV, sp);
  };
  auto b1_piece = [&](auto PC, auto BC, auto TC) {
    constexpr int pair = decltype(PC)::value, base = decltype(BC)::value, ty = decltype(TC)::value;
    dma<base + ty * 4096, ty * 1024>(mB1, vB1, rQKV, ((pair + rotp) & 7) * 196608 + sB1);
  };
  const int hd = w & 3, blk2 = w >> 2, kq = w & 3, mh = w >> 2;
  const int cs0 = j < 4 ? 22 * j : 88 + 21 * (j - 4);
  const int cend = cs0 + (j < 4 ? 22 : 21);
  v4u xr[10];
  const int rx2 = (0x4310 >> (4 * (j & 3))) & 7;
  auto load_x = [&](int i0, int i1) {
    const int r0 = 64 * g - 16 + w;
#pragma unroll
    for (int i = i0; i < i1; ++i) {
      int bp = (i >> 1) + rx2; bp = bp >= 5 ? bp - 5 : bp;
      const u32 so = (u32)max(r0 + 16 * bp + 8 * (i & 1), 0) * 1024u;
      LD16(xr[i], vl16, rX, so, 0);
    }
  };
  load_x(0, 5);
  STAMP(1);
  const int T0 = 12 * (g >> 4) + 3 * (w & 3), rcb = min(cs0 + T0, 168);
  if (w < 4) {
    asm volatile("s_waitcnt vmcnt(5)" : "+v"(cv), "+v"(wm[0]), "+v"(wm[1]), "+v"(wm[2]), "+v"(wm[3]), "+v"(ep0)::"memory");
    const u32 tg = __builtin_amdgcn_readfirstlane(ep0) + 1u;
    const f32x4 cf = {blo(cv.x), bhi(cv.x), blo(cv.y), bhi(cv.y)};
    float sc[4];
#pragma unroll
    for (int r = 0; r < 4; ++r) sc[r] = cf[r] * __builtin_amdgcn_rcpf(1.f + __builtin_amdgcn_exp2f(cf[r] * -1.4426950408889634f));
    f32x2 v[4];
#pragma unroll
    for (int i = 0; i < 4; ++i) v[i] = (unp(wm[0][i]) * sc[0] + unp(wm[1][i]) * sc[1]) + (unp(wm[2][i]) * sc[2] + unp(wm[3][i]) * sc[3]);
    const float a0 = sw32_add(v[0].x, v[2].x), a1 = sw32_add(v[0].y, v[2].y), a2 = sw32_add(v[1].x, v[3].x), a3 = sw32_add(v[1].y, v[3].y);
    float t0 = sw16_add(a0, a2), t1 = sw16_add(a1, a3);
    t0 += dppf<0x128>(t0); t1 += dppf<0x128>(t1);
    f32x2 o = {t0, t1};
    *(LAS f32x2*)(L + L_RED + w * 256 + (lane & 7) * 32 + g4 * 8) = o;
    LGW();
    u32 old = 0;
    if (lane == 0) old = __hip_atomic_fetch_add((LAS u32*)(L + L_CNT + 28), 1u, __ATOMIC_RELAXED, __HIP_MEMORY_SCOPE_WORKGROUP);
    if (__builtin_amdgcn_readfirstlane(old) == 3u) {
      asm volatile("" ::: "memory");
      float s = 0.f;
#pragma unroll
      for (int i = 0; i < 4; ++i) s += *(LAS float*)(L + L_RED + i * 256 + lane * 4);
      v2u gr; gr.x = asu(s); gr.y = tg;
      if (b < 192) __builtin_amdgcn_raw_buffer_store_b64(gr, rWS, lane * 8, WS_MODG + (mq * 3072 + cl * 64) * 8, 16);
    }
  }
  STAMP(2);
  load_x(5, 10);
  stage_b1(IC<0>{}, IC<L_XY>{});
  if (w == 6) touch(lane < 24 ? WS_FLAG + (lane >> 3) * 65536 + g * 2048 + (lane & 7) * 128
                              : lane < 28 ? WS_STAT + g * 4096 + j * 512 + (lane & 3) * 128 : lane == 28 ? WS_MODF + (1024 + 8 * b) * 8 : WS_RFLAG + j * 128, rWS, 0);
  int afo[4];
#pragma unroll
  for (int i = 0; i < 4; ++i) { afo[i] = blk2 * 32768 + c16 * 1024 + (((4 * i + g4) ^ c16) << 4); PIN(afo[i]); }
  const u64 am0 = __builtin_amdgcn_ballot_w64((c16 & 14) >= 4 * g4), am1 = __builtin_amdgcn_ballot_w64((c16 & 14) >= 4 * g4 + 2);
  u32 ahm = (lane & 1) ? 0xffff0000u : 0xffffu; PIN(ahm);
  u32 vOst = c16 * 16 + (2 * (hd & 1) + (g4 >> 1)) * 256 + (g4 & 1) * 16384; PIN(vOst);
  int tB1 = g4 * 1024 + 64 * (c16 >> 2) + 8 * (lane & 3) + 32 * ((hd & 1) ^ (g4 & 1)) + 256 * (hd >> 1); PIN(tB1);
  int hw0 = w * 1024 + ((((lane - 8 * rotp) & 63) ^ w) << 4); PIN(hw0);
#define vfl ((opq(vl16) & 112u) << 3)
  STAMP(3);
  v4u bw[48];
  auto read_b1 = [&](int pair, int base, int off) {
#pragma unroll
    for (int kk = 0; kk < 2; ++kk)
#pragma unroll
      for (int ty = 0; ty < 3; ++ty) {
        LAS char* a = L + base + (off + kk * 12288 + ty * 4096);
        bw[(2 * pair + kk) * 3 + ty] = cat(trd(a), trd(a + 512));
      }
  };
  f32x2 xn[10][4];
  v4u ga_[4]; v2u pv;
  const u32 sMG = WS_MODG + w * 1024;
  {
    float u[10];
    sfor<0, 10>([&](auto I) {
      constexpr int i = decltype(I)::value;
      if constexpr (i == 0) asm volatile("s_waitcnt vmcnt(11)" : "+v"(xr[0]), "+v"(xr[1]), "+v"(ep)::"memory");
      if constexpr (i == 2) asm volatile("s_waitcnt vmcnt(9)" : "+v"(xr[2]), "+v"(xr[3])::"memory");
      if constexpr (i == 4) asm volatile("s_waitcnt vmcnt(10)" : "+v"(xr[4]), "+v"(xr[5])::"memory");
      if constexpr (i == 6) asm volatile("s_waitcnt vmcnt(8)" : "+v"(xr[6]), "+v"(xr[7])::"memory");
      if constexpr (i == 8) asm volatile("s_waitcnt vmcnt(9)" : "+v"(xr[8]), "+v"(xr[9])::"memory");
      float s = 0.f, q = 0.f;
#pragma unroll
      for (int e = 0; e < 4; ++e) { s = dot2(xr[i][e], 0x3f803f80u, s); q = dot2(xr[i][e], xr[i][e], q); }
      u[i] = sw32_add(s, q);
      PIN(u[i]);
      if constexpr (i == 3) stage_b1(IC<1>{}, IC<L_XY + 24576>{});
      if constexpr (i == 7) stage_b1(IC<2>{}, IC<L_XY + 49152>{});
      if constexpr (i == 9) {
        const u32 s0 = sMG, s1 = sMG + 24576, s2 = sMG + 49152, s3 = sMG + 73728;
        ld16s<0>(ga_[0], vl16, rWS, s0); ld16s<0>(ga_[1], vl16, rWS, s1); ld16s<0>(ga_[2], vl16, rWS, s2); ld16s<0>(ga_[3], vl16, rWS, s3);
      }
    });
    float t[5];
#pragma unroll
    for (int i = 0; i < 5; ++i) t[i] = row16_sum(sw16_add(u[i], u[i + 5]));
    float v = t[0];
#pragma unroll
    for (int i = 1; i < 5; ++i) v = c16 == i ? t[i] : v;
    auto r = __builtin_amdgcn_permlane32_swap(asu(v), asu(v), false, false);
    const float mean = asf(r[0]) * (1.f / 512.f);
    const float var = fmaxf(asf(r[1]) * (1.f / 512.f) - mean * mean, 0.f);
    float rs = __builtin_amdgcn_rsqf(var + 1e-5f), nm = -mean * rs;
    PIN(rs); PIN(nm);
    STAMP(4);
    { const u32 vo = ((lane >> 3) & 3) * 24576 + (lane & 7) * 8, so = WS_MODG + (1024 + 8 * b) * 8; LD8S(pv, vo, rWS, so, 0); }
    if (w >= 4) {
      const int u = lane >> 2, row = (u & 3) | ((u >> 1) & 4) | ((u << 1) & 8);
      const u32 vo = row * 2752 + (lane & 3) * 16, so = (g & 15) * (32 * 2752) + rcb * 16, mb = L_Z + 49152 + (w & 3) * 4096;
      dma<0, 0>(mb, vo, rW1, so); dma<1024, 16 * 2752>(mb, vo, rW1, so); dma<2048, 0>(mb, vo, rW3, so); dma<3072, 16 * 2752>(mb, vo, rW3, so);
    } else {
      const int u = lane >> 1, row = (u & 3) | ((u >> 1) & 4) | ((u << 1) & 8) | (u & 16);
      const u32 vo = row * 1024 + (lane & 1) * 16, so = g * 32768 + j * 128 + w * 32, mb = L_Z + 65536 + w * 2048;
      dma<0, 0>(mb, vo, rW2, so); dma<1024, 0>(mb, vo, rW2, so + (g < 11 ? 32 * 32768 : 0));
      dma<0, 0>(76800 + w * 1024, vo, rWO, (g & 15) * 32768 + j * 128 + w * 32);
    }
    sfor<0, 10>([&](auto I) {
      constexpr int i = decltype(I)::value;
      if constexpr (i == 2) stage_b1(IC<3>{}, IC<L_Z>{});
      if constexpr (i == 6) stage_b1(IC<4>{}, IC<L_Z + 24576>{});
      const float a = rdl(rs, (i / 5) * 16 + i % 5), c = rdl(nm, (i / 5) * 16 + i % 5);
#pragma unroll
      for (int e = 0; e < 4; ++e) { xn[i][e] = unp(xr[i][e]) * a + c; PIN(xn[i][e]); }
    });
  }
  const u32 tag = __builtin_amdgcn_readfirstlane(ep) + 1u;
  u32 m1; asm("s_lshr_b32 %0, 1, %1" : "=s"(m1) : "s"(w) : "scc");
#define RAISE(fo) raise_flag(rWS, (fo), tag, vl16, m1)
#define SIGNAL(idx, fo) signal_flag(rWS, L_CNT + 4 * (idx), (fo), tag)
  STAMP(5);
  asm volatile("s_waitcnt vmcnt(9)" : "+v"(ga_[0]), "+v"(ga_[1]), "+v"(ga_[2]), "+v"(ga_[3]), "+v"(pv)::"memory");
  auto gsum = [&](v4u (&a)[4], f32x2& o) -> u32 {
    o.x = (asf(a[0].x) + asf(a[1].x)) + (asf(a[2].x) + asf(a[3].x)); o.y = (asf(a[0].z) + asf(a[1].z)) + (asf(a[2].z) + asf(a[3].z));
    return ((a[0].y ^ tag) | (a[0].w ^ tag) | (a[1].y ^ tag) | (a[1].w ^ tag)) | ((a[2].y ^ tag) | (a[2].w ^ tag) | (a[3].y ^ tag) | (a[3].w ^ tag));
  };
  auto gather = [&](u32 voff, u32 soff, f32x2& o) {
    for (u32 sp = 0; sp < SPIN_CAP; ++sp) {
      asm volatile("" ::: "memory");
      v4u a[4];
#pragma unroll
      for (int q = 0; q < 4; ++q) a[q] = __builtin_amdgcn_raw_buffer_load_b128(rWS, voff, soff + q * 24576, 16);
      if (!__builtin_amdgcn_ballot_w64(gsum(a, o) != 0u)) break;
    }
  };
  const float one = w >= 4 ? 1.f : 0.f;
  {
    f32x2 mA;
    if (__builtin_amdgcn_ballot_w64(gsum(ga_, mA) != 0u)) gather(vl16, sMG, mA);
    *(LAS f32x2*)(L + L_MODA + w * 512 + (vl16 >> 1)) = mA + one;
  }
  BAR();
  if (w == 7) {
    const u32 vo = ((lane >> 3) & 3) * 24576 + (lane & 7) * 8, so = WS_MODG + (1024 + 8 * b) * 8;
    for (u32 sp = 0; sp < SPIN_CAP && __builtin_amdgcn_ballot_w64(pv.y != tag); ++sp) {
      asm volatile("" ::: "memory");
      pv = __builtin_amdgcn_raw_buffer_load_b64(rWS, vo, so, 16);
    }
    float sv = asf(pv.x);
    sv += dppf<0x128>(sv); sv = sw16_add(sv, sv);
    pv.x = asu(sv);
    __builtin_amdgcn_raw_buffer_store_b64(pv, rWS, (lane & 7) * 8, WS_MODF + (1024 + 8 * b) * 8, 16);
  }
  STAMP(6);
  int vma = L_MODA + 2 * vl16; PIN(vma);
  const f32x4 ms0 = *(LAS f32x4*)(L + vma), ms1 = *(LAS f32x4*)(L + vma + 16);
  const f32x4 ma0 = *(LAS f32x4*)(L + vma + 2048), ma1 = *(LAS f32x4*)(L + vma + 2064);
  int tz0 = tB1 + L_Z; PIN(tz0);
  { int t2 = tB1 + 49152; PIN(t2); read_b1(0, tB1, 0); read_b1(1, tB1, 24576); read_b1(2, t2, 0); }
  {
    VMW(6);
    if (w >= 4) {
      LAS char* a0 = L + L_Z + 49152 + (w & 3) * 4096 + ((lane >> 1) & 1) * 2048 + 64 * ((c16 >> 2) + 4 * (g4 & 1) + 16 * (g4 >> 1)) + 8 * (lane & 1);
#pragma unroll
      for (int ct = 0; ct < 3; ++ct) {
        const int c = min(cs0 + T0 + ct - rcb, 3) * 16;
        const v4u f = cat(trd(a0 + c), trd(a0 + c + 512));
        const u32 sf = WS_B4T + ((j * 16 + (g & 15)) * 24 + T0 + ct) * 1024;
        asm volatile("buffer_store_dwordx4 %0, %1, %2, %3 offen sc1\n\ts_nop 1" ::"v"(f), "v"(vl16), "s"(rWS), "s"(sf) : "memory");
      }
    }
    else {
      LAS char* a0 = L + L_Z + 65536 + w * 2048 + 32 * ((c16 >> 2) + 4 * (g4 & 1) + 16 * (g4 >> 1)) + 8 * (lane & 3);
#pragma unroll
      for (int i = 0; i < 2; ++i) {
        const v4u f = cat(trd(a0 + i * 1024), trd(a0 + i * 1024 + 256));
        const u32 sf = WS_B5T + ((j * 43 + g + (i && g < 11 ? 32 : 0)) * 4 + w) * 1024;
        asm volatile("buffer_store_dwordx4 %0, %1, %2, %3 offen sc1\n\ts_nop 1" ::"v"(f), "v"(vl16), "s"(rWS), "s"(sf) : "memory");
      }
      LAS char* a1 = L + 76800 + w * 1024 + 32 * ((c16 >> 2) + 4 * (g4 & 1) + 16 * (g4 >> 1)) + 8 * (lane & 3);
      const v4u f = cat(trd(a1), trd(a1 + 256));
      const u32 sf = WS_B3T + ((j * 16 + (g & 15)) * 4 + w) * 1024;
      asm volatile("buffer_store_dwordx4 %0, %1, %2, %3 offen sc1\n\ts_nop 1" ::"v"(f), "v"(vl16), "s"(rWS), "s"(sf) : "memory");
    }
  }
  BAR();
  STAMP(7);
  {
    int hw1 = (hw0 ^ 128) + 8192; PIN(hw1);
    sfor<0, 10>([&](auto I) {
      constexpr int i = decltype(I)::value;
      v4u o;
      o.x = pk2(xn[i][0] * ma0.xy + ms0.xy); o.y = pk2(xn[i][1] * ma0.zw + ms0.zw);
      o.z = pk2(xn[i][2] * ma1.xy + ms1.xy); o.w = pk2(xn[i][3] * ma1.zw + ms1.zw);
      int bp = (i >> 1) + rx2; bp = bp >= 5 ? bp - 5 : bp;
      *(LAS v4u*)(L + ((i & 1) ? hw1 : hw0) + bp * 16384) = o;
      SCHED();
    });
  }
  VMW(0);
  BAR();
  RAISE(WS_RFLAG + (j * 32 + g) * 4);
  u32 flr;
  STAMP(8);
  __builtin_amdgcn_s_setprio(2);

  const f32x4 z4 = {0.f, 0.f, 0.f, 0.f};
#define AFRAG(off, ks) (*(LAS v4u*)(L + afo[(ks) & 3] + ((off) + ((ks) >> 2) * 256)))
  const int ch0 = cs0 + 3 * w;
  constexpr int ND = NODMA ? 0 : 1;
  v4u b4[48];
  const u32 sB4 = WS_B4T + (j * 16 * 24 + 3 * w) * 1024;
  auto load_b4 = [&](auto NC) {
    constexpr int n = decltype(NC)::value, ks = n / 3, ct = n % 3;
    const u32 so = sB4 + ((ks + 2 * rotp) & 15) * 24576;
    v4u& r = b4[n];
    if constexpr (ct == 0) LD16S(r, vl16, rWS, so, 0); else if constexpr (ct == 1) LD16S(r, vl16, rWS, so, 1024); else LD16S(r, vl16, rWS, so, 2048);
  };
  auto landed_b4 = [&](auto AC, auto BC) {
#pragma unroll
    for (int i = decltype(AC)::value; i < decltype(BC)::value; ++i) { v4u& r = b4[i]; PIN(r); }
  };
  v4u b3[16];
  auto load_b3 = [&](auto NC) { constexpr int n = decltype(NC)::value; ld16s<(n & 3) * 1024>(b3[n], vl16, rWS, WS_B3T + (j * 16 + 4 * kq + (n >> 2)) * 4096); };
  v4u gb_, gg_;
  u32 vgg, fl0, vRow;
  int pw, pr;
  f32x4 aKh = z4, aVh = z4, aQ = z4, aK = z4, aV = z4, bQ = z4, bK = z4, bV = z4;
  {
    v4u ah = AFRAG(0, 0), a1 = AFRAG(16384, 0), a2 = AFRAG(32768, 0);
    int l3, c3, g3; f32x2 mB, mG;
    auto job = [&](auto NC) {
      constexpr int n = decltype(NC)::value;
      if constexpr (n == 3) { l3 = opq(vl16) >> 4; c3 = l3 & 15; g3 = l3 >> 4; PIN(c3); PIN(g3); vgg = (vl16 < 512 ? 8192 : 20480 - 512) + 512 * j + vl16; PIN(vgg); }
      if constexpr (n == 7) { vRow = (8 * w + (l3 >> 3)) * 1024 + (l3 & 7) * 16; PIN(vRow); }
      if constexpr (n == 8) { pr = (8 * w + (l3 >> 3)) * 256 + (((2 * (l3 & 7)) ^ ((l3 >> 3) & 7)) << 4); PIN(pr); }
      if constexpr (n == 10) { pw = kq * 16384 + (32 * mh + c3) * 256 + ((g3 ^ (c3 & 3)) << 4) + ((c3 & 4) << 4); PIN(pw); }
      if constexpr (n == 31) { mB.x = asf(gb_.x) + one; PIN(mB.x); mB.y = asf(gb_.z) + one; PIN(mB.y); *(LAS f32x2*)(L + L_MODA + w * 512 + (vl16 >> 1)) = mB; }
      if constexpr (n == 32) { mG.x = asf(gg_.x); mG.y = asf(gg_.z); *(LAS f32x2*)(L + L_GAB + (vl16 >> 1)) = mG; }
      if constexpr (n == 34) { fl0 = (gb_.y ^ tag) | (gb_.w ^ tag) | (gg_.y ^ tag) | (gg_.w ^ tag); PIN(fl0); }
      if constexpr (n == 7) { const u32 s0 = WS_MODF + 12288 + w * 1024; LD16S(gb_, vl16, rWS, s0, 0); }
      if constexpr (n == 9) { const u32 s0 = WS_MODF; LD16S(gg_, vgg, rWS, s0, 0); }
      if constexpr (n < 6 && !(n & 1)) b1_piece(IC<5>{}, IC<L_Z + 49152>{}, IC<n / 2>{});
      if constexpr (n == 6) arrive(L_CNT + 8);
      if constexpr (n == 10) await8(L_CNT + 8);
      if constexpr (n >= 10 && n < 22 && !(n & 1)) b1_piece(IC<6 + (n - 10) / 6>{}, IC<L_Z + ((n - 10) / 6) * 24576>{}, IC<((n - 10) / 2) % 3>{});
      if constexpr (n == 21) { const u32 vr = (vl16 >> 2) & 124, sr = WS_RFLAG + j * 128; LD4S(flr, vr, rWS, sr); }
      if constexpr (n == 26) {
        asm volatile("s_waitcnt vmcnt(0)" : "+v"(gb_), "+v"(gg_), "+v"(flr)::"memory");
        arrive(L_CNT + 12);
        const u32 vr = (vl16 >> 2) & 124, sr = WS_RFLAG + j * 128; u32 cnt = SPIN_CAP;
        asm volatile("1:\n\tv_cmp_ne_u32 vcc, %5, %0\n\ts_cbranch_vccz 2f\n\tbuffer_load_dword %0, %2, %3, %4 offen sc1\n\ts_waitcnt vmcnt(0)\n\t"
                     "s_sub_u32 %1, %1, 1\n\ts_cbranch_scc0 1b\n\t2:" : "+v"(flr), "+s"(cnt) : "v"(vr), "s"(rWS), "s"(sr), "s"(tag) : "vcc", "scc", "memory");
      }
      if constexpr (n == 28) { await8(L_CNT + 12); int tz2 = tz0 + 49152; PIN(tz2); read_b1(5, tz2, 0); }
      if constexpr (n == 32) read_b1(6, tz0, 0);
      if constexpr (n == 37) read_b1(7, tz0, 24576);
      if constexpr (n >= 27 && n < 47 && (n & 1)) load_b3(IC<(n - 27) / 2>{});
    };
    sfor<0, 16>([&](auto KS) {
      constexpr int ks = decltype(KS)::value;
      KSTAMP(1, ks);
      if constexpr (ks == 0) read_b1(3, tz0, 0);
      if constexpr (ks == 1) read_b1(4, tz0, 24576);
      SCHED();
      v4u nh = ah, n1 = a1, n2 = a2;
      aKh = mfma32(bw[ks * 3 + 1], ah, aKh);
      aVh = mfma32(ah, bw[ks * 3 + 2], aVh);
      if constexpr (ks < 15) nh = AFRAG(0, ks + 1);
      job(IC<3 * ks>{});
      SCHED();
      aQ = mfma32(bw[ks * 3 + 0], a1, aQ);
      aK = mfma32(bw[ks * 3 + 1], a1, aK);
      aV = mfma32(a1, bw[ks * 3 + 2], aV);
      if constexpr (ks < 15) n1 = AFRAG(16384, ks + 1);
      job(IC<3 * ks + 1>{});
      SCHED();
      bQ = mfma32(bw[ks * 3 + 0], a2, bQ);
      bK = mfma32(bw[ks * 3 + 1], a2, bK);
      bV = mfma32(a2, bw[ks * 3 + 2], bV);
      if constexpr (ks < 15) n2 = AFRAG(32768, ks + 1);
      job(IC<3 * ks + 2>{});
      ah = nh; a1 = n1; a2 = n2;
      SCHED();
    });
  }
  __builtin_amdgcn_s_setprio(0);
  STAMP(9); KSTAMP(1, 16);
  {
    const u32 sOst = WS_O + ((g * 4 + 2 * blk2) * 16 + 2 * j + (hd >> 1)) * 1024;
    const v2u k1p = pk4(aK), v1p = pk4(aV);
    const v2u o0 = attend(pk4(aKh), pk4(aVh), k1p, v1p, pk4(aQ), am0, am1, ahm, g == 0 && blk2 == 0);
    const v2u o1 = attend(k1p, v1p, pk4(bK), pk4(bV), pk4(bQ), am0, am1, ahm, false);
    const auto sx = __builtin_amdgcn_permlane16_swap(o0.x, o1.x, false, false), sy = __builtin_amdgcn_permlane16_swap(o0.y, o1.y, false, false);
    const v4u ov = {sx[0], sy[0], sx[1], sy[1]};
    asm volatile("buffer_store_dwordx4 %0, %1, %2, %3 offen sc1\n\ts_nop 1" ::"v"(ov), "v"(vOst), "s"(rWS), "s"(sOst) : "memory");
  }
  if (__builtin_amdgcn_ballot_w64(fl0 != 0u)) {
    for (u32 sp = 0; sp < SPIN_CAP; ++sp) {
      asm volatile("" ::: "memory");
      gb_ = __builtin_amdgcn_raw_buffer_load_b128(rWS, vl16, WS_MODF + 12288 + w * 1024, 16);
      gg_ = __builtin_amdgcn_raw_buffer_load_b128(rWS, vgg, WS_MODF, 16);
      if (!__builtin_amdgcn_ballot_w64(((gb_.y ^ tag) | (gb_.w ^ tag) | (gg_.y ^ tag) | (gg_.w ^ tag)) != 0u)) break;
    }
    const f32x2 mB = {asf(gb_.x) + one, asf(gb_.z) + one}, mG = {asf(gg_.x), asf(gg_.z)};
    *(LAS f32x2*)(L + L_MODA + w * 512 + (vl16 >> 1)) = mB;
    *(LAS f32x2*)(L + L_GAB + (vl16 >> 1)) = mG;
  }
  STAMP(10);
  VMW(0);
  BAR();
  RAISE(flag_off(0, g, 0) + j * 128);
  STAMP(11);
  if (b == 0 && w == 7) {
    for (u32 sp = 0; sp < SPIN_CAP; ++sp) {
      asm volatile("" ::: "memory");
      const v4u v = __builtin_amdgcn_raw_buffer_load_b128(rWS, vl16, WS_RFLAG, 16);
      if (__all(v.x == tag && v.y == tag && v.z == tag && v.w == tag)) break;
    }
    __builtin_amdgcn_raw_buffer_store_b32(tag, rWS, 0, WS_EPOCH, 16);
  }

  v4u xv;
  { const u32 sx = g * 65536 + j * 128; LD16(xv, vRow, rX, sx, 0); }
  sfor<10, 16>(load_b3);
  const u64 care2 = 0x0101010101010101ull * (3u << (2 * kq));
  poll_flags(rWS, vfl, flag_off(0, g, 0), care2, tag);
  PIN(xv);
  v4u af[8];
  {
    const u32 so = WS_O + ((g * 4 + 2 * mh) * 16 + 4 * kq) * 1024, so1 = so + 16384;
    LD16S(af[0], vl16, rWS, so, 0); LD16S(af[1], vl16, rWS, so, 1024); LD16S(af[2], vl16, rWS, so, 2048); LD16S(af[3], vl16, rWS, so, 3072);
    LD16S(af[4], vl16, rWS, so1, 0); LD16S(af[5], vl16, rWS, so1, 1024); LD16S(af[6], vl16, rWS, so1, 2048); LD16S(af[7], vl16, rWS, so1, 3072);
  }
  STAMP(12);
  sfor<0, 2>([&](auto M) {
    constexpr int m = decltype(M)::value;
    asm volatile("s_waitcnt vmcnt(%c4)" : "+v"(af[4 * m]), "+v"(af[4 * m + 1]), "+v"(af[4 * m + 2]), "+v"(af[4 * m + 3]) : "i"(m == 0 ? 4 : 2) : "memory");
    if constexpr (m == 0) {
#pragma unroll
      for (int i = 0; i < 16; ++i) PIN(b3[i]);
    }
    f32x4 acc[4] = {z4, z4, z4, z4};
    sfor<0, 4>([&](auto SC) {
      constexpr int sx = decltype(SC)::value;
#pragma unroll
      for (int n = 0; n < 4; ++n) acc[n] = mfma32(b3[sx * 4 + n], af[4 * m + sx], acc[n]);
      if constexpr (sx & 1) load_b4(IC<2 * m + (sx >> 1)>{});
      SCHED();
    });
#pragma unroll
    for (int n = 0; n < 4; ++n) *(LAS f32x4*)(L + ((n & 1) ? (pw ^ 64) : pw) + (m * 4096 + (n >> 1) * 128)) = acc[n];
  });
  BAR();
  STAMP(13);
  v4u x1p;
  {
    f32x4 y0 = *(LAS f32x4*)(L + pr), y1 = *(LAS f32x4*)(L + (pr ^ 16));
#pragma unroll
    for (int q = 1; q < 4; ++q) { y0 += *(LAS f32x4*)(L + pr + q * 16384); y1 += *(LAS f32x4*)(L + (pr ^ 16) + q * 16384); }
    const int ga = L_GAB + ((vl16 & 112) << 1);
    const f32x4 g0 = *(LAS f32x4*)(L + ga), g1 = *(LAS f32x4*)(L + ga + 16);
    const f32x4 x0 = f32x4{blo(xv.x), bhi(xv.x), blo(xv.y), bhi(xv.y)} + g0 * y0, x1 = f32x4{blo(xv.z), bhi(xv.z), blo(xv.w), bhi(xv.w)} + g1 * y1;
    { const v4u o = cat(pk4(x0), pk4(x1)); x1p = o; const u32 so = WS_X1 + g * 65536 + j * 128;
      asm volatile("buffer_store_dwordx4 %0, %1, %2, %3 offen sc1\n\ts_nop 1" ::"v"(o), "v"(vRow), "s"(rWS), "s"(so) : "memory"); }
    const f32x4 sv = x0 + x1;
    float mean = (sv.x + sv.y) + (sv.z + sv.w);
    mean += dppf<0xB1>(mean); mean += dppf<0x4E>(mean); mean += dppf<0x141>(mean);
    mean *= 1.f / 64.f;
    const f32x4 d0 = x0 - mean, d1 = x1 - mean, dd = d0 * d0 + d1 * d1;
    float m2 = (dd.x + dd.y) + (dd.z + dd.w);
    m2 += dppf<0xB1>(m2); m2 += dppf<0x4E>(m2); m2 += dppf<0x141>(m2);
    v2u st; st.x = asu(mean); st.y = asu(m2);
    __builtin_amdgcn_raw_buffer_store_b64(st, rWS, (vRow >> 10) << 3, WS_STAT + g * 4096 + j * 512, 16);
  }
  STAMP(14);
  VMW(0);
  BAR();
  RAISE(flag_off(1, g, 0) + j * 128);
  sfor<4, 9>(load_b4);
  STAMP(15);

  u32 hmask[3];
#pragma unroll
  for (int ct = 0; ct < 3; ++ct)
    asm("s_cmp_lt_u32 %1, %2\n\ts_cselect_b32 %0, -1, 0" : "=s"(hmask[ct]) : "s"((u32)(ch0 + ct)), "s"((u32)cend) : "scc");
#pragma unroll
  for (int i = 0; i < 4; ++i) { afo[i] -= blk2 * 32768; PIN(afo[i]); }
  int vm2 = L_MODA + 2 * vl16; PIN(vm2);
  int h2w = w * 8192 + (((vl16 - 128 * rotp) & 1023) ^ ((8 * w & 15) << 4)); PIN(h2w);
  const f32x4 ls0 = *(LAS f32x4*)(L + vm2), ls1 = *(LAS f32x4*)(L + vm2 + 16), la0 = *(LAS f32x4*)(L + vm2 + 2048), la1 = *(LAS f32x4*)(L + vm2 + 2064);
  poll_flags(rWS, vfl, flag_off(1, g, 0), ~0ull, tag);
  landed_b4(IC<0>{}, IC<9>{});
  {
    v4u x1v[8]; v2u stv;
    const u32 so = WS_X1 + g * 65536 + w * 8192, so2 = so + 4096;
    { const u32 vs = ((vl16 & 112) << 5) + ((vl16 >> 7) << 3), ss = WS_STAT + g * 4096 + w * 64; LD8S(stv, vs, rWS, ss, 0); }
    LD16S(x1v[0], vl16, rWS, so, 0); LD16S(x1v[1], vl16, rWS, so, 1024); LD16S(x1v[2], vl16, rWS, so, 2048); LD16S(x1v[3], vl16, rWS, so, 3072);
    LD16S(x1v[4], vl16, rWS, so2, 0); LD16S(x1v[5], vl16, rWS, so2, 1024); LD16S(x1v[6], vl16, rWS, so2, 2048); LD16S(x1v[7], vl16, rWS, so2, 3072);
    sfor<9, 12>(load_b4);
    asm volatile("s_waitcnt vmcnt(11)" : "+v"(stv)::"memory");
    const float mj = asf(stv.x);
    float mean = mj;
    mean += dppf<0xB1>(mean); mean += dppf<0x4E>(mean); mean += dppf<0x141>(mean);
    mean *= 0.125f;
    const float dm = mj - mean;
    float m2 = asf(stv.y) + 64.f * dm * dm;
    m2 += dppf<0xB1>(m2); m2 += dppf<0x4E>(m2); m2 += dppf<0x141>(m2);
    const float l2r = __builtin_amdgcn_rsqf(m2 * (1.f / 512.f) + 1e-5f), l2n = -mean * l2r;
    sfor<0, 8>([&](auto I) {
      constexpr int i = decltype(I)::value;
      v4u& xx = x1v[i];
      asm volatile("s_waitcnt vmcnt(%c1)" : "+v"(xx) : "i"(10 - i) : "memory");
      const float rs = rdl(l2r, 8 * i), nn = rdl(l2n, 8 * i);
      v4u o;
      o.x = pk2((unp(xx.x) * rs + nn) * la0.xy + ls0.xy); o.y = pk2((unp(xx.y) * rs + nn) * la0.zw + ls0.zw);
      o.z = pk2((unp(xx.z) * rs + nn) * la1.xy + ls1.xy); o.w = pk2((unp(xx.w) * rs + nn) * la1.zw + ls1.zw);
      *(LAS v4u*)(L + (h2w ^ (i << 4)) + i * 1024) = o;
    });
  }
  BAR();
  STAMP(16);
  __builtin_amdgcn_s_setprio(2);
  const int kb5 = 5 * w + min(w, 3);
  u64 care5 = 0x0101010101010101ull * ((0x80c06030180c0603ull >> (8 * w)) & 255u);
  asm volatile("" : "+s"(care5));
  v4u b5[24];
  const u32 sB5 = WS_B5T + (j * 43 + kb5) * 4096, d5 = (w + 5) >> 3;
  auto load_b5 = [&](auto NC) {
    constexpr int n = decltype(NC)::value, sx = n / 4, t = n % 4;
    ld16s<t * 1024>(b5[n], vl16, rWS, sB5 + (sx == 5 ? (5 - d5) * 4096 : sx * 4096));
  };
  f32x4 acc4[3][4];
#pragma unroll
  for (int ct = 0; ct < 3; ++ct)
#pragma unroll
    for (int m = 0; m < 4; ++m) acc4[ct][m] = z4;
  u32 vH; int pw5;
  {
    v4u a[4] = {AFRAG(0, 0), AFRAG(16384, 0), AFRAG(32768, 0), AFRAG(49152, 0)};
    sfor<0, 16>([&](auto KS) {
      constexpr int ks = decltype(KS)::value;
      KSTAMP(3, ks);
      if constexpr (ks >= 3 && ks < 13) { VMW(9); landed_b4(IC<3 * ks>{}, IC<3 * ks + 3>{}); }
      if constexpr (ks >= 13) { VMW(9); landed_b4(IC<3 * ks>{}, IC<3 * ks + 3>{}); }
      v4u nx[4] = {a[0], a[1], a[2], a[3]};
      sfor<0, 4>([&](auto MC) {
        constexpr int m = decltype(MC)::value;
        if constexpr (ks < 15) nx[m] = AFRAG(16384 * m, ks + 1);
        if constexpr (m < 3) { if constexpr (ks < 12) load_b4(IC<12 + 3 * ks + m>{}); else load_b5(IC<3 * (ks - 12) + m>{}); }
        SCHED();
#pragma unroll
        for (int ct = 0; ct < 3; ++ct) acc4[ct][m] = mfma32(b4[ks * 3 + ct], a[m], acc4[ct][m]);
        if constexpr (ks == 8 && m == 0) { vH = (vl16 & 255) + (((vl16 >> 7) & 2) + (vl16 >> 9)) * (172 * 256); PIN(vH); }
        if constexpr (ks == 9 && m == 0) { const int l5 = opq(vl16) >> 4, c5 = l5 & 15, g5 = l5 >> 4;
          pw5 = w * 16384 + c5 * 256 + ((c5 & 4) << 4) + ((g5 ^ (c5 & 3)) << 4); PIN(pw5); }
        SCHED();
      });
#pragma unroll
      for (int m = 0; m < 4; ++m) a[m] = nx[m];
    });
  }
#pragma unroll
  for (int ct = 0; ct < 3; ++ct)
#pragma unroll
    for (int m = 0; m < 4; ++m) PIN(acc4[ct][m]);
  __builtin_amdgcn_s_setprio(0);
  STAMP(17); KSTAMP(3, 16);
#pragma unroll
  for (int ct = 0; ct < 3; ++ct) {
    v2u o[2];
#pragma unroll
    for (int mp = 0; mp < 2; ++mp) {
      f32x4 G, U, R;
#pragma unroll
      for (int e = 0; e < 4; ++e) {
        auto r = __builtin_amdgcn_permlane32_swap(asu(acc4[ct][2 * mp][e]), asu(acc4[ct][2 * mp + 1][e]), false, false);
        G[e] = asf(r[0]); U[e] = asf(r[1]);
      }
#pragma unroll
      for (int e = 0; e < 4; ++e) R[e] = qp(qp(G[e] * U[e]) * __builtin_amdgcn_rcpf(1.f + __builtin_amdgcn_exp2f(qp(G[e] * -1.4426950408889634f))));
      o[mp] = pk4(R);
    }
    const auto sx = __builtin_amdgcn_permlane16_swap(o[0].x, o[1].x, false, false), sy = __builtin_amdgcn_permlane16_swap(o[0].y, o[1].y, false, false);
    const v4u ov = {sx[0], sy[0], sx[1], sy[1]};
    const u32 so = WS_HH + (g * 4 * 172 + ch0 + ct) * 256;
    asm volatile("s_mov_b32 exec_lo, %4\n\ts_mov_b32 exec_hi, %4\n\tbuffer_store_dwordx4 %0, %1, %2, %3 offen sc1\n\ts_mov_b64 exec, -1\n\ts_nop 0"
                 ::"v"(ov), "v"(vH), "s"(rWS), "s"(so), "s"(hmask[ct]) : "memory");
  }
  STAMP(18);
  sfor<12, 16>(load_b5);
  VMW(4);
  BAR();
  RAISE(flag_off(2, g, 0) + j * 128);
  STAMP(19);
  sfor<16, 22>(load_b5);
  poll_flags(rWS, vfl, flag_off(2, g, 0), care5, tag);
#pragma unroll
  for (int i = 0; i < 22; ++i) PIN(b5[i]);
  v4u hf[16];
  auto load_h = [&](auto NC) {
    constexpr int n = decltype(NC)::value, sx = n / 4, m = n % 4;
    ld16s<0>(hf[n & 15], vl16, rWS, WS_HH + ((g * 4 + m) * 172 + 4 * kb5) * 256 + (sx == 5 ? (5 - d5) * 1024 : sx * 1024));
  };
  sfor<0, 16>(load_h);
  sfor<22, 24>(load_b5);
  STAMP(20);
  {
    f32x4 acc[4][4];
#pragma unroll
    for (int m = 0; m < 4; ++m)
#pragma unroll
      for (int n = 0; n < 4; ++n) acc[m][n] = z4;
    sfor<0, 6>([&](auto SC) {
      constexpr int sx = decltype(SC)::value, r = (sx & 3) * 4;
      constexpr int nw = sx == 0 ? 14 : sx == 1 ? 14 : sx == 2 ? 14 : sx == 3 ? 10 : sx == 4 ? 4 : 0;
      asm volatile("s_waitcnt vmcnt(%c4)" : "+v"(hf[r]), "+v"(hf[r + 1]), "+v"(hf[r + 2]), "+v"(hf[r + 3]) : "i"(nw) : "memory");
      if constexpr (sx == 4) { PIN(b5[22]); PIN(b5[23]); }
      if constexpr (sx == 5) { if (w >= 3) { hf[r] = v4u{0u, 0u, 0u, 0u}; hf[r + 1] = v4u{0u, 0u, 0u, 0u}; hf[r + 2] = v4u{0u, 0u, 0u, 0u}; hf[r + 3] = v4u{0u, 0u, 0u, 0u}; } }
      sfor<0, 4>([&](auto MC) {
        constexpr int m = decltype(MC)::value;
#pragma unroll
        for (int n = 0; n < 4; ++n) acc[m][n] = mfma32(b5[sx * 4 + n], hf[r + m], acc[m][n]);
        SCHED();
        if constexpr (sx < 2) load_h(IC<16 + 4 * sx + m>{});
        SCHED();
      });
    });
#pragma unroll
    for (int m = 0; m < 4; ++m)
#pragma unroll
      for (int n = 0; n < 4; ++n) *(LAS f32x4*)(L + ((n & 1) ? (pw5 ^ 64) : pw5) + (m * 4096 + (n >> 1) * 128)) = acc[m][n];
  }
  BAR();
  STAMP(21);
  {
    const int l6 = opq(vl16) >> 4;
    const int p0 = opq((8 * w + (l6 >> 3)) * 256 + (((2 * (l6 & 7)) ^ ((l6 >> 3) & 7)) << 4)), p1 = opq(p0 + 65536);
    f32x4 y0 = *(LAS f32x4*)(L + p0), y1 = *(LAS f32x4*)(L + (p0 ^ 16));
#pragma unroll
    for (int q = 1; q < 8; ++q) { const int pb = q < 4 ? p0 : p1; y0 += *(LAS f32x4*)(L + pb + (q & 3) * 16384); y1 += *(LAS f32x4*)(L + (pb ^ 16) + (q & 3) * 16384); }
    const int gb = L_GAB + 256 + (l6 & 7) * 32;
    const f32x4 g0 = *(LAS f32x4*)(L + gb), g1 = *(LAS f32x4*)(L + gb + 16);
    const f32x4 o0 = f32x4{blo(x1p.x), bhi(x1p.x), blo(x1p.y), bhi(x1p.y)} + g0 * y0, o1 = f32x4{blo(x1p.z), bhi(x1p.z), blo(x1p.w), bhi(x1p.w)} + g1 * y1;
    const v4u o = cat(pk4(o0), pk4(o1));
    const u32 vo = (8 * w + (l6 >> 3)) * 1024 + (l6 & 7) * 16, so = g * 65536 + j * 128;
    asm volatile("buffer_store_dwordx4 %0, %1, %2, %3 offen sc1\n\ts_nop 1" ::"v"(o), "v"(vo), "s"(rOUT), "s"(so) : "memory");
  }
  STAMP(22);
  STAMP_DUMP();
}
}

extern "C" void kernel_launch(void* const* d_in, const int* in_sizes, int n_in, void* d_out, int out_size, void* d_ws, size_t ws_size,
                              hipStream_t stream) {
  Params p;
  p.x = (const u16*)d_in[0]; p.wqkv = (const u16*)d_in[1]; p.wo = (const u16*)d_in[2];
  p.w1 = (const u16*)d_in[4]; p.w3 = (const u16*)d_in[5]; p.w2 = (const u16*)d_in[6];
  p.c = (const u16*)d_in[7]; p.wmod = (const u16*)d_in[8];
  p.out = (u16*)d_out; p.ws = (char*)d_ws;
  (void)hipFuncSetAttribute((const void*)fused_block, hipFuncAttributeMaxDynamicSharedMemorySize, L_TOTAL);
  fused_block<<<256, 512, L_TOTAL, stream>>>(p);
}
```

```cpp
#include <hip/hip_runtime.h>
#include <stdint.h>

#define KDBG 0
#define XMAP 2

#define DI __device__ __forceinline__
#define GAS __attribute__((address_space(1)))
#define LAS __attribute__((address_space(3)))
typedef unsigned u32;
typedef unsigned short u16;
typedef unsigned long long u64;
typedef u32 v4u __attribute__((ext_vector_type(4)));
typedef u32 v2u __attribute__((ext_vector_type(2)));
typedef float f32x4 __attribute__((ext_vector_type(4)));
typedef float f32x2 __attribute__((ext_vector_type(2)));
typedef __bf16 bf16x8 __attribute__((ext_vector_type(8)));
typedef __bf16 bf16x2 __attribute__((ext_vector_type(2)));
typedef short s16x4 __attribute__((ext_vector_type(4)));
typedef __amdgpu_buffer_rsrc_t rsrc_t;
typedef GAS u32 gu32;
typedef GAS u64 gu64;
#define RLX_AGENT __ATOMIC_RELAXED, __HIP_MEMORY_SCOPE_AGENT

namespace {
constexpr int S_ = 2048, D_ = 512, F_ = 1376;
constexpr u32 WS_EPOCH = 0;
constexpr u32 WS_FLAG = 32768;
constexpr u32 WS_STAT = 262144;
constexpr u32 WS_MODG = 524288;
constexpr u32 WS_MODF = 655360;
constexpr u32 WS_O = 1u << 20;
constexpr u32 WS_X1 = 3u << 20;
constexpr u32 WS_HH = 5u << 20;
constexpr u32 WS_RFLAG = 8192;
constexpr u32 WS_B4T = 11u << 20;
constexpr u32 WS_B5T = 14u << 20;
constexpr u32 WS_B3T = (15u << 20) + (512u << 10);
constexpr u32 WS_DBG = 16u << 20;
constexpr u32 WS_BYTES = 32u << 20;
constexpr int L_XY = 0;
constexpr int L_Z = 81920;
constexpr int L_MISC = 158720;
constexpr int L_TOTAL = 163840;
constexpr int L_RED = 73728;
constexpr int L_MODA = L_MISC;
constexpr int L_CNT = L_MISC + 4096;
constexpr int L_GAB = L_MISC + 4096 + 64;
constexpr int L_JUNK = L_MISC + 4096 + 64 + 512;
constexpr int L_STAMP = 156672;
constexpr u32 SPIN_CAP = 1u << 18;

DI f32x4 mfma32(v4u a, v4u b, f32x4 c) {
  return __builtin_amdgcn_mfma_f32_16x16x32_bf16(__builtin_bit_cast(bf16x8, a), __builtin_bit_cast(bf16x8, b), c, 0, 0, 0);
}
DI f32x4 mfma16(v2u a, v2u b, f32x4 c) {
  return __builtin_amdgcn_mfma_f32_16x16x16bf16_1k(__builtin_bit_cast(s16x4, a), __builtin_bit_cast(s16x4, b), c, 0, 0, 0);
}
DI u32 pk(float lo, float hi) { f32x2 v = {lo, hi}; return __builtin_bit_cast(u32, __builtin_convertvector(v, bf16x2)); }
DI u32 pk2(f32x2 v) { return __builtin_bit_cast(u32, __builtin_convertvector(v, bf16x2)); }
DI v2u pk4(f32x4 v) { v2u r; r.x = pk(v.x, v.y); r.y = pk(v.z, v.w); return r; }
DI float asf(u32 u) { return __builtin_bit_cast(float, u); }
DI u32 asu(float f) { return __builtin_bit_cast(u32, f); }
DI float blo(u32 u) { return asf(u << 16); }
DI float bhi(u32 u) { return asf(u & 0xffff0000u); }
DI f32x2 unp(u32 u) { f32x2 r = {blo(u), bhi(u)}; return r; }
DI float dot2(u32 a, u32 b, float c) { return __builtin_amdgcn_fdot2_f32_bf16(__builtin_bit_cast(bf16x2, a), __builtin_bit_cast(bf16x2, b), c, false); }
DI v2u trd(LAS char* p) { return __builtin_bit_cast(v2u, __builtin_amdgcn_ds_read_tr16_b64_v4i16((LAS s16x4*)p)); }
DI v4u cat(v2u a, v2u b) { v4u r; r.x = a.x; r.y = a.y; r.z = b.x; r.w = b.y; return r; }
template <int MI, int SI> DI void dma(u32 mb, u32 voff, rsrc_t srd, u32 sb) {
  u32 t;
  asm volatile("s_add_u32 m0, %1, %c2\n\ts_add_u32 %0, %3, %c4\n\tbuffer_load_dwordx4 %5, %6, %0 offen lds"
               : "=&s"(t) : "s"(mb), "i"(MI), "s"(sb), "i"(SI), "v"(voff), "s"(srd) : "scc");
}
DI void touch(u32 voff, rsrc_t srd, u32 sb) {
  asm volatile("s_mov_b32 m0, %c0\n\ts_nop 0\n\tbuffer_load_dword %1, %2, %3 offen sc1 lds" ::"i"(L_JUNK), "v"(voff), "s"(srd), "s"(sb));
}
#define BAR() asm volatile("s_waitcnt lgkmcnt(0)\n\ts_barrier" ::: "memory")
#define VMW(n) asm volatile("s_waitcnt vmcnt(" #n ")" ::: "memory")
#define LGW() asm volatile("s_waitcnt lgkmcnt(0)" ::: "memory")
#define SCHED() __builtin_amdgcn_sched_barrier(0)
#define LD16(dst, voff, srd, soff, IMM) asm volatile("buffer_load_dwordx4 %0, %1, %2, %3 offen offset:" #IMM : "=v"(dst) : "v"(voff), "s"(srd), "s"(soff))
#define LD16S(dst, voff, srd, soff, IMM) asm volatile("buffer_load_dwordx4 %0, %1, %2, %3 offen offset:" #IMM " sc1" : "=v"(dst) : "v"(voff), "s"(srd), "s"(soff))
#define LD8(dst, voff, srd, soff, IMM) asm volatile("buffer_load_dwordx2 %0, %1, %2, %3 offen offset:" #IMM : "=v"(dst) : "v"(voff), "s"(srd), "s"(soff))
#define LD8S(dst, voff, srd, soff, IMM) asm volatile("buffer_load_dwordx2 %0, %1, %2, %3 offen offset:" #IMM " sc1" : "=v"(dst) : "v"(voff), "s"(srd), "s"(soff))
#define LD4S(dst, voff, srd, soff) asm volatile("buffer_load_dword %0, %1, %2, %3 offen sc1" : "=v"(dst) : "v"(voff), "s"(srd), "s"(soff))

template <int IMM> DI void ld16s(v4u& r, u32 v, rsrc_t rs, u32 so) { asm volatile("buffer_load_dwordx4 %0, %1, %2, %3 offen offset:%c4 sc1" : "=v"(r) : "v"(v), "s"(rs), "s"(so), "i"(IMM)); }
template <int CTRL> DI float dppf(float v) { return asf(__builtin_amdgcn_update_dpp(0, asu(v), CTRL, 0xf, 0xf, true)); }
DI float row16_sum(float v) { v += dppf<0x128>(v); v += dppf<0x124>(v); v += dppf<0x122>(v); v += dppf<0x121>(v); return v; }
DI float sw32_add(float a, float b) { auto r = __builtin_amdgcn_permlane32_swap(asu(a), asu(b), false, false); return asf(r[0]) + asf(r[1]); }
DI float sw16_add(float a, float b) { auto r = __builtin_amdgcn_permlane16_swap(asu(a), asu(b), false, false); return asf(r[0]) + asf(r[1]); }
DI float x16_max(float v) { auto r = __builtin_amdgcn_permlane16_swap(asu(v), asu(v), false, false); return fmaxf(asf(r[0]), asf(r[1])); }
DI float x32_max(float v) { auto r = __builtin_amdgcn_permlane32_swap(asu(v), asu(v), false, false); return fmaxf(asf(r[0]), asf(r[1])); }
DI int fresh_lane() { int l; asm volatile("v_mbcnt_lo_u32_b32 %0, -1, 0\n\tv_mbcnt_hi_u32_b32 %0, -1, %0" : "=v"(l)); return l; }
DI float rdl(float v, int l) { return asf(__builtin_amdgcn_readlane(asu(v), l)); }

struct Params {
  const u16 *x, *wqkv, *wo, *w1, *w3, *w2, *c, *wmod;
  u16* out;
  char* ws;
};

#define LOOPSEL 0
#define STAMP(i) do {} while (0)
#define KSTAMP(lp, i) do {} while (0)
#define STAMP_DUMP() do {} while (0)

DI u32 flag_off(int phase, int g, int h) { return WS_FLAG + ((phase * 32 + g) * 2 + h) * 1024; }
DI void poll_flags(rsrc_t rWS, u32 vfl, u32 foff, u64 care, u32 tag) {
  for (u32 sp = 0; sp < SPIN_CAP; ++sp) {
    asm volatile("" ::: "memory");
    const u32 v = __builtin_amdgcn_raw_buffer_load_b32(rWS, vfl, foff, 16);
    if (!(__builtin_amdgcn_ballot_w64(v != tag) & care)) break;
  }
  VMW(0);
}
DI void raise_flag(rsrc_t rWS, u32 foff, u32 vtag, u32 vzero, u32 m1) {
  asm volatile("s_mov_b32 exec_hi, 0\n\ts_mov_b32 exec_lo, %4\n\tbuffer_store_dword %0, %1, %2, %3 offen sc1\n\ts_mov_b64 exec, -1" ::"v"(vtag), "v"(vzero), "s"(rWS), "s"(foff), "s"(m1) : "memory");
}
DI void arrive(u32 cnt_addr) {
  u32 a = cnt_addr, one = 1u;
  asm volatile("s_waitcnt lgkmcnt(0)\n\ts_mov_b64 exec, 1\n\tds_add_u32 %0, %1\n\ts_mov_b64 exec, -1" ::"v"(a), "v"(one) : "memory");
}
DI void await8(u32 cnt_addr) {
  u32 a = cnt_addr, t, sv;
  asm volatile("1:\n\tds_read_b32 %0, %2\n\ts_waitcnt lgkmcnt(0)\n\tv_readfirstlane_b32 %1, %0\n\ts_nop 0\n\ts_cmp_lt_u32 %1, 8\n\ts_cbranch_scc1 1b"
               : "=&v"(t), "=&s"(sv) : "v"(a) : "scc", "memory");
}
#define PIN(x) asm volatile("" : "+v"(x))
DI float qp(float r) { PIN(r); return r; }
DI u32 opq(u32 v) { asm volatile("" : "+v"(v)); return v; }

DI float sel(u64 m, float a, float b) { return __builtin_amdgcn_inverse_ballot_w64(m) ? a : b; }
DI v2u attend(v2u kp, v2u vp, v2u kc, v2u vc, v2u q, u64 m0, u64 m1, u32 hm, bool noprev) {
  const f32x4 z = {0.f, 0.f, 0.f, 0.f};
  const f32x4 sp = mfma16(kp, q, z);
  const f32x4 sc = mfma16(kc, q, z);
  const float K2 = 0.25f * 1.4426950408889634f;
  const u64 par = 0xaaaaaaaaaaaaaaaaull;
  float p0 = sel(par, sp[1], sp[0]), p1 = sel(par, sp[3], sp[2]);
  if (noprev) { p0 = -INFINITY; p1 = -INFINITY; }
  const float t0 = sel(m0, sel(par, sc[1], sc[0]), p0), t1 = sel(m1, sel(par, sc[3], sc[2]), p1);
  const float m = x32_max(x16_max(fmaxf(t0, t1)));
  const float mk = -m * K2;
  const float e0 = __builtin_amdgcn_exp2f(__builtin_fmaf(t0, K2, mk)), e1 = __builtin_amdgcn_exp2f(__builtin_fmaf(t1, K2, mk));
  const u32 w0 = pk(e0, e0) & hm, w1 = pk(e1, e1) & hm;
  v4u pf;
  pf.x = __builtin_amdgcn_inverse_ballot_w64(m0) ? 0u : w0; pf.y = __builtin_amdgcn_inverse_ballot_w64(m1) ? 0u : w1;
  pf.z = __builtin_amdgcn_inverse_ballot_w64(m0) ? w0 : 0u; pf.w = __builtin_amdgcn_inverse_ballot_w64(m1) ? w1 : 0u;
  const f32x4 o = mfma32(cat(vp, vc), pf, z);
  float sum = e0 + e1;
  sum = sw16_add(sum, sum); sum = sw32_add(sum, sum);
  const float inv = __builtin_amdgcn_rcpf(sum);
  return pk4(f32x4{qp(o.x * inv), qp(o.y * inv), qp(o.z * inv), qp(o.w * inv)});
}

template <int N> using IC = std::integral_constant<int, N>;
template <int I, int N, class F> DI void sfor(F&& f) { if constexpr (I < N) { f(IC<I>{}); sfor<I + 1, N>(f); } }

__global__ void __launch_bounds__(512, 2) fused_block(Params p) {
  extern __shared__ __attribute__((aligned(16))) char smem[];
  LAS char* L = (LAS char*)smem;
  const int tid = threadIdx.x, lane = tid & 63, w = __builtin_amdgcn_readfirstlane(tid >> 6);
  const int c16 = lane & 15, g4 = lane >> 4;
  const int b = blockIdx.x;
  int g, j;
  if (XMAP == 0) { g = (b & 7) * 4 + ((b >> 3) & 3); j = b >> 5; }
  else if (XMAP == 2) { const int x = b & 7, r = b >> 3; g = (x >> 1) * 8 + (r & 7); j = (x & 1) * 4 + (r >> 3); }
  else { const int x = b & 7, r = b >> 3; g = (x >> 2) * 16 + (r & 15); j = (x & 3) * 2 + (r >> 4); }
  const rsrc_t rX = __builtin_amdgcn_make_buffer_rsrc((void*)p.x, 0, S_ * D_ * 2, 0x00020000);
  const rsrc_t rQKV = __builtin_amdgcn_make_buffer_rsrc((void*)p.wqkv, 0, D_ * 3 * D_ * 2, 0x00020000);
  const rsrc_t rWO = __builtin_amdgcn_make_buffer_rsrc((void*)p.wo, 0, D_ * D_ * 2, 0x00020000);
  const rsrc_t rW1 = __builtin_amdgcn_make_buffer_rsrc((void*)p.w1, 0, D_ * F_ * 2, 0x00020000);
  const rsrc_t rW3 = __builtin_amdgcn_make_buffer_rsrc((void*)p.w3, 0, D_ * F_ * 2, 0x00020000);
  const rsrc_t rW2 = __builtin_amdgcn_make_buffer_rsrc((void*)p.w2, 0, F_ * D_ * 2, 0x00020000);
  const rsrc_t rC = __builtin_amdgcn_make_buffer_rsrc((void*)p.c, 0, D_ * 2, 0x00020000);
  const rsrc_t rMOD = __builtin_amdgcn_make_buffer_rsrc((void*)p.wmod, 0, D_ * 6 * D_ * 2, 0x00020000);
  const rsrc_t rWS = __builtin_amdgcn_make_buffer_rsrc((void*)p.ws, 0, WS_BYTES, 0x00020000);
  const rsrc_t rOUT = __builtin_amdgcn_make_buffer_rsrc((void*)p.out, 0, S_ * D_ * 2, 0x00020000);
  STAMP(0);

  u32 ep;
  { const u32 vz = 0; asm volatile("buffer_load_dword %0, %1, %2, 0 offen sc1" : "=v"(ep) : "v"(vz), "s"(rWS)); }
  u32 vl16 = lane * 16; PIN(vl16);
  const int bm = b < 192 ? b : b - 192, mq = bm / 48, cl = bm - 48 * mq;
  v4u wm[4]; v2u cv; u32 ep0;
  if (w < 4) {
    const u32 vm = (tid >> 3) * 24576 + (tid & 7) * 16, s0 = mq * (128 * 6144) + cl * 128, s1 = s0 + 6144, s2 = s0 + 12288, s3 = s0 + 18432;
    LD16(wm[0], vm, rMOD, s0, 0); LD16(wm[1], vm, rMOD, s1, 0); LD16(wm[2], vm, rMOD, s2, 0); LD16(wm[3], vm, rMOD, s3, 0);
    const u32 vc = (tid >> 3) * 8, scq = mq * 256;
    LD8(cv, vc, rC, scq, 0);
    const u32 vz = 0; asm volatile("buffer_load_dword %0, %1, %2, 0 offen sc1" : "=v"(ep0) : "v"(vz), "s"(rWS));
  }
  if (tid < 16) *(LAS u32*)(L + L_CNT + tid * 4) = 0u;
  BAR();
  const int st_qd = lane >> 2, st_r = (st_qd & 3) + 4 * (st_qd >> 3);
  const int st_col = ((st_qd >> 2) & 1) * 64 + (((lane & 3) ^ ((w & 1) << 1)) << 4);
  const int rotp = g & 7;
  u32 vB1 = st_r * 3072 + st_col; PIN(vB1);
  const u32 mB1 = (w >> 2) * 12288 + (w & 3) * 1024, sB1 = (w >> 2) * 98304 + (w & 3) * 24576 + j * 128;
  auto stage_b1 = [&](auto PC, auto BC) {
    constexpr int pair = decltype(PC)::value, base = decltype(BC)::value;
    const u32 sp = ((pair + rotp) & 7) * 196608 + sB1;
    dma<base, 0>(mB1, vB1, rQKV, sp); dma<base + 4096, 1024>(mB1, vB1, rQKV, sp); dma<base + 8192, 2048>(mB1, vB1, rQKV, sp);
  };
  auto b1_piece = [&](auto PC, auto BC, auto TC) {
    constexpr int pair = decltype(PC)::value, base = decltype(BC)::value, ty = decltype(TC)::value;
    dma<base + ty * 4096, ty * 1024>(mB1, vB1, rQKV, ((pair + rotp) & 7) * 196608 + sB1);
  };
  const int hd = w & 3, blk2 = w >> 2, kq = w & 3, mh = w >> 2;
  const int cs0 = j < 4 ? 22 * j : 88 + 21 * (j - 4);
  const int cend = cs0 + (j < 4 ? 22 : 21);
  v4u xr[10];
  const int rx2 = (0x4310 >> (4 * (j & 3))) & 7;
  auto load_x = [&](int i0, int i1) {
    const int r0 = 64 * g - 16 + w;
#pragma unroll
    for (int i = i0; i < i1; ++i) {
      int bp = (i >> 1) + rx2; bp = bp >= 5 ? bp - 5 : bp;
      const u32 so = (u32)max(r0 + 16 * bp + 8 * (i & 1), 0) * 1024u;
      LD16(xr[i], vl16, rX, so, 0);
    }
  };
  load_x(0, 5);
  STAMP(1);
  const int T0 = 12 * (g >> 4) + 3 * (w & 3), rcb = min(cs0 + T0, 168);
  if (w < 4) {
    asm volatile("s_waitcnt vmcnt(5)" : "+v"(cv), "+v"(wm[0]), "+v"(wm[1]), "+v"(wm[2]), "+v"(wm[3]), "+v"(ep0)::"memory");
    const u32 tg = __builtin_amdgcn_readfirstlane(ep0) + 1u;
    const f32x4 cf = {blo(cv.x), bhi(cv.x), blo(cv.y), bhi(cv.y)};
    float sc[4];
#pragma unroll
    for (int r = 0; r < 4; ++r) sc[r] = cf[r] * __builtin_amdgcn_rcpf(1.f + __builtin_amdgcn_exp2f(cf[r] * -1.4426950408889634f));
    f32x2 v[4];
#pragma unroll
    for (int i = 0; i < 4; ++i) v[i] = (unp(wm[0][i]) * sc[0] + unp(wm[1][i]) * sc[1]) + (unp(wm[2][i]) * sc[2] + unp(wm[3][i]) * sc[3]);
    const float a0 = sw32_add(v[0].x, v[2].x), a1 = sw32_add(v[0].y, v[2].y), a2 = sw32_add(v[1].x, v[3].x), a3 = sw32_add(v[1].y, v[3].y);
    float t0 = sw16_add(a0, a2), t1 = sw16_add(a1, a3);
    t0 += dppf<0x128>(t0); t1 += dppf<0x128>(t1);
    f32x2 o = {t0, t1};
    *(LAS f32x2*)(L + L_RED + w * 256 + (lane & 7) * 32 + g4 * 8) = o;
    LGW();
    u32 old = 0;
    if (lane == 0) old = __hip_atomic_fetch_add((LAS u32*)(L + L_CNT + 28), 1u, __ATOMIC_RELAXED, __HIP_MEMORY_SCOPE_WORKGROUP);
    if (__builtin_amdgcn_readfirstlane(old) == 3u) {
      asm volatile("" ::: "memory");
      float s = 0.f;
#pragma unroll
      for (int i = 0; i < 4; ++i) s += *(LAS float*)(L + L_RED + i * 256 + lane * 4);
      v2u gr; gr.x = asu(s); gr.y = tg;
      if (b < 192) __builtin_amdgcn_raw_buffer_store_b64(gr, rWS, lane * 8, WS_MODG + (mq * 3072 + cl * 64) * 8, 16);
    }
  }
  STAMP(2);
  load_x(5, 10);
  stage_b1(IC<0>{}, IC<L_XY>{});
  if (w == 6) touch(lane < 24 ? WS_FLAG + (lane >> 3) * 65536 + g * 2048 + (lane & 7) * 128
                              : lane < 28 ? WS_STAT + g * 4096 + j * 512 + (lane & 3) * 128 : lane == 28 ? WS_MODF + (1024 + 8 * b) * 8 : WS_RFLAG + j * 128, rWS, 0);
  int afo[4];
#pragma unroll
  for (int i = 0; i < 4; ++i) { afo[i] = blk2 * 32768 + c16 * 1024 + (((4 * i + g4) ^ c16) << 4); PIN(afo[i]); }
  const u64 am0 = __builtin_amdgcn_ballot_w64((c16 & 14) >= 4 * g4), am1 = __builtin_amdgcn_ballot_w64((c16 & 14) >= 4 * g4 + 2);
  u32 ahm = (lane & 1) ? 0xffff0000u : 0xffffu; PIN(ahm);
  u32 vOst = c16 * 16 + (2 * (hd & 1) + (g4 >> 1)) * 256 + (g4 & 1) * 16384; PIN(vOst);
  int tB1 = g4 * 1024 + 64 * (c16 >> 2) + 8 * (lane & 3) + 32 * ((hd & 1) ^ (g4 & 1)) + 256 * (hd >> 1); PIN(tB1);
  int hw0 = w * 1024 + ((((lane - 8 * rotp) & 63) ^ w) << 4); PIN(hw0);
#define vfl ((opq(vl16) & 112u) << 3)
  STAMP(3);
  v4u bw[48];
  auto read_b1 = [&](int pair, int base, int off) {
#pragma unroll
    for (int kk = 0; kk < 2; ++kk)
#pragma unroll
      for (int ty = 0; ty < 3; ++ty) {
        LAS char* a = L + base + (off + kk * 12288 + ty * 4096);
        bw[(2 * pair + kk) * 3 + ty] = cat(trd(a), trd(a + 512));
      }
  };
  f32x2 xn[10][4];
  v4u ga_[4]; v2u pv;
  const u32 sMG = WS_MODG + w * 1024;
  {
    float u[10];
    sfor<0, 10>([&](auto I) {
      constexpr int i = decltype(I)::value;
      if constexpr (i == 0) asm volatile("s_waitcnt vmcnt(11)" : "+v"(xr[0]), "+v"(xr[1]), "+v"(ep)::"memory");
      if constexpr (i == 2) asm volatile("s_waitcnt vmcnt(9)" : "+v"(xr[2]), "+v"(xr[3])::"memory");
      if constexpr (i == 4) asm volatile("s_waitcnt vmcnt(10)" : "+v"(xr[4]), "+v"(xr[5])::"memory");
      if constexpr (i == 6) asm volatile("s_waitcnt vmcnt(8)" : "+v"(xr[6]), "+v"(xr[7])::"memory");
      if constexpr (i == 8) asm volatile("s_waitcnt vmcnt(9)" : "+v"(xr[8]), "+v"(xr[9])::"memory");
      float s = 0.f, q = 0.f;
#pragma unroll
      for (int e = 0; e < 4; ++e) { s = dot2(xr[i][e], 0x3f803f80u, s); q = dot2(xr[i][e], xr[i][e], q); }
      u[i] = sw32_add(s, q);
      PIN(u[i]);
      if constexpr (i == 3) stage_b1(IC<1>{}, IC<L_XY + 24576>{});
      if constexpr (i == 7) stage_b1(IC<2>{}, IC<L_XY + 49152>{});
      if constexpr (i == 9) {
        const u32 s0 = sMG, s1 = sMG + 24576, s2 = sMG + 49152, s3 = sMG + 73728;
        ld16s<0>(ga_[0], vl16, rWS, s0); ld16s<0>(ga_[1], vl16, rWS, s1); ld16s<0>(ga_[2], vl16, rWS, s2); ld16s<0>(ga_[3], vl16, rWS, s3);
      }
    });
    float t[5];
#pragma unroll
    for (int i = 0; i < 5; ++i) t[i] = row16_sum(sw16_add(u[i], u[i + 5]));
    float v = t[0];
#pragma unroll
    for (int i = 1; i < 5; ++i) v = c16 == i ? t[i] : v;
    auto r = __builtin_amdgcn_permlane32_swap(asu(v), asu(v), false, false);
    const float mean = asf(r[0]) * (1.f / 512.f);
    const float var = fmaxf(asf(r[1]) * (1.f / 512.f) - mean * mean, 0.f);
    float rs = __builtin_amdgcn_rsqf(var + 1e-5f), nm = -mean * rs;
    PIN(rs); PIN(nm);
    STAMP(4);
    { const u32 vo = ((lane >> 3) & 3) * 24576 + (lane & 7) * 8, so = WS_MODG + (1024 + 8 * b) * 8; LD8S(pv, vo, rWS, so, 0); }
    if (w >= 4) {
      const int u = lane >> 2, row = (u & 3) | ((u >> 1) & 4) | ((u << 1) & 8);
      const u32 vo = row * 2752 + (lane & 3) * 16, so = (g & 15) * (32 * 2752) + rcb * 16, mb = L_Z + 49152 + (w & 3) * 4096;
      dma<0, 0>(mb, vo, rW1, so); dma<1024, 16 * 2752>(mb, vo, rW1, so); dma<2048, 0>(mb, vo, rW3, so); dma<3072, 16 * 2752>(mb, vo, rW3, so);
    } else {
      const int u = lane >> 1, row = (u & 3) | ((u >> 1) & 4) | ((u << 1) & 8) | (u & 16);
      const u32 vo = row * 1024 + (lane & 1) * 16, so = g * 32768 + j * 128 + w * 32, mb = L_Z + 65536 + w * 2048;
      dma<0, 0>(mb, vo, rW2, so); dma<1024, 0>(mb, vo, rW2, so + (g < 11 ? 32 * 32768 : 0));
      dma<0, 0>(76800 + w * 1024, vo, rWO, (g & 15) * 32768 + j * 128 + w * 32);
    }
    sfor<0, 10>([&](auto I) {
      constexpr int i = decltype(I)::value;
      if constexpr (i == 2) stage_b1(IC<3>{}, IC<L_Z>{});
      if constexpr (i == 6) stage_b1(IC<4>{}, IC<L_Z + 24576>{});
      const float a = rdl(rs, (i / 5) * 16 + i % 5), c = rdl(nm, (i / 5) * 16 + i % 5);
#pragma unroll
      for (int e = 0; e < 4; ++e) { xn[i][e] = unp(xr[i][e]) * a + c; PIN(xn[i][e]); }
    });
  }
  const u32 tag = __builtin_amdgcn_readfirstlane(ep) + 1u;
  u32 m1; asm("s_lshr_b32 %0, 1, %1" : "=s"(m1) : "s"(w) : "scc");
#define RAISE(fo) raise_flag(rWS, (fo), tag, vl16, m1)
  STAMP(5);
  asm volatile("s_waitcnt vmcnt(9)" : "+v"(ga_[0]), "+v"(ga_[1]), "+v"(ga_[2]), "+v"(ga_[3]), "+v"(pv)::"memory");
  auto gsum = [&](v4u (&a)[4], f32x2& o) -> u32 {
    o.x = (asf(a[0].x) + asf(a[1].x)) + (asf(a[2].x) + asf(a[3].x)); o.y = (asf(a[0].z) + asf(a[1].z)) + (asf(a[2].z) + asf(a[3].z));
    return ((a[0].y ^ tag) | (a[0].w ^ tag) | (a[1].y ^ tag) | (a[1].w ^ tag)) | ((a[2].y ^ tag) | (a[2].w ^ tag) | (a[3].y ^ tag) | (a[3].w ^ tag));
  };
  auto gather = [&](u32 voff, u32 soff, f32x2& o) {
    for (u32 sp = 0; sp < SPIN_CAP; ++sp) {
      asm volatile("" ::: "memory");
      v4u a[4];
#pragma unroll
      for (int q = 0; q < 4; ++q) a[q] = __builtin_amdgcn_raw_buffer_load_b128(rWS, voff, soff + q * 24576, 16);
      if (!__builtin_amdgcn_ballot_w64(gsum(a, o) != 0u)) break;
    }
  };
  const float one = w >= 4 ? 1.f : 0.f;
  {
    f32x2 mA;
    if (__builtin_amdgcn_ballot_w64(gsum(ga_, mA) != 0u)) gather(vl16, sMG, mA);
    *(LAS f32x2*)(L + L_MODA + w * 512 + (vl16 >> 1)) = mA + one;
  }
  BAR();
  if (w == 7) {
    const u32 vo = ((lane >> 3) & 3) * 24576 + (lane & 7) * 8, so = WS_MODG + (1024 + 8 * b) * 8;
    for (u32 sp = 0; sp < SPIN_CAP && __builtin_amdgcn_ballot_w64(pv.y != tag); ++sp) {
      asm volatile("" ::: "memory");
      pv = __builtin_amdgcn_raw_buffer_load_b64(rWS, vo, so, 16);
    }
    float sv = asf(pv.x);
    sv += dppf<0x128>(sv); sv = sw16_add(sv, sv);
    pv.x = asu(sv);
    __builtin_amdgcn_raw_buffer_store_b64(pv, rWS, (lane & 7) * 8, WS_MODF + (1024 + 8 * b) * 8, 16);
  }
  STAMP(6);
  int vma = L_MODA + 2 * vl16; PIN(vma);
  const f32x4 ms0 = *(LAS f32x4*)(L + vma), ms1 = *(LAS f32x4*)(L + vma + 16);
  const f32x4 ma0 = *(LAS f32x4*)(L + vma + 2048), ma1 = *(LAS f32x4*)(L + vma + 2064);
  int tz0 = tB1 + L_Z; PIN(tz0);
  { int t2 = tB1 + 49152; PIN(t2); read_b1(0, tB1, 0); read_b1(1, tB1, 24576); read_b1(2, t2, 0); }
  {
    VMW(6);
    if (w >= 4) {
      LAS char* a0 = L + L_Z + 49152 + (w & 3) * 4096 + ((lane >> 1) & 1) * 2048 + 64 * ((c16 >> 2) + 4 * (g4 & 1) + 16 * (g4 >> 1)) + 8 * (lane & 1);
#pragma unroll
      for (int ct = 0; ct < 3; ++ct) {
        const int c = min(cs0 + T0 + ct - rcb, 3) * 16;
        const v4u f = cat(trd(a0 + c), trd(a0 + c + 512));
        const u32 sf = WS_B4T + ((j * 16 + (g & 15)) * 24 + T0 + ct) * 1024;
        asm volatile("buffer_store_dwordx4 %0, %1, %2, %3 offen sc1\n\ts_nop 1" ::"v"(f), "v"(vl16), "s"(rWS), "s"(sf) : "memory");
      }
    }
    else {
      LAS char* a0 = L + L_Z + 65536 + w * 2048 + 32 * ((c16 >> 2) + 4 * (g4 & 1) + 16 * (g4 >> 1)) + 8 * (lane & 3);
#pragma unroll
      for (int i = 0; i < 2; ++i) {
        const v4u f = cat(trd(a0 + i * 1024), trd(a0 + i * 1024 + 256));
        const u32 sf = WS_B5T + ((j * 43 + g + (i && g < 11 ? 32 : 0)) * 4 + w) * 1024;
        asm volatile("buffer_store_dwordx4 %0, %1, %2, %3 offen sc1\n\ts_nop 1" ::"v"(f), "v"(vl16), "s"(rWS), "s"(sf) : "memory");
      }
      LAS char* a1 = L + 76800 + w * 1024 + 32 * ((c16 >> 2) + 4 * (g4 & 1) + 16 * (g4 >> 1)) + 8 * (lane & 3);
      const v4u f = cat(trd(a1), trd(a1 + 256));
      const u32 sf = WS_B3T + ((j * 16 + (g & 15)) * 4 + w) * 1024;
      asm volatile("buffer_store_dwordx4 %0, %1, %2, %3 offen sc1\n\ts_nop 1" ::"v"(f), "v"(vl16), "s"(rWS), "s"(sf) : "memory");
    }
  }
  BAR();
  STAMP(7);
  {
    int hw1 = (hw0 ^ 128) + 8192; PIN(hw1);
    sfor<0, 10>([&](auto I) {
      constexpr int i = decltype(I)::value;
      v4u o;
      o.x = pk2(xn[i][0] * ma0.xy + ms0.xy); o.y = pk2(xn[i][1] * ma0.zw + ms0.zw);
      o.z = pk2(xn[i][2] * ma1.xy + ms1.xy); o.w = pk2(xn[i][3] * ma1.zw + ms1.zw);
      int bp = (i >> 1) + rx2; bp = bp >= 5 ? bp - 5 : bp;
      *(LAS v4u*)(L + ((i & 1) ? hw1 : hw0) + bp * 16384) = o;
      SCHED();
    });
  }
  VMW(0);
  BAR();
  RAISE(WS_RFLAG + (j * 32 + g) * 4);
  u32 flr;
  STAMP(8);
  __builtin_amdgcn_s_setprio(2);

  const f32x4 z4 = {0.f, 0.f, 0.f, 0.f};
#define AFRAG(off, ks) (*(LAS v4u*)(L + afo[(ks) & 3] + ((off) + ((ks) >> 2) * 256)))
  const int ch0 = cs0 + 3 * w;
  v4u b4[48];
  const u32 sB4 = WS_B4T + (j * 16 * 24 + 3 * w) * 1024;
  auto load_b4 = [&](auto NC) {
    constexpr int n = decltype(NC)::value, ks = n / 3, ct = n % 3;
    const u32 so = sB4 + ks * 24576;
    v4u& r = b4[n];
    if constexpr (ct == 0) LD16S(r, vl16, rWS, so, 0); else if constexpr (ct == 1) LD16S(r, vl16, rWS, so, 1024); else LD16S(r, vl16, rWS, so, 2048);
  };
  auto landed_b4 = [&](auto AC, auto BC) {
#pragma unroll
    for (int i = decltype(AC)::value; i < decltype(BC)::value; ++i) { v4u& r = b4[i]; PIN(r); }
  };
  v4u b3[16];
  auto load_b3 = [&](auto NC) { constexpr int n = decltype(NC)::value; ld16s<(n & 3) * 1024>(b3[n], vl16, rWS, WS_B3T + (j * 16 + 4 * kq + (n >> 2)) * 4096); };
  v4u gb_, gg_;
  u32 vgg, fl0, vRow;
  int pw, pr;
  f32x4 aKh = z4, aVh = z4, aQ = z4, aK = z4, aV = z4, bQ = z4, bK = z4, bV = z4;
  {
    v4u ah = AFRAG(0, 0), a1 = AFRAG(16384, 0), a2 = AFRAG(32768, 0);
    int l3, c3, g3; f32x2 mB, mG;
    auto job = [&](auto NC) {
      constexpr int n = decltype(NC)::value;
      if constexpr (n == 3) { l3 = opq(vl16) >> 4; c3 = l3 & 15; g3 = l3 >> 4; PIN(c3); PIN(g3); vgg = (vl16 < 512 ? 8192 : 20480 - 512) + 512 * j + vl16; PIN(vgg); }
      if constexpr (n == 7) { vRow = (8 * w + (l3 >> 3)) * 1024 + (l3 & 7) * 16; PIN(vRow); }
      if constexpr (n == 8) { pr = (8 * w + (l3 >> 3)) * 256 + (((2 * (l3 & 7)) ^ ((l3 >> 3) & 7)) << 4); PIN(pr); }
      if constexpr (n == 10) { pw = kq * 16384 + (32 * mh + c3) * 256 + ((g3 ^ (c3 & 3)) << 4) + ((c3 & 4) << 4); PIN(pw); }
      if constexpr (n == 31) { mB.x = asf(gb_.x) + one; PIN(mB.x); mB.y = asf(gb_.z) + one; PIN(mB.y); *(LAS f32x2*)(L + L_MODA + w * 512 + (vl16 >> 1)) = mB; }
      if constexpr (n == 32) { mG.x = asf(gg_.x); mG.y = asf(gg_.z); *(LAS f32x2*)(L + L_GAB + (vl16 >> 1)) = mG; }
      if constexpr (n == 34) { fl0 = (gb_.y ^ tag) | (gb_.w ^ tag) | (gg_.y ^ tag) | (gg_.w ^ tag); PIN(fl0); }
      if constexpr (n == 7) { const u32 s0 = WS_MODF + 12288 + w * 1024; LD16S(gb_, vl16, rWS, s0, 0); }
      if constexpr (n == 9) { const u32 s0 = WS_MODF; LD16S(gg_, vgg, rWS, s0, 0); }
      if constexpr (n < 6 && !(n & 1)) b1_piece(IC<5>{}, IC<L_Z + 49152>{}, IC<n / 2>{});
      if constexpr (n == 6) arrive(L_CNT + 8);
      if constexpr (n == 10) await8(L_CNT + 8);
      if constexpr (n >= 10 && n < 22 && !(n & 1)) b1_piece(IC<6 + (n - 10) / 6>{}, IC<L_Z + ((n - 10) / 6) * 24576>{}, IC<((n - 10) / 2) % 3>{});
      if constexpr (n == 21) { const u32 vr = (vl16 >> 2) & 124, sr = WS_RFLAG + j * 128; LD4S(flr, vr, rWS, sr); }
      if constexpr (n == 26) {
        asm volatile("s_waitcnt vmcnt(0)" : "+v"(gb_), "+v"(gg_), "+v"(flr)::"memory");
        arrive(L_CNT + 12);
        const u32 vr = (vl16 >> 2) & 124, sr = WS_RFLAG + j * 128; u32 cnt = SPIN_CAP;
        asm volatile("1:\n\tv_cmp_ne_u32 vcc, %5, %0\n\ts_cbranch_vccz 2f\n\tbuffer_load_dword %0, %2, %3, %4 offen sc1\n\ts_waitcnt vmcnt(0)\n\t"
                     "s_sub_u32 %1, %1, 1\n\ts_cbranch_scc0 1b\n\t2:" : "+v"(flr), "+s"(cnt) : "v"(vr), "s"(rWS), "s"(sr), "s"(tag) : "vcc", "scc", "memory");
      }
      if constexpr (n == 28) { await8(L_CNT + 12); int tz2 = tz0 + 49152; PIN(tz2); read_b1(5, tz2, 0); }
      if constexpr (n == 32) read_b1(6, tz0, 0);
      if constexpr (n == 37) read_b1(7, tz0, 24576);
      if constexpr (n >= 27 && n < 47 && (n & 1)) load_b3(IC<(n - 27) / 2>{});
    };
    sfor<0, 16>([&](auto KS) {
      constexpr int ks = decltype(KS)::value;
      KSTAMP(1, ks);
      if constexpr (ks == 0) read_b1(3, tz0, 0);
      if constexpr (ks == 1) read_b1(4, tz0, 24576);
      SCHED();
      v4u nh = ah, n1 = a1, n2 = a2;
      aKh = mfma32(bw[ks * 3 + 1], ah, aKh);
      aVh = mfma32(ah, bw[ks * 3 + 2], aVh);
      if constexpr (ks < 15) nh = AFRAG(0, ks + 1);
      job(IC<3 * ks>{});
      SCHED();
      aQ = mfma32(bw[ks * 3 + 0], a1, aQ);
      aK = mfma32(bw[ks * 3 + 1], a1, aK);
      aV = mfma32(a1, bw[ks * 3 + 2], aV);
      if constexpr (ks < 15) n1 = AFRAG(16384, ks + 1);
      job(IC<3 * ks + 1>{});
      SCHED();
      bQ = mfma32(bw[ks * 3 + 0], a2, bQ);
      bK = mfma32(bw[ks * 3 + 1], a2, bK);
      bV = mfma32(a2, bw[ks * 3 + 2], bV);
      if constexpr (ks < 15) n2 = AFRAG(32768, ks + 1);
      job(IC<3 * ks + 2>{});
      ah = nh; a1 = n1; a2 = n2;
      SCHED();
    });
  }
  __builtin_amdgcn_s_setprio(0);
  STAMP(9); KSTAMP(1, 16);
  {
    const u32 sOst = WS_O + ((g * 4 + 2 * blk2) * 16 + 2 * j + (hd >> 1)) * 1024;
    const v2u k1p = pk4(aK), v1p = pk4(aV);
    const v2u o0 = attend(pk4(aKh), pk4(aVh), k1p, v1p, pk4(aQ), am0, am1, ahm, g == 0 && blk2 == 0);
    const v2u o1 = attend(k1p, v1p, pk4(bK), pk4(bV), pk4(bQ), am0, am1, ahm, false);
    const auto sx = __builtin_amdgcn_permlane16_swap(o0.x, o1.x, false, false), sy = __builtin_amdgcn_permlane16_swap(o0.y, o1.y, false, false);
    const v4u ov = {sx[0], sy[0], sx[1], sy[1]};
    asm volatile("buffer_store_dwordx4 %0, %1, %2, %3 offen sc1\n\ts_nop 1" ::"v"(ov), "v"(vOst), "s"(rWS), "s"(sOst) : "memory");
  }
  if (__builtin_amdgcn_ballot_w64(fl0 != 0u)) {
    for (u32 sp = 0; sp < SPIN_CAP; ++sp) {
      asm volatile("" ::: "memory");
      gb_ = __builtin_amdgcn_raw_buffer_load_b128(rWS, vl16, WS_MODF + 12288 + w * 1024, 16);
      gg_ = __builtin_amdgcn_raw_buffer_load_b128(rWS, vgg, WS_MODF, 16);
      if (!__builtin_amdgcn_ballot_w64(((gb_.y ^ tag) | (gb_.w ^ tag) | (gg_.y ^ tag) | (gg_.w ^ tag)) != 0u)) break;
    }
    const f32x2 mB = {asf(gb_.x) + one, asf(gb_.z) + one}, mG = {asf(gg_.x), asf(gg_.z)};
    *(LAS f32x2*)(L + L_MODA + w * 512 + (vl16 >> 1)) = mB;
    *(LAS f32x2*)(L + L_GAB + (vl16 >> 1)) = mG;
  }
  STAMP(10);
  VMW(0);
  BAR();
  RAISE(flag_off(0, g, 0) + j * 128);
  STAMP(11);
  if (b == 0 && w == 7) {
    for (u32 sp = 0; sp < SPIN_CAP; ++sp) {
      asm volatile("" ::: "memory");
      const v4u v = __builtin_amdgcn_raw_buffer_load_b128(rWS, vl16, WS_RFLAG, 16);
      if (__all(v.x == tag && v.y == tag && v.z == tag && v.w == tag)) break;
    }
    __builtin_amdgcn_raw_buffer_store_b32(tag, rWS, 0, WS_EPOCH, 16);
  }

  v4u xv;
  { const u32 sx = g * 65536 + j * 128; LD16(xv, vRow, rX, sx, 0); }
  sfor<10, 16>(load_b3);
  const u64 care2 = 0x0101010101010101ull * (3u << (2 * kq));
  poll_flags(rWS, vfl, flag_off(0, g, 0), care2, tag);
  PIN(xv);
  v4u af[8];
  {
    const u32 so = WS_O + ((g * 4 + 2 * mh) * 16 + 4 * kq) * 1024, so1 = so + 16384;
    LD16S(af[0], vl16, rWS, so, 0); LD16S(af[1], vl16, rWS, so, 1024); LD16S(af[2], vl16, rWS, so, 2048); LD16S(af[3], vl16, rWS, so, 3072);
    LD16S(af[4], vl16, rWS, so1, 0); LD16S(af[5], vl16, rWS, so1, 1024); LD16S(af[6], vl16, rWS, so1, 2048); LD16S(af[7], vl16, rWS, so1, 3072);
  }
  STAMP(12);
  sfor<0, 2>([&](auto M) {
    constexpr int m = decltype(M)::value;
    asm volatile("s_waitcnt vmcnt(%c4)" : "+v"(af[4 * m]), "+v"(af[4 * m + 1]), "+v"(af[4 * m + 2]), "+v"(af[4 * m + 3]) : "i"(m == 0 ? 4 : 2) : "memory");
    if constexpr (m == 0) {
#pragma unroll
      for (int i = 0; i < 16; ++i) PIN(b3[i]);
    }
    f32x4 acc[4] = {z4, z4, z4, z4};
    sfor<0, 4>([&](auto SC) {
      constexpr int sx = decltype(SC)::value;
#pragma unroll
      for (int n = 0; n < 4; ++n) acc[n] = mfma32(b3[sx * 4 + n], af[4 * m + sx], acc[n]);
      if constexpr (sx & 1) load_b4(IC<2 * m + (sx >> 1)>{});
      SCHED();
    });
#pragma unroll
    for (int n = 0; n < 4; ++n) *(LAS f32x4*)(L + ((n & 1) ? (pw ^ 64) : pw) + (m * 4096 + (n >> 1) * 128)) = acc[n];
  });
  BAR();
  STAMP(13);
  v4u x1p;
  {
    f32x4 y0 = *(LAS f32x4*)(L + pr), y1 = *(LAS f32x4*)(L + (pr ^ 16));
#pragma unroll
    for (int q = 1; q < 4; ++q) { y0 += *(LAS f32x4*)(L + pr + q * 16384); y1 += *(LAS f32x4*)(L + (pr ^ 16) + q * 16384); }
    const int ga = L_GAB + ((vl16 & 112) << 1);
    const f32x4 g0 = *(LAS f32x4*)(L + ga), g1 = *(LAS f32x4*)(L + ga + 16);
    const f32x4 x0 = f32x4{blo(xv.x), bhi(xv.x), blo(xv.y), bhi(xv.y)} + g0 * y0, x1 = f32x4{blo(xv.z), bhi(xv.z), blo(xv.w), bhi(xv.w)} + g1 * y1;
    { const v4u o = cat(pk4(x0), pk4(x1)); x1p = o; const u32 so = WS_X1 + g * 65536 + j * 128;
      asm volatile("buffer_store_dwordx4 %0, %1, %2, %3 offen sc1\n\ts_nop 1" ::"v"(o), "v"(vRow), "s"(rWS), "s"(so) : "memory"); }
    const f32x4 sv = x0 + x1;
    float mean = (sv.x + sv.y) + (sv.z + sv.w);
    mean += dppf<0xB1>(mean); mean += dppf<0x4E>(mean); mean += dppf<0x141>(mean);
    mean *= 1.f / 64.f;
    const f32x4 d0 = x0 - mean, d1 = x1 - mean, dd = d0 * d0 + d1 * d1;
    float m2 = (dd.x + dd.y) + (dd.z + dd.w);
    m2 += dppf<0xB1>(m2); m2 += dppf<0x4E>(m2); m2 += dppf<0x141>(m2);
    v2u st; st.x = asu(mean); st.y = asu(m2);
    __builtin_amdgcn_raw_buffer_store_b64(st, rWS, (vRow >> 10) << 3, WS_STAT + g * 4096 + j * 512, 16);
  }
  STAMP(14);
  VMW(0);
  BAR();
  RAISE(flag_off(1, g, 0) + j * 128);
  sfor<4, 9>(load_b4);
  STAMP(15);

  u32 hmask[3];
#pragma unroll
  for (int ct = 0; ct < 3; ++ct)
    asm("s_cmp_lt_u32 %1, %2\n\ts_cselect_b32 %0, -1, 0" : "=s"(hmask[ct]) : "s"((u32)(ch0 + ct)), "s"((u32)cend) : "scc");
#pragma unroll
  for (int i = 0; i < 4; ++i) { afo[i] -= blk2 * 32768; PIN(afo[i]); }
  int vm2 = L_MODA + 2 * vl16; PIN(vm2);
  int h2w = w * 8192 + (vl16 ^ ((8 * w & 15) << 4)); PIN(h2w);
  const f32x4 ls0 = *(LAS f32x4*)(L + vm2), ls1 = *(LAS f32x4*)(L + vm2 + 16), la0 = *(LAS f32x4*)(L + vm2 + 2048), la1 = *(LAS f32x4*)(L + vm2 + 2064);
  poll_flags(rWS, vfl, flag_off(1, g, 0), ~0ull, tag);
  landed_b4(IC<0>{}, IC<9>{});
  {
    v4u x1v[8]; v2u stv;
    const u32 so = WS_X1 + g * 65536 + w * 8192, so2 = so + 4096;
    { const u32 vs = ((vl16 & 112) << 5) + ((vl16 >> 7) << 3), ss = WS_STAT + g * 4096 + w * 64; LD8S(stv, vs, rWS, ss, 0); }
    LD16S(x1v[0], vl16, rWS, so, 0); LD16S(x1v[1], vl16, rWS, so, 1024); LD16S(x1v[2], vl16, rWS, so, 2048); LD16S(x1v[3], vl16, rWS, so, 3072);
    LD16S(x1v[4], vl16, rWS, so2, 0); LD16S(x1v[5], vl16, rWS, so2, 1024); LD16S(x1v[6], vl16, rWS, so2, 2048); LD16S(x1v[7], vl16, rWS, so2, 3072);
    sfor<9, 12>(load_b4);
    asm volatile("s_waitcnt vmcnt(11)" : "+v"(stv)::"memory");
    const float mj = asf(stv.x);
    float mean = mj;
    mean += dppf<0xB1>(mean); mean += dppf<0x4E>(mean); mean += dppf<0x141>(mean);
    mean *= 0.125f;
    const float dm = mj - mean;
    float m2 = asf(stv.y) + 64.f * dm * dm;
    m2 += dppf<0xB1>(m2); m2 += dppf<0x4E>(m2); m2 += dppf<0x141>(m2);
    const float l2r = __builtin_amdgcn_rsqf(m2 * (1.f / 512.f) + 1e-5f), l2n = -mean * l2r;
    sfor<0, 8>([&](auto I) {
      constexpr int i = decltype(I)::value;
      v4u& xx = x1v[i];
      asm volatile("s_waitcnt vmcnt(%c1)" : "+v"(xx) : "i"(10 - i) : "memory");
      const float rs = rdl(l2r, 8 * i), nn = rdl(l2n, 8 * i);
      v4u o;
      o.x = pk2((unp(xx.x) * rs + nn) * la0.xy + ls0.xy); o.y = pk2((unp(xx.y) * rs + nn) * la0.zw + ls0.zw);
      o.z = pk2((unp(xx.z) * rs + nn) * la1.xy + ls1.xy); o.w = pk2((unp(xx.w) * rs + nn) * la1.zw + ls1.zw);
      *(LAS v4u*)(L + (h2w ^ (i << 4)) + i * 1024) = o;
    });
  }
  BAR();
  STAMP(16);
  __builtin_amdgcn_s_setprio(2);
  const int kb5 = 5 * w + min(w, 3);
  u64 care5 = 0x0101010101010101ull * ((0x80c06030180c0603ull >> (8 * w)) & 255u);
  asm volatile("" : "+s"(care5));
  v4u b5[24];
  const u32 sB5 = WS_B5T + (j * 43 + kb5) * 4096, d5 = (w + 5) >> 3;
  auto load_b5 = [&](auto NC) {
    constexpr int n = decltype(NC)::value, sx = n / 4, t = n % 4;
    ld16s<t * 1024>(b5[n], vl16, rWS, sB5 + (sx == 5 ? (5 - d5) * 4096 : sx * 4096));
  };
  f32x4 acc4[3][4];
#pragma unroll
  for (int ct = 0; ct < 3; ++ct)
#pragma unroll
    for (int m = 0; m < 4; ++m) acc4[ct][m] = z4;
  u32 vH; int pw5;
  {
    v4u a[4] = {AFRAG(0, 0), AFRAG(16384, 0), AFRAG(32768, 0), AFRAG(49152, 0)};
    sfor<0, 16>([&](auto KS) {
      constexpr int ks = decltype(KS)::value;
      KSTAMP(3, ks);
      if constexpr (ks >= 3 && ks < 13) { VMW(9); landed_b4(IC<3 * ks>{}, IC<3 * ks + 3>{}); }
      if constexpr (ks >= 13) { VMW(9); landed_b4(IC<3 * ks>{}, IC<3 * ks + 3>{}); }
      v4u nx[4] = {a[0], a[1], a[2], a[3]};
      sfor<0, 4>([&](auto MC) {
        constexpr int m = decltype(MC)::value;
        if constexpr (ks < 15) nx[m] = AFRAG(16384 * m, ks + 1);
        if constexpr (m < 3) { if constexpr (ks < 12) load_b4(IC<12 + 3 * ks + m>{}); else load_b5(IC<3 * (ks - 12) + m>{}); }
        SCHED();
#pragma unroll
        for (int ct = 0; ct < 3; ++ct) acc4[ct][m] = mfma32(b4[ks * 3 + ct], a[m], acc4[ct][m]);
        if constexpr (ks == 8 && m == 0) { vH = (vl16 & 255) + (((vl16 >> 7) & 2) + (vl16 >> 9)) * (172 * 256); PIN(vH); }
        if constexpr (ks == 9 && m == 0) { const int l5 = opq(vl16) >> 4, c5 = l5 & 15, g5 = l5 >> 4;
          pw5 = w * 16384 + c5 * 256 + ((c5 & 4) << 4) + ((g5 ^ (c5 & 3)) << 4); PIN(pw5); }
        SCHED();
      });
#pragma unroll
      for (int m = 0; m < 4; ++m) a[m] = nx[m];
    });
  }
#pragma unroll
  for (int ct = 0; ct < 3; ++ct)
#pragma unroll
    for (int m = 0; m < 4; ++m) PIN(acc4[ct][m]);
  __builtin_amdgcn_s_setprio(0);
  STAMP(17); KSTAMP(3, 16);
#pragma unroll
  for (int ct = 0; ct < 3; ++ct) {
    v2u o[2];
#pragma unroll
    for (int mp = 0; mp < 2; ++mp) {
      f32x4 G, U, R;
#pragma unroll
      for (int e = 0; e < 4; ++e) {
        auto r = __builtin_amdgcn_permlane32_swap(asu(acc4[ct][2 * mp][e]), asu(acc4[ct][2 * mp + 1][e]), false, false);
        G[e] = asf(r[0]); U[e] = asf(r[1]);
      }
#pragma unroll
      for (int e = 0; e < 4; ++e) R[e] = qp(qp(G[e] * U[e]) * __builtin_amdgcn_rcpf(1.f + __builtin_amdgcn_exp2f(qp(G[e] * -1.4426950408889634f))));
      o[mp] = pk4(R);
    }
    const auto sx = __builtin_amdgcn_permlane16_swap(o[0].x, o[1].x, false, false), sy = __builtin_amdgcn_permlane16_swap(o[0].y, o[1].y, false, false);
    const v4u ov = {sx[0], sy[0], sx[1], sy[1]};
    const u32 so = WS_HH + (g * 4 * 172 + ch0 + ct) * 256;
    asm volatile("s_mov_b32 exec_lo, %4\n\ts_mov_b32 exec_hi, %4\n\tbuffer_store_dwordx4 %0, %1, %2, %3 offen sc1\n\ts_mov_b64 exec, -1\n\ts_nop 0"
                 ::"v"(ov), "v"(vH), "s"(rWS), "s"(so), "s"(hmask[ct]) : "memory");
  }
  STAMP(18);
  sfor<12, 16>(load_b5);
  VMW(4);
  BAR();
  RAISE(flag_off(2, g, 0) + j * 128);
  STAMP(19);
  sfor<16, 22>(load_b5);
  poll_flags(rWS, vfl, flag_off(2, g, 0), care5, tag);
#pragma unroll
  for (int i = 0; i < 22; ++i) PIN(b5[i]);
  v4u hf[16];
  auto load_h = [&](auto NC) {
    constexpr int n = decltype(NC)::value, sx = n / 4, m = n % 4;
    ld16s<0>(hf[n & 15], vl16, rWS, WS_HH + ((g * 4 + m) * 172 + 4 * kb5) * 256 + (sx == 5 ? (5 - d5) * 1024 : sx * 1024));
  };
  sfor<0, 16>(load_h);
  sfor<22, 24>(load_b5);
  STAMP(20);
  {
    f32x4 acc[4][4];
#pragma unroll
    for (int m = 0; m < 4; ++m)
#pragma unroll
      for (int n = 0; n < 4; ++n) acc[m][n] = z4;
    sfor<0, 6>([&](auto SC) {
      constexpr int sx = decltype(SC)::value, r = (sx & 3) * 4;
      constexpr int nw = sx == 0 ? 14 : sx == 1 ? 14 : sx == 2 ? 14 : sx == 3 ? 10 : sx == 4 ? 4 : 0;
      asm volatile("s_waitcnt vmcnt(%c4)" : "+v"(hf[r]), "+v"(hf[r + 1]), "+v"(hf[r + 2]), "+v"(hf[r + 3]) : "i"(nw) : "memory");
      if constexpr (sx == 4) { PIN(b5[22]); PIN(b5[23]); }
      if constexpr (sx == 5) { if (w >= 3) { hf[r] = v4u{0u, 0u, 0u, 0u}; hf[r + 1] = v4u{0u, 0u, 0u, 0u}; hf[r + 2] = v4u{0u, 0u, 0u, 0u}; hf[r + 3] = v4u{0u, 0u, 0u, 0u}; } }
      sfor<0, 4>([&](auto MC) {
        constexpr int m = decltype(MC)::value;
#pragma unroll
        for (int n = 0; n < 4; ++n) acc[m][n] = mfma32(b5[sx * 4 + n], hf[r + m], acc[m][n]);
        SCHED();
        if constexpr (sx < 2) load_h(IC<16 + 4 * sx + m>{});
        SCHED();
      });
    });
#pragma unroll
    for (int m = 0; m < 4; ++m)
#pragma unroll
      for (int n = 0; n < 4; ++n) *(LAS f32x4*)(L + ((n & 1) ? (pw5 ^ 64) : pw5) + (m * 4096 + (n >> 1) * 128)) = acc[m][n];
  }
  BAR();
  STAMP(21);
  {
    const int l6 = opq(vl16) >> 4;
    const int p0 = opq((8 * w + (l6 >> 3)) * 256 + (((2 * (l6 & 7)) ^ ((l6 >> 3) & 7)) << 4)), p1 = opq(p0 + 65536);
    f32x4 y0 = *(LAS f32x4*)(L + p0), y1 = *(LAS f32x4*)(L + (p0 ^ 16));
#pragma unroll
    for (int q = 1; q < 8; ++q) { const int pb = q < 4 ? p0 : p1; y0 += *(LAS f32x4*)(L + pb + (q & 3) * 16384); y1 += *(LAS f32x4*)(L + (pb ^ 16) + (q & 3) * 16384); }
    const int gb = L_GAB + 256 + (l6 & 7) * 32;
    const f32x4 g0 = *(LAS f32x4*)(L + gb), g1 = *(LAS f32x4*)(L + gb + 16);
    const f32x4 o0 = f32x4{blo(x1p.x), bhi(x1p.x), blo(x1p.y), bhi(x1p.y)} + g0 * y0, o1 = f32x4{blo(x1p.z), bhi(x1p.z), blo(x1p.w), bhi(x1p.w)} + g1 * y1;
    const v4u o = cat(pk4(o0), pk4(o1));
    const u32 vo = (8 * w + (l6 >> 3)) * 1024 + (l6 & 7) * 16, so = g * 65536 + j * 128;
    asm volatile("buffer_store_dwordx4 %0, %1, %2, %3 offen sc1\n\ts_nop 1" ::"v"(o), "v"(vo), "s"(rOUT), "s"(so) : "memory");
  }
  STAMP(22);
  STAMP_DUMP();
}
}

extern "C" void kernel_launch(void* const* d_in, const int* in_sizes, int n_in, void* d_out, int out_size, void* d_ws, size_t ws_size,
                              hipStream_t stream) {
  Params p;
  p.x = (const u16*)d_in[0]; p.wqkv = (const u16*)d_in[1]; p.wo = (const u16*)d_in[2];
  p.w1 = (const u16*)d_in[4]; p.w3 = (const u16*)d_in[5]; p.w2 = (const u16*)d_in[6];
  p.c = (const u16*)d_in[7]; p.wmod = (const u16*)d_in[8];
  p.out = (u16*)d_out; p.ws = (char*)d_ws;
  (void)hipFuncSetAttribute((const void*)fused_block, hipFuncAttributeMaxDynamicSharedMemorySize, L_TOTAL);
  fused_block<<<256, 512, L_TOTAL, stream>>>(p);
}
```
